```python
import torch

def _softmax(x, axis=-1):
    return torch.softmax(x, dim=axis)
def _gelu(x):
    return torch.nn.functional.gelu(x, approximate="tanh")
def _silu(x):
    return torch.nn.functional.silu(x)
def _sigmoid(x):
    return torch.sigmoid(x)
def _layernorm(x, axis=-1, eps=1e-5):
    m = x.mean(dim=axis, keepdim=True)
    v = x.var(dim=axis, keepdim=True, unbiased=False)
    return (x - m) / torch.sqrt(v + eps)
def _scatter_add(tgt, idx, src, axis=0):
    return tgt.clone().index_add_(axis, idx, src)
def _segment_sum(src, seg, n):
    out = torch.zeros((n,) + src.shape[1:], dtype=src.dtype)
    return out.index_add_(0, seg, src)
def _pad(x, pw):
    flat = tuple(v for pair in reversed(pw) for v in pair)
    return torch.nn.functional.pad(x, flat)

kernel_name = 'standalone__gqa_softmax__near_ridge'

def setup_inputs():

    _w = (lambda a: (lambda t: t.to(torch.bfloat16) if t.is_floating_point() else t)(torch.as_tensor(a)))
    B, Hq, Hkv, Sq, Sk, D = 8, 32, 32, 432, 1024, 192
    return {
        "q": _w((lambda *s: torch.randn(*s, dtype=torch.bfloat16) * 0.8)(B, Hq, Sq, D)),
        "k": _w((lambda *s: torch.randn(*s, dtype=torch.bfloat16) * 0.8)(B, Hkv, Sk, D)),
        "v": _w((lambda *s: torch.randn(*s, dtype=torch.bfloat16) * 0.05)(B, Hkv, Sk, D)),
    }

def reference(q, k, v):
    B, Hq, Sq, D = q.shape
    Hkv = k.shape[1]
    rep = Hq // Hkv
    k = (lambda x, n, axis: torch.repeat_interleave(x, n, dim=axis))(k, rep, axis=1)
    v = (lambda x, n, axis: torch.repeat_interleave(x, n, dim=axis))(v, rep, axis=1)
    scale = D ** -0.5
    scores = torch.einsum("bhqd,bhkd->bhqk", q, k) * scale
    p = _softmax(scores, axis=-1)
    return torch.einsum("bhqk,bhkd->bhqd", p, v)
```

```cpp
#include <hip/hip_runtime.h>
#include <hip/hip_bf16.h>
typedef float f32x16 __attribute__((ext_vector_type(16)));
typedef __bf16 bf16x8 __attribute__((ext_vector_type(8)));
typedef unsigned short u16;
typedef unsigned u32x4 __attribute__((ext_vector_type(4)));
constexpr int SQ=432, SK=1024, D=192;
__global__ __launch_bounds__(256) void attn_base(const u16* __restrict__ Q, const u16* __restrict__ K, const u16* __restrict__ V, u16* __restrict__ O) {
  __shared__ __attribute__((aligned(16))) u16 Ks[32*(D+8)];
  __shared__ __attribute__((aligned(16))) u16 Vs[32*(D+8)];
  const int tid=threadIdx.x, w=tid>>6, l=tid&63, r=l&31, h=l>>5;
  const long bh=blockIdx.y;
  int row=blockIdx.x*128+w*32+r; int rowc=row<SQ?row:SQ-1;
  bf16x8 q[12];
  for(int s=0;s<12;s++) q[s]=*(const bf16x8*)(Q+(bh*SQ+rowc)*D+16*s+8*h);
  f32x16 o[6]={}; float m=-1e30f, lsum=0.f;
  const float c=1.4426950408889634f*0.07216878364870322f;
  for(int j=0;j<SK/32;j++){
    __syncthreads();
    for(int i=tid;i<32*24;i+=256){int kr=i/24, cc=i%24;
      *(u32x4*)(Ks+kr*(D+8)+cc*8)=*(const u32x4*)(K+(bh*SK+j*32+kr)*D+cc*8);
      *(u32x4*)(Vs+kr*(D+8)+cc*8)=*(const u32x4*)(V+(bh*SK+j*32+kr)*D+cc*8);}
    __syncthreads();
    f32x16 s={};
    #pragma unroll
    for(int t=0;t<12;t++){ bf16x8 kf=*(const bf16x8*)(Ks+r*(D+8)+16*t+8*h); s=__builtin_amdgcn_mfma_f32_32x32x16_bf16(kf,q[t],s,0,0,0);}
    float mx=s[0]; for(int i=1;i<16;i++) mx=fmaxf(mx,s[i]);
    mx=fmaxf(mx,__shfl_xor(mx,32));
    float mn=fmaxf(m,mx); float al=exp2f((m-mn)*c); m=mn;
    float rs=0; float p[16];
    for(int i=0;i<16;i++){p[i]=exp2f((s[i]-mn)*c); rs+=p[i];}
    rs+=__shfl_xor(rs,32); lsum=lsum*al+rs;
    #pragma unroll
    for(int d=0;d<6;d++) for(int i=0;i<16;i++) o[d][i]*=al;
    #pragma unroll
    for(int ks=0;ks<2;ks++){
      bf16x8 pb; for(int i=0;i<8;i++) pb[i]=(__bf16)p[8*ks+i];
      #pragma unroll
      for(int d=0;d<6;d++){
        bf16x8 vf;
        for(int i=0;i<8;i++){int key=16*ks+8*(i>>2)+4*h+(i&3); u16 x=Vs[key*(D+8)+32*d+r]; vf[i]=__builtin_bit_cast(__bf16,x);}
        o[d]=__builtin_amdgcn_mfma_f32_32x32x16_bf16(vf,pb,o[d],0,0,0);
      }
    }
  }
  if(row<SQ){ float il=1.f/lsum;
    #pragma unroll
    for(int d=0;d<6;d++) for(int i=0;i<16;i++){int dd=32*d+(i&3)+8*(i>>2)+4*h; __bf16 x=(__bf16)(o[d][i]*il); O[(bh*SQ+row)*D+dd]=__builtin_bit_cast(u16,x);} }
}
extern "C" void kernel_launch(void* const* d_in, const int* in_sizes, int n_in, void* d_out, int out_size, void* d_ws, size_t ws_size, hipStream_t stream) {
  attn_base<<<dim3(4,256),256,0,stream>>>((const u16*)d_in[0],(const u16*)d_in[1],(const u16*)d_in[2],(u16*)d_out);
}
```

```cpp
#include <hip/hip_runtime.h>

__global__ __launch_bounds__(256, 1) void attn_fwd_d192(const void* __restrict__ q, const void* __restrict__ k, const void* __restrict__ v,
                                                        void* __restrict__ o) {
  __shared__ __attribute__((aligned(1024))) char lds[163840];
  const unsigned wave = __builtin_amdgcn_readfirstlane(threadIdx.x >> 6);
  const unsigned ldsb = __builtin_amdgcn_readfirstlane((unsigned)(size_t)lds);
  asm volatile(
    "s_load_dwordx8 s[8:15], %0, 0x0\n"
    "v_mbcnt_lo_u32_b32 v244, -1, 0\n"
    "v_mbcnt_hi_u32_b32 v244, -1, v244\n"
    "s_mov_b32 s53, %2\n"
    "s_and_b32 s80, %1, 7\n"
    "s_lshl_b32 s80, s80, 4\n"
    "s_lshr_b32 s59, %1, 4\n"
    "s_add_u32 s80, s80, s59\n"
    "s_bfe_u32 s52, %1, 0x10003\n"
    "v_lshrrev_b32 v245, 3, v244\n"
    "v_and_b32 v246, 7, v244\n"
    "v_mul_u32_u24 v247, 384, v245\n"
    "v_lshl_add_u32 v240, v246, 4, v247\n"
    "s_mul_i32 s60, s53, 24576\n"
    "s_mul_i32 s61, s53, 18432\n"
    "s_add_u32 s61, s61, 98304\n"
    "s_mul_i32 s59, s80, 331776\n"
    "s_cmp_eq_u32 s52, 0\n"
    "s_cselect_b32 s54, s60, s61\n"
    "s_cselect_b32 s55, s61, s60\n"
    "s_add_u32 s54, s54, s59\n"
    "s_add_u32 s55, s55, s59\n"
    "s_add_u32 s55, s55, 165888\n"
    "s_mul_i32 s85, s53, 6144\n"
    "s_getpc_b64 s[92:93]\n"
    "LSTART_%=:\n"
    "s_and_b32 s93, s93, 0xffff\n"
    "s_mov_b32 s94, LEND_%=-LSTART_%=\n"
    "s_mov_b32 s95, 0x20000\n"
    "v_lshlrev_b32 v253, 7, v244\n"
    "s_cmp_eq_u32 s53, 3\n"
    "s_mov_b32 s62, LPRO0_3_%=-LSTART_%=\n"
    "s_cselect_b32 s62, LPRO0_2_%=-LSTART_%=, s62\n"
    "s_cmp_eq_u32 s52, 0\n"
    "s_cselect_b32 s62, LPRO0_4_%=-LSTART_%=, s62\n"
    "s_cmp_eq_u32 s53, 3\n"
    "s_mov_b32 s63, LPEEL_3_%=-LSTART_%=\n"
    "s_cselect_b32 s63, LPEEL_2_%=-LSTART_%=, s63\n"
    "s_cmp_eq_u32 s52, 0\n"
    "s_cselect_b32 s63, LPEEL_4_%=-LSTART_%=, s63\n"
    "s_lshl_b32 s61, s52, 2\n"
    "s_add_u32 s61, s61, s53\n"
    "s_cmp_eq_u32 s53, 0\n"
    "s_cselect_b32 s60, s62, 0x7000000\n"
    "s_cmp_eq_u32 s61, 7\n"
    "s_cselect_b32 s62, s62, s60\n"
    "s_cmp_eq_u32 s53, 1\n"
    "s_cselect_b32 s60, s63, 0x7000000\n"
    "s_cmp_eq_u32 s61, 7\n"
    "s_cselect_b32 s63, s63, s60\n"
    "buffer_load_dword v254, v253, s[92:95], s62 offen\n"
    "buffer_load_dword v254, v253, s[92:95], s63 offen\n"
    "v_bfe_u32 v245, v244, 2, 3\n"
    "v_mul_u32_u24 v245, 384, v245\n"
    "v_lshrrev_b32 v246, 5, v244\n"
    "v_lshl_add_u32 v245, v246, 6, v245\n"
    "v_bfe_u32 v246, v244, 4, 1\n"
    "v_lshlrev_b32 v246, 1, v246\n"
    "v_and_b32 v247, 3, v244\n"
    "v_xor_b32 v246, v246, v247\n"
    "v_lshl_add_u32 v245, v246, 4, v245\n"
    "v_add_u32 v239, s85, v245\n"
    "v_add_u32 v238, 896, v239\n"
    "v_add_u32 v237, 1792, v239\n"
    "s_waitcnt lgkmcnt(0)\n"
    "s_mov_b64 s[28:29], s[8:9]\n"
    "s_and_b32 s29, s29, 0xffff\n"
    "s_mov_b32 s30, 42467328\n"
    "s_mov_b32 s31, 0x20000\n"
    "s_add_u32 s58, s54, 0\n"
    "buffer_load_dwordx4 v[0:3], v240, s[28:31], s58 offen offset:0\n"
    "buffer_load_dwordx4 v[4:7], v240, s[28:31], s58 offen offset:3072\n"
    "buffer_load_dwordx4 v[8:11], v240, s[28:31], s58 offen offset:128\n"
    "buffer_load_dwordx4 v[12:15], v240, s[28:31], s58 offen offset:3200\n"
    "buffer_load_dwordx4 v[16:19], v240, s[28:31], s58 offen offset:256\n"
    "buffer_load_dwordx4 v[20:23], v240, s[28:31], s58 offen offset:3328\n"
    "s_mul_i32 s80, s80, 786432\n"
    "s_mov_b64 s[20:21], s[10:11]\n"
    "s_add_u32 s20, s20, s80\n"
    "s_addc_u32 s21, s21, 0\n"
    "s_sub_u32 s20, s20, 1792\n"
    "s_subb_u32 s21, s21, 0\n"
    "s_and_b32 s21, s21, 0xffff\n"
    "s_mov_b32 s22, 788224\n"
    "s_mov_b32 s23, 0x20000\n"
    "s_mov_b32 s56, %3\n"
    "s_mul_i32 s85, s53, 6144\n"
    "s_add_u32 s47, s85, s56\n"
    "s_mov_b32 s40, 0\n"
    "s_mul_i32 s44, s52, 0\n"
    "s_add_u32 m0, s40, s47\n"
    "s_nop 0\n"
    "buffer_load_dwordx4 v237, s[20:23], s44 offen offset:0 lds\n"
    "buffer_load_dwordx4 v238, s[20:23], s44 offen offset:1024 lds\n"
    "buffer_load_dwordx4 v239, s[20:23], s44 offen offset:2048 lds\n"
    "s_add_u32 m0, m0, 3072\n"
    "s_add_u32 s45, s44, 3072\n"
    "buffer_load_dwordx4 v237, s[20:23], s45 offen offset:0 lds\n"
    "buffer_load_dwordx4 v238, s[20:23], s45 offen offset:1024 lds\n"
    "buffer_load_dwordx4 v239, s[20:23], s45 offen offset:2048 lds\n"
    "s_add_u32 s59, s54, 6144\n"
    "buffer_load_dwordx4 v[24:27], v240, s[28:31], s59 offen offset:0\n"
    "buffer_load_dwordx4 v[28:31], v240, s[28:31], s59 offen offset:3072\n"
    "buffer_load_dwordx4 v[32:35], v240, s[28:31], s59 offen offset:128\n"
    "buffer_load_dwordx4 v[36:39], v240, s[28:31], s59 offen offset:3200\n"
    "buffer_load_dwordx4 v[40:43], v240, s[28:31], s59 offen offset:256\n"
    "buffer_load_dwordx4 v[44:47], v240, s[28:31], s59 offen offset:3328\n"
    "s_lshl_b32 s62, s52, 2\n"
    "s_add_u32 s62, s62, s53\n"
    "s_cmp_eq_u32 s62, 7\n"
    "s_cbranch_scc1 LQDONE_%=\n"
    "s_add_u32 s60, s54, 12288\n"
    "buffer_load_dwordx4 v[48:51], v240, s[28:31], s60 offen offset:0\n"
    "buffer_load_dwordx4 v[52:55], v240, s[28:31], s60 offen offset:3072\n"
    "buffer_load_dwordx4 v[56:59], v240, s[28:31], s60 offen offset:128\n"
    "buffer_load_dwordx4 v[60:63], v240, s[28:31], s60 offen offset:3200\n"
    "buffer_load_dwordx4 v[64:67], v240, s[28:31], s60 offen offset:256\n"
    "buffer_load_dwordx4 v[68:71], v240, s[28:31], s60 offen offset:3328\n"
    "s_cmp_eq_u32 s52, 1\n"
    "s_cbranch_scc1 LQDONE_%=\n"
    "s_add_u32 s61, s54, 18432\n"
    "buffer_load_dwordx4 v[72:75], v240, s[28:31], s61 offen offset:0\n"
    "buffer_load_dwordx4 v[76:79], v240, s[28:31], s61 offen offset:3072\n"
    "buffer_load_dwordx4 v[80:83], v240, s[28:31], s61 offen offset:128\n"
    "buffer_load_dwordx4 v[84:87], v240, s[28:31], s61 offen offset:3200\n"
    "buffer_load_dwordx4 v[88:91], v240, s[28:31], s61 offen offset:256\n"
    "buffer_load_dwordx4 v[92:95], v240, s[28:31], s61 offen offset:3328\n"
    "LQDONE_%=:\n"
    "s_mov_b64 s[24:25], s[12:13]\n"
    "s_add_u32 s24, s24, s80\n"
    "s_addc_u32 s25, s25, 0\n"
    "s_sub_u32 s24, s24, 1792\n"
    "s_subb_u32 s25, s25, 0\n"
    "s_and_b32 s25, s25, 0xffff\n"
    "s_mov_b32 s26, 788224\n"
    "s_mov_b32 s27, 0x20000\n"
    "s_mov_b64 s[36:37], s[14:15]\n"
    "s_and_b32 s37, s37, 0xffff\n"
    "s_mov_b32 s38, 42467328\n"
    "s_mov_b32 s39, 0x20000\n"
    "s_mov_b32 s49, 0x47800000\n"
    "s_mov_b32 s17, 0xffff0000\n"
    "s_mov_b32 s50, 0x3dd53b95\n"
    "s_mov_b32 s51, 0\n"
    "s_mov_b32 s41, 0\n"
    "s_lshl_b32 s34, s53, 12\n"
    "s_add_u32 s34, s34, 147456\n"
    "s_add_u32 s34, s34, s56\n"
    "s_mov_b32 s35, 0x7000000\n"
    "v_lshrrev_b32 v245, 3, v244\n"
    "v_and_b32 v246, 7, v244\n"
    "v_xor_b32 v247, v245, v246\n"
    "v_lshlrev_b32 v247, 4, v247\n"
    "v_lshl_add_u32 v247, v245, 7, v247\n"
    "v_add_u32 v242, s34, v247\n"
    "v_lshrrev_b32 v245, 4, v244\n"
    "v_xor_b32 v247, v245, v246\n"
    "v_lshlrev_b32 v247, 4, v247\n"
    "v_and_b32 v245, 15, v244\n"
    "v_lshl_add_u32 v247, v245, 7, v247\n"
    "v_add_u32 v243, s34, v247\n"
    "v_bfe_u32 v245, v244, 3, 1\n"
    "v_mul_u32_u24 v245, 3072, v245\n"
    "v_lshl_add_u32 v245, v246, 6, v245\n"
    "v_bfe_u32 v246, v244, 2, 1\n"
    "v_lshlrev_b32 v246, 1, v246\n"
    "v_lshrrev_b32 v247, 4, v244\n"
    "v_xor_b32 v246, v246, v247\n"
    "v_lshl_add_u32 v245, v246, 4, v245\n"
    "v_add_u32 v233, s56, v245\n"
    "v_lshrrev_b32 v245, 5, v244\n"
    "v_mul_u32_u24 v245, 3072, v245\n"
    "v_bfe_u32 v246, v244, 2, 3\n"
    "v_lshl_add_u32 v245, v246, 6, v245\n"
    "v_bfe_u32 v246, v244, 4, 1\n"
    "v_lshl_add_u32 v245, v246, 5, v245\n"
    "v_and_b32 v246, 3, v244\n"
    "v_lshl_add_u32 v245, v246, 3, v245\n"
    "v_add_u32 v245, 73728, v245\n"
    "v_add_u32 v234, s56, v245\n"
    "s_cmp_eq_u32 s52, 0\n"
    "s_mov_b32 s58, 0x5e432f10\n"
    "s_cselect_b32 s58, 0x76543210, s58\n"
    "s_mov_b32 s59, 0xcba98d76\n"
    "s_cselect_b32 s59, 0xfedcba98, s59\n"
    "s_mov_b32 s60, 0x76543210\n"
    "s_cselect_b32 s60, 0x81765432, s60\n"
    "s_mov_b32 s61, 0xfedcba98\n"
    "s_cselect_b32 s61, 0xfedcb0a9, s61\n"
    "v_cmp_gt_u32 vcc, 16, v244\n"
    "v_mov_b32 v248, s60\n"
    "v_mov_b32 v249, s61\n"
    "v_mov_b32 v245, s58\n"
    "v_mov_b32 v246, s59\n"
    "v_cndmask_b32 v248, v248, v245, vcc\n"
    "v_cndmask_b32 v249, v249, v246, vcc\n"
    "v_and_b32 v245, 15, v244\n"
    "v_lshlrev_b32 v245, 2, v245\n"
    "v_lshrrev_b64 v[248:249], v245, v[248:249]\n"
    "v_and_b32 v245, 15, v248\n"
    "v_and_b32 v246, 48, v244\n"
    "v_add_u32 v245, v245, v246\n"
    "v_mul_u32_u24 v252, 24576, v245\n"
    "s_mov_b32 s87, 0\n"
    "LDISPATCH_%=:\n"
    "s_cmp_eq_u32 s51, s52\n"
    "s_cselect_b32 s59, 1, 0\n"
    "s_lshl_b32 s58, 3072, s59\n"
    "s_mul_i32 s58, s58, s53\n"
    "s_sub_u32 s60, s58, s85\n"
    "s_mov_b32 s85, s58\n"
    "v_add_u32 v237, s60, v237\n"
    "v_add_u32 v238, s60, v238\n"
    "v_add_u32 v239, s60, v239\n"
    "s_lshl_b32 s46, s51, 4\n"
    "s_add_u32 s46, s46, 17\n"
    "s_add_u32 s47, s58, s56\n"
    "s_add_u32 s48, s47, 73728\n"
    "s_cmp_eq_u32 s59, 1\n"
    "s_cbranch_scc1 LITEM_4_%=\n"
    "s_cmp_eq_u32 s53, 3\n"
    "s_cbranch_scc1 LITEM_2_%=\n"
    "s_branch LITEM_3_%=\n"
    "LNEXT_%=:\n"
    "s_cmp_eq_u32 s51, 1\n"
    "s_cbranch_scc1 LEND_%=\n"
    "s_mov_b32 s51, 1\n"
    "s_mov_b32 s54, s55\n"
    "s_mov_b32 s55, s35\n"
    "s_branch LDISPATCH_%=\n"
    "LITEM_4_%=:\n"
    "s_cmp_eq_u32 s51, 0\n"
    "s_cbranch_scc0 LPRO1_4_%=\n"
    "LPRO0_4_%=:\n"
    "s_mov_b32 s84, s44\n"
    "s_add_u32 s87, s87, 1\n"
    "v_readlane_b32 s44, v252, s87\n"
    "s_mov_b32 s86, LFIN0_4_%=-LSTART_%=+13631488\n"
    "v_cmp_gt_u32 vcc, 1280, v253\n"
    "v_mov_b32 v232, s35\n"
    "s_nop 0\n"
    "v_cndmask_b32 v255, v232, v253, vcc\n"
    "s_sub_u32 s79, s55, 0\n"
    "v_xor_b32 v232, 64, v243\n"
    "v_add_u32 v235, s41, v233\n"
    "s_waitcnt vmcnt(24)\n"
    "v_lshlrev_b32 v216, 16, v0\n"
    "v_and_b32 v217, s17, v0\n"
    "v_lshlrev_b32 v218, 16, v1\n"
    "v_and_b32 v219, s17, v1\n"
    "v_lshlrev_b32 v220, 16, v2\n"
    "v_and_b32 v221, s17, v2\n"
    "v_lshlrev_b32 v222, 16, v3\n"
    "v_and_b32 v223, s17, v3\n"
    "v_pk_mul_f32 v[216:217], v[216:217], s[50:51] op_sel_hi:[1,0]\n"
    "v_pk_mul_f32 v[218:219], v[218:219], s[50:51] op_sel_hi:[1,0]\n"
    "v_pk_mul_f32 v[220:221], v[220:221], s[50:51] op_sel_hi:[1,0]\n"
    "v_pk_mul_f32 v[222:223], v[222:223], s[50:51] op_sel_hi:[1,0]\n"
    "v_cvt_pk_bf16_f32 v0, v216, v217\n"
    "v_cvt_pk_bf16_f32 v1, v218, v219\n"
    "v_cvt_pk_bf16_f32 v2, v220, v221\n"
    "v_cvt_pk_bf16_f32 v3, v222, v223\n"
    "v_lshlrev_b32 v224, 16, v4\n"
    "v_and_b32 v225, s17, v4\n"
    "v_lshlrev_b32 v226, 16, v5\n"
    "v_and_b32 v227, s17, v5\n"
    "v_lshlrev_b32 v228, 16, v6\n"
    "v_and_b32 v229, s17, v6\n"
    "v_lshlrev_b32 v230, 16, v7\n"
    "v_and_b32 v231, s17, v7\n"
    "v_pk_mul_f32 v[224:225], v[224:225], s[50:51] op_sel_hi:[1,0]\n"
    "v_pk_mul_f32 v[226:227], v[226:227], s[50:51] op_sel_hi:[1,0]\n"
    "v_pk_mul_f32 v[228:229], v[228:229], s[50:51] op_sel_hi:[1,0]\n"
    "v_pk_mul_f32 v[230:231], v[230:231], s[50:51] op_sel_hi:[1,0]\n"
    "v_cvt_pk_bf16_f32 v4, v224, v225\n"
    "v_cvt_pk_bf16_f32 v5, v226, v227\n"
    "v_cvt_pk_bf16_f32 v6, v228, v229\n"
    "v_cvt_pk_bf16_f32 v7, v230, v231\n"
    "ds_write_b128 v242, v[0:3] offset:0\n"
    "ds_write_b128 v242, v[4:7] offset:1024\n"
    "v_lshlrev_b32 v216, 16, v8\n"
    "v_and_b32 v217, s17, v8\n"
    "v_lshlrev_b32 v218, 16, v9\n"
    "v_and_b32 v219, s17, v9\n"
    "v_lshlrev_b32 v220, 16, v10\n"
    "v_and_b32 v221, s17, v10\n"
    "v_lshlrev_b32 v222, 16, v11\n"
    "v_and_b32 v223, s17, v11\n"
    "v_pk_mul_f32 v[216:217], v[216:217], s[50:51] op_sel_hi:[1,0]\n"
    "v_pk_mul_f32 v[218:219], v[218:219], s[50:51] op_sel_hi:[1,0]\n"
    "v_pk_mul_f32 v[220:221], v[220:221], s[50:51] op_sel_hi:[1,0]\n"
    "v_pk_mul_f32 v[222:223], v[222:223], s[50:51] op_sel_hi:[1,0]\n"
    "v_cvt_pk_bf16_f32 v8, v216, v217\n"
    "v_cvt_pk_bf16_f32 v9, v218, v219\n"
    "v_cvt_pk_bf16_f32 v10, v220, v221\n"
    "v_cvt_pk_bf16_f32 v11, v222, v223\n"
    "v_lshlrev_b32 v224, 16, v12\n"
    "v_and_b32 v225, s17, v12\n"
    "v_lshlrev_b32 v226, 16, v13\n"
    "v_and_b32 v227, s17, v13\n"
    "v_lshlrev_b32 v228, 16, v14\n"
    "v_and_b32 v229, s17, v14\n"
    "v_lshlrev_b32 v230, 16, v15\n"
    "v_and_b32 v231, s17, v15\n"
    "v_pk_mul_f32 v[224:225], v[224:225], s[50:51] op_sel_hi:[1,0]\n"
    "v_pk_mul_f32 v[226:227], v[226:227], s[50:51] op_sel_hi:[1,0]\n"
    "v_pk_mul_f32 v[228:229], v[228:229], s[50:51] op_sel_hi:[1,0]\n"
    "v_pk_mul_f32 v[230:231], v[230:231], s[50:51] op_sel_hi:[1,0]\n"
    "v_cvt_pk_bf16_f32 v12, v224, v225\n"
    "v_cvt_pk_bf16_f32 v13, v226, v227\n"
    "v_cvt_pk_bf16_f32 v14, v228, v229\n"
    "v_cvt_pk_bf16_f32 v15, v230, v231\n"
    "ds_read_b128 v[0:3], v243 offset:0\n"
    "ds_read_b128 v[4:7], v232 offset:0\n"
    "ds_write_b128 v242, v[8:11] offset:2048\n"
    "ds_write_b128 v242, v[12:15] offset:3072\n"
    "v_lshlrev_b32 v216, 16, v16\n"
    "v_and_b32 v217, s17, v16\n"
    "v_lshlrev_b32 v218, 16, v17\n"
    "v_and_b32 v219, s17, v17\n"
    "v_lshlrev_b32 v220, 16, v18\n"
    "v_and_b32 v221, s17, v18\n"
    "v_lshlrev_b32 v222, 16, v19\n"
    "v_and_b32 v223, s17, v19\n"
    "v_pk_mul_f32 v[216:217], v[216:217], s[50:51] op_sel_hi:[1,0]\n"
    "v_pk_mul_f32 v[218:219], v[218:219], s[50:51] op_sel_hi:[1,0]\n"
    "v_pk_mul_f32 v[220:221], v[220:221], s[50:51] op_sel_hi:[1,0]\n"
    "v_pk_mul_f32 v[222:223], v[222:223], s[50:51] op_sel_hi:[1,0]\n"
    "v_cvt_pk_bf16_f32 v16, v216, v217\n"
    "v_cvt_pk_bf16_f32 v17, v218, v219\n"
    "v_cvt_pk_bf16_f32 v18, v220, v221\n"
    "v_cvt_pk_bf16_f32 v19, v222, v223\n"
    "v_lshlrev_b32 v224, 16, v20\n"
    "v_and_b32 v225, s17, v20\n"
    "v_lshlrev_b32 v226, 16, v21\n"
    "v_and_b32 v227, s17, v21\n"
    "v_lshlrev_b32 v228, 16, v22\n"
    "v_and_b32 v229, s17, v22\n"
    "v_lshlrev_b32 v230, 16, v23\n"
    "v_and_b32 v231, s17, v23\n"
    "v_pk_mul_f32 v[224:225], v[224:225], s[50:51] op_sel_hi:[1,0]\n"
    "v_pk_mul_f32 v[226:227], v[226:227], s[50:51] op_sel_hi:[1,0]\n"
    "v_pk_mul_f32 v[228:229], v[228:229], s[50:51] op_sel_hi:[1,0]\n"
    "v_pk_mul_f32 v[230:231], v[230:231], s[50:51] op_sel_hi:[1,0]\n"
    "v_cvt_pk_bf16_f32 v20, v224, v225\n"
    "v_cvt_pk_bf16_f32 v21, v226, v227\n"
    "v_cvt_pk_bf16_f32 v22, v228, v229\n"
    "v_cvt_pk_bf16_f32 v23, v230, v231\n"
    "ds_read_b128 v[8:11], v243 offset:2048\n"
    "ds_read_b128 v[12:15], v232 offset:2048\n"
    "ds_write_b128 v242, v[16:19] offset:0\n"
    "ds_write_b128 v242, v[20:23] offset:1024\n"
    "s_waitcnt vmcnt(12)\n"
    "s_barrier\n"
    "ds_read_b128 a[224:227], v235 offset:0\n"
    "ds_read_b128 a[228:231], v235 offset:512\n"
    "ds_read_b128 a[232:235], v235 offset:1024\n"
    "ds_read_b128 a[236:239], v235 offset:1536\n"
    "ds_read_b128 a[240:243], v235 offset:2048\n"
    "ds_read_b128 a[244:247], v235 offset:2560\n"
    "ds_read_b128 a[248:251], v235 offset:6144\n"
    "ds_read_b128 a[252:255], v235 offset:6656\n"
    "ds_read_b128 a[192:195], v235 offset:7168\n"
    "ds_read_b128 a[196:199], v235 offset:7680\n"
    "ds_read_b128 a[200:203], v235 offset:8192\n"
    "ds_read_b128 a[204:207], v235 offset:8704\n"
    "v_lshlrev_b32 v216, 16, v24\n"
    "v_and_b32 v217, s17, v24\n"
    "v_lshlrev_b32 v218, 16, v25\n"
    "v_and_b32 v219, s17, v25\n"
    "v_lshlrev_b32 v220, 16, v26\n"
    "v_and_b32 v221, s17, v26\n"
    "v_lshlrev_b32 v222, 16, v27\n"
    "v_and_b32 v223, s17, v27\n"
    "v_pk_mul_f32 v[216:217], v[216:217], s[50:51] op_sel_hi:[1,0]\n"
    "v_pk_mul_f32 v[218:219], v[218:219], s[50:51] op_sel_hi:[1,0]\n"
    "v_pk_mul_f32 v[220:221], v[220:221], s[50:51] op_sel_hi:[1,0]\n"
    "v_pk_mul_f32 v[222:223], v[222:223], s[50:51] op_sel_hi:[1,0]\n"
    "v_cvt_pk_bf16_f32 v24, v216, v217\n"
    "v_cvt_pk_bf16_f32 v25, v218, v219\n"
    "v_cvt_pk_bf16_f32 v26, v220, v221\n"
    "v_cvt_pk_bf16_f32 v27, v222, v223\n"
    "v_lshlrev_b32 v224, 16, v28\n"
    "v_and_b32 v225, s17, v28\n"
    "v_lshlrev_b32 v226, 16, v29\n"
    "v_and_b32 v227, s17, v29\n"
    "v_lshlrev_b32 v228, 16, v30\n"
    "v_and_b32 v229, s17, v30\n"
    "v_lshlrev_b32 v230, 16, v31\n"
    "v_and_b32 v231, s17, v31\n"
    "v_pk_mul_f32 v[224:225], v[224:225], s[50:51] op_sel_hi:[1,0]\n"
    "v_pk_mul_f32 v[226:227], v[226:227], s[50:51] op_sel_hi:[1,0]\n"
    "v_pk_mul_f32 v[228:229], v[228:229], s[50:51] op_sel_hi:[1,0]\n"
    "v_pk_mul_f32 v[230:231], v[230:231], s[50:51] op_sel_hi:[1,0]\n"
    "v_cvt_pk_bf16_f32 v28, v224, v225\n"
    "v_cvt_pk_bf16_f32 v29, v226, v227\n"
    "v_cvt_pk_bf16_f32 v30, v228, v229\n"
    "v_cvt_pk_bf16_f32 v31, v230, v231\n"
    "ds_read_b128 v[16:19], v243 offset:0\n"
    "ds_read_b128 v[20:23], v232 offset:0\n"
    "ds_write_b128 v242, v[24:27] offset:2048\n"
    "ds_write_b128 v242, v[28:31] offset:3072\n"
    "v_lshlrev_b32 v216, 16, v32\n"
    "v_and_b32 v217, s17, v32\n"
    "v_lshlrev_b32 v218, 16, v33\n"
    "v_and_b32 v219, s17, v33\n"
    "v_lshlrev_b32 v220, 16, v34\n"
    "v_and_b32 v221, s17, v34\n"
    "v_lshlrev_b32 v222, 16, v35\n"
    "v_and_b32 v223, s17, v35\n"
    "v_pk_mul_f32 v[216:217], v[216:217], s[50:51] op_sel_hi:[1,0]\n"
    "v_pk_mul_f32 v[218:219], v[218:219], s[50:51] op_sel_hi:[1,0]\n"
    "v_pk_mul_f32 v[220:221], v[220:221], s[50:51] op_sel_hi:[1,0]\n"
    "v_pk_mul_f32 v[222:223], v[222:223], s[50:51] op_sel_hi:[1,0]\n"
    "s_waitcnt lgkmcnt(4)\n"
    "v_mfma_f32_16x16x32_bf16 v[96:99], a[224:227], v[0:3], 0\n"
    "v_cvt_pk_bf16_f32 v32, v216, v217\n"
    "v_cvt_pk_bf16_f32 v33, v218, v219\n"
    "v_mfma_f32_16x16x32_bf16 v[96:99], a[228:231], v[4:7], v[96:99]\n"
    "v_cvt_pk_bf16_f32 v34, v220, v221\n"
    "v_cvt_pk_bf16_f32 v35, v222, v223\n"
    "v_mfma_f32_16x16x32_bf16 v[96:99], a[232:235], v[8:11], v[96:99]\n"
    "v_lshlrev_b32 v224, 16, v36\n"
    "v_and_b32 v225, s17, v36\n"
    "v_mfma_f32_16x16x32_bf16 v[96:99], a[236:239], v[12:15], v[96:99]\n"
    "v_lshlrev_b32 v226, 16, v37\n"
    "v_and_b32 v227, s17, v37\n"
    "v_lshlrev_b32 v228, 16, v38\n"
    "v_and_b32 v229, s17, v38\n"
    "v_lshlrev_b32 v230, 16, v39\n"
    "v_and_b32 v231, s17, v39\n"
    "v_pk_mul_f32 v[224:225], v[224:225], s[50:51] op_sel_hi:[1,0]\n"
    "v_pk_mul_f32 v[226:227], v[226:227], s[50:51] op_sel_hi:[1,0]\n"
    "v_pk_mul_f32 v[228:229], v[228:229], s[50:51] op_sel_hi:[1,0]\n"
    "v_pk_mul_f32 v[230:231], v[230:231], s[50:51] op_sel_hi:[1,0]\n"
    "s_waitcnt lgkmcnt(2)\n"
    "v_mfma_f32_16x16x32_bf16 v[96:99], a[240:243], v[16:19], v[96:99]\n"
    "v_cvt_pk_bf16_f32 v36, v224, v225\n"
    "v_cvt_pk_bf16_f32 v37, v226, v227\n"
    "v_mfma_f32_16x16x32_bf16 v[96:99], a[244:247], v[20:23], v[96:99]\n"
    "v_cvt_pk_bf16_f32 v38, v228, v229\n"
    "v_cvt_pk_bf16_f32 v39, v230, v231\n"
    "ds_read_b128 v[24:27], v243 offset:2048\n"
    "ds_read_b128 v[28:31], v232 offset:2048\n"
    "ds_write_b128 v242, v[32:35] offset:0\n"
    "ds_write_b128 v242, v[36:39] offset:1024\n"
    "v_lshlrev_b32 v216, 16, v40\n"
    "v_and_b32 v217, s17, v40\n"
    "v_lshlrev_b32 v218, 16, v41\n"
    "v_and_b32 v219, s17, v41\n"
    "v_lshlrev_b32 v220, 16, v42\n"
    "v_and_b32 v221, s17, v42\n"
    "v_lshlrev_b32 v222, 16, v43\n"
    "v_and_b32 v223, s17, v43\n"
    "v_pk_mul_f32 v[216:217], v[216:217], s[50:51] op_sel_hi:[1,0]\n"
    "v_pk_mul_f32 v[218:219], v[218:219], s[50:51] op_sel_hi:[1,0]\n"
    "v_pk_mul_f32 v[220:221], v[220:221], s[50:51] op_sel_hi:[1,0]\n"
    "v_pk_mul_f32 v[222:223], v[222:223], s[50:51] op_sel_hi:[1,0]\n"
    "v_mfma_f32_16x16x32_bf16 v[100:103], a[248:251], v[0:3], 0\n"
    "v_cvt_pk_bf16_f32 v40, v216, v217\n"
    "v_cvt_pk_bf16_f32 v41, v218, v219\n"
    "v_mfma_f32_16x16x32_bf16 v[100:103], a[252:255], v[4:7], v[100:103]\n"
    "v_cvt_pk_bf16_f32 v42, v220, v221\n"
    "v_cvt_pk_bf16_f32 v43, v222, v223\n"
    "v_mfma_f32_16x16x32_bf16 v[100:103], a[192:195], v[8:11], v[100:103]\n"
    "v_lshlrev_b32 v224, 16, v44\n"
    "v_and_b32 v225, s17, v44\n"
    "v_mfma_f32_16x16x32_bf16 v[100:103], a[196:199], v[12:15], v[100:103]\n"
    "v_lshlrev_b32 v226, 16, v45\n"
    "v_and_b32 v227, s17, v45\n"
    "v_lshlrev_b32 v228, 16, v46\n"
    "v_and_b32 v229, s17, v46\n"
    "v_lshlrev_b32 v230, 16, v47\n"
    "v_and_b32 v231, s17, v47\n"
    "v_pk_mul_f32 v[224:225], v[224:225], s[50:51] op_sel_hi:[1,0]\n"
    "v_pk_mul_f32 v[226:227], v[226:227], s[50:51] op_sel_hi:[1,0]\n"
    "v_pk_mul_f32 v[228:229], v[228:229], s[50:51] op_sel_hi:[1,0]\n"
    "v_pk_mul_f32 v[230:231], v[230:231], s[50:51] op_sel_hi:[1,0]\n"
    "v_mfma_f32_16x16x32_bf16 v[100:103], a[200:203], v[16:19], v[100:103]\n"
    "v_cvt_pk_bf16_f32 v44, v224, v225\n"
    "v_cvt_pk_bf16_f32 v45, v226, v227\n"
    "v_mfma_f32_16x16x32_bf16 v[100:103], a[204:207], v[20:23], v[100:103]\n"
    "v_cvt_pk_bf16_f32 v46, v228, v229\n"
    "v_cvt_pk_bf16_f32 v47, v230, v231\n"
    "ds_read_b128 v[32:35], v243 offset:0\n"
    "ds_read_b128 v[36:39], v232 offset:0\n"
    "ds_write_b128 v242, v[40:43] offset:2048\n"
    "ds_write_b128 v242, v[44:47] offset:3072\n"
    "s_waitcnt vmcnt(6)\n"
    "s_mul_i32 s62, s53, 3072\n"
    "s_sub_u32 s63, s62, s85\n"
    "s_add_u32 s62, s62, s56\n"
    "v_add_u32 v128, s63, v237\n"
    "v_add_u32 v129, s63, v238\n"
    "v_add_u32 v130, s63, v239\n"
    "s_add_u32 m0, s62, 73728\n"
    "s_add_u32 s45, s84, 0\n"
    "buffer_load_dwordx4 v128, s[24:27], s45 offen offset:0 lds\n"
    "v_lshlrev_b32 v216, 16, v48\n"
    "v_max3_f32 v244, v96, v97, v98\n"
    "v_and_b32 v217, s17, v48\n"
    "v_max3_f32 v244, v244, v99, v100\n"
    "v_lshlrev_b32 v218, 16, v49\n"
    "v_max3_f32 v244, v244, v101, v102\n"
    "v_and_b32 v219, s17, v49\n"
    "v_max_f32 v244, v244, v103\n"
    "v_lshlrev_b32 v220, 16, v50\n"
    "v_mov_b32 v245, v244\n"
    "v_and_b32 v221, s17, v50\n"
    "s_nop 0\n"
    "v_permlane32_swap_b32 v244, v245\n"
    "v_lshlrev_b32 v222, 16, v51\n"
    "v_max_f32 v244, v244, v245\n"
    "v_and_b32 v223, s17, v51\n"
    "v_mov_b32 v245, v244\n"
    "v_pk_mul_f32 v[216:217], v[216:217], s[50:51] op_sel_hi:[1,0]\n"
    "s_nop 0\n"
    "v_permlane16_swap_b32 v244, v245\n"
    "v_pk_mul_f32 v[218:219], v[218:219], s[50:51] op_sel_hi:[1,0]\n"
    "v_max_f32 v244, v244, v245\n"
    "v_pk_mul_f32 v[220:221], v[220:221], s[50:51] op_sel_hi:[1,0]\n"
    "v_sub_f32 v192, 0, v244\n"
    "v_pk_mul_f32 v[222:223], v[222:223], s[50:51] op_sel_hi:[1,0]\n"
    "v_sub_f32 v193, 0, v244\n"
    "v_cvt_pk_bf16_f32 v48, v216, v217\n"
    "v_sub_f32 v194, 0, v244\n"
    "v_cvt_pk_bf16_f32 v49, v218, v219\n"
    "v_sub_f32 v195, 0, v244\n"
    "v_cvt_pk_bf16_f32 v50, v220, v221\n"
    "v_sub_f32 v96, v96, v244\n"
    "v_cvt_pk_bf16_f32 v51, v222, v223\n"
    "v_sub_f32 v97, v97, v244\n"
    "v_lshlrev_b32 v224, 16, v52\n"
    "v_sub_f32 v98, v98, v244\n"
    "v_and_b32 v225, s17, v52\n"
    "v_sub_f32 v99, v99, v244\n"
    "v_lshlrev_b32 v226, 16, v53\n"
    "v_sub_f32 v100, v100, v244\n"
    "v_and_b32 v227, s17, v53\n"
    "v_sub_f32 v101, v101, v244\n"
    "v_lshlrev_b32 v228, 16, v54\n"
    "v_sub_f32 v102, v102, v244\n"
    "v_and_b32 v229, s17, v54\n"
    "v_sub_f32 v103, v103, v244\n"
    "v_lshlrev_b32 v230, 16, v55\n"
    "v_and_b32 v231, s17, v55\n"
    "v_pk_mul_f32 v[224:225], v[224:225], s[50:51] op_sel_hi:[1,0]\n"
    "v_pk_mul_f32 v[226:227], v[226:227], s[50:51] op_sel_hi:[1,0]\n"
    "v_pk_mul_f32 v[228:229], v[228:229], s[50:51] op_sel_hi:[1,0]\n"
    "v_pk_mul_f32 v[230:231], v[230:231], s[50:51] op_sel_hi:[1,0]\n"
    "v_cvt_pk_bf16_f32 v52, v224, v225\n"
    "v_cvt_pk_bf16_f32 v53, v226, v227\n"
    "v_cvt_pk_bf16_f32 v54, v228, v229\n"
    "v_cvt_pk_bf16_f32 v55, v230, v231\n"
    "ds_read_b128 v[40:43], v243 offset:2048\n"
    "ds_read_b128 v[44:47], v232 offset:2048\n"
    "ds_write_b128 v242, v[48:51] offset:0\n"
    "ds_write_b128 v242, v[52:55] offset:1024\n"
    "buffer_load_dwordx4 v129, s[24:27], s45 offen offset:1024 lds\n"
    "v_lshlrev_b32 v216, 16, v56\n"
    "v_and_b32 v217, s17, v56\n"
    "v_lshlrev_b32 v218, 16, v57\n"
    "v_and_b32 v219, s17, v57\n"
    "v_lshlrev_b32 v220, 16, v58\n"
    "v_and_b32 v221, s17, v58\n"
    "v_lshlrev_b32 v222, 16, v59\n"
    "v_and_b32 v223, s17, v59\n"
    "v_pk_mul_f32 v[216:217], v[216:217], s[50:51] op_sel_hi:[1,0]\n"
    "v_pk_mul_f32 v[218:219], v[218:219], s[50:51] op_sel_hi:[1,0]\n"
    "v_pk_mul_f32 v[220:221], v[220:221], s[50:51] op_sel_hi:[1,0]\n"
    "v_pk_mul_f32 v[222:223], v[222:223], s[50:51] op_sel_hi:[1,0]\n"
    "s_waitcnt lgkmcnt(4)\n"
    "v_mfma_f32_16x16x32_bf16 v[104:107], a[224:227], v[24:27], 0\n"
    "v_cvt_pk_bf16_f32 v56, v216, v217\n"
    "v_cvt_pk_bf16_f32 v57, v218, v219\n"
    "v_mfma_f32_16x16x32_bf16 v[104:107], a[228:231], v[28:31], v[104:107]\n"
    "v_cvt_pk_bf16_f32 v58, v220, v221\n"
    "v_cvt_pk_bf16_f32 v59, v222, v223\n"
    "v_mfma_f32_16x16x32_bf16 v[104:107], a[232:235], v[32:35], v[104:107]\n"
    "v_lshlrev_b32 v224, 16, v60\n"
    "v_and_b32 v225, s17, v60\n"
    "v_mfma_f32_16x16x32_bf16 v[104:107], a[236:239], v[36:39], v[104:107]\n"
    "v_lshlrev_b32 v226, 16, v61\n"
    "v_and_b32 v227, s17, v61\n"
    "v_lshlrev_b32 v228, 16, v62\n"
    "v_and_b32 v229, s17, v62\n"
    "v_lshlrev_b32 v230, 16, v63\n"
    "v_and_b32 v231, s17, v63\n"
    "v_pk_mul_f32 v[224:225], v[224:225], s[50:51] op_sel_hi:[1,0]\n"
    "v_pk_mul_f32 v[226:227], v[226:227], s[50:51] op_sel_hi:[1,0]\n"
    "v_pk_mul_f32 v[228:229], v[228:229], s[50:51] op_sel_hi:[1,0]\n"
    "v_pk_mul_f32 v[230:231], v[230:231], s[50:51] op_sel_hi:[1,0]\n"
    "s_waitcnt lgkmcnt(1)\n"
    "v_mfma_f32_16x16x32_bf16 v[104:107], a[240:243], v[40:43], v[104:107]\n"
    "v_cvt_pk_bf16_f32 v60, v224, v225\n"
    "v_cvt_pk_bf16_f32 v61, v226, v227\n"
    "v_mfma_f32_16x16x32_bf16 v[104:107], a[244:247], v[44:47], v[104:107]\n"
    "v_cvt_pk_bf16_f32 v62, v228, v229\n"
    "v_cvt_pk_bf16_f32 v63, v230, v231\n"
    "ds_read_b128 v[48:51], v243 offset:0\n"
    "ds_read_b128 v[52:55], v232 offset:0\n"
    "ds_write_b128 v242, v[56:59] offset:2048\n"
    "ds_write_b128 v242, v[60:63] offset:3072\n"
    "buffer_load_dwordx4 v130, s[24:27], s45 offen offset:2048 lds\n"
    "v_lshlrev_b32 v216, 16, v64\n"
    "v_and_b32 v217, s17, v64\n"
    "v_lshlrev_b32 v218, 16, v65\n"
    "v_and_b32 v219, s17, v65\n"
    "v_lshlrev_b32 v220, 16, v66\n"
    "v_and_b32 v221, s17, v66\n"
    "v_lshlrev_b32 v222, 16, v67\n"
    "v_and_b32 v223, s17, v67\n"
    "v_pk_mul_f32 v[216:217], v[216:217], s[50:51] op_sel_hi:[1,0]\n"
    "v_pk_mul_f32 v[218:219], v[218:219], s[50:51] op_sel_hi:[1,0]\n"
    "v_pk_mul_f32 v[220:221], v[220:221], s[50:51] op_sel_hi:[1,0]\n"
    "v_pk_mul_f32 v[222:223], v[222:223], s[50:51] op_sel_hi:[1,0]\n"
    "v_mfma_f32_16x16x32_bf16 v[108:111], a[248:251], v[24:27], 0\n"
    "v_cvt_pk_bf16_f32 v64, v216, v217\n"
    "v_cvt_pk_bf16_f32 v65, v218, v219\n"
    "v_mfma_f32_16x16x32_bf16 v[108:111], a[252:255], v[28:31], v[108:111]\n"
    "v_cvt_pk_bf16_f32 v66, v220, v221\n"
    "v_cvt_pk_bf16_f32 v67, v222, v223\n"
    "v_mfma_f32_16x16x32_bf16 v[108:111], a[192:195], v[32:35], v[108:111]\n"
    "v_lshlrev_b32 v224, 16, v68\n"
    "v_and_b32 v225, s17, v68\n"
    "v_mfma_f32_16x16x32_bf16 v[108:111], a[196:199], v[36:39], v[108:111]\n"
    "v_lshlrev_b32 v226, 16, v69\n"
    "v_and_b32 v227, s17, v69\n"
    "v_lshlrev_b32 v228, 16, v70\n"
    "v_and_b32 v229, s17, v70\n"
    "v_lshlrev_b32 v230, 16, v71\n"
    "v_and_b32 v231, s17, v71\n"
    "v_pk_mul_f32 v[224:225], v[224:225], s[50:51] op_sel_hi:[1,0]\n"
    "v_pk_mul_f32 v[226:227], v[226:227], s[50:51] op_sel_hi:[1,0]\n"
    "v_pk_mul_f32 v[228:229], v[228:229], s[50:51] op_sel_hi:[1,0]\n"
    "v_pk_mul_f32 v[230:231], v[230:231], s[50:51] op_sel_hi:[1,0]\n"
    "v_mfma_f32_16x16x32_bf16 v[108:111], a[200:203], v[40:43], v[108:111]\n"
    "v_cvt_pk_bf16_f32 v68, v224, v225\n"
    "v_cvt_pk_bf16_f32 v69, v226, v227\n"
    "v_mfma_f32_16x16x32_bf16 v[108:111], a[204:207], v[44:47], v[108:111]\n"
    "v_cvt_pk_bf16_f32 v70, v228, v229\n"
    "v_cvt_pk_bf16_f32 v71, v230, v231\n"
    "ds_read_b128 v[56:59], v243 offset:2048\n"
    "ds_read_b128 v[60:63], v232 offset:2048\n"
    "ds_write_b128 v242, v[64:67] offset:0\n"
    "ds_write_b128 v242, v[68:71] offset:1024\n"
    "ds_read_b128 v[64:67], v243 offset:0\n"
    "ds_read_b128 v[68:71], v232 offset:0\n"
    "v_max3_f32 v246, v104, v105, v106\n"
    "v_max3_f32 v246, v246, v107, v108\n"
    "v_max3_f32 v246, v246, v109, v110\n"
    "v_max_f32 v246, v246, v111\n"
    "v_mov_b32 v247, v246\n"
    "s_nop 1\n"
    "v_permlane32_swap_b32 v246, v247\n"
    "v_max_f32 v246, v246, v247\n"
    "v_mov_b32 v247, v246\n"
    "s_waitcnt lgkmcnt(6)\n"
    "v_mfma_f32_16x16x32_bf16 v[112:115], a[224:227], v[48:51], 0\n"
    "v_permlane16_swap_b32 v246, v247\n"
    "v_max_f32 v246, v246, v247\n"
    "v_sub_f32 v196, 0, v246\n"
    "v_mfma_f32_16x16x32_bf16 v[112:115], a[228:231], v[52:55], v[112:115]\n"
    "v_sub_f32 v197, 0, v246\n"
    "v_sub_f32 v198, 0, v246\n"
    "v_sub_f32 v199, 0, v246\n"
    "s_waitcnt lgkmcnt(5)\n"
    "v_mfma_f32_16x16x32_bf16 v[112:115], a[232:235], v[56:59], v[112:115]\n"
    "v_sub_f32 v104, v104, v246\n"
    "v_sub_f32 v105, v105, v246\n"
    "v_sub_f32 v106, v106, v246\n"
    "s_waitcnt lgkmcnt(4)\n"
    "v_mfma_f32_16x16x32_bf16 v[112:115], a[236:239], v[60:63], v[112:115]\n"
    "v_sub_f32 v107, v107, v246\n"
    "v_sub_f32 v108, v108, v246\n"
    "v_sub_f32 v109, v109, v246\n"
    "s_waitcnt lgkmcnt(1)\n"
    "v_mfma_f32_16x16x32_bf16 v[112:115], a[240:243], v[64:67], v[112:115]\n"
    "v_sub_f32 v110, v110, v246\n"
    "v_sub_f32 v111, v111, v246\n"
    "v_exp_f32 v216, v96\n"
    "s_waitcnt lgkmcnt(0)\n"
    "v_mfma_f32_16x16x32_bf16 v[112:115], a[244:247], v[68:71], v[112:115]\n"
    "v_exp_f32 v217, v97\n"
    "v_exp_f32 v218, v98\n"
    "v_exp_f32 v219, v99\n"
    "v_mfma_f32_16x16x32_bf16 v[116:119], a[248:251], v[48:51], 0\n"
    "v_cvt_pk_bf16_f32 v160, v216, v217\n"
    "v_cvt_pk_bf16_f32 v161, v218, v219\n"
    "v_add_f32 v212, v216, v217\n"
    "v_mfma_f32_16x16x32_bf16 v[116:119], a[252:255], v[52:55], v[116:119]\n"
    "v_add_f32 v212, v212, v218\n"
    "v_add_f32 v212, v212, v219\n"
    "v_exp_f32 v216, v100\n"
    "v_mfma_f32_16x16x32_bf16 v[116:119], a[192:195], v[56:59], v[116:119]\n"
    "v_exp_f32 v217, v101\n"
    "v_exp_f32 v218, v102\n"
    "v_exp_f32 v219, v103\n"
    "v_mfma_f32_16x16x32_bf16 v[116:119], a[196:199], v[60:63], v[116:119]\n"
    "v_cvt_pk_bf16_f32 v162, v216, v217\n"
    "v_cvt_pk_bf16_f32 v163, v218, v219\n"
    "v_add_f32 v212, v212, v216\n"
    "v_mfma_f32_16x16x32_bf16 v[116:119], a[200:203], v[64:67], v[116:119]\n"
    "v_add_f32 v212, v212, v217\n"
    "v_add_f32 v212, v212, v218\n"
    "v_add_f32 v212, v212, v219\n"
    "v_mfma_f32_16x16x32_bf16 v[116:119], a[204:207], v[68:71], v[116:119]\n"
    "v_exp_f32 v220, v104\n"
    "v_exp_f32 v221, v105\n"
    "v_exp_f32 v222, v106\n"
    "v_exp_f32 v223, v107\n"
    "v_cvt_pk_bf16_f32 v164, v220, v221\n"
    "v_cvt_pk_bf16_f32 v165, v222, v223\n"
    "v_add_f32 v213, v220, v221\n"
    "v_add_f32 v213, v213, v222\n"
    "v_add_f32 v213, v213, v223\n"
    "v_exp_f32 v220, v108\n"
    "v_exp_f32 v221, v109\n"
    "v_exp_f32 v222, v110\n"
    "v_exp_f32 v223, v111\n"
    "v_cvt_pk_bf16_f32 v166, v220, v221\n"
    "v_cvt_pk_bf16_f32 v167, v222, v223\n"
    "v_add_f32 v213, v213, v220\n"
    "v_add_f32 v213, v213, v221\n"
    "v_add_f32 v213, v213, v222\n"
    "v_add_f32 v213, v213, v223\n"
    "v_max3_f32 v248, v112, v113, v114\n"
    "v_max3_f32 v248, v248, v115, v116\n"
    "v_max3_f32 v248, v248, v117, v118\n"
    "v_max_f32 v248, v248, v119\n"
    "v_mov_b32 v249, v248\n"
    "s_nop 1\n"
    "v_permlane32_swap_b32 v248, v249\n"
    "v_max_f32 v248, v248, v249\n"
    "v_mov_b32 v249, v248\n"
    "s_nop 1\n"
    "v_permlane16_swap_b32 v248, v249\n"
    "v_max_f32 v248, v248, v249\n"
    "v_sub_f32 v200, 0, v248\n"
    "v_sub_f32 v201, 0, v248\n"
    "v_sub_f32 v202, 0, v248\n"
    "v_sub_f32 v203, 0, v248\n"
    "v_sub_f32 v112, v112, v248\n"
    "v_sub_f32 v113, v113, v248\n"
    "v_sub_f32 v114, v114, v248\n"
    "v_sub_f32 v115, v115, v248\n"
    "v_sub_f32 v116, v116, v248\n"
    "v_sub_f32 v117, v117, v248\n"
    "v_sub_f32 v118, v118, v248\n"
    "v_sub_f32 v119, v119, v248\n"
    "v_exp_f32 v224, v112\n"
    "v_exp_f32 v225, v113\n"
    "v_exp_f32 v226, v114\n"
    "v_exp_f32 v227, v115\n"
    "v_cvt_pk_bf16_f32 v168, v224, v225\n"
    "v_cvt_pk_bf16_f32 v169, v226, v227\n"
    "v_add_f32 v214, v224, v225\n"
    "v_add_f32 v214, v214, v226\n"
    "v_add_f32 v214, v214, v227\n"
    "v_exp_f32 v224, v116\n"
    "v_exp_f32 v225, v117\n"
    "v_exp_f32 v226, v118\n"
    "v_exp_f32 v227, v119\n"
    "v_cvt_pk_bf16_f32 v170, v224, v225\n"
    "v_cvt_pk_bf16_f32 v171, v226, v227\n"
    "v_add_f32 v214, v214, v224\n"
    "v_add_f32 v214, v214, v225\n"
    "v_add_f32 v214, v214, v226\n"
    "v_add_f32 v214, v214, v227\n"
    "s_waitcnt vmcnt(3)\n"
    "s_add_u32 m0, s62, 24576\n"
    "s_add_u32 s45, s44, 0\n"
    "buffer_load_dwordx4 v128, s[20:23], s45 offen offset:0 lds\n"
    "v_lshlrev_b32 v216, 16, v72\n"
    "v_and_b32 v217, s17, v72\n"
    "v_lshlrev_b32 v218, 16, v73\n"
    "v_and_b32 v219, s17, v73\n"
    "v_lshlrev_b32 v220, 16, v74\n"
    "v_and_b32 v221, s17, v74\n"
    "v_lshlrev_b32 v222, 16, v75\n"
    "v_and_b32 v223, s17, v75\n"
    "v_pk_mul_f32 v[216:217], v[216:217], s[50:51] op_sel_hi:[1,0]\n"
    "v_pk_mul_f32 v[218:219], v[218:219], s[50:51] op_sel_hi:[1,0]\n"
    "v_pk_mul_f32 v[220:221], v[220:221], s[50:51] op_sel_hi:[1,0]\n"
    "buffer_load_dwordx4 v129, s[20:23], s45 offen offset:1024 lds\n"
    "v_pk_mul_f32 v[222:223], v[222:223], s[50:51] op_sel_hi:[1,0]\n"
    "v_cvt_pk_bf16_f32 v72, v216, v217\n"
    "v_cvt_pk_bf16_f32 v73, v218, v219\n"
    "v_cvt_pk_bf16_f32 v74, v220, v221\n"
    "v_cvt_pk_bf16_f32 v75, v222, v223\n"
    "v_lshlrev_b32 v224, 16, v76\n"
    "v_and_b32 v225, s17, v76\n"
    "v_lshlrev_b32 v226, 16, v77\n"
    "v_and_b32 v227, s17, v77\n"
    "v_lshlrev_b32 v228, 16, v78\n"
    "v_and_b32 v229, s17, v78\n"
    "v_lshlrev_b32 v230, 16, v79\n"
    "v_and_b32 v231, s17, v79\n"
    "buffer_load_dwordx4 v130, s[20:23], s45 offen offset:2048 lds\n"
    "v_pk_mul_f32 v[224:225], v[224:225], s[50:51] op_sel_hi:[1,0]\n"
    "v_pk_mul_f32 v[226:227], v[226:227], s[50:51] op_sel_hi:[1,0]\n"
    "v_pk_mul_f32 v[228:229], v[228:229], s[50:51] op_sel_hi:[1,0]\n"
    "v_pk_mul_f32 v[230:231], v[230:231], s[50:51] op_sel_hi:[1,0]\n"
    "v_cvt_pk_bf16_f32 v76, v224, v225\n"
    "v_cvt_pk_bf16_f32 v77, v226, v227\n"
    "v_cvt_pk_bf16_f32 v78, v228, v229\n"
    "v_cvt_pk_bf16_f32 v79, v230, v231\n"
    "ds_write_b128 v242, v[72:75] offset:2048\n"
    "ds_write_b128 v242, v[76:79] offset:3072\n"
    "s_add_u32 m0, s62, 86016\n"
    "s_add_u32 s45, s84, 12288\n"
    "buffer_load_dwordx4 v128, s[24:27], s45 offen offset:0 lds\n"
    "v_lshlrev_b32 v216, 16, v80\n"
    "v_and_b32 v217, s17, v80\n"
    "v_lshlrev_b32 v218, 16, v81\n"
    "v_and_b32 v219, s17, v81\n"
    "v_lshlrev_b32 v220, 16, v82\n"
    "v_and_b32 v221, s17, v82\n"
    "v_lshlrev_b32 v222, 16, v83\n"
    "v_and_b32 v223, s17, v83\n"
    "v_pk_mul_f32 v[216:217], v[216:217], s[50:51] op_sel_hi:[1,0]\n"
    "v_pk_mul_f32 v[218:219], v[218:219], s[50:51] op_sel_hi:[1,0]\n"
    "v_pk_mul_f32 v[220:221], v[220:221], s[50:51] op_sel_hi:[1,0]\n"
    "buffer_load_dwordx4 v129, s[24:27], s45 offen offset:1024 lds\n"
    "v_pk_mul_f32 v[222:223], v[222:223], s[50:51] op_sel_hi:[1,0]\n"
    "v_cvt_pk_bf16_f32 v80, v216, v217\n"
    "v_cvt_pk_bf16_f32 v81, v218, v219\n"
    "v_cvt_pk_bf16_f32 v82, v220, v221\n"
    "v_cvt_pk_bf16_f32 v83, v222, v223\n"
    "v_lshlrev_b32 v224, 16, v84\n"
    "v_and_b32 v225, s17, v84\n"
    "v_lshlrev_b32 v226, 16, v85\n"
    "v_and_b32 v227, s17, v85\n"
    "v_lshlrev_b32 v228, 16, v86\n"
    "v_and_b32 v229, s17, v86\n"
    "v_lshlrev_b32 v230, 16, v87\n"
    "v_and_b32 v231, s17, v87\n"
    "buffer_load_dwordx4 v130, s[24:27], s45 offen offset:2048 lds\n"
    "v_pk_mul_f32 v[224:225], v[224:225], s[50:51] op_sel_hi:[1,0]\n"
    "v_pk_mul_f32 v[226:227], v[226:227], s[50:51] op_sel_hi:[1,0]\n"
    "v_pk_mul_f32 v[228:229], v[228:229], s[50:51] op_sel_hi:[1,0]\n"
    "v_pk_mul_f32 v[230:231], v[230:231], s[50:51] op_sel_hi:[1,0]\n"
    "v_cvt_pk_bf16_f32 v84, v224, v225\n"
    "v_cvt_pk_bf16_f32 v85, v226, v227\n"
    "v_cvt_pk_bf16_f32 v86, v228, v229\n"
    "v_cvt_pk_bf16_f32 v87, v230, v231\n"
    "ds_read_b128 v[72:75], v243 offset:2048\n"
    "ds_read_b128 v[76:79], v232 offset:2048\n"
    "ds_write_b128 v242, v[80:83] offset:0\n"
    "ds_write_b128 v242, v[84:87] offset:1024\n"
    "s_add_u32 m0, s62, 36864\n"
    "s_add_u32 s45, s44, 12288\n"
    "buffer_load_dwordx4 v128, s[20:23], s45 offen offset:0 lds\n"
    "v_lshlrev_b32 v216, 16, v88\n"
    "v_and_b32 v217, s17, v88\n"
    "v_lshlrev_b32 v218, 16, v89\n"
    "v_and_b32 v219, s17, v89\n"
    "v_lshlrev_b32 v220, 16, v90\n"
    "v_and_b32 v221, s17, v90\n"
    "v_lshlrev_b32 v222, 16, v91\n"
    "v_and_b32 v223, s17, v91\n"
    "v_pk_mul_f32 v[216:217], v[216:217], s[50:51] op_sel_hi:[1,0]\n"
    "v_pk_mul_f32 v[218:219], v[218:219], s[50:51] op_sel_hi:[1,0]\n"
    "v_pk_mul_f32 v[220:221], v[220:221], s[50:51] op_sel_hi:[1,0]\n"
    "buffer_load_dwordx4 v129, s[20:23], s45 offen offset:1024 lds\n"
    "v_pk_mul_f32 v[222:223], v[222:223], s[50:51] op_sel_hi:[1,0]\n"
    "v_cvt_pk_bf16_f32 v88, v216, v217\n"
    "v_cvt_pk_bf16_f32 v89, v218, v219\n"
    "v_cvt_pk_bf16_f32 v90, v220, v221\n"
    "v_cvt_pk_bf16_f32 v91, v222, v223\n"
    "v_lshlrev_b32 v224, 16, v92\n"
    "v_and_b32 v225, s17, v92\n"
    "v_lshlrev_b32 v226, 16, v93\n"
    "v_and_b32 v227, s17, v93\n"
    "v_lshlrev_b32 v228, 16, v94\n"
    "v_and_b32 v229, s17, v94\n"
    "v_lshlrev_b32 v230, 16, v95\n"
    "v_and_b32 v231, s17, v95\n"
    "buffer_load_dwordx4 v130, s[20:23], s45 offen offset:2048 lds\n"
    "s_mov_b32 s84, s44\n"
    "s_add_u32 s87, s87, 1\n"
    "v_readlane_b32 s44, v252, s87\n"
    "s_mov_b32 s40, 49152\n"
    "s_mov_b32 s42, 24576\n"
    "v_pk_mul_f32 v[224:225], v[224:225], s[50:51] op_sel_hi:[1,0]\n"
    "v_pk_mul_f32 v[226:227], v[226:227], s[50:51] op_sel_hi:[1,0]\n"
    "v_pk_mul_f32 v[228:229], v[228:229], s[50:51] op_sel_hi:[1,0]\n"
    "v_pk_mul_f32 v[230:231], v[230:231], s[50:51] op_sel_hi:[1,0]\n"
    "v_cvt_pk_bf16_f32 v92, v224, v225\n"
    "v_cvt_pk_bf16_f32 v93, v226, v227\n"
    "v_cvt_pk_bf16_f32 v94, v228, v229\n"
    "v_cvt_pk_bf16_f32 v95, v230, v231\n"
    "ds_read_b128 v[80:83], v243 offset:0\n"
    "ds_read_b128 v[84:87], v232 offset:0\n"
    "ds_write_b128 v242, v[88:91] offset:2048\n"
    "ds_write_b128 v242, v[92:95] offset:3072\n"
    "ds_read_b128 a[208:211], v235 offset:14336\n"
    "ds_read_b128 a[212:215], v235 offset:14848\n"
    "ds_read_b128 a[216:219], v235 offset:18432\n"
    "ds_read_b128 a[220:223], v235 offset:18944\n"
    "v_add_u32 v236, s41, v234\n"
    "v_xor_b32 v241, 32, v236\n"
    "s_mov_b32 s43, s40\n"
    "s_mov_b32 s40, s41\n"
    "s_mov_b32 s41, s42\n"
    "s_mov_b32 s42, s43\n"
    "ds_read_b128 v[88:91], v243 offset:2048\n"
    "ds_read_b128 v[92:95], v232 offset:2048\n"
    "s_waitcnt lgkmcnt(10)\n"
    "v_mfma_f32_16x16x32_bf16 v[120:123], a[224:227], v[72:75], 0\n"
    "v_mfma_f32_16x16x32_bf16 v[120:123], a[228:231], v[76:79], v[120:123]\n"
    "s_waitcnt lgkmcnt(9)\n"
    "v_mfma_f32_16x16x32_bf16 v[120:123], a[232:235], v[80:83], v[120:123]\n"
    "s_waitcnt lgkmcnt(8)\n"
    "v_mfma_f32_16x16x32_bf16 v[120:123], a[236:239], v[84:87], v[120:123]\n"
    "s_waitcnt lgkmcnt(1)\n"
    "v_mfma_f32_16x16x32_bf16 v[120:123], a[240:243], v[88:91], v[120:123]\n"
    "s_waitcnt lgkmcnt(0)\n"
    "v_mfma_f32_16x16x32_bf16 v[120:123], a[244:247], v[92:95], v[120:123]\n"
    "v_mfma_f32_16x16x32_bf16 v[124:127], a[248:251], v[72:75], 0\n"
    "v_mfma_f32_16x16x32_bf16 v[124:127], a[252:255], v[76:79], v[124:127]\n"
    "v_mfma_f32_16x16x32_bf16 v[124:127], a[192:195], v[80:83], v[124:127]\n"
    "v_mfma_f32_16x16x32_bf16 v[124:127], a[196:199], v[84:87], v[124:127]\n"
    "v_mfma_f32_16x16x32_bf16 v[124:127], a[200:203], v[88:91], v[124:127]\n"
    "v_mfma_f32_16x16x32_bf16 v[124:127], a[204:207], v[92:95], v[124:127]\n"
    "ds_read_b128 a[224:227], v235 offset:19456\n"
    "ds_read_b128 a[228:231], v235 offset:19968\n"
    "ds_read_b128 a[232:235], v235 offset:20480\n"
    "ds_read_b128 a[236:239], v235 offset:20992\n"
    "ds_read_b128 a[192:195], v235 offset:12288\n"
    "ds_read_b128 a[196:199], v235 offset:12800\n"
    "ds_read_b128 a[200:203], v235 offset:13312\n"
    "ds_read_b128 a[204:207], v235 offset:13824\n"
    "v_max3_f32 v250, v120, v121, v122\n"
    "v_max3_f32 v250, v250, v123, v124\n"
    "v_max3_f32 v250, v250, v125, v126\n"
    "v_max_f32 v250, v250, v127\n"
    "v_mov_b32 v251, v250\n"
    "s_nop 1\n"
    "v_permlane32_swap_b32 v250, v251\n"
    "v_max_f32 v250, v250, v251\n"
    "v_mov_b32 v251, v250\n"
    "s_nop 1\n"
    "v_permlane16_swap_b32 v250, v251\n"
    "v_max_f32 v250, v250, v251\n"
    "s_waitcnt vmcnt(9)\n"
    "s_waitcnt lgkmcnt(0)\n"
    "s_branch LPEEL_4_%=\n"
    "LPRO1_4_%=:\n"
    "s_mov_b32 s86, LFIN1_4_%=-LSTART_%=+13631488\n"
    "v_cmp_gt_u32 vcc, 1280, v253\n"
    "v_mov_b32 v232, s35\n"
    "s_nop 0\n"
    "v_cndmask_b32 v255, v232, v253, vcc\n"
    "s_sub_u32 s79, s55, 0\n"
    "v_xor_b32 v232, 64, v243\n"
    "v_add_u32 v235, s41, v233\n"
    "s_add_u32 s60, s54, 12288\n"
    "buffer_load_dwordx4 v[64:67], v240, s[28:31], s60 offen offset:256\n"
    "buffer_load_dwordx4 v[68:71], v240, s[28:31], s60 offen offset:3328\n"
    "s_add_u32 s61, s54, 18432\n"
    "buffer_load_dwordx4 v[72:75], v240, s[28:31], s61 offen offset:0\n"
    "buffer_load_dwordx4 v[76:79], v240, s[28:31], s61 offen offset:3072\n"
    "buffer_load_dwordx4 v[80:83], v240, s[28:31], s61 offen offset:128\n"
    "buffer_load_dwordx4 v[84:87], v240, s[28:31], s61 offen offset:3200\n"
    "s_waitcnt vmcnt(28)\n"
    "s_barrier\n"
    "ds_read_b128 a[224:227], v235 offset:0\n"
    "ds_read_b128 a[228:231], v235 offset:512\n"
    "ds_read_b128 a[232:235], v235 offset:1024\n"
    "ds_read_b128 a[236:239], v235 offset:1536\n"
    "ds_read_b128 a[240:243], v235 offset:2048\n"
    "ds_read_b128 a[244:247], v235 offset:2560\n"
    "ds_read_b128 a[248:251], v235 offset:6144\n"
    "ds_read_b128 a[252:255], v235 offset:6656\n"
    "ds_read_b128 a[192:195], v235 offset:7168\n"
    "ds_read_b128 a[196:199], v235 offset:7680\n"
    "ds_read_b128 a[200:203], v235 offset:8192\n"
    "ds_read_b128 a[204:207], v235 offset:8704\n"
    "buffer_load_dwordx4 v[88:91], v240, s[28:31], s61 offen offset:256\n"
    "v_lshlrev_b32 v216, 16, v0\n"
    "v_and_b32 v217, s17, v0\n"
    "v_lshlrev_b32 v218, 16, v1\n"
    "v_and_b32 v219, s17, v1\n"
    "v_lshlrev_b32 v220, 16, v2\n"
    "v_and_b32 v221, s17, v2\n"
    "v_lshlrev_b32 v222, 16, v3\n"
    "v_and_b32 v223, s17, v3\n"
    "v_pk_mul_f32 v[216:217], v[216:217], s[50:51] op_sel_hi:[1,0]\n"
    "v_pk_mul_f32 v[218:219], v[218:219], s[50:51] op_sel_hi:[1,0]\n"
    "v_pk_mul_f32 v[220:221], v[220:221], s[50:51] op_sel_hi:[1,0]\n"
    "v_pk_mul_f32 v[222:223], v[222:223], s[50:51] op_sel_hi:[1,0]\n"
    "v_cvt_pk_bf16_f32 v0, v216, v217\n"
    "v_cvt_pk_bf16_f32 v1, v218, v219\n"
    "v_cvt_pk_bf16_f32 v2, v220, v221\n"
    "v_cvt_pk_bf16_f32 v3, v222, v223\n"
    "buffer_load_dwordx4 v[92:95], v240, s[28:31], s61 offen offset:3328\n"
    "v_lshlrev_b32 v224, 16, v4\n"
    "v_and_b32 v225, s17, v4\n"
    "v_lshlrev_b32 v226, 16, v5\n"
    "v_and_b32 v227, s17, v5\n"
    "v_lshlrev_b32 v228, 16, v6\n"
    "v_and_b32 v229, s17, v6\n"
    "v_lshlrev_b32 v230, 16, v7\n"
    "v_and_b32 v231, s17, v7\n"
    "v_pk_mul_f32 v[224:225], v[224:225], s[50:51] op_sel_hi:[1,0]\n"
    "v_pk_mul_f32 v[226:227], v[226:227], s[50:51] op_sel_hi:[1,0]\n"
    "v_pk_mul_f32 v[228:229], v[228:229], s[50:51] op_sel_hi:[1,0]\n"
    "v_pk_mul_f32 v[230:231], v[230:231], s[50:51] op_sel_hi:[1,0]\n"
    "v_cvt_pk_bf16_f32 v4, v224, v225\n"
    "v_cvt_pk_bf16_f32 v5, v226, v227\n"
    "v_cvt_pk_bf16_f32 v6, v228, v229\n"
    "v_cvt_pk_bf16_f32 v7, v230, v231\n"
    "ds_write_b128 v242, v[0:3] offset:0\n"
    "ds_write_b128 v242, v[4:7] offset:1024\n"
    "v_lshlrev_b32 v216, 16, v8\n"
    "v_and_b32 v217, s17, v8\n"
    "v_lshlrev_b32 v218, 16, v9\n"
    "v_and_b32 v219, s17, v9\n"
    "v_lshlrev_b32 v220, 16, v10\n"
    "v_and_b32 v221, s17, v10\n"
    "v_lshlrev_b32 v222, 16, v11\n"
    "v_and_b32 v223, s17, v11\n"
    "v_pk_mul_f32 v[216:217], v[216:217], s[50:51] op_sel_hi:[1,0]\n"
    "v_pk_mul_f32 v[218:219], v[218:219], s[50:51] op_sel_hi:[1,0]\n"
    "v_pk_mul_f32 v[220:221], v[220:221], s[50:51] op_sel_hi:[1,0]\n"
    "v_pk_mul_f32 v[222:223], v[222:223], s[50:51] op_sel_hi:[1,0]\n"
    "v_cvt_pk_bf16_f32 v8, v216, v217\n"
    "v_cvt_pk_bf16_f32 v9, v218, v219\n"
    "v_cvt_pk_bf16_f32 v10, v220, v221\n"
    "v_cvt_pk_bf16_f32 v11, v222, v223\n"
    "v_lshlrev_b32 v224, 16, v12\n"
    "v_and_b32 v225, s17, v12\n"
    "v_lshlrev_b32 v226, 16, v13\n"
    "v_and_b32 v227, s17, v13\n"
    "v_lshlrev_b32 v228, 16, v14\n"
    "v_and_b32 v229, s17, v14\n"
    "v_lshlrev_b32 v230, 16, v15\n"
    "v_and_b32 v231, s17, v15\n"
    "v_pk_mul_f32 v[224:225], v[224:225], s[50:51] op_sel_hi:[1,0]\n"
    "v_pk_mul_f32 v[226:227], v[226:227], s[50:51] op_sel_hi:[1,0]\n"
    "v_pk_mul_f32 v[228:229], v[228:229], s[50:51] op_sel_hi:[1,0]\n"
    "v_pk_mul_f32 v[230:231], v[230:231], s[50:51] op_sel_hi:[1,0]\n"
    "v_cvt_pk_bf16_f32 v12, v224, v225\n"
    "v_cvt_pk_bf16_f32 v13, v226, v227\n"
    "v_cvt_pk_bf16_f32 v14, v228, v229\n"
    "v_cvt_pk_bf16_f32 v15, v230, v231\n"
    "ds_read_b128 v[0:3], v243 offset:0\n"
    "ds_read_b128 v[4:7], v232 offset:0\n"
    "ds_write_b128 v242, v[8:11] offset:2048\n"
    "ds_write_b128 v242, v[12:15] offset:3072\n"
    "v_lshlrev_b32 v216, 16, v16\n"
    "v_and_b32 v217, s17, v16\n"
    "v_lshlrev_b32 v218, 16, v17\n"
    "v_and_b32 v219, s17, v17\n"
    "v_lshlrev_b32 v220, 16, v18\n"
    "v_and_b32 v221, s17, v18\n"
    "v_lshlrev_b32 v222, 16, v19\n"
    "v_and_b32 v223, s17, v19\n"
    "v_pk_mul_f32 v[216:217], v[216:217], s[50:51] op_sel_hi:[1,0]\n"
    "v_pk_mul_f32 v[218:219], v[218:219], s[50:51] op_sel_hi:[1,0]\n"
    "v_pk_mul_f32 v[220:221], v[220:221], s[50:51] op_sel_hi:[1,0]\n"
    "v_pk_mul_f32 v[222:223], v[222:223], s[50:51] op_sel_hi:[1,0]\n"
    "v_cvt_pk_bf16_f32 v16, v216, v217\n"
    "v_cvt_pk_bf16_f32 v17, v218, v219\n"
    "v_cvt_pk_bf16_f32 v18, v220, v221\n"
    "v_cvt_pk_bf16_f32 v19, v222, v223\n"
    "v_lshlrev_b32 v224, 16, v20\n"
    "v_and_b32 v225, s17, v20\n"
    "v_lshlrev_b32 v226, 16, v21\n"
    "v_and_b32 v227, s17, v21\n"
    "v_lshlrev_b32 v228, 16, v22\n"
    "v_and_b32 v229, s17, v22\n"
    "v_lshlrev_b32 v230, 16, v23\n"
    "v_and_b32 v231, s17, v23\n"
    "v_pk_mul_f32 v[224:225], v[224:225], s[50:51] op_sel_hi:[1,0]\n"
    "v_pk_mul_f32 v[226:227], v[226:227], s[50:51] op_sel_hi:[1,0]\n"
    "v_pk_mul_f32 v[228:229], v[228:229], s[50:51] op_sel_hi:[1,0]\n"
    "v_pk_mul_f32 v[230:231], v[230:231], s[50:51] op_sel_hi:[1,0]\n"
    "v_cvt_pk_bf16_f32 v20, v224, v225\n"
    "v_cvt_pk_bf16_f32 v21, v226, v227\n"
    "v_cvt_pk_bf16_f32 v22, v228, v229\n"
    "v_cvt_pk_bf16_f32 v23, v230, v231\n"
    "ds_read_b128 v[8:11], v243 offset:2048\n"
    "ds_read_b128 v[12:15], v232 offset:2048\n"
    "ds_write_b128 v242, v[16:19] offset:0\n"
    "ds_write_b128 v242, v[20:23] offset:1024\n"
    "s_waitcnt vmcnt(24)\n"
    "v_lshlrev_b32 v216, 16, v24\n"
    "v_and_b32 v217, s17, v24\n"
    "v_lshlrev_b32 v218, 16, v25\n"
    "v_and_b32 v219, s17, v25\n"
    "v_lshlrev_b32 v220, 16, v26\n"
    "v_and_b32 v221, s17, v26\n"
    "v_lshlrev_b32 v222, 16, v27\n"
    "v_and_b32 v223, s17, v27\n"
    "v_pk_mul_f32 v[216:217], v[216:217], s[50:51] op_sel_hi:[1,0]\n"
    "v_pk_mul_f32 v[218:219], v[218:219], s[50:51] op_sel_hi:[1,0]\n"
    "v_pk_mul_f32 v[220:221], v[220:221], s[50:51] op_sel_hi:[1,0]\n"
    "v_pk_mul_f32 v[222:223], v[222:223], s[50:51] op_sel_hi:[1,0]\n"
    "v_cvt_pk_bf16_f32 v24, v216, v217\n"
    "v_cvt_pk_bf16_f32 v25, v218, v219\n"
    "v_cvt_pk_bf16_f32 v26, v220, v221\n"
    "v_cvt_pk_bf16_f32 v27, v222, v223\n"
    "v_lshlrev_b32 v224, 16, v28\n"
    "v_and_b32 v225, s17, v28\n"
    "v_lshlrev_b32 v226, 16, v29\n"
    "v_and_b32 v227, s17, v29\n"
    "v_lshlrev_b32 v228, 16, v30\n"
    "v_and_b32 v229, s17, v30\n"
    "v_lshlrev_b32 v230, 16, v31\n"
    "v_and_b32 v231, s17, v31\n"
    "v_pk_mul_f32 v[224:225], v[224:225], s[50:51] op_sel_hi:[1,0]\n"
    "v_pk_mul_f32 v[226:227], v[226:227], s[50:51] op_sel_hi:[1,0]\n"
    "v_pk_mul_f32 v[228:229], v[228:229], s[50:51] op_sel_hi:[1,0]\n"
    "v_pk_mul_f32 v[230:231], v[230:231], s[50:51] op_sel_hi:[1,0]\n"
    "v_cvt_pk_bf16_f32 v28, v224, v225\n"
    "v_cvt_pk_bf16_f32 v29, v226, v227\n"
    "v_cvt_pk_bf16_f32 v30, v228, v229\n"
    "v_cvt_pk_bf16_f32 v31, v230, v231\n"
    "ds_read_b128 v[16:19], v243 offset:0\n"
    "ds_read_b128 v[20:23], v232 offset:0\n"
    "ds_write_b128 v242, v[24:27] offset:2048\n"
    "ds_write_b128 v242, v[28:31] offset:3072\n"
    "v_lshlrev_b32 v216, 16, v32\n"
    "v_and_b32 v217, s17, v32\n"
    "v_lshlrev_b32 v218, 16, v33\n"
    "v_and_b32 v219, s17, v33\n"
    "v_lshlrev_b32 v220, 16, v34\n"
    "v_and_b32 v221, s17, v34\n"
    "v_lshlrev_b32 v222, 16, v35\n"
    "v_and_b32 v223, s17, v35\n"
    "v_pk_mul_f32 v[216:217], v[216:217], s[50:51] op_sel_hi:[1,0]\n"
    "v_pk_mul_f32 v[218:219], v[218:219], s[50:51] op_sel_hi:[1,0]\n"
    "v_pk_mul_f32 v[220:221], v[220:221], s[50:51] op_sel_hi:[1,0]\n"
    "v_pk_mul_f32 v[222:223], v[222:223], s[50:51] op_sel_hi:[1,0]\n"
    "s_waitcnt lgkmcnt(4)\n"
    "v_mfma_f32_16x16x32_bf16 v[96:99], a[224:227], v[0:3], 0\n"
    "v_cvt_pk_bf16_f32 v32, v216, v217\n"
    "v_cvt_pk_bf16_f32 v33, v218, v219\n"
    "v_mfma_f32_16x16x32_bf16 v[96:99], a[228:231], v[4:7], v[96:99]\n"
    "v_cvt_pk_bf16_f32 v34, v220, v221\n"
    "v_cvt_pk_bf16_f32 v35, v222, v223\n"
    "v_mfma_f32_16x16x32_bf16 v[96:99], a[232:235], v[8:11], v[96:99]\n"
    "v_lshlrev_b32 v224, 16, v36\n"
    "v_and_b32 v225, s17, v36\n"
    "v_mfma_f32_16x16x32_bf16 v[96:99], a[236:239], v[12:15], v[96:99]\n"
    "v_lshlrev_b32 v226, 16, v37\n"
    "v_and_b32 v227, s17, v37\n"
    "v_lshlrev_b32 v228, 16, v38\n"
    "v_and_b32 v229, s17, v38\n"
    "v_lshlrev_b32 v230, 16, v39\n"
    "v_and_b32 v231, s17, v39\n"
    "v_pk_mul_f32 v[224:225], v[224:225], s[50:51] op_sel_hi:[1,0]\n"
    "v_pk_mul_f32 v[226:227], v[226:227], s[50:51] op_sel_hi:[1,0]\n"
    "v_pk_mul_f32 v[228:229], v[228:229], s[50:51] op_sel_hi:[1,0]\n"
    "v_pk_mul_f32 v[230:231], v[230:231], s[50:51] op_sel_hi:[1,0]\n"
    "s_waitcnt lgkmcnt(2)\n"
    "v_mfma_f32_16x16x32_bf16 v[96:99], a[240:243], v[16:19], v[96:99]\n"
    "v_cvt_pk_bf16_f32 v36, v224, v225\n"
    "v_cvt_pk_bf16_f32 v37, v226, v227\n"
    "v_mfma_f32_16x16x32_bf16 v[96:99], a[244:247], v[20:23], v[96:99]\n"
    "v_cvt_pk_bf16_f32 v38, v228, v229\n"
    "v_cvt_pk_bf16_f32 v39, v230, v231\n"
    "ds_read_b128 v[24:27], v243 offset:2048\n"
    "ds_read_b128 v[28:31], v232 offset:2048\n"
    "ds_write_b128 v242, v[32:35] offset:0\n"
    "ds_write_b128 v242, v[36:39] offset:1024\n"
    "v_lshlrev_b32 v216, 16, v40\n"
    "v_and_b32 v217, s17, v40\n"
    "v_lshlrev_b32 v218, 16, v41\n"
    "v_and_b32 v219, s17, v41\n"
    "v_lshlrev_b32 v220, 16, v42\n"
    "v_and_b32 v221, s17, v42\n"
    "v_lshlrev_b32 v222, 16, v43\n"
    "v_and_b32 v223, s17, v43\n"
    "v_pk_mul_f32 v[216:217], v[216:217], s[50:51] op_sel_hi:[1,0]\n"
    "v_pk_mul_f32 v[218:219], v[218:219], s[50:51] op_sel_hi:[1,0]\n"
    "v_pk_mul_f32 v[220:221], v[220:221], s[50:51] op_sel_hi:[1,0]\n"
    "v_pk_mul_f32 v[222:223], v[222:223], s[50:51] op_sel_hi:[1,0]\n"
    "v_mfma_f32_16x16x32_bf16 v[100:103], a[248:251], v[0:3], 0\n"
    "v_cvt_pk_bf16_f32 v40, v216, v217\n"
    "v_cvt_pk_bf16_f32 v41, v218, v219\n"
    "v_mfma_f32_16x16x32_bf16 v[100:103], a[252:255], v[4:7], v[100:103]\n"
    "v_cvt_pk_bf16_f32 v42, v220, v221\n"
    "v_cvt_pk_bf16_f32 v43, v222, v223\n"
    "v_mfma_f32_16x16x32_bf16 v[100:103], a[192:195], v[8:11], v[100:103]\n"
    "v_lshlrev_b32 v224, 16, v44\n"
    "v_and_b32 v225, s17, v44\n"
    "v_mfma_f32_16x16x32_bf16 v[100:103], a[196:199], v[12:15], v[100:103]\n"
    "v_lshlrev_b32 v226, 16, v45\n"
    "v_and_b32 v227, s17, v45\n"
    "v_lshlrev_b32 v228, 16, v46\n"
    "v_and_b32 v229, s17, v46\n"
    "v_lshlrev_b32 v230, 16, v47\n"
    "v_and_b32 v231, s17, v47\n"
    "v_pk_mul_f32 v[224:225], v[224:225], s[50:51] op_sel_hi:[1,0]\n"
    "v_pk_mul_f32 v[226:227], v[226:227], s[50:51] op_sel_hi:[1,0]\n"
    "v_pk_mul_f32 v[228:229], v[228:229], s[50:51] op_sel_hi:[1,0]\n"
    "v_pk_mul_f32 v[230:231], v[230:231], s[50:51] op_sel_hi:[1,0]\n"
    "v_mfma_f32_16x16x32_bf16 v[100:103], a[200:203], v[16:19], v[100:103]\n"
    "v_cvt_pk_bf16_f32 v44, v224, v225\n"
    "v_cvt_pk_bf16_f32 v45, v226, v227\n"
    "v_mfma_f32_16x16x32_bf16 v[100:103], a[204:207], v[20:23], v[100:103]\n"
    "v_cvt_pk_bf16_f32 v46, v228, v229\n"
    "v_cvt_pk_bf16_f32 v47, v230, v231\n"
    "ds_read_b128 v[32:35], v243 offset:0\n"
    "ds_read_b128 v[36:39], v232 offset:0\n"
    "ds_write_b128 v242, v[40:43] offset:2048\n"
    "ds_write_b128 v242, v[44:47] offset:3072\n"
    "s_waitcnt vmcnt(6)\n"
    "v_lshlrev_b32 v216, 16, v48\n"
    "v_max3_f32 v244, v96, v97, v98\n"
    "v_and_b32 v217, s17, v48\n"
    "v_max3_f32 v244, v244, v99, v100\n"
    "v_lshlrev_b32 v218, 16, v49\n"
    "v_max3_f32 v244, v244, v101, v102\n"
    "v_and_b32 v219, s17, v49\n"
    "v_max_f32 v244, v244, v103\n"
    "v_lshlrev_b32 v220, 16, v50\n"
    "v_mov_b32 v245, v244\n"
    "v_and_b32 v221, s17, v50\n"
    "s_nop 0\n"
    "v_permlane32_swap_b32 v244, v245\n"
    "v_lshlrev_b32 v222, 16, v51\n"
    "v_max_f32 v244, v244, v245\n"
    "v_and_b32 v223, s17, v51\n"
    "v_mov_b32 v245, v244\n"
    "v_pk_mul_f32 v[216:217], v[216:217], s[50:51] op_sel_hi:[1,0]\n"
    "s_nop 0\n"
    "v_permlane16_swap_b32 v244, v245\n"
    "v_pk_mul_f32 v[218:219], v[218:219], s[50:51] op_sel_hi:[1,0]\n"
    "v_max_f32 v244, v244, v245\n"
    "v_pk_mul_f32 v[220:221], v[220:221], s[50:51] op_sel_hi:[1,0]\n"
    "v_sub_f32 v192, 0, v244\n"
    "v_pk_mul_f32 v[222:223], v[222:223], s[50:51] op_sel_hi:[1,0]\n"
    "v_sub_f32 v193, 0, v244\n"
    "v_cvt_pk_bf16_f32 v48, v216, v217\n"
    "v_sub_f32 v194, 0, v244\n"
    "v_cvt_pk_bf16_f32 v49, v218, v219\n"
    "v_sub_f32 v195, 0, v244\n"
    "v_cvt_pk_bf16_f32 v50, v220, v221\n"
    "v_sub_f32 v96, v96, v244\n"
    "v_cvt_pk_bf16_f32 v51, v222, v223\n"
    "v_sub_f32 v97, v97, v244\n"
    "v_lshlrev_b32 v224, 16, v52\n"
    "v_sub_f32 v98, v98, v244\n"
    "v_and_b32 v225, s17, v52\n"
    "v_sub_f32 v99, v99, v244\n"
    "v_lshlrev_b32 v226, 16, v53\n"
    "v_sub_f32 v100, v100, v244\n"
    "v_and_b32 v227, s17, v53\n"
    "v_sub_f32 v101, v101, v244\n"
    "v_lshlrev_b32 v228, 16, v54\n"
    "v_sub_f32 v102, v102, v244\n"
    "v_and_b32 v229, s17, v54\n"
    "v_sub_f32 v103, v103, v244\n"
    "v_lshlrev_b32 v230, 16, v55\n"
    "v_and_b32 v231, s17, v55\n"
    "v_pk_mul_f32 v[224:225], v[224:225], s[50:51] op_sel_hi:[1,0]\n"
    "v_pk_mul_f32 v[226:227], v[226:227], s[50:51] op_sel_hi:[1,0]\n"
    "v_pk_mul_f32 v[228:229], v[228:229], s[50:51] op_sel_hi:[1,0]\n"
    "v_pk_mul_f32 v[230:231], v[230:231], s[50:51] op_sel_hi:[1,0]\n"
    "v_cvt_pk_bf16_f32 v52, v224, v225\n"
    "v_cvt_pk_bf16_f32 v53, v226, v227\n"
    "v_cvt_pk_bf16_f32 v54, v228, v229\n"
    "v_cvt_pk_bf16_f32 v55, v230, v231\n"
    "ds_read_b128 v[40:43], v243 offset:2048\n"
    "ds_read_b128 v[44:47], v232 offset:2048\n"
    "ds_write_b128 v242, v[48:51] offset:0\n"
    "ds_write_b128 v242, v[52:55] offset:1024\n"
    "v_lshlrev_b32 v216, 16, v56\n"
    "v_and_b32 v217, s17, v56\n"
    "v_lshlrev_b32 v218, 16, v57\n"
    "v_and_b32 v219, s17, v57\n"
    "v_lshlrev_b32 v220, 16, v58\n"
    "v_and_b32 v221, s17, v58\n"
    "v_lshlrev_b32 v222, 16, v59\n"
    "v_and_b32 v223, s17, v59\n"
    "v_pk_mul_f32 v[216:217], v[216:217], s[50:51] op_sel_hi:[1,0]\n"
    "v_pk_mul_f32 v[218:219], v[218:219], s[50:51] op_sel_hi:[1,0]\n"
    "v_pk_mul_f32 v[220:221], v[220:221], s[50:51] op_sel_hi:[1,0]\n"
    "v_pk_mul_f32 v[222:223], v[222:223], s[50:51] op_sel_hi:[1,0]\n"
    "s_waitcnt lgkmcnt(4)\n"
    "v_mfma_f32_16x16x32_bf16 v[104:107], a[224:227], v[24:27], 0\n"
    "v_cvt_pk_bf16_f32 v56, v216, v217\n"
    "v_cvt_pk_bf16_f32 v57, v218, v219\n"
    "v_mfma_f32_16x16x32_bf16 v[104:107], a[228:231], v[28:31], v[104:107]\n"
    "v_cvt_pk_bf16_f32 v58, v220, v221\n"
    "v_cvt_pk_bf16_f32 v59, v222, v223\n"
    "v_mfma_f32_16x16x32_bf16 v[104:107], a[232:235], v[32:35], v[104:107]\n"
    "v_lshlrev_b32 v224, 16, v60\n"
    "v_and_b32 v225, s17, v60\n"
    "v_mfma_f32_16x16x32_bf16 v[104:107], a[236:239], v[36:39], v[104:107]\n"
    "v_lshlrev_b32 v226, 16, v61\n"
    "v_and_b32 v227, s17, v61\n"
    "v_lshlrev_b32 v228, 16, v62\n"
    "v_and_b32 v229, s17, v62\n"
    "v_lshlrev_b32 v230, 16, v63\n"
    "v_and_b32 v231, s17, v63\n"
    "v_pk_mul_f32 v[224:225], v[224:225], s[50:51] op_sel_hi:[1,0]\n"
    "v_pk_mul_f32 v[226:227], v[226:227], s[50:51] op_sel_hi:[1,0]\n"
    "v_pk_mul_f32 v[228:229], v[228:229], s[50:51] op_sel_hi:[1,0]\n"
    "v_pk_mul_f32 v[230:231], v[230:231], s[50:51] op_sel_hi:[1,0]\n"
    "s_waitcnt lgkmcnt(2)\n"
    "v_mfma_f32_16x16x32_bf16 v[104:107], a[240:243], v[40:43], v[104:107]\n"
    "v_cvt_pk_bf16_f32 v60, v224, v225\n"
    "v_cvt_pk_bf16_f32 v61, v226, v227\n"
    "v_mfma_f32_16x16x32_bf16 v[104:107], a[244:247], v[44:47], v[104:107]\n"
    "v_cvt_pk_bf16_f32 v62, v228, v229\n"
    "v_cvt_pk_bf16_f32 v63, v230, v231\n"
    "ds_read_b128 v[48:51], v243 offset:0\n"
    "ds_read_b128 v[52:55], v232 offset:0\n"
    "ds_write_b128 v242, v[56:59] offset:2048\n"
    "ds_write_b128 v242, v[60:63] offset:3072\n"
    "v_lshlrev_b32 v216, 16, v64\n"
    "v_and_b32 v217, s17, v64\n"
    "v_lshlrev_b32 v218, 16, v65\n"
    "v_and_b32 v219, s17, v65\n"
    "v_lshlrev_b32 v220, 16, v66\n"
    "v_and_b32 v221, s17, v66\n"
    "v_lshlrev_b32 v222, 16, v67\n"
    "v_and_b32 v223, s17, v67\n"
    "v_pk_mul_f32 v[216:217], v[216:217], s[50:51] op_sel_hi:[1,0]\n"
    "v_pk_mul_f32 v[218:219], v[218:219], s[50:51] op_sel_hi:[1,0]\n"
    "v_pk_mul_f32 v[220:221], v[220:221], s[50:51] op_sel_hi:[1,0]\n"
    "v_pk_mul_f32 v[222:223], v[222:223], s[50:51] op_sel_hi:[1,0]\n"
    "v_mfma_f32_16x16x32_bf16 v[108:111], a[248:251], v[24:27], 0\n"
    "v_cvt_pk_bf16_f32 v64, v216, v217\n"
    "v_cvt_pk_bf16_f32 v65, v218, v219\n"
    "v_mfma_f32_16x16x32_bf16 v[108:111], a[252:255], v[28:31], v[108:111]\n"
    "v_cvt_pk_bf16_f32 v66, v220, v221\n"
    "v_cvt_pk_bf16_f32 v67, v222, v223\n"
    "v_mfma_f32_16x16x32_bf16 v[108:111], a[192:195], v[32:35], v[108:111]\n"
    "v_lshlrev_b32 v224, 16, v68\n"
    "v_and_b32 v225, s17, v68\n"
    "v_mfma_f32_16x16x32_bf16 v[108:111], a[196:199], v[36:39], v[108:111]\n"
    "v_lshlrev_b32 v226, 16, v69\n"
    "v_and_b32 v227, s17, v69\n"
    "v_lshlrev_b32 v228, 16, v70\n"
    "v_and_b32 v229, s17, v70\n"
    "v_lshlrev_b32 v230, 16, v71\n"
    "v_and_b32 v231, s17, v71\n"
    "v_pk_mul_f32 v[224:225], v[224:225], s[50:51] op_sel_hi:[1,0]\n"
    "v_pk_mul_f32 v[226:227], v[226:227], s[50:51] op_sel_hi:[1,0]\n"
    "v_pk_mul_f32 v[228:229], v[228:229], s[50:51] op_sel_hi:[1,0]\n"
    "v_pk_mul_f32 v[230:231], v[230:231], s[50:51] op_sel_hi:[1,0]\n"
    "v_mfma_f32_16x16x32_bf16 v[108:111], a[200:203], v[40:43], v[108:111]\n"
    "v_cvt_pk_bf16_f32 v68, v224, v225\n"
    "v_cvt_pk_bf16_f32 v69, v226, v227\n"
    "v_mfma_f32_16x16x32_bf16 v[108:111], a[204:207], v[44:47], v[108:111]\n"
    "v_cvt_pk_bf16_f32 v70, v228, v229\n"
    "v_cvt_pk_bf16_f32 v71, v230, v231\n"
    "ds_read_b128 v[56:59], v243 offset:2048\n"
    "ds_read_b128 v[60:63], v232 offset:2048\n"
    "ds_write_b128 v242, v[64:67] offset:0\n"
    "ds_write_b128 v242, v[68:71] offset:1024\n"
    "ds_read_b128 v[64:67], v243 offset:0\n"
    "ds_read_b128 v[68:71], v232 offset:0\n"
    "v_max3_f32 v246, v104, v105, v106\n"
    "v_max3_f32 v246, v246, v107, v108\n"
    "v_max3_f32 v246, v246, v109, v110\n"
    "v_max_f32 v246, v246, v111\n"
    "v_mov_b32 v247, v246\n"
    "s_nop 1\n"
    "v_permlane32_swap_b32 v246, v247\n"
    "v_max_f32 v246, v246, v247\n"
    "v_mov_b32 v247, v246\n"
    "s_waitcnt lgkmcnt(6)\n"
    "v_mfma_f32_16x16x32_bf16 v[112:115], a[224:227], v[48:51], 0\n"
    "v_permlane16_swap_b32 v246, v247\n"
    "v_max_f32 v246, v246, v247\n"
    "v_sub_f32 v196, 0, v246\n"
    "v_mfma_f32_16x16x32_bf16 v[112:115], a[228:231], v[52:55], v[112:115]\n"
    "v_sub_f32 v197, 0, v246\n"
    "v_sub_f32 v198, 0, v246\n"
    "v_sub_f32 v199, 0, v246\n"
    "s_waitcnt lgkmcnt(5)\n"
    "v_mfma_f32_16x16x32_bf16 v[112:115], a[232:235], v[56:59], v[112:115]\n"
    "v_sub_f32 v104, v104, v246\n"
    "v_sub_f32 v105, v105, v246\n"
    "v_sub_f32 v106, v106, v246\n"
    "s_waitcnt lgkmcnt(4)\n"
    "v_mfma_f32_16x16x32_bf16 v[112:115], a[236:239], v[60:63], v[112:115]\n"
    "v_sub_f32 v107, v107, v246\n"
    "v_sub_f32 v108, v108, v246\n"
    "v_sub_f32 v109, v109, v246\n"
    "s_waitcnt lgkmcnt(1)\n"
    "v_mfma_f32_16x16x32_bf16 v[112:115], a[240:243], v[64:67], v[112:115]\n"
    "v_sub_f32 v110, v110, v246\n"
    "v_sub_f32 v111, v111, v246\n"
    "v_exp_f32 v216, v96\n"
    "s_waitcnt lgkmcnt(0)\n"
    "v_mfma_f32_16x16x32_bf16 v[112:115], a[244:247], v[68:71], v[112:115]\n"
    "v_exp_f32 v217, v97\n"
    "v_exp_f32 v218, v98\n"
    "v_exp_f32 v219, v99\n"
    "v_mfma_f32_16x16x32_bf16 v[116:119], a[248:251], v[48:51], 0\n"
    "v_cvt_pk_bf16_f32 v160, v216, v217\n"
    "v_cvt_pk_bf16_f32 v161, v218, v219\n"
    "v_add_f32 v212, v216, v217\n"
    "v_mfma_f32_16x16x32_bf16 v[116:119], a[252:255], v[52:55], v[116:119]\n"
    "v_add_f32 v212, v212, v218\n"
    "v_add_f32 v212, v212, v219\n"
    "v_exp_f32 v216, v100\n"
    "v_mfma_f32_16x16x32_bf16 v[116:119], a[192:195], v[56:59], v[116:119]\n"
    "v_exp_f32 v217, v101\n"
    "v_exp_f32 v218, v102\n"
    "v_exp_f32 v219, v103\n"
    "v_mfma_f32_16x16x32_bf16 v[116:119], a[196:199], v[60:63], v[116:119]\n"
    "v_cvt_pk_bf16_f32 v162, v216, v217\n"
    "v_cvt_pk_bf16_f32 v163, v218, v219\n"
    "v_add_f32 v212, v212, v216\n"
    "v_mfma_f32_16x16x32_bf16 v[116:119], a[200:203], v[64:67], v[116:119]\n"
    "v_add_f32 v212, v212, v217\n"
    "v_add_f32 v212, v212, v218\n"
    "v_add_f32 v212, v212, v219\n"
    "v_mfma_f32_16x16x32_bf16 v[116:119], a[204:207], v[68:71], v[116:119]\n"
    "v_exp_f32 v220, v104\n"
    "v_exp_f32 v221, v105\n"
    "v_exp_f32 v222, v106\n"
    "v_exp_f32 v223, v107\n"
    "v_cvt_pk_bf16_f32 v164, v220, v221\n"
    "v_cvt_pk_bf16_f32 v165, v222, v223\n"
    "v_add_f32 v213, v220, v221\n"
    "v_add_f32 v213, v213, v222\n"
    "v_add_f32 v213, v213, v223\n"
    "v_exp_f32 v220, v108\n"
    "v_exp_f32 v221, v109\n"
    "v_exp_f32 v222, v110\n"
    "v_exp_f32 v223, v111\n"
    "v_cvt_pk_bf16_f32 v166, v220, v221\n"
    "v_cvt_pk_bf16_f32 v167, v222, v223\n"
    "v_add_f32 v213, v213, v220\n"
    "v_add_f32 v213, v213, v221\n"
    "v_add_f32 v213, v213, v222\n"
    "v_add_f32 v213, v213, v223\n"
    "v_max3_f32 v248, v112, v113, v114\n"
    "v_max3_f32 v248, v248, v115, v116\n"
    "v_max3_f32 v248, v248, v117, v118\n"
    "v_max_f32 v248, v248, v119\n"
    "v_mov_b32 v249, v248\n"
    "s_nop 1\n"
    "v_permlane32_swap_b32 v248, v249\n"
    "v_max_f32 v248, v248, v249\n"
    "v_mov_b32 v249, v248\n"
    "s_nop 1\n"
    "v_permlane16_swap_b32 v248, v249\n"
    "v_max_f32 v248, v248, v249\n"
    "v_sub_f32 v200, 0, v248\n"
    "v_sub_f32 v201, 0, v248\n"
    "v_sub_f32 v202, 0, v248\n"
    "v_sub_f32 v203, 0, v248\n"
    "v_sub_f32 v112, v112, v248\n"
    "v_sub_f32 v113, v113, v248\n"
    "v_sub_f32 v114, v114, v248\n"
    "v_sub_f32 v115, v115, v248\n"
    "v_sub_f32 v116, v116, v248\n"
    "v_sub_f32 v117, v117, v248\n"
    "v_sub_f32 v118, v118, v248\n"
    "v_sub_f32 v119, v119, v248\n"
    "v_exp_f32 v224, v112\n"
    "v_exp_f32 v225, v113\n"
    "v_exp_f32 v226, v114\n"
    "v_exp_f32 v227, v115\n"
    "v_cvt_pk_bf16_f32 v168, v224, v225\n"
    "v_cvt_pk_bf16_f32 v169, v226, v227\n"
    "v_add_f32 v214, v224, v225\n"
    "v_add_f32 v214, v214, v226\n"
    "v_add_f32 v214, v214, v227\n"
    "v_exp_f32 v224, v116\n"
    "v_exp_f32 v225, v117\n"
    "v_exp_f32 v226, v118\n"
    "v_exp_f32 v227, v119\n"
    "v_cvt_pk_bf16_f32 v170, v224, v225\n"
    "v_cvt_pk_bf16_f32 v171, v226, v227\n"
    "v_add_f32 v214, v214, v224\n"
    "v_add_f32 v214, v214, v225\n"
    "v_add_f32 v214, v214, v226\n"
    "v_add_f32 v214, v214, v227\n"
    "s_waitcnt vmcnt(0)\n"
    "v_lshlrev_b32 v216, 16, v72\n"
    "v_and_b32 v217, s17, v72\n"
    "v_lshlrev_b32 v218, 16, v73\n"
    "v_and_b32 v219, s17, v73\n"
    "v_lshlrev_b32 v220, 16, v74\n"
    "v_and_b32 v221, s17, v74\n"
    "v_lshlrev_b32 v222, 16, v75\n"
    "v_and_b32 v223, s17, v75\n"
    "v_pk_mul_f32 v[216:217], v[216:217], s[50:51] op_sel_hi:[1,0]\n"
    "v_pk_mul_f32 v[218:219], v[218:219], s[50:51] op_sel_hi:[1,0]\n"
    "v_pk_mul_f32 v[220:221], v[220:221], s[50:51] op_sel_hi:[1,0]\n"
    "v_pk_mul_f32 v[222:223], v[222:223], s[50:51] op_sel_hi:[1,0]\n"
    "v_cvt_pk_bf16_f32 v72, v216, v217\n"
    "v_cvt_pk_bf16_f32 v73, v218, v219\n"
    "v_cvt_pk_bf16_f32 v74, v220, v221\n"
    "v_cvt_pk_bf16_f32 v75, v222, v223\n"
    "v_lshlrev_b32 v224, 16, v76\n"
    "v_and_b32 v225, s17, v76\n"
    "v_lshlrev_b32 v226, 16, v77\n"
    "v_and_b32 v227, s17, v77\n"
    "v_lshlrev_b32 v228, 16, v78\n"
    "v_and_b32 v229, s17, v78\n"
    "v_lshlrev_b32 v230, 16, v79\n"
    "v_and_b32 v231, s17, v79\n"
    "v_pk_mul_f32 v[224:225], v[224:225], s[50:51] op_sel_hi:[1,0]\n"
    "v_pk_mul_f32 v[226:227], v[226:227], s[50:51] op_sel_hi:[1,0]\n"
    "v_pk_mul_f32 v[228:229], v[228:229], s[50:51] op_sel_hi:[1,0]\n"
    "v_pk_mul_f32 v[230:231], v[230:231], s[50:51] op_sel_hi:[1,0]\n"
    "v_cvt_pk_bf16_f32 v76, v224, v225\n"
    "v_cvt_pk_bf16_f32 v77, v226, v227\n"
    "v_cvt_pk_bf16_f32 v78, v228, v229\n"
    "v_cvt_pk_bf16_f32 v79, v230, v231\n"
    "ds_write_b128 v242, v[72:75] offset:2048\n"
    "ds_write_b128 v242, v[76:79] offset:3072\n"
    "v_lshlrev_b32 v216, 16, v80\n"
    "v_and_b32 v217, s17, v80\n"
    "v_lshlrev_b32 v218, 16, v81\n"
    "v_and_b32 v219, s17, v81\n"
    "v_lshlrev_b32 v220, 16, v82\n"
    "v_and_b32 v221, s17, v82\n"
    "v_lshlrev_b32 v222, 16, v83\n"
    "v_and_b32 v223, s17, v83\n"
    "v_pk_mul_f32 v[216:217], v[216:217], s[50:51] op_sel_hi:[1,0]\n"
    "v_pk_mul_f32 v[218:219], v[218:219], s[50:51] op_sel_hi:[1,0]\n"
    "v_pk_mul_f32 v[220:221], v[220:221], s[50:51] op_sel_hi:[1,0]\n"
    "v_pk_mul_f32 v[222:223], v[222:223], s[50:51] op_sel_hi:[1,0]\n"
    "v_cvt_pk_bf16_f32 v80, v216, v217\n"
    "v_cvt_pk_bf16_f32 v81, v218, v219\n"
    "v_cvt_pk_bf16_f32 v82, v220, v221\n"
    "v_cvt_pk_bf16_f32 v83, v222, v223\n"
    "v_lshlrev_b32 v224, 16, v84\n"
    "v_and_b32 v225, s17, v84\n"
    "v_lshlrev_b32 v226, 16, v85\n"
    "v_and_b32 v227, s17, v85\n"
    "v_lshlrev_b32 v228, 16, v86\n"
    "v_and_b32 v229, s17, v86\n"
    "v_lshlrev_b32 v230, 16, v87\n"
    "v_and_b32 v231, s17, v87\n"
    "v_pk_mul_f32 v[224:225], v[224:225], s[50:51] op_sel_hi:[1,0]\n"
    "v_pk_mul_f32 v[226:227], v[226:227], s[50:51] op_sel_hi:[1,0]\n"
    "v_pk_mul_f32 v[228:229], v[228:229], s[50:51] op_sel_hi:[1,0]\n"
    "v_pk_mul_f32 v[230:231], v[230:231], s[50:51] op_sel_hi:[1,0]\n"
    "v_cvt_pk_bf16_f32 v84, v224, v225\n"
    "v_cvt_pk_bf16_f32 v85, v226, v227\n"
    "v_cvt_pk_bf16_f32 v86, v228, v229\n"
    "v_cvt_pk_bf16_f32 v87, v230, v231\n"
    "ds_read_b128 v[72:75], v243 offset:2048\n"
    "ds_read_b128 v[76:79], v232 offset:2048\n"
    "ds_write_b128 v242, v[80:83] offset:0\n"
    "ds_write_b128 v242, v[84:87] offset:1024\n"
    "v_lshlrev_b32 v216, 16, v88\n"
    "v_and_b32 v217, s17, v88\n"
    "v_lshlrev_b32 v218, 16, v89\n"
    "v_and_b32 v219, s17, v89\n"
    "v_lshlrev_b32 v220, 16, v90\n"
    "v_and_b32 v221, s17, v90\n"
    "v_lshlrev_b32 v222, 16, v91\n"
    "v_and_b32 v223, s17, v91\n"
    "v_pk_mul_f32 v[216:217], v[216:217], s[50:51] op_sel_hi:[1,0]\n"
    "v_pk_mul_f32 v[218:219], v[218:219], s[50:51] op_sel_hi:[1,0]\n"
    "v_pk_mul_f32 v[220:221], v[220:221], s[50:51] op_sel_hi:[1,0]\n"
    "v_pk_mul_f32 v[222:223], v[222:223], s[50:51] op_sel_hi:[1,0]\n"
    "v_cvt_pk_bf16_f32 v88, v216, v217\n"
    "v_cvt_pk_bf16_f32 v89, v218, v219\n"
    "v_cvt_pk_bf16_f32 v90, v220, v221\n"
    "v_cvt_pk_bf16_f32 v91, v222, v223\n"
    "v_lshlrev_b32 v224, 16, v92\n"
    "v_and_b32 v225, s17, v92\n"
    "v_lshlrev_b32 v226, 16, v93\n"
    "v_and_b32 v227, s17, v93\n"
    "v_lshlrev_b32 v228, 16, v94\n"
    "v_and_b32 v229, s17, v94\n"
    "v_lshlrev_b32 v230, 16, v95\n"
    "v_and_b32 v231, s17, v95\n"
    "v_pk_mul_f32 v[224:225], v[224:225], s[50:51] op_sel_hi:[1,0]\n"
    "v_pk_mul_f32 v[226:227], v[226:227], s[50:51] op_sel_hi:[1,0]\n"
    "v_pk_mul_f32 v[228:229], v[228:229], s[50:51] op_sel_hi:[1,0]\n"
    "v_pk_mul_f32 v[230:231], v[230:231], s[50:51] op_sel_hi:[1,0]\n"
    "v_cvt_pk_bf16_f32 v92, v224, v225\n"
    "v_cvt_pk_bf16_f32 v93, v226, v227\n"
    "v_cvt_pk_bf16_f32 v94, v228, v229\n"
    "v_cvt_pk_bf16_f32 v95, v230, v231\n"
    "ds_read_b128 v[80:83], v243 offset:0\n"
    "ds_read_b128 v[84:87], v232 offset:0\n"
    "ds_write_b128 v242, v[88:91] offset:2048\n"
    "ds_write_b128 v242, v[92:95] offset:3072\n"
    "ds_read_b128 a[208:211], v235 offset:14336\n"
    "ds_read_b128 a[212:215], v235 offset:14848\n"
    "ds_read_b128 a[216:219], v235 offset:18432\n"
    "ds_read_b128 a[220:223], v235 offset:18944\n"
    "v_add_u32 v236, s41, v234\n"
    "v_xor_b32 v241, 32, v236\n"
    "s_mov_b32 s43, s40\n"
    "s_mov_b32 s40, s41\n"
    "s_mov_b32 s41, s42\n"
    "s_mov_b32 s42, s43\n"
    "ds_read_b128 v[88:91], v243 offset:2048\n"
    "ds_read_b128 v[92:95], v232 offset:2048\n"
    "s_waitcnt lgkmcnt(10)\n"
    "v_mfma_f32_16x16x32_bf16 v[120:123], a[224:227], v[72:75], 0\n"
    "v_mfma_f32_16x16x32_bf16 v[120:123], a[228:231], v[76:79], v[120:123]\n"
    "s_waitcnt lgkmcnt(9)\n"
    "v_mfma_f32_16x16x32_bf16 v[120:123], a[232:235], v[80:83], v[120:123]\n"
    "s_waitcnt lgkmcnt(8)\n"
    "v_mfma_f32_16x16x32_bf16 v[120:123], a[236:239], v[84:87], v[120:123]\n"
    "s_waitcnt lgkmcnt(1)\n"
    "v_mfma_f32_16x16x32_bf16 v[120:123], a[240:243], v[88:91], v[120:123]\n"
    "s_waitcnt lgkmcnt(0)\n"
    "v_mfma_f32_16x16x32_bf16 v[120:123], a[244:247], v[92:95], v[120:123]\n"
    "v_mfma_f32_16x16x32_bf16 v[124:127], a[248:251], v[72:75], 0\n"
    "v_mfma_f32_16x16x32_bf16 v[124:127], a[252:255], v[76:79], v[124:127]\n"
    "v_mfma_f32_16x16x32_bf16 v[124:127], a[192:195], v[80:83], v[124:127]\n"
    "v_mfma_f32_16x16x32_bf16 v[124:127], a[196:199], v[84:87], v[124:127]\n"
    "v_mfma_f32_16x16x32_bf16 v[124:127], a[200:203], v[88:91], v[124:127]\n"
    "v_mfma_f32_16x16x32_bf16 v[124:127], a[204:207], v[92:95], v[124:127]\n"
    "ds_read_b128 a[224:227], v235 offset:19456\n"
    "ds_read_b128 a[228:231], v235 offset:19968\n"
    "ds_read_b128 a[232:235], v235 offset:20480\n"
    "ds_read_b128 a[236:239], v235 offset:20992\n"
    "ds_read_b128 a[192:195], v235 offset:12288\n"
    "ds_read_b128 a[196:199], v235 offset:12800\n"
    "ds_read_b128 a[200:203], v235 offset:13312\n"
    "ds_read_b128 a[204:207], v235 offset:13824\n"
    "v_max3_f32 v250, v120, v121, v122\n"
    "v_max3_f32 v250, v250, v123, v124\n"
    "v_max3_f32 v250, v250, v125, v126\n"
    "v_max_f32 v250, v250, v127\n"
    "v_mov_b32 v251, v250\n"
    "s_nop 1\n"
    "v_permlane32_swap_b32 v250, v251\n"
    "v_max_f32 v250, v250, v251\n"
    "v_mov_b32 v251, v250\n"
    "s_nop 1\n"
    "v_permlane16_swap_b32 v250, v251\n"
    "v_max_f32 v250, v250, v251\n"
    "s_waitcnt lgkmcnt(0)\n"
    "LPEEL_4_%=:\n"
    "s_barrier\n"
    "s_mov_b32 s57, 3\n"
    "v_mfma_f32_16x16x32_bf16 v[128:131], a[192:195], v[0:3], v[192:195]\n"
    "ds_read_b64_tr_b16 a[240:241], v236 offset:0\n"
    "v_sub_f32 v204, 0, v250\n"
    "v_mfma_f32_16x16x32_bf16 v[128:131], a[196:199], v[4:7], v[128:131]\n"
    "ds_read_b64_tr_b16 a[242:243], v236 offset:6144\n"
    "v_add_u32 v235, s41, v233\n"
    "v_mfma_f32_16x16x32_bf16 v[128:131], a[200:203], v[8:11], v[128:131]\n"
    "v_sub_f32 v205, 0, v250\n"
    "v_sub_f32 v206, 0, v250\n"
    "v_mfma_f32_16x16x32_bf16 v[128:131], a[204:207], v[12:15], v[128:131]\n"
    "ds_read_b64_tr_b16 a[244:245], v241 offset:0\n"
    "s_add_u32 m0, s42, s47\n"
    "v_mfma_f32_16x16x32_bf16 v[128:131], a[208:211], v[16:19], v[128:131]\n"
    "ds_read_b64_tr_b16 a[246:247], v241 offset:6144\n"
    "v_sub_f32 v207, 0, v250\n"
    "v_mfma_f32_16x16x32_bf16 v[128:131], a[212:215], v[20:23], v[128:131]\n"
    "buffer_load_dwordx4 v237, s[20:23], s44 offen offset:0 lds\n"
    "v_mfma_f32_16x16x32_bf16 v[136:139], a[192:195], v[24:27], v[196:199]\n"
    "ds_read_b64_tr_b16 a[248:249], v236 offset:512\n"
    "v_sub_f32 v120, v120, v250\n"
    "v_mfma_f32_16x16x32_bf16 v[136:139], a[196:199], v[28:31], v[136:139]\n"
    "buffer_load_dword v254, v255, s[28:31], s79 offen\n"
    "v_mfma_f32_16x16x32_bf16 v[136:139], a[200:203], v[32:35], v[136:139]\n"
    "ds_read_b64_tr_b16 a[250:251], v236 offset:6656\n"
    "s_add_u32 s79, s79, 1280\n"
    "v_mfma_f32_16x16x32_bf16 v[136:139], a[204:207], v[36:39], v[136:139]\n"
    "ds_read_b64_tr_b16 a[252:253], v241 offset:512\n"
    "v_sub_f32 v121, v121, v250\n"
    "v_mfma_f32_16x16x32_bf16 v[136:139], a[208:211], v[40:43], v[136:139]\n"
    "buffer_load_dwordx4 v238, s[20:23], s44 offen offset:1024 lds\n"
    "v_mfma_f32_16x16x32_bf16 v[136:139], a[212:215], v[44:47], v[136:139]\n"
    "ds_read_b64_tr_b16 a[254:255], v241 offset:6656\n"
    "v_sub_f32 v122, v122, v250\n"
    "v_mfma_f32_16x16x32_bf16 v[144:147], a[192:195], v[48:51], v[200:203]\n"
    "buffer_load_dword v254, v253, s[92:95], s86 offen\n"
    "v_mfma_f32_16x16x32_bf16 v[144:147], a[196:199], v[52:55], v[144:147]\n"
    "buffer_load_dwordx4 v239, s[20:23], s44 offen offset:2048 lds\n"
    "v_mfma_f32_16x16x32_bf16 v[144:147], a[200:203], v[56:59], v[144:147]\n"
    "s_add_u32 m0, m0, 3072\n"
    "s_add_u32 s45, s44, 3072\n"
    "v_mfma_f32_16x16x32_bf16 v[144:147], a[204:207], v[60:63], v[144:147]\n"
    "buffer_load_dwordx4 v237, s[20:23], s45 offen offset:0 lds\n"
    "v_mfma_f32_16x16x32_bf16 v[144:147], a[208:211], v[64:67], v[144:147]\n"
    "buffer_load_dwordx4 v238, s[20:23], s45 offen offset:1024 lds\n"
    "v_mfma_f32_16x16x32_bf16 v[144:147], a[212:215], v[68:71], v[144:147]\n"
    "buffer_load_dwordx4 v239, s[20:23], s45 offen offset:2048 lds\n"
    "v_mfma_f32_16x16x32_bf16 v[152:155], a[192:195], v[72:75], v[204:207]\n"
    "ds_read_b64_tr_b16 a[192:193], v236 offset:1024\n"
    "s_add_u32 m0, s41, s48\n"
    "v_mfma_f32_16x16x32_bf16 v[152:155], a[196:199], v[76:79], v[152:155]\n"
    "ds_read_b64_tr_b16 a[194:195], v236 offset:7168\n"
    "v_sub_f32 v123, v123, v250\n"
    "v_mfma_f32_16x16x32_bf16 v[152:155], a[200:203], v[80:83], v[152:155]\n"
    "ds_read_b64_tr_b16 a[196:197], v241 offset:1024\n"
    "s_sub_u32 s86, s86, 1048576\n"
    "v_mfma_f32_16x16x32_bf16 v[152:155], a[204:207], v[84:87], v[152:155]\n"
    "ds_read_b64_tr_b16 a[198:199], v241 offset:7168\n"
    "v_sub_f32 v124, v124, v250\n"
    "v_mfma_f32_16x16x32_bf16 v[152:155], a[208:211], v[88:91], v[152:155]\n"
    "ds_read_b64_tr_b16 a[200:201], v236 offset:1536\n"
    "v_sub_f32 v125, v125, v250\n"
    "v_mfma_f32_16x16x32_bf16 v[152:155], a[212:215], v[92:95], v[152:155]\n"
    "ds_read_b64_tr_b16 a[202:203], v236 offset:7680\n"
    "v_sub_f32 v126, v126, v250\n"
    "v_mfma_f32_16x16x32_bf16 v[132:135], a[216:219], v[0:3], v[192:195]\n"
    "ds_read_b64_tr_b16 a[204:205], v241 offset:1536\n"
    "v_sub_f32 v127, v127, v250\n"
    "v_mfma_f32_16x16x32_bf16 v[132:135], a[220:223], v[4:7], v[132:135]\n"
    "ds_read_b64_tr_b16 a[206:207], v241 offset:7680\n"
    "v_mfma_f32_16x16x32_bf16 v[132:135], a[224:227], v[8:11], v[132:135]\n"
    "ds_read_b64_tr_b16 a[208:209], v236 offset:2048\n"
    "v_mfma_f32_16x16x32_bf16 v[132:135], a[228:231], v[12:15], v[132:135]\n"
    "ds_read_b64_tr_b16 a[210:211], v236 offset:8192\n"
    "v_mfma_f32_16x16x32_bf16 v[132:135], a[232:235], v[16:19], v[132:135]\n"
    "ds_read_b64_tr_b16 a[212:213], v241 offset:2048\n"
    "v_mfma_f32_16x16x32_bf16 v[132:135], a[236:239], v[20:23], v[132:135]\n"
    "buffer_load_dwordx4 v237, s[24:27], s84 offen offset:0 lds\n"
    "v_mfma_f32_16x16x32_bf16 v[140:143], a[216:219], v[24:27], v[196:199]\n"
    "ds_read_b64_tr_b16 a[214:215], v241 offset:8192\n"
    "v_mfma_f32_16x16x32_bf16 v[140:143], a[220:223], v[28:31], v[140:143]\n"
    "buffer_load_dwordx4 v238, s[24:27], s84 offen offset:1024 lds\n"
    "v_mfma_f32_16x16x32_bf16 v[140:143], a[224:227], v[32:35], v[140:143]\n"
    "buffer_load_dwordx4 v239, s[24:27], s84 offen offset:2048 lds\n"
    "v_mfma_f32_16x16x32_bf16 v[140:143], a[228:231], v[36:39], v[140:143]\n"
    "s_add_u32 m0, m0, 3072\n"
    "s_add_u32 s45, s84, 3072\n"
    "v_mfma_f32_16x16x32_bf16 v[140:143], a[232:235], v[40:43], v[140:143]\n"
    "buffer_load_dwordx4 v237, s[24:27], s45 offen offset:0 lds\n"
    "v_mfma_f32_16x16x32_bf16 v[140:143], a[236:239], v[44:47], v[140:143]\n"
    "buffer_load_dwordx4 v238, s[24:27], s45 offen offset:1024 lds\n"
    "v_mfma_f32_16x16x32_bf16 v[148:151], a[216:219], v[48:51], v[200:203]\n"
    "buffer_load_dwordx4 v239, s[24:27], s45 offen offset:2048 lds\n"
    "v_mfma_f32_16x16x32_bf16 v[148:151], a[220:223], v[52:55], v[148:151]\n"
    "s_mov_b32 s84, s44\n"
    "v_mfma_f32_16x16x32_bf16 v[148:151], a[224:227], v[56:59], v[148:151]\n"
    "s_add_u32 s87, s87, 1\n"
    "v_mfma_f32_16x16x32_bf16 v[148:151], a[228:231], v[60:63], v[148:151]\n"
    "v_readlane_b32 s44, v252, s87\n"
    "v_mfma_f32_16x16x32_bf16 v[148:151], a[232:235], v[64:67], v[148:151]\n"
    "v_exp_f32 v216, v120\n"
    "v_mfma_f32_16x16x32_bf16 v[148:151], a[236:239], v[68:71], v[148:151]\n"
    "v_exp_f32 v217, v121\n"
    "v_mfma_f32_16x16x32_bf16 v[156:159], a[216:219], v[72:75], v[204:207]\n"
    "ds_read_b64_tr_b16 a[216:217], v236 offset:2560\n"
    "v_cvt_pk_bf16_f32 v172, v216, v217\n"
    "v_mfma_f32_16x16x32_bf16 v[156:159], a[220:223], v[76:79], v[156:159]\n"
    "ds_read_b64_tr_b16 a[218:219], v236 offset:8704\n"
    "s_add_u32 m0, s40, s47\n"
    "v_mfma_f32_16x16x32_bf16 v[156:159], a[224:227], v[80:83], v[156:159]\n"
    "ds_read_b64_tr_b16 a[220:221], v241 offset:2560\n"
    "v_add_f32 v215, v216, v217\n"
    "v_mfma_f32_16x16x32_bf16 v[156:159], a[228:231], v[84:87], v[156:159]\n"
    "ds_read_b64_tr_b16 a[222:223], v241 offset:8704\n"
    "v_mfma_f32_16x16x32_bf16 v[156:159], a[232:235], v[88:91], v[156:159]\n"
    "buffer_load_dwordx4 v237, s[20:23], s44 offen offset:0 lds\n"
    "v_mfma_f32_16x16x32_bf16 v[156:159], a[236:239], v[92:95], v[156:159]\n"
    "buffer_load_dwordx4 v238, s[20:23], s44 offen offset:1024 lds\n"
    "buffer_load_dwordx4 v239, s[20:23], s44 offen offset:2048 lds\n"
    "v_exp_f32 v218, v122\n"
    "v_exp_f32 v219, v123\n"
    "s_add_u32 m0, m0, 3072\n"
    "s_add_u32 s45, s44, 3072\n"
    "buffer_load_dwordx4 v237, s[20:23], s45 offen offset:0 lds\n"
    "v_exp_f32 v220, v124\n"
    "buffer_load_dwordx4 v238, s[20:23], s45 offen offset:1024 lds\n"
    "v_exp_f32 v221, v125\n"
    "buffer_load_dwordx4 v239, s[20:23], s45 offen offset:2048 lds\n"
    "v_exp_f32 v222, v126\n"
    "v_exp_f32 v223, v127\n"
    "v_cvt_pk_bf16_f32 v173, v218, v219\n"
    "v_add_f32 v215, v215, v218\n"
    "v_add_f32 v215, v215, v219\n"
    "v_cvt_pk_bf16_f32 v174, v220, v221\n"
    "v_add_f32 v215, v215, v220\n"
    "v_add_f32 v215, v215, v221\n"
    "v_cvt_pk_bf16_f32 v175, v222, v223\n"
    "v_add_f32 v215, v215, v222\n"
    "v_add_f32 v215, v215, v223\n"
    "s_waitcnt vmcnt(26)\n"
    "s_barrier\n"
    "s_waitcnt lgkmcnt(1)\n"
    "v_mfma_f32_16x16x32_bf16 a[0:3], a[240:243], v[160:163], 0\n"
    "v_exp_f32 v216, v128\n"
    "v_mfma_f32_16x16x32_bf16 a[48:51], a[240:243], v[164:167], 0\n"
    "ds_read_b128 a[224:227], v235 offset:0\n"
    "s_add_u32 m0, s42, s48\n"
    "v_mfma_f32_16x16x32_bf16 a[96:99], a[240:243], v[168:171], 0\n"
    "buffer_load_dwordx4 v237, s[24:27], s84 offen offset:0 lds\n"
    "v_mfma_f32_16x16x32_bf16 a[144:147], a[240:243], v[172:175], 0\n"
    "v_exp_f32 v217, v129\n"
    "v_mfma_f32_16x16x32_bf16 a[4:7], a[244:247], v[160:163], 0\n"
    "ds_read_b128 a[228:231], v235 offset:512\n"
    "v_mov_b32 v208, v212\n"
    "v_mfma_f32_16x16x32_bf16 a[52:55], a[244:247], v[164:167], 0\n"
    "v_exp_f32 v218, v130\n"
    "v_mfma_f32_16x16x32_bf16 a[100:103], a[244:247], v[168:171], 0\n"
    "v_exp_f32 v219, v131\n"
    "v_mfma_f32_16x16x32_bf16 a[148:151], a[244:247], v[172:175], 0\n"
    "ds_read_b128 a[232:235], v235 offset:1024\n"
    "v_mov_b32 v209, v213\n"
    "v_mfma_f32_16x16x32_bf16 a[8:11], a[248:251], v[160:163], 0\n"
    "v_exp_f32 v220, v136\n"
    "v_mfma_f32_16x16x32_bf16 a[56:59], a[248:251], v[164:167], 0\n"
    "buffer_load_dwordx4 v238, s[24:27], s84 offen offset:1024 lds\n"
    "v_mfma_f32_16x16x32_bf16 a[104:107], a[248:251], v[168:171], 0\n"
    "ds_read_b128 a[236:239], v235 offset:1536\n"
    "v_mov_b32 v210, v214\n"
    "v_mfma_f32_16x16x32_bf16 a[152:155], a[248:251], v[172:175], 0\n"
    "v_exp_f32 v221, v137\n"
    "v_mfma_f32_16x16x32_bf16 a[12:15], a[252:255], v[160:163], 0\n"
    "v_exp_f32 v222, v138\n"
    "v_mfma_f32_16x16x32_bf16 a[60:63], a[252:255], v[164:167], 0\n"
    "ds_read_b128 a[240:243], v235 offset:2048\n"
    "v_mov_b32 v211, v215\n"
    "v_mfma_f32_16x16x32_bf16 a[108:111], a[252:255], v[168:171], 0\n"
    "v_exp_f32 v223, v139\n"
    "v_mfma_f32_16x16x32_bf16 a[156:159], a[252:255], v[172:175], 0\n"
    "v_exp_f32 v224, v144\n"
    "v_mfma_f32_16x16x32_bf16 a[16:19], a[192:195], v[160:163], 0\n"
    "ds_read_b128 a[244:247], v235 offset:2560\n"
    "v_cvt_pk_bf16_f32 v176, v216, v217\n"
    "v_mfma_f32_16x16x32_bf16 a[64:67], a[192:195], v[164:167], 0\n"
    "buffer_load_dwordx4 v239, s[24:27], s84 offen offset:2048 lds\n"
    "v_mfma_f32_16x16x32_bf16 a[112:115], a[192:195], v[168:171], 0\n"
    "v_exp_f32 v225, v145\n"
    "v_mfma_f32_16x16x32_bf16 a[160:163], a[192:195], v[172:175], 0\n"
    "ds_read_b128 a[248:251], v235 offset:6144\n"
    "s_add_u32 m0, m0, 3072\n"
    "v_mfma_f32_16x16x32_bf16 a[20:23], a[196:199], v[160:163], 0\n"
    "v_exp_f32 v226, v146\n"
    "v_mfma_f32_16x16x32_bf16 a[68:71], a[196:199], v[164:167], 0\n"
    "v_exp_f32 v227, v147\n"
    "v_mfma_f32_16x16x32_bf16 a[116:119], a[196:199], v[168:171], 0\n"
    "ds_read_b128 a[252:255], v235 offset:6656\n"
    "s_add_u32 s45, s84, 3072\n"
    "v_mfma_f32_16x16x32_bf16 a[164:167], a[196:199], v[172:175], 0\n"
    "v_add_f32 v212, v216, v217\n"
    "v_cvt_pk_bf16_f32 v177, v218, v219\n"
    "v_mfma_f32_16x16x32_bf16 a[24:27], a[200:203], v[160:163], 0\n"
    "v_exp_f32 v228, v152\n"
    "v_mfma_f32_16x16x32_bf16 a[72:75], a[200:203], v[164:167], 0\n"
    "ds_read_b128 a[192:195], v235 offset:7168\n"
    "v_add_f32 v212, v212, v218\n"
    "v_mfma_f32_16x16x32_bf16 a[120:123], a[200:203], v[168:171], 0\n"
    "buffer_load_dwordx4 v237, s[24:27], s45 offen offset:0 lds\n"
    "v_mfma_f32_16x16x32_bf16 a[168:171], a[200:203], v[172:175], 0\n"
    "v_exp_f32 v229, v153\n"
    "v_mfma_f32_16x16x32_bf16 a[28:31], a[204:207], v[160:163], 0\n"
    "ds_read_b128 a[196:199], v235 offset:7680\n"
    "v_add_f32 v212, v212, v219\n"
    "v_mfma_f32_16x16x32_bf16 a[76:79], a[204:207], v[164:167], 0\n"
    "v_exp_f32 v230, v154\n"
    "v_mfma_f32_16x16x32_bf16 a[124:127], a[204:207], v[168:171], 0\n"
    "v_exp_f32 v231, v155\n"
    "v_mfma_f32_16x16x32_bf16 a[172:175], a[204:207], v[172:175], 0\n"
    "ds_read_b128 a[200:203], v235 offset:8192\n"
    "v_cvt_pk_bf16_f32 v180, v220, v221\n"
    "v_mfma_f32_16x16x32_bf16 a[32:35], a[208:211], v[160:163], 0\n"
    "buffer_load_dwordx4 v238, s[24:27], s45 offen offset:1024 lds\n"
    "v_mfma_f32_16x16x32_bf16 a[80:83], a[208:211], v[164:167], 0\n"
    "v_add_f32 v213, v220, v221\n"
    "v_cvt_pk_bf16_f32 v181, v222, v223\n"
    "v_mfma_f32_16x16x32_bf16 a[128:131], a[208:211], v[168:171], 0\n"
    "ds_read_b128 a[204:207], v235 offset:8704\n"
    "v_mfma_f32_16x16x32_bf16 a[176:179], a[208:211], v[172:175], 0\n"
    "v_exp_f32 v216, v132\n"
    "v_mfma_f32_16x16x32_bf16 a[36:39], a[212:215], v[160:163], 0\n"
    "v_exp_f32 v217, v133\n"
    "v_mfma_f32_16x16x32_bf16 a[84:87], a[212:215], v[164:167], 0\n"
    "v_add_f32 v213, v213, v222\n"
    "v_add_f32 v213, v213, v223\n"
    "v_mfma_f32_16x16x32_bf16 a[132:135], a[212:215], v[168:171], 0\n"
    "v_cvt_pk_bf16_f32 v184, v224, v225\n"
    "v_add_f32 v214, v224, v225\n"
    "v_mfma_f32_16x16x32_bf16 a[180:183], a[212:215], v[172:175], 0\n"
    "v_exp_f32 v218, v134\n"
    "v_mfma_f32_16x16x32_bf16 a[40:43], a[216:219], v[160:163], 0\n"
    "buffer_load_dwordx4 v239, s[24:27], s45 offen offset:2048 lds\n"
    "v_mfma_f32_16x16x32_bf16 a[88:91], a[216:219], v[164:167], 0\n"
    "v_exp_f32 v219, v135\n"
    "v_mfma_f32_16x16x32_bf16 a[136:139], a[216:219], v[168:171], 0\n"
    "v_cvt_pk_bf16_f32 v185, v226, v227\n"
    "v_add_f32 v214, v214, v226\n"
    "v_mfma_f32_16x16x32_bf16 a[184:187], a[216:219], v[172:175], 0\n"
    "v_exp_f32 v220, v140\n"
    "s_waitcnt lgkmcnt(1)\n"
    "v_mfma_f32_16x16x32_bf16 a[44:47], a[220:223], v[160:163], 0\n"
    "v_exp_f32 v221, v141\n"
    "v_mfma_f32_16x16x32_bf16 a[92:95], a[220:223], v[164:167], 0\n"
    "v_add_f32 v214, v214, v227\n"
    "v_cvt_pk_bf16_f32 v188, v228, v229\n"
    "v_mfma_f32_16x16x32_bf16 a[140:143], a[220:223], v[168:171], 0\n"
    "v_add_f32 v215, v228, v229\n"
    "v_cvt_pk_bf16_f32 v189, v230, v231\n"
    "v_mfma_f32_16x16x32_bf16 a[188:191], a[220:223], v[172:175], 0\n"
    "v_exp_f32 v222, v142\n"
    "s_waitcnt vmcnt(29)\n"
    "s_barrier\n"
    "v_mfma_f32_16x16x32_bf16 v[96:99], a[224:227], v[0:3], v[192:195]\n"
    "ds_read_b64_tr_b16 a[208:209], v236 offset:12288\n"
    "s_mov_b32 s84, s44\n"
    "v_mfma_f32_16x16x32_bf16 v[96:99], a[228:231], v[4:7], v[96:99]\n"
    "ds_read_b64_tr_b16 a[210:211], v236 offset:18432\n"
    "s_add_u32 s87, s87, 1\n"
    "v_mfma_f32_16x16x32_bf16 v[96:99], a[232:235], v[8:11], v[96:99]\n"
    "v_exp_f32 v223, v143\n"
    "v_mfma_f32_16x16x32_bf16 v[96:99], a[236:239], v[12:15], v[96:99]\n"
    "ds_read_b64_tr_b16 a[212:213], v241 offset:12288\n"
    "v_readlane_b32 s44, v252, s87\n"
    "v_mfma_f32_16x16x32_bf16 v[96:99], a[240:243], v[16:19], v[96:99]\n"
    "ds_read_b64_tr_b16 a[214:215], v241 offset:18432\n"
    "v_add_f32 v215, v215, v230\n"
    "v_mfma_f32_16x16x32_bf16 v[96:99], a[244:247], v[20:23], v[96:99]\n"
    "v_add_f32 v215, v215, v231\n"
    "v_cvt_pk_bf16_f32 v178, v216, v217\n"
    "v_mfma_f32_16x16x32_bf16 v[104:107], a[224:227], v[24:27], v[196:199]\n"
    "ds_read_b64_tr_b16 a[216:217], v236 offset:12800\n"
    "v_add_f32 v212, v212, v216\n"
    "v_mfma_f32_16x16x32_bf16 v[104:107], a[228:231], v[28:31], v[104:107]\n"
    "v_exp_f32 v224, v148\n"
    "v_mfma_f32_16x16x32_bf16 v[104:107], a[232:235], v[32:35], v[104:107]\n"
    "ds_read_b64_tr_b16 a[218:219], v236 offset:18944\n"
    "s_cmp_gt_u32 s87, s46\n"
    "v_mfma_f32_16x16x32_bf16 v[104:107], a[236:239], v[36:39], v[104:107]\n"
    "ds_read_b64_tr_b16 a[220:221], v241 offset:12800\n"
    "s_cselect_b32 s71, -1.0, s49\n"
    "v_mfma_f32_16x16x32_bf16 v[104:107], a[240:243], v[40:43], v[104:107]\n"
    "v_exp_f32 v225, v149\n"
    "v_mfma_f32_16x16x32_bf16 v[104:107], a[244:247], v[44:47], v[104:107]\n"
    "ds_read_b64_tr_b16 a[222:223], v241 offset:18944\n"
    "v_add_f32 v212, v212, v217\n"
    "v_mfma_f32_16x16x32_bf16 v[112:115], a[224:227], v[48:51], v[200:203]\n"
    "v_exp_f32 v226, v150\n"
    "v_mfma_f32_16x16x32_bf16 v[112:115], a[228:231], v[52:55], v[112:115]\n"
    "v_exp_f32 v227, v151\n"
    "v_mfma_f32_16x16x32_bf16 v[112:115], a[232:235], v[56:59], v[112:115]\n"
    "v_cvt_pk_bf16_f32 v179, v218, v219\n"
    "v_add_f32 v212, v212, v218\n"
    "v_mfma_f32_16x16x32_bf16 v[112:115], a[236:239], v[60:63], v[112:115]\n"
    "v_add_f32 v212, v212, v219\n"
    "v_cvt_pk_bf16_f32 v182, v220, v221\n"
    "v_mfma_f32_16x16x32_bf16 v[112:115], a[240:243], v[64:67], v[112:115]\n"
    "v_exp_f32 v228, v156\n"
    "v_mfma_f32_16x16x32_bf16 v[112:115], a[244:247], v[68:71], v[112:115]\n"
    "v_exp_f32 v229, v157\n"
    "v_mfma_f32_16x16x32_bf16 v[120:123], a[224:227], v[72:75], v[204:207]\n"
    "ds_read_b64_tr_b16 a[224:225], v236 offset:13312\n"
    "v_add_f32 v213, v213, v220\n"
    "v_mfma_f32_16x16x32_bf16 v[120:123], a[228:231], v[76:79], v[120:123]\n"
    "ds_read_b64_tr_b16 a[226:227], v236 offset:19456\n"
    "v_add_f32 v213, v213, v221\n"
    "v_mfma_f32_16x16x32_bf16 v[120:123], a[232:235], v[80:83], v[120:123]\n"
    "ds_read_b64_tr_b16 a[228:229], v241 offset:13312\n"
    "v_cvt_pk_bf16_f32 v183, v222, v223\n"
    "v_mfma_f32_16x16x32_bf16 v[120:123], a[236:239], v[84:87], v[120:123]\n"
    "ds_read_b64_tr_b16 a[230:231], v241 offset:19456\n"
    "v_add_f32 v213, v213, v222\n"
    "v_mfma_f32_16x16x32_bf16 v[120:123], a[240:243], v[88:91], v[120:123]\n"
    "ds_read_b64_tr_b16 a[232:233], v236 offset:13824\n"
    "v_add_f32 v213, v213, v223\n"
    "v_mfma_f32_16x16x32_bf16 v[120:123], a[244:247], v[92:95], v[120:123]\n"
    "ds_read_b64_tr_b16 a[234:235], v236 offset:19968\n"
    "v_cvt_pk_bf16_f32 v186, v224, v225\n"
    "v_mfma_f32_16x16x32_bf16 v[100:103], a[248:251], v[0:3], v[192:195]\n"
    "ds_read_b64_tr_b16 a[236:237], v241 offset:13824\n"
    "v_add_f32 v214, v214, v224\n"
    "v_mfma_f32_16x16x32_bf16 v[100:103], a[252:255], v[4:7], v[100:103]\n"
    "ds_read_b64_tr_b16 a[238:239], v241 offset:19968\n"
    "v_mfma_f32_16x16x32_bf16 v[100:103], a[192:195], v[8:11], v[100:103]\n"
    "ds_read_b64_tr_b16 a[240:241], v236 offset:14336\n"
    "v_add_f32 v214, v214, v225\n"
    "v_mfma_f32_16x16x32_bf16 v[100:103], a[196:199], v[12:15], v[100:103]\n"
    "ds_read_b64_tr_b16 a[242:243], v236 offset:20480\n"
    "v_cvt_pk_bf16_f32 v187, v226, v227\n"
    "v_mfma_f32_16x16x32_bf16 v[100:103], a[200:203], v[16:19], v[100:103]\n"
    "ds_read_b64_tr_b16 a[244:245], v241 offset:14336\n"
    "v_add_f32 v214, v214, v226\n"
    "s_waitcnt lgkmcnt(10)\n"
    "v_mfma_f32_16x16x32_bf16 v[100:103], a[204:207], v[20:23], v[100:103]\n"
    "v_exp_f32 v230, v158\n"
    "v_mfma_f32_16x16x32_bf16 v[108:111], a[248:251], v[24:27], v[196:199]\n"
    "ds_read_b64_tr_b16 a[246:247], v241 offset:20480\n"
    "v_add_f32 v214, v214, v227\n"
    "v_mfma_f32_16x16x32_bf16 v[108:111], a[252:255], v[28:31], v[108:111]\n"
    "v_exp_f32 v231, v159\n"
    "v_mfma_f32_16x16x32_bf16 v[108:111], a[192:195], v[32:35], v[108:111]\n"
    "v_cvt_pk_bf16_f32 v190, v228, v229\n"
    "v_add_f32 v215, v215, v228\n"
    "v_mfma_f32_16x16x32_bf16 v[108:111], a[196:199], v[36:39], v[108:111]\n"
    "v_add_f32 v215, v215, v229\n"
    "v_cvt_pk_bf16_f32 v191, v230, v231\n"
    "v_mfma_f32_16x16x32_bf16 v[108:111], a[200:203], v[40:43], v[108:111]\n"
    "v_add_f32 v215, v215, v230\n"
    "v_add_f32 v215, v215, v231\n"
    "v_mfma_f32_16x16x32_bf16 v[108:111], a[204:207], v[44:47], v[108:111]\n"
    "v_mfma_f32_16x16x32_bf16 v[116:119], a[248:251], v[48:51], v[200:203]\n"
    "v_mfma_f32_16x16x32_bf16 v[116:119], a[252:255], v[52:55], v[116:119]\n"
    "v_mfma_f32_16x16x32_bf16 v[116:119], a[192:195], v[56:59], v[116:119]\n"
    "v_mfma_f32_16x16x32_bf16 v[116:119], a[196:199], v[60:63], v[116:119]\n"
    "v_mfma_f32_16x16x32_bf16 v[116:119], a[200:203], v[64:67], v[116:119]\n"
    "v_mfma_f32_16x16x32_bf16 v[116:119], a[204:207], v[68:71], v[116:119]\n"
    "v_mfma_f32_16x16x32_bf16 v[124:127], a[248:251], v[72:75], v[204:207]\n"
    "ds_read_b64_tr_b16 a[248:249], v236 offset:14848\n"
    "v_max3_f32 v232, v212, v213, v214\n"
    "v_mfma_f32_16x16x32_bf16 v[124:127], a[252:255], v[76:79], v[124:127]\n"
    "ds_read_b64_tr_b16 a[250:251], v236 offset:20992\n"
    "v_max_f32 v232, v232, v215\n"
    "v_mfma_f32_16x16x32_bf16 v[124:127], a[192:195], v[80:83], v[124:127]\n"
    "ds_read_b64_tr_b16 a[252:253], v241 offset:14848\n"
    "v_cmp_nge_f32 vcc, s49, v232\n"
    "v_mfma_f32_16x16x32_bf16 v[124:127], a[196:199], v[84:87], v[124:127]\n"
    "ds_read_b64_tr_b16 a[254:255], v241 offset:20992\n"
    "v_mfma_f32_16x16x32_bf16 v[124:127], a[200:203], v[88:91], v[124:127]\n"
    "v_mfma_f32_16x16x32_bf16 v[124:127], a[204:207], v[92:95], v[124:127]\n"
    "v_add_u32 v236, s41, v234\n"
    "s_cbranch_vccnz LRARE_o_4_%=\n"
    "LRET_o_4_P0_%=:\n"
    "s_waitcnt vmcnt(26)\n"
    "s_barrier\n"
    "v_mfma_f32_16x16x32_bf16 a[0:3], a[208:211], v[176:179], a[0:3]\n"
    "v_exp_f32 v216, v96\n"
    "v_mfma_f32_16x16x32_bf16 a[48:51], a[208:211], v[180:183], a[48:51]\n"
    "ds_read_b128 a[192:195], v235 offset:12288\n"
    "v_xor_b32 v241, 32, v236\n"
    "v_mfma_f32_16x16x32_bf16 a[96:99], a[208:211], v[184:187], a[96:99]\n"
    "v_exp_f32 v217, v97\n"
    "v_mfma_f32_16x16x32_bf16 a[144:147], a[208:211], v[188:191], a[144:147]\n"
    "v_exp_f32 v218, v98\n"
    "v_mfma_f32_16x16x32_bf16 a[4:7], a[212:215], v[176:179], a[4:7]\n"
    "ds_read_b128 a[196:199], v235 offset:12800\n"
    "s_mov_b32 s43, s40\n"
    "v_mfma_f32_16x16x32_bf16 a[52:55], a[212:215], v[180:183], a[52:55]\n"
    "v_exp_f32 v219, v99\n"
    "v_mfma_f32_16x16x32_bf16 a[100:103], a[212:215], v[184:187], a[100:103]\n"
    "v_exp_f32 v220, v104\n"
    "v_mfma_f32_16x16x32_bf16 a[148:151], a[212:215], v[188:191], a[148:151]\n"
    "ds_read_b128 a[200:203], v235 offset:13312\n"
    "s_mov_b32 s40, s41\n"
    "v_mfma_f32_16x16x32_bf16 a[8:11], a[216:219], v[176:179], a[8:11]\n"
    "v_exp_f32 v221, v105\n"
    "v_mfma_f32_16x16x32_bf16 a[56:59], a[216:219], v[180:183], a[56:59]\n"
    "v_exp_f32 v222, v106\n"
    "v_mfma_f32_16x16x32_bf16 a[104:107], a[216:219], v[184:187], a[104:107]\n"
    "ds_read_b128 a[204:207], v235 offset:13824\n"
    "s_mov_b32 s41, s42\n"
    "v_mfma_f32_16x16x32_bf16 a[152:155], a[216:219], v[188:191], a[152:155]\n"
    "v_exp_f32 v223, v107\n"
    "v_mfma_f32_16x16x32_bf16 a[12:15], a[220:223], v[176:179], a[12:15]\n"
    "v_add_f32 v208, v208, v212\n"
    "v_add_f32 v209, v209, v213\n"
    "v_mfma_f32_16x16x32_bf16 a[60:63], a[220:223], v[180:183], a[60:63]\n"
    "ds_read_b128 a[208:211], v235 offset:14336\n"
    "s_mov_b32 s42, s43\n"
    "v_mfma_f32_16x16x32_bf16 a[108:111], a[220:223], v[184:187], a[108:111]\n"
    "v_exp_f32 v224, v112\n"
    "v_mfma_f32_16x16x32_bf16 a[156:159], a[220:223], v[188:191], a[156:159]\n"
    "v_add_f32 v210, v210, v214\n"
    "s_waitcnt lgkmcnt(3)\n"
    "v_mfma_f32_16x16x32_bf16 a[16:19], a[224:227], v[176:179], a[16:19]\n"
    "ds_read_b128 a[212:215], v235 offset:14848\n"
    "v_add_f32 v211, v211, v215\n"
    "v_mfma_f32_16x16x32_bf16 a[64:67], a[224:227], v[180:183], a[64:67]\n"
    "v_exp_f32 v225, v113\n"
    "v_mfma_f32_16x16x32_bf16 a[112:115], a[224:227], v[184:187], a[112:115]\n"
    "v_exp_f32 v226, v114\n"
    "v_mfma_f32_16x16x32_bf16 a[160:163], a[224:227], v[188:191], a[160:163]\n"
    "ds_read_b128 a[216:219], v235 offset:18432\n"
    "v_cvt_pk_bf16_f32 v160, v216, v217\n"
    "v_mfma_f32_16x16x32_bf16 a[20:23], a[228:231], v[176:179], a[20:23]\n"
    "v_exp_f32 v227, v115\n"
    "v_mfma_f32_16x16x32_bf16 a[68:71], a[228:231], v[180:183], a[68:71]\n"
    "v_add_f32 v212, v216, v217\n"
    "v_cvt_pk_bf16_f32 v161, v218, v219\n"
    "v_mfma_f32_16x16x32_bf16 a[116:119], a[228:231], v[184:187], a[116:119]\n"
    "ds_read_b128 a[220:223], v235 offset:18944\n"
    "v_add_f32 v212, v212, v218\n"
    "v_mfma_f32_16x16x32_bf16 a[164:167], a[228:231], v[188:191], a[164:167]\n"
    "v_exp_f32 v228, v120\n"
    "v_mfma_f32_16x16x32_bf16 a[24:27], a[232:235], v[176:179], a[24:27]\n"
    "v_exp_f32 v229, v121\n"
    "v_mfma_f32_16x16x32_bf16 a[72:75], a[232:235], v[180:183], a[72:75]\n"
    "ds_read_b128 a[224:227], v235 offset:19456\n"
    "v_add_f32 v212, v212, v219\n"
    "v_mfma_f32_16x16x32_bf16 a[120:123], a[232:235], v[184:187], a[120:123]\n"
    "v_exp_f32 v230, v122\n"
    "v_mfma_f32_16x16x32_bf16 a[168:171], a[232:235], v[188:191], a[168:171]\n"
    "v_exp_f32 v231, v123\n"
    "v_mfma_f32_16x16x32_bf16 a[28:31], a[236:239], v[176:179], a[28:31]\n"
    "ds_read_b128 a[228:231], v235 offset:19968\n"
    "v_cvt_pk_bf16_f32 v164, v220, v221\n"
    "v_mfma_f32_16x16x32_bf16 a[76:79], a[236:239], v[180:183], a[76:79]\n"
    "v_add_f32 v213, v220, v221\n"
    "v_cvt_pk_bf16_f32 v165, v222, v223\n"
    "v_mfma_f32_16x16x32_bf16 a[124:127], a[236:239], v[184:187], a[124:127]\n"
    "v_exp_f32 v216, v100\n"
    "v_mfma_f32_16x16x32_bf16 a[172:175], a[236:239], v[188:191], a[172:175]\n"
    "ds_read_b128 a[232:235], v235 offset:20480\n"
    "v_add_f32 v213, v213, v222\n"
    "v_mfma_f32_16x16x32_bf16 a[32:35], a[240:243], v[176:179], a[32:35]\n"
    "v_exp_f32 v217, v101\n"
    "v_mfma_f32_16x16x32_bf16 a[80:83], a[240:243], v[180:183], a[80:83]\n"
    "v_add_f32 v213, v213, v223\n"
    "v_cvt_pk_bf16_f32 v168, v224, v225\n"
    "v_mfma_f32_16x16x32_bf16 a[128:131], a[240:243], v[184:187], a[128:131]\n"
    "ds_read_b128 a[236:239], v235 offset:20992\n"
    "v_add_f32 v214, v224, v225\n"
    "v_mfma_f32_16x16x32_bf16 a[176:179], a[240:243], v[188:191], a[176:179]\n"
    "v_exp_f32 v218, v102\n"
    "v_mfma_f32_16x16x32_bf16 a[36:39], a[244:247], v[176:179], a[36:39]\n"
    "v_exp_f32 v219, v103\n"
    "v_mfma_f32_16x16x32_bf16 a[84:87], a[244:247], v[180:183], a[84:87]\n"
    "v_cvt_pk_bf16_f32 v169, v226, v227\n"
    "v_add_f32 v214, v214, v226\n"
    "v_mfma_f32_16x16x32_bf16 a[132:135], a[244:247], v[184:187], a[132:135]\n"
    "v_exp_f32 v220, v108\n"
    "v_mfma_f32_16x16x32_bf16 a[180:183], a[244:247], v[188:191], a[180:183]\n"
    "v_exp_f32 v221, v109\n"
    "v_mfma_f32_16x16x32_bf16 a[40:43], a[248:251], v[176:179], a[40:43]\n"
    "v_add_f32 v214, v214, v227\n"
    "v_cvt_pk_bf16_f32 v172, v228, v229\n"
    "v_mfma_f32_16x16x32_bf16 a[88:91], a[248:251], v[180:183], a[88:91]\n"
    "v_add_f32 v215, v228, v229\n"
    "v_cvt_pk_bf16_f32 v173, v230, v231\n"
    "v_mfma_f32_16x16x32_bf16 a[136:139], a[248:251], v[184:187], a[136:139]\n"
    "v_exp_f32 v222, v110\n"
    "v_mfma_f32_16x16x32_bf16 a[184:187], a[248:251], v[188:191], a[184:187]\n"
    "v_exp_f32 v223, v111\n"
    "v_mfma_f32_16x16x32_bf16 a[44:47], a[252:255], v[176:179], a[44:47]\n"
    "v_add_f32 v215, v215, v230\n"
    "v_add_f32 v215, v215, v231\n"
    "v_mfma_f32_16x16x32_bf16 a[92:95], a[252:255], v[180:183], a[92:95]\n"
    "v_cvt_pk_bf16_f32 v162, v216, v217\n"
    "s_waitcnt vmcnt(12) lgkmcnt(0)\n"
    "v_mfma_f32_16x16x32_bf16 a[140:143], a[252:255], v[184:187], a[140:143]\n"
    "s_barrier\n"
    "v_mfma_f32_16x16x32_bf16 a[188:191], a[252:255], v[188:191], a[188:191]\n"
    "v_exp_f32 v224, v116\n"
    "s_mov_b32 s57, 0\n"
    "s_branch LLOOPA_4_%=\n"
    ".p2align 6\n"
    "LLOOP_4_%=:\n"
    "v_mfma_f32_16x16x32_bf16 a[0:3], a[240:243], v[160:163], a[0:3]\n"
    "v_exp_f32 v216, v128\n"
    "v_mfma_f32_16x16x32_bf16 a[48:51], a[240:243], v[164:167], a[48:51]\n"
    "ds_read_b128 a[224:227], v235 offset:0\n"
    "s_add_u32 m0, s42, s48\n"
    "v_mfma_f32_16x16x32_bf16 a[96:99], a[240:243], v[168:171], a[96:99]\n"
    "buffer_load_dwordx4 v237, s[24:27], s84 offen offset:0 lds\n"
    "v_mfma_f32_16x16x32_bf16 a[144:147], a[240:243], v[172:175], a[144:147]\n"
    "v_exp_f32 v217, v129\n"
    "v_mfma_f32_16x16x32_bf16 a[4:7], a[244:247], v[160:163], a[4:7]\n"
    "ds_read_b128 a[228:231], v235 offset:512\n"
    "v_add_f32 v208, v208, v212\n"
    "v_mfma_f32_16x16x32_bf16 a[52:55], a[244:247], v[164:167], a[52:55]\n"
    "v_exp_f32 v218, v130\n"
    "v_mfma_f32_16x16x32_bf16 a[100:103], a[244:247], v[168:171], a[100:103]\n"
    "v_exp_f32 v219, v131\n"
    "v_mfma_f32_16x16x32_bf16 a[148:151], a[244:247], v[172:175], a[148:151]\n"
    "ds_read_b128 a[232:235], v235 offset:1024\n"
    "v_add_f32 v209, v209, v213\n"
    "v_mfma_f32_16x16x32_bf16 a[8:11], a[248:251], v[160:163], a[8:11]\n"
    "v_exp_f32 v220, v136\n"
    "v_mfma_f32_16x16x32_bf16 a[56:59], a[248:251], v[164:167], a[56:59]\n"
    "buffer_load_dwordx4 v238, s[24:27], s84 offen offset:1024 lds\n"
    "v_mfma_f32_16x16x32_bf16 a[104:107], a[248:251], v[168:171], a[104:107]\n"
    "ds_read_b128 a[236:239], v235 offset:1536\n"
    "v_add_f32 v210, v210, v214\n"
    "v_mfma_f32_16x16x32_bf16 a[152:155], a[248:251], v[172:175], a[152:155]\n"
    "v_exp_f32 v221, v137\n"
    "v_mfma_f32_16x16x32_bf16 a[12:15], a[252:255], v[160:163], a[12:15]\n"
    "v_exp_f32 v222, v138\n"
    "v_mfma_f32_16x16x32_bf16 a[60:63], a[252:255], v[164:167], a[60:63]\n"
    "ds_read_b128 a[240:243], v235 offset:2048\n"
    "v_add_f32 v211, v211, v215\n"
    "v_mfma_f32_16x16x32_bf16 a[108:111], a[252:255], v[168:171], a[108:111]\n"
    "v_exp_f32 v223, v139\n"
    "v_mfma_f32_16x16x32_bf16 a[156:159], a[252:255], v[172:175], a[156:159]\n"
    "v_exp_f32 v224, v144\n"
    "v_mfma_f32_16x16x32_bf16 a[16:19], a[192:195], v[160:163], a[16:19]\n"
    "ds_read_b128 a[244:247], v235 offset:2560\n"
    "v_cvt_pk_bf16_f32 v176, v216, v217\n"
    "v_mfma_f32_16x16x32_bf16 a[64:67], a[192:195], v[164:167], a[64:67]\n"
    "buffer_load_dwordx4 v239, s[24:27], s84 offen offset:2048 lds\n"
    "v_mfma_f32_16x16x32_bf16 a[112:115], a[192:195], v[168:171], a[112:115]\n"
    "v_exp_f32 v225, v145\n"
    "v_mfma_f32_16x16x32_bf16 a[160:163], a[192:195], v[172:175], a[160:163]\n"
    "ds_read_b128 a[248:251], v235 offset:6144\n"
    "s_add_u32 m0, m0, 3072\n"
    "v_mfma_f32_16x16x32_bf16 a[20:23], a[196:199], v[160:163], a[20:23]\n"
    "v_exp_f32 v226, v146\n"
    "v_mfma_f32_16x16x32_bf16 a[68:71], a[196:199], v[164:167], a[68:71]\n"
    "v_exp_f32 v227, v147\n"
    "v_mfma_f32_16x16x32_bf16 a[116:119], a[196:199], v[168:171], a[116:119]\n"
    "ds_read_b128 a[252:255], v235 offset:6656\n"
    "s_add_u32 s45, s84, 3072\n"
    "v_mfma_f32_16x16x32_bf16 a[164:167], a[196:199], v[172:175], a[164:167]\n"
    "v_add_f32 v212, v216, v217\n"
    "v_cvt_pk_bf16_f32 v177, v218, v219\n"
    "v_mfma_f32_16x16x32_bf16 a[24:27], a[200:203], v[160:163], a[24:27]\n"
    "v_exp_f32 v228, v152\n"
    "v_mfma_f32_16x16x32_bf16 a[72:75], a[200:203], v[164:167], a[72:75]\n"
    "ds_read_b128 a[192:195], v235 offset:7168\n"
    "v_add_f32 v212, v212, v218\n"
    "v_mfma_f32_16x16x32_bf16 a[120:123], a[200:203], v[168:171], a[120:123]\n"
    "buffer_load_dwordx4 v237, s[24:27], s45 offen offset:0 lds\n"
    "v_mfma_f32_16x16x32_bf16 a[168:171], a[200:203], v[172:175], a[168:171]\n"
    "v_exp_f32 v229, v153\n"
    "v_mfma_f32_16x16x32_bf16 a[28:31], a[204:207], v[160:163], a[28:31]\n"
    "ds_read_b128 a[196:199], v235 offset:7680\n"
    "v_add_f32 v212, v212, v219\n"
    "v_mfma_f32_16x16x32_bf16 a[76:79], a[204:207], v[164:167], a[76:79]\n"
    "v_exp_f32 v230, v154\n"
    "v_mfma_f32_16x16x32_bf16 a[124:127], a[204:207], v[168:171], a[124:127]\n"
    "v_exp_f32 v231, v155\n"
    "v_mfma_f32_16x16x32_bf16 a[172:175], a[204:207], v[172:175], a[172:175]\n"
    "ds_read_b128 a[200:203], v235 offset:8192\n"
    "v_cvt_pk_bf16_f32 v180, v220, v221\n"
    "v_mfma_f32_16x16x32_bf16 a[32:35], a[208:211], v[160:163], a[32:35]\n"
    "buffer_load_dwordx4 v238, s[24:27], s45 offen offset:1024 lds\n"
    "v_mfma_f32_16x16x32_bf16 a[80:83], a[208:211], v[164:167], a[80:83]\n"
    "v_add_f32 v213, v220, v221\n"
    "v_cvt_pk_bf16_f32 v181, v222, v223\n"
    "v_mfma_f32_16x16x32_bf16 a[128:131], a[208:211], v[168:171], a[128:131]\n"
    "ds_read_b128 a[204:207], v235 offset:8704\n"
    "s_waitcnt lgkmcnt(4)\n"
    "v_mfma_f32_16x16x32_bf16 a[176:179], a[208:211], v[172:175], a[176:179]\n"
    "v_exp_f32 v216, v132\n"
    "v_mfma_f32_16x16x32_bf16 a[36:39], a[212:215], v[160:163], a[36:39]\n"
    "v_exp_f32 v217, v133\n"
    "v_mfma_f32_16x16x32_bf16 a[84:87], a[212:215], v[164:167], a[84:87]\n"
    "v_add_f32 v213, v213, v222\n"
    "v_add_f32 v213, v213, v223\n"
    "v_mfma_f32_16x16x32_bf16 a[132:135], a[212:215], v[168:171], a[132:135]\n"
    "v_cvt_pk_bf16_f32 v184, v224, v225\n"
    "v_add_f32 v214, v224, v225\n"
    "v_mfma_f32_16x16x32_bf16 a[180:183], a[212:215], v[172:175], a[180:183]\n"
    "v_exp_f32 v218, v134\n"
    "v_mfma_f32_16x16x32_bf16 a[40:43], a[216:219], v[160:163], a[40:43]\n"
    "buffer_load_dwordx4 v239, s[24:27], s45 offen offset:2048 lds\n"
    "v_mfma_f32_16x16x32_bf16 a[88:91], a[216:219], v[164:167], a[88:91]\n"
    "v_exp_f32 v219, v135\n"
    "v_mfma_f32_16x16x32_bf16 a[136:139], a[216:219], v[168:171], a[136:139]\n"
    "v_cvt_pk_bf16_f32 v185, v226, v227\n"
    "v_add_f32 v214, v214, v226\n"
    "v_mfma_f32_16x16x32_bf16 a[184:187], a[216:219], v[172:175], a[184:187]\n"
    "v_exp_f32 v220, v140\n"
    "v_mfma_f32_16x16x32_bf16 a[44:47], a[220:223], v[160:163], a[44:47]\n"
    "v_exp_f32 v221, v141\n"
    "v_mfma_f32_16x16x32_bf16 a[92:95], a[220:223], v[164:167], a[92:95]\n"
    "v_add_f32 v214, v214, v227\n"
    "v_cvt_pk_bf16_f32 v188, v228, v229\n"
    "v_mfma_f32_16x16x32_bf16 a[140:143], a[220:223], v[168:171], a[140:143]\n"
    "v_add_f32 v215, v228, v229\n"
    "v_cvt_pk_bf16_f32 v189, v230, v231\n"
    "v_mfma_f32_16x16x32_bf16 a[188:191], a[220:223], v[172:175], a[188:191]\n"
    "v_exp_f32 v222, v142\n"
    "v_mfma_f32_16x16x32_bf16 v[96:99], a[224:227], v[0:3], v[192:195]\n"
    "ds_read_b64_tr_b16 a[208:209], v236 offset:12288\n"
    "s_mov_b32 s84, s44\n"
    "v_mfma_f32_16x16x32_bf16 v[96:99], a[228:231], v[4:7], v[96:99]\n"
    "ds_read_b64_tr_b16 a[210:211], v236 offset:18432\n"
    "s_add_u32 s87, s87, 1\n"
    "v_mfma_f32_16x16x32_bf16 v[96:99], a[232:235], v[8:11], v[96:99]\n"
    "v_exp_f32 v223, v143\n"
    "v_mfma_f32_16x16x32_bf16 v[96:99], a[236:239], v[12:15], v[96:99]\n"
    "ds_read_b64_tr_b16 a[212:213], v241 offset:12288\n"
    "v_readlane_b32 s44, v252, s87\n"
    "v_mfma_f32_16x16x32_bf16 v[96:99], a[240:243], v[16:19], v[96:99]\n"
    "ds_read_b64_tr_b16 a[214:215], v241 offset:18432\n"
    "v_add_f32 v215, v215, v230\n"
    "v_mfma_f32_16x16x32_bf16 v[96:99], a[244:247], v[20:23], v[96:99]\n"
    "v_add_f32 v215, v215, v231\n"
    "v_cvt_pk_bf16_f32 v178, v216, v217\n"
    "v_mfma_f32_16x16x32_bf16 v[104:107], a[224:227], v[24:27], v[196:199]\n"
    "ds_read_b64_tr_b16 a[216:217], v236 offset:12800\n"
    "v_add_f32 v212, v212, v216\n"
    "v_mfma_f32_16x16x32_bf16 v[104:107], a[228:231], v[28:31], v[104:107]\n"
    "v_exp_f32 v224, v148\n"
    "v_mfma_f32_16x16x32_bf16 v[104:107], a[232:235], v[32:35], v[104:107]\n"
    "ds_read_b64_tr_b16 a[218:219], v236 offset:18944\n"
    "s_cmp_gt_u32 s87, s46\n"
    "v_mfma_f32_16x16x32_bf16 v[104:107], a[236:239], v[36:39], v[104:107]\n"
    "ds_read_b64_tr_b16 a[220:221], v241 offset:12800\n"
    "s_cselect_b32 s71, -1.0, s49\n"
    "v_mfma_f32_16x16x32_bf16 v[104:107], a[240:243], v[40:43], v[104:107]\n"
    "v_exp_f32 v225, v149\n"
    "v_mfma_f32_16x16x32_bf16 v[104:107], a[244:247], v[44:47], v[104:107]\n"
    "ds_read_b64_tr_b16 a[222:223], v241 offset:18944\n"
    "v_add_f32 v212, v212, v217\n"
    "v_mfma_f32_16x16x32_bf16 v[112:115], a[224:227], v[48:51], v[200:203]\n"
    "v_exp_f32 v226, v150\n"
    "v_mfma_f32_16x16x32_bf16 v[112:115], a[228:231], v[52:55], v[112:115]\n"
    "v_exp_f32 v227, v151\n"
    "v_mfma_f32_16x16x32_bf16 v[112:115], a[232:235], v[56:59], v[112:115]\n"
    "v_cvt_pk_bf16_f32 v179, v218, v219\n"
    "v_add_f32 v212, v212, v218\n"
    "v_mfma_f32_16x16x32_bf16 v[112:115], a[236:239], v[60:63], v[112:115]\n"
    "v_add_f32 v212, v212, v219\n"
    "v_cvt_pk_bf16_f32 v182, v220, v221\n"
    "v_mfma_f32_16x16x32_bf16 v[112:115], a[240:243], v[64:67], v[112:115]\n"
    "v_exp_f32 v228, v156\n"
    "v_mfma_f32_16x16x32_bf16 v[112:115], a[244:247], v[68:71], v[112:115]\n"
    "v_exp_f32 v229, v157\n"
    "v_mfma_f32_16x16x32_bf16 v[120:123], a[224:227], v[72:75], v[204:207]\n"
    "ds_read_b64_tr_b16 a[224:225], v236 offset:13312\n"
    "v_add_f32 v213, v213, v220\n"
    "v_mfma_f32_16x16x32_bf16 v[120:123], a[228:231], v[76:79], v[120:123]\n"
    "ds_read_b64_tr_b16 a[226:227], v236 offset:19456\n"
    "v_add_f32 v213, v213, v221\n"
    "v_mfma_f32_16x16x32_bf16 v[120:123], a[232:235], v[80:83], v[120:123]\n"
    "ds_read_b64_tr_b16 a[228:229], v241 offset:13312\n"
    "v_cvt_pk_bf16_f32 v183, v222, v223\n"
    "v_mfma_f32_16x16x32_bf16 v[120:123], a[236:239], v[84:87], v[120:123]\n"
    "ds_read_b64_tr_b16 a[230:231], v241 offset:19456\n"
    "v_add_f32 v213, v213, v222\n"
    "v_mfma_f32_16x16x32_bf16 v[120:123], a[240:243], v[88:91], v[120:123]\n"
    "ds_read_b64_tr_b16 a[232:233], v236 offset:13824\n"
    "v_add_f32 v213, v213, v223\n"
    "v_mfma_f32_16x16x32_bf16 v[120:123], a[244:247], v[92:95], v[120:123]\n"
    "ds_read_b64_tr_b16 a[234:235], v236 offset:19968\n"
    "v_cvt_pk_bf16_f32 v186, v224, v225\n"
    "v_mfma_f32_16x16x32_bf16 v[100:103], a[248:251], v[0:3], v[192:195]\n"
    "ds_read_b64_tr_b16 a[236:237], v241 offset:13824\n"
    "v_add_f32 v214, v214, v224\n"
    "v_mfma_f32_16x16x32_bf16 v[100:103], a[252:255], v[4:7], v[100:103]\n"
    "ds_read_b64_tr_b16 a[238:239], v241 offset:19968\n"
    "s_waitcnt lgkmcnt(8)\n"
    "v_mfma_f32_16x16x32_bf16 v[100:103], a[192:195], v[8:11], v[100:103]\n"
    "ds_read_b64_tr_b16 a[240:241], v236 offset:14336\n"
    "v_add_f32 v214, v214, v225\n"
    "v_mfma_f32_16x16x32_bf16 v[100:103], a[196:199], v[12:15], v[100:103]\n"
    "ds_read_b64_tr_b16 a[242:243], v236 offset:20480\n"
    "v_cvt_pk_bf16_f32 v187, v226, v227\n"
    "v_mfma_f32_16x16x32_bf16 v[100:103], a[200:203], v[16:19], v[100:103]\n"
    "ds_read_b64_tr_b16 a[244:245], v241 offset:14336\n"
    "v_add_f32 v214, v214, v226\n"
    "v_mfma_f32_16x16x32_bf16 v[100:103], a[204:207], v[20:23], v[100:103]\n"
    "v_exp_f32 v230, v158\n"
    "v_mfma_f32_16x16x32_bf16 v[108:111], a[248:251], v[24:27], v[196:199]\n"
    "ds_read_b64_tr_b16 a[246:247], v241 offset:20480\n"
    "v_add_f32 v214, v214, v227\n"
    "v_mfma_f32_16x16x32_bf16 v[108:111], a[252:255], v[28:31], v[108:111]\n"
    "v_exp_f32 v231, v159\n"
    "v_mfma_f32_16x16x32_bf16 v[108:111], a[192:195], v[32:35], v[108:111]\n"
    "v_cvt_pk_bf16_f32 v190, v228, v229\n"
    "v_add_f32 v215, v215, v228\n"
    "v_mfma_f32_16x16x32_bf16 v[108:111], a[196:199], v[36:39], v[108:111]\n"
    "v_add_f32 v215, v215, v229\n"
    "v_cvt_pk_bf16_f32 v191, v230, v231\n"
    "v_mfma_f32_16x16x32_bf16 v[108:111], a[200:203], v[40:43], v[108:111]\n"
    "v_add_f32 v215, v215, v230\n"
    "v_add_f32 v215, v215, v231\n"
    "v_mfma_f32_16x16x32_bf16 v[108:111], a[204:207], v[44:47], v[108:111]\n"
    "v_mfma_f32_16x16x32_bf16 v[116:119], a[248:251], v[48:51], v[200:203]\n"
    "v_mfma_f32_16x16x32_bf16 v[116:119], a[252:255], v[52:55], v[116:119]\n"
    "v_mfma_f32_16x16x32_bf16 v[116:119], a[192:195], v[56:59], v[116:119]\n"
    "v_mfma_f32_16x16x32_bf16 v[116:119], a[196:199], v[60:63], v[116:119]\n"
    "v_mfma_f32_16x16x32_bf16 v[116:119], a[200:203], v[64:67], v[116:119]\n"
    "v_mfma_f32_16x16x32_bf16 v[116:119], a[204:207], v[68:71], v[116:119]\n"
    "v_mfma_f32_16x16x32_bf16 v[124:127], a[248:251], v[72:75], v[204:207]\n"
    "ds_read_b64_tr_b16 a[248:249], v236 offset:14848\n"
    "v_max3_f32 v232, v212, v213, v214\n"
    "v_mfma_f32_16x16x32_bf16 v[124:127], a[252:255], v[76:79], v[124:127]\n"
    "ds_read_b64_tr_b16 a[250:251], v236 offset:20992\n"
    "v_max_f32 v232, v232, v215\n"
    "v_mfma_f32_16x16x32_bf16 v[124:127], a[192:195], v[80:83], v[124:127]\n"
    "ds_read_b64_tr_b16 a[252:253], v241 offset:14848\n"
    "v_cmp_nge_f32 vcc, s49, v232\n"
    "v_mfma_f32_16x16x32_bf16 v[124:127], a[196:199], v[84:87], v[124:127]\n"
    "ds_read_b64_tr_b16 a[254:255], v241 offset:20992\n"
    "v_mfma_f32_16x16x32_bf16 v[124:127], a[200:203], v[88:91], v[124:127]\n"
    "v_mfma_f32_16x16x32_bf16 v[124:127], a[204:207], v[92:95], v[124:127]\n"
    "v_add_u32 v236, s41, v234\n"
    "s_cbranch_vccnz LRARE_o_4_%=\n"
    "LRET_o_4_L_%=:\n"
    "v_mfma_f32_16x16x32_bf16 a[0:3], a[208:211], v[176:179], a[0:3]\n"
    "v_exp_f32 v216, v96\n"
    "v_mfma_f32_16x16x32_bf16 a[48:51], a[208:211], v[180:183], a[48:51]\n"
    "ds_read_b128 a[192:195], v235 offset:12288\n"
    "v_xor_b32 v241, 32, v236\n"
    "v_mfma_f32_16x16x32_bf16 a[96:99], a[208:211], v[184:187], a[96:99]\n"
    "v_exp_f32 v217, v97\n"
    "v_mfma_f32_16x16x32_bf16 a[144:147], a[208:211], v[188:191], a[144:147]\n"
    "v_exp_f32 v218, v98\n"
    "v_mfma_f32_16x16x32_bf16 a[4:7], a[212:215], v[176:179], a[4:7]\n"
    "ds_read_b128 a[196:199], v235 offset:12800\n"
    "s_mov_b32 s43, s40\n"
    "v_mfma_f32_16x16x32_bf16 a[52:55], a[212:215], v[180:183], a[52:55]\n"
    "v_exp_f32 v219, v99\n"
    "v_mfma_f32_16x16x32_bf16 a[100:103], a[212:215], v[184:187], a[100:103]\n"
    "v_exp_f32 v220, v104\n"
    "v_mfma_f32_16x16x32_bf16 a[148:151], a[212:215], v[188:191], a[148:151]\n"
    "ds_read_b128 a[200:203], v235 offset:13312\n"
    "s_mov_b32 s40, s41\n"
    "v_mfma_f32_16x16x32_bf16 a[8:11], a[216:219], v[176:179], a[8:11]\n"
    "v_exp_f32 v221, v105\n"
    "v_mfma_f32_16x16x32_bf16 a[56:59], a[216:219], v[180:183], a[56:59]\n"
    "v_exp_f32 v222, v106\n"
    "v_mfma_f32_16x16x32_bf16 a[104:107], a[216:219], v[184:187], a[104:107]\n"
    "ds_read_b128 a[204:207], v235 offset:13824\n"
    "s_mov_b32 s41, s42\n"
    "v_mfma_f32_16x16x32_bf16 a[152:155], a[216:219], v[188:191], a[152:155]\n"
    "v_exp_f32 v223, v107\n"
    "v_mfma_f32_16x16x32_bf16 a[12:15], a[220:223], v[176:179], a[12:15]\n"
    "v_add_f32 v208, v208, v212\n"
    "v_add_f32 v209, v209, v213\n"
    "v_mfma_f32_16x16x32_bf16 a[60:63], a[220:223], v[180:183], a[60:63]\n"
    "ds_read_b128 a[208:211], v235 offset:14336\n"
    "s_mov_b32 s42, s43\n"
    "v_mfma_f32_16x16x32_bf16 a[108:111], a[220:223], v[184:187], a[108:111]\n"
    "v_exp_f32 v224, v112\n"
    "v_mfma_f32_16x16x32_bf16 a[156:159], a[220:223], v[188:191], a[156:159]\n"
    "v_add_f32 v210, v210, v214\n"
    "s_waitcnt lgkmcnt(3)\n"
    "v_mfma_f32_16x16x32_bf16 a[16:19], a[224:227], v[176:179], a[16:19]\n"
    "ds_read_b128 a[212:215], v235 offset:14848\n"
    "v_add_f32 v211, v211, v215\n"
    "v_mfma_f32_16x16x32_bf16 a[64:67], a[224:227], v[180:183], a[64:67]\n"
    "v_exp_f32 v225, v113\n"
    "v_mfma_f32_16x16x32_bf16 a[112:115], a[224:227], v[184:187], a[112:115]\n"
    "v_exp_f32 v226, v114\n"
    "v_mfma_f32_16x16x32_bf16 a[160:163], a[224:227], v[188:191], a[160:163]\n"
    "ds_read_b128 a[216:219], v235 offset:18432\n"
    "v_cvt_pk_bf16_f32 v160, v216, v217\n"
    "v_mfma_f32_16x16x32_bf16 a[20:23], a[228:231], v[176:179], a[20:23]\n"
    "v_exp_f32 v227, v115\n"
    "v_mfma_f32_16x16x32_bf16 a[68:71], a[228:231], v[180:183], a[68:71]\n"
    "v_add_f32 v212, v216, v217\n"
    "v_cvt_pk_bf16_f32 v161, v218, v219\n"
    "v_mfma_f32_16x16x32_bf16 a[116:119], a[228:231], v[184:187], a[116:119]\n"
    "ds_read_b128 a[220:223], v235 offset:18944\n"
    "v_add_f32 v212, v212, v218\n"
    "v_mfma_f32_16x16x32_bf16 a[164:167], a[228:231], v[188:191], a[164:167]\n"
    "v_exp_f32 v228, v120\n"
    "v_mfma_f32_16x16x32_bf16 a[24:27], a[232:235], v[176:179], a[24:27]\n"
    "v_exp_f32 v229, v121\n"
    "v_mfma_f32_16x16x32_bf16 a[72:75], a[232:235], v[180:183], a[72:75]\n"
    "ds_read_b128 a[224:227], v235 offset:19456\n"
    "v_add_f32 v212, v212, v219\n"
    "v_mfma_f32_16x16x32_bf16 a[120:123], a[232:235], v[184:187], a[120:123]\n"
    "v_exp_f32 v230, v122\n"
    "v_mfma_f32_16x16x32_bf16 a[168:171], a[232:235], v[188:191], a[168:171]\n"
    "v_exp_f32 v231, v123\n"
    "v_mfma_f32_16x16x32_bf16 a[28:31], a[236:239], v[176:179], a[28:31]\n"
    "ds_read_b128 a[228:231], v235 offset:19968\n"
    "v_cvt_pk_bf16_f32 v164, v220, v221\n"
    "v_mfma_f32_16x16x32_bf16 a[76:79], a[236:239], v[180:183], a[76:79]\n"
    "v_add_f32 v213, v220, v221\n"
    "v_cvt_pk_bf16_f32 v165, v222, v223\n"
    "v_mfma_f32_16x16x32_bf16 a[124:127], a[236:239], v[184:187], a[124:127]\n"
    "v_exp_f32 v216, v100\n"
    "v_mfma_f32_16x16x32_bf16 a[172:175], a[236:239], v[188:191], a[172:175]\n"
    "ds_read_b128 a[232:235], v235 offset:20480\n"
    "v_add_f32 v213, v213, v222\n"
    "v_mfma_f32_16x16x32_bf16 a[32:35], a[240:243], v[176:179], a[32:35]\n"
    "v_exp_f32 v217, v101\n"
    "v_mfma_f32_16x16x32_bf16 a[80:83], a[240:243], v[180:183], a[80:83]\n"
    "v_add_f32 v213, v213, v223\n"
    "v_cvt_pk_bf16_f32 v168, v224, v225\n"
    "v_mfma_f32_16x16x32_bf16 a[128:131], a[240:243], v[184:187], a[128:131]\n"
    "ds_read_b128 a[236:239], v235 offset:20992\n"
    "v_add_f32 v214, v224, v225\n"
    "v_mfma_f32_16x16x32_bf16 a[176:179], a[240:243], v[188:191], a[176:179]\n"
    "v_exp_f32 v218, v102\n"
    "v_mfma_f32_16x16x32_bf16 a[36:39], a[244:247], v[176:179], a[36:39]\n"
    "v_exp_f32 v219, v103\n"
    "v_mfma_f32_16x16x32_bf16 a[84:87], a[244:247], v[180:183], a[84:87]\n"
    "v_cvt_pk_bf16_f32 v169, v226, v227\n"
    "v_add_f32 v214, v214, v226\n"
    "v_mfma_f32_16x16x32_bf16 a[132:135], a[244:247], v[184:187], a[132:135]\n"
    "v_exp_f32 v220, v108\n"
    "v_mfma_f32_16x16x32_bf16 a[180:183], a[244:247], v[188:191], a[180:183]\n"
    "v_exp_f32 v221, v109\n"
    "v_mfma_f32_16x16x32_bf16 a[40:43], a[248:251], v[176:179], a[40:43]\n"
    "v_add_f32 v214, v214, v227\n"
    "v_cvt_pk_bf16_f32 v172, v228, v229\n"
    "v_mfma_f32_16x16x32_bf16 a[88:91], a[248:251], v[180:183], a[88:91]\n"
    "v_add_f32 v215, v228, v229\n"
    "v_cvt_pk_bf16_f32 v173, v230, v231\n"
    "v_mfma_f32_16x16x32_bf16 a[136:139], a[248:251], v[184:187], a[136:139]\n"
    "v_exp_f32 v222, v110\n"
    "v_mfma_f32_16x16x32_bf16 a[184:187], a[248:251], v[188:191], a[184:187]\n"
    "v_exp_f32 v223, v111\n"
    "v_mfma_f32_16x16x32_bf16 a[44:47], a[252:255], v[176:179], a[44:47]\n"
    "v_add_f32 v215, v215, v230\n"
    "v_add_f32 v215, v215, v231\n"
    "v_mfma_f32_16x16x32_bf16 a[92:95], a[252:255], v[180:183], a[92:95]\n"
    "v_cvt_pk_bf16_f32 v162, v216, v217\n"
    "s_waitcnt vmcnt(14) lgkmcnt(0)\n"
    "v_mfma_f32_16x16x32_bf16 a[140:143], a[252:255], v[184:187], a[140:143]\n"
    "s_barrier\n"
    "v_mfma_f32_16x16x32_bf16 a[188:191], a[252:255], v[188:191], a[188:191]\n"
    "v_exp_f32 v224, v116\n"
    "LLOOPA_4_%=:\n"
    "v_mfma_f32_16x16x32_bf16 v[128:131], a[192:195], v[0:3], v[192:195]\n"
    "ds_read_b64_tr_b16 a[240:241], v236 offset:0\n"
    "s_add_u32 m0, s40, s47\n"
    "v_mfma_f32_16x16x32_bf16 v[128:131], a[196:199], v[4:7], v[128:131]\n"
    "ds_read_b64_tr_b16 a[242:243], v236 offset:6144\n"
    "v_add_u32 v235, s41, v233\n"
    "v_mfma_f32_16x16x32_bf16 v[128:131], a[200:203], v[8:11], v[128:131]\n"
    "v_exp_f32 v225, v117\n"
    "v_mfma_f32_16x16x32_bf16 v[128:131], a[204:207], v[12:15], v[128:131]\n"
    "ds_read_b64_tr_b16 a[244:245], v241 offset:0\n"
    "v_add_f32 v212, v212, v216\n"
    "v_mfma_f32_16x16x32_bf16 v[128:131], a[208:211], v[16:19], v[128:131]\n"
    "ds_read_b64_tr_b16 a[246:247], v241 offset:6144\n"
    "v_add_f32 v212, v212, v217\n"
    "v_mfma_f32_16x16x32_bf16 v[128:131], a[212:215], v[20:23], v[128:131]\n"
    "buffer_load_dwordx4 v237, s[20:23], s44 offen offset:0 lds\n"
    "v_mfma_f32_16x16x32_bf16 v[136:139], a[192:195], v[24:27], v[196:199]\n"
    "ds_read_b64_tr_b16 a[248:249], v236 offset:512\n"
    "v_cvt_pk_bf16_f32 v163, v218, v219\n"
    "v_mfma_f32_16x16x32_bf16 v[136:139], a[196:199], v[28:31], v[136:139]\n"
    "buffer_load_dword v254, v255, s[28:31], s79 offen\n"
    "v_mfma_f32_16x16x32_bf16 v[136:139], a[200:203], v[32:35], v[136:139]\n"
    "ds_read_b64_tr_b16 a[250:251], v236 offset:6656\n"
    "s_add_u32 s79, s79, 1280\n"
    "v_mfma_f32_16x16x32_bf16 v[136:139], a[204:207], v[36:39], v[136:139]\n"
    "ds_read_b64_tr_b16 a[252:253], v241 offset:512\n"
    "v_add_f32 v212, v212, v218\n"
    "v_mfma_f32_16x16x32_bf16 v[136:139], a[208:211], v[40:43], v[136:139]\n"
    "buffer_load_dwordx4 v238, s[20:23], s44 offen offset:1024 lds\n"
    "v_mfma_f32_16x16x32_bf16 v[136:139], a[212:215], v[44:47], v[136:139]\n"
    "ds_read_b64_tr_b16 a[254:255], v241 offset:6656\n"
    "v_add_f32 v212, v212, v219\n"
    "v_mfma_f32_16x16x32_bf16 v[144:147], a[192:195], v[48:51], v[200:203]\n"
    "buffer_load_dword v254, v253, s[92:95], s86 offen\n"
    "v_mfma_f32_16x16x32_bf16 v[144:147], a[196:199], v[52:55], v[144:147]\n"
    "v_exp_f32 v226, v118\n"
    "v_mfma_f32_16x16x32_bf16 v[144:147], a[200:203], v[56:59], v[144:147]\n"
    "v_exp_f32 v227, v119\n"
    "v_mfma_f32_16x16x32_bf16 v[144:147], a[204:207], v[60:63], v[144:147]\n"
    "v_exp_f32 v228, v124\n"
    "v_mfma_f32_16x16x32_bf16 v[144:147], a[208:211], v[64:67], v[144:147]\n"
    "v_exp_f32 v229, v125\n"
    "v_mfma_f32_16x16x32_bf16 v[144:147], a[212:215], v[68:71], v[144:147]\n"
    "buffer_load_dwordx4 v239, s[20:23], s44 offen offset:2048 lds\n"
    "v_mfma_f32_16x16x32_bf16 v[152:155], a[192:195], v[72:75], v[204:207]\n"
    "ds_read_b64_tr_b16 a[192:193], v236 offset:1024\n"
    "s_add_u32 m0, m0, 3072\n"
    "v_mfma_f32_16x16x32_bf16 v[152:155], a[196:199], v[76:79], v[152:155]\n"
    "ds_read_b64_tr_b16 a[194:195], v236 offset:7168\n"
    "s_add_u32 s45, s44, 3072\n"
    "v_mfma_f32_16x16x32_bf16 v[152:155], a[200:203], v[80:83], v[152:155]\n"
    "ds_read_b64_tr_b16 a[196:197], v241 offset:1024\n"
    "s_sub_u32 s86, s86, 1048576\n"
    "v_mfma_f32_16x16x32_bf16 v[152:155], a[204:207], v[84:87], v[152:155]\n"
    "ds_read_b64_tr_b16 a[198:199], v241 offset:7168\n"
    "v_cvt_pk_bf16_f32 v166, v220, v221\n"
    "v_mfma_f32_16x16x32_bf16 v[152:155], a[208:211], v[88:91], v[152:155]\n"
    "ds_read_b64_tr_b16 a[200:201], v236 offset:1536\n"
    "v_add_f32 v213, v213, v220\n"
    "v_mfma_f32_16x16x32_bf16 v[152:155], a[212:215], v[92:95], v[152:155]\n"
    "ds_read_b64_tr_b16 a[202:203], v236 offset:7680\n"
    "v_add_f32 v213, v213, v221\n"
    "v_mfma_f32_16x16x32_bf16 v[132:135], a[216:219], v[0:3], v[192:195]\n"
    "ds_read_b64_tr_b16 a[204:205], v241 offset:1536\n"
    "v_cvt_pk_bf16_f32 v167, v222, v223\n"
    "v_mfma_f32_16x16x32_bf16 v[132:135], a[220:223], v[4:7], v[132:135]\n"
    "ds_read_b64_tr_b16 a[206:207], v241 offset:7680\n"
    "v_add_f32 v213, v213, v222\n"
    "v_mfma_f32_16x16x32_bf16 v[132:135], a[224:227], v[8:11], v[132:135]\n"
    "ds_read_b64_tr_b16 a[208:209], v236 offset:2048\n"
    "v_add_f32 v213, v213, v223\n"
    "v_mfma_f32_16x16x32_bf16 v[132:135], a[228:231], v[12:15], v[132:135]\n"
    "ds_read_b64_tr_b16 a[210:211], v236 offset:8192\n"
    "v_cvt_pk_bf16_f32 v170, v224, v225\n"
    "v_mfma_f32_16x16x32_bf16 v[132:135], a[232:235], v[16:19], v[132:135]\n"
    "ds_read_b64_tr_b16 a[212:213], v241 offset:2048\n"
    "v_add_f32 v214, v214, v224\n"
    "v_mfma_f32_16x16x32_bf16 v[132:135], a[236:239], v[20:23], v[132:135]\n"
    "buffer_load_dwordx4 v237, s[20:23], s45 offen offset:0 lds\n"
    "v_mfma_f32_16x16x32_bf16 v[140:143], a[216:219], v[24:27], v[196:199]\n"
    "ds_read_b64_tr_b16 a[214:215], v241 offset:8192\n"
    "v_add_f32 v214, v214, v225\n"
    "v_mfma_f32_16x16x32_bf16 v[140:143], a[220:223], v[28:31], v[140:143]\n"
    "buffer_load_dwordx4 v238, s[20:23], s45 offen offset:1024 lds\n"
    "v_mfma_f32_16x16x32_bf16 v[140:143], a[224:227], v[32:35], v[140:143]\n"
    "v_exp_f32 v230, v126\n"
    "v_mfma_f32_16x16x32_bf16 v[140:143], a[228:231], v[36:39], v[140:143]\n"
    "v_exp_f32 v231, v127\n"
    "v_mfma_f32_16x16x32_bf16 v[140:143], a[232:235], v[40:43], v[140:143]\n"
    "v_cvt_pk_bf16_f32 v171, v226, v227\n"
    "v_add_f32 v214, v214, v226\n"
    "v_mfma_f32_16x16x32_bf16 v[140:143], a[236:239], v[44:47], v[140:143]\n"
    "v_add_f32 v214, v214, v227\n"
    "v_cvt_pk_bf16_f32 v174, v228, v229\n"
    "v_mfma_f32_16x16x32_bf16 v[148:151], a[216:219], v[48:51], v[200:203]\n"
    "v_add_f32 v215, v215, v228\n"
    "v_add_f32 v215, v215, v229\n"
    "v_mfma_f32_16x16x32_bf16 v[148:151], a[220:223], v[52:55], v[148:151]\n"
    "buffer_load_dwordx4 v239, s[20:23], s45 offen offset:2048 lds\n"
    "v_mfma_f32_16x16x32_bf16 v[148:151], a[224:227], v[56:59], v[148:151]\n"
    "v_cvt_pk_bf16_f32 v175, v230, v231\n"
    "v_add_f32 v215, v215, v230\n"
    "v_mfma_f32_16x16x32_bf16 v[148:151], a[228:231], v[60:63], v[148:151]\n"
    "v_add_f32 v215, v215, v231\n"
    "v_mfma_f32_16x16x32_bf16 v[148:151], a[232:235], v[64:67], v[148:151]\n"
    "v_mfma_f32_16x16x32_bf16 v[148:151], a[236:239], v[68:71], v[148:151]\n"
    "v_mfma_f32_16x16x32_bf16 v[156:159], a[216:219], v[72:75], v[204:207]\n"
    "ds_read_b64_tr_b16 a[216:217], v236 offset:2560\n"
    "v_max3_f32 v232, v212, v213, v214\n"
    "v_mfma_f32_16x16x32_bf16 v[156:159], a[220:223], v[76:79], v[156:159]\n"
    "ds_read_b64_tr_b16 a[218:219], v236 offset:8704\n"
    "v_max_f32 v232, v232, v215\n"
    "v_mfma_f32_16x16x32_bf16 v[156:159], a[224:227], v[80:83], v[156:159]\n"
    "ds_read_b64_tr_b16 a[220:221], v241 offset:2560\n"
    "v_cmp_nge_f32 vcc, s71, v232\n"
    "v_mfma_f32_16x16x32_bf16 v[156:159], a[228:231], v[84:87], v[156:159]\n"
    "ds_read_b64_tr_b16 a[222:223], v241 offset:8704\n"
    "v_mfma_f32_16x16x32_bf16 v[156:159], a[232:235], v[88:91], v[156:159]\n"
    "v_mfma_f32_16x16x32_bf16 v[156:159], a[236:239], v[92:95], v[156:159]\n"
    "s_waitcnt lgkmcnt(4)\n"
    "s_cbranch_vccz LLOOP_4_%=\n"
    "v_cmp_nge_f32 vcc, s49, v232\n"
    "s_cmp_le_u32 s87, s46\n"
    "s_cbranch_vccnz LRARE_e_4_%=\n"
    "s_cbranch_scc1 LLOOP_4_%=\n"
    "s_branch LFINX_4_%=\n"
    "LRET_e_4_L_%=:\n"
    "s_cmp_le_u32 s87, s46\n"
    "s_cbranch_scc1 LLOOP_4_%=\n"
    "LFINX_4_%=:\n"
    "s_cmp_eq_u32 s51, 0\n"
    "s_cbranch_scc0 LFIN1_4_%=\n"
    "LFIN0_4_%=:\n"
    "s_mov_b32 s57, 1\n"
    "s_add_u32 s58, s55, 0\n"
    "s_add_u32 s59, s55, 6144\n"
    "s_add_u32 s60, s55, 12288\n"
    "s_cmp_eq_u32 s53, 3\n"
    "s_cselect_b32 s60, s35, s60\n"
    "s_cmp_eq_u32 s53, 3\n"
    "s_mov_b32 s62, LPRO1_3_%=-LSTART_%=\n"
    "s_cselect_b32 s62, LPRO1_2_%=-LSTART_%=, s62\n"
    "s_bfe_u32 s63, s53, 65536\n"
    "s_cmp_eq_u32 s53, 3\n"
    "s_cselect_b32 s63, 0, s63\n"
    "s_lshl_b32 s63, s63, 13\n"
    "s_add_u32 s62, s62, s63\n"
    "s_waitcnt lgkmcnt(4)\n"
    "v_mfma_f32_16x16x32_bf16 a[0:3], a[240:243], v[160:163], a[0:3]\n"
    "ds_read_b64_tr_b16 a[224:225], v236 offset:12288\n"
    "v_add_f32 v208, v208, v212\n"
    "v_mfma_f32_16x16x32_bf16 a[48:51], a[240:243], v[164:167], a[48:51]\n"
    "ds_read_b64_tr_b16 a[226:227], v236 offset:18432\n"
    "v_add_f32 v209, v209, v213\n"
    "v_mfma_f32_16x16x32_bf16 a[96:99], a[240:243], v[168:171], a[96:99]\n"
    "buffer_load_dwordx4 v[0:3], v240, s[28:31], s58 offen offset:0\n"
    "v_mfma_f32_16x16x32_bf16 a[144:147], a[240:243], v[172:175], a[144:147]\n"
    "ds_read_b64_tr_b16 a[228:229], v241 offset:12288\n"
    "v_add_f32 v210, v210, v214\n"
    "v_mfma_f32_16x16x32_bf16 a[4:7], a[244:247], v[160:163], a[4:7]\n"
    "ds_read_b64_tr_b16 a[230:231], v241 offset:18432\n"
    "v_add_f32 v211, v211, v215\n"
    "v_mfma_f32_16x16x32_bf16 a[52:55], a[244:247], v[164:167], a[52:55]\n"
    "v_exp_f32 v216, v128\n"
    "v_mfma_f32_16x16x32_bf16 a[100:103], a[244:247], v[168:171], a[100:103]\n"
    "ds_read_b64_tr_b16 a[232:233], v236 offset:12800\n"
    "v_mfma_f32_16x16x32_bf16 a[148:151], a[244:247], v[172:175], a[148:151]\n"
    "ds_read_b64_tr_b16 a[234:235], v236 offset:18944\n"
    "v_mfma_f32_16x16x32_bf16 a[8:11], a[248:251], v[160:163], a[8:11]\n"
    "buffer_load_dwordx4 v[4:7], v240, s[28:31], s58 offen offset:3072\n"
    "v_mfma_f32_16x16x32_bf16 a[56:59], a[248:251], v[164:167], a[56:59]\n"
    "ds_read_b64_tr_b16 a[236:237], v241 offset:12800\n"
    "v_mfma_f32_16x16x32_bf16 a[104:107], a[248:251], v[168:171], a[104:107]\n"
    "ds_read_b64_tr_b16 a[238:239], v241 offset:18944\n"
    "v_mfma_f32_16x16x32_bf16 a[152:155], a[248:251], v[172:175], a[152:155]\n"
    "v_exp_f32 v217, v129\n"
    "v_mfma_f32_16x16x32_bf16 a[12:15], a[252:255], v[160:163], a[12:15]\n"
    "ds_read_b64_tr_b16 a[240:241], v236 offset:13312\n"
    "v_cvt_pk_bf16_f32 v176, v216, v217\n"
    "v_mfma_f32_16x16x32_bf16 a[60:63], a[252:255], v[164:167], a[60:63]\n"
    "ds_read_b64_tr_b16 a[242:243], v236 offset:19456\n"
    "v_add_f32 v212, v216, v217\n"
    "v_mfma_f32_16x16x32_bf16 a[108:111], a[252:255], v[168:171], a[108:111]\n"
    "buffer_load_dwordx4 v[8:11], v240, s[28:31], s58 offen offset:128\n"
    "v_mfma_f32_16x16x32_bf16 a[156:159], a[252:255], v[172:175], a[156:159]\n"
    "ds_read_b64_tr_b16 a[244:245], v241 offset:13312\n"
    "v_mfma_f32_16x16x32_bf16 a[16:19], a[192:195], v[160:163], a[16:19]\n"
    "ds_read_b64_tr_b16 a[246:247], v241 offset:19456\n"
    "v_mfma_f32_16x16x32_bf16 a[64:67], a[192:195], v[164:167], a[64:67]\n"
    "v_exp_f32 v218, v130\n"
    "v_mfma_f32_16x16x32_bf16 a[112:115], a[192:195], v[168:171], a[112:115]\n"
    "ds_read_b64_tr_b16 a[248:249], v236 offset:13824\n"
    "v_mfma_f32_16x16x32_bf16 a[160:163], a[192:195], v[172:175], a[160:163]\n"
    "ds_read_b64_tr_b16 a[250:251], v236 offset:19968\n"
    "v_mfma_f32_16x16x32_bf16 a[20:23], a[196:199], v[160:163], a[20:23]\n"
    "buffer_load_dwordx4 v[12:15], v240, s[28:31], s58 offen offset:3200\n"
    "v_mfma_f32_16x16x32_bf16 a[68:71], a[196:199], v[164:167], a[68:71]\n"
    "ds_read_b64_tr_b16 a[252:253], v241 offset:13824\n"
    "v_mfma_f32_16x16x32_bf16 a[116:119], a[196:199], v[168:171], a[116:119]\n"
    "ds_read_b64_tr_b16 a[254:255], v241 offset:19968\n"
    "v_mfma_f32_16x16x32_bf16 a[164:167], a[196:199], v[172:175], a[164:167]\n"
    "v_exp_f32 v219, v131\n"
    "v_mfma_f32_16x16x32_bf16 a[24:27], a[200:203], v[160:163], a[24:27]\n"
    "ds_read_b64_tr_b16 a[192:193], v236 offset:14336\n"
    "v_cvt_pk_bf16_f32 v177, v218, v219\n"
    "v_mfma_f32_16x16x32_bf16 a[72:75], a[200:203], v[164:167], a[72:75]\n"
    "ds_read_b64_tr_b16 a[194:195], v236 offset:20480\n"
    "v_add_f32 v212, v212, v218\n"
    "v_mfma_f32_16x16x32_bf16 a[120:123], a[200:203], v[168:171], a[120:123]\n"
    "buffer_load_dwordx4 v[16:19], v240, s[28:31], s58 offen offset:256\n"
    "v_mfma_f32_16x16x32_bf16 a[168:171], a[200:203], v[172:175], a[168:171]\n"
    "ds_read_b64_tr_b16 a[196:197], v241 offset:14336\n"
    "v_add_f32 v212, v212, v219\n"
    "v_mfma_f32_16x16x32_bf16 a[28:31], a[204:207], v[160:163], a[28:31]\n"
    "ds_read_b64_tr_b16 a[198:199], v241 offset:20480\n"
    "v_mfma_f32_16x16x32_bf16 a[76:79], a[204:207], v[164:167], a[76:79]\n"
    "v_exp_f32 v220, v136\n"
    "v_mfma_f32_16x16x32_bf16 a[124:127], a[204:207], v[168:171], a[124:127]\n"
    "ds_read_b64_tr_b16 a[200:201], v236 offset:14848\n"
    "v_mfma_f32_16x16x32_bf16 a[172:175], a[204:207], v[172:175], a[172:175]\n"
    "ds_read_b64_tr_b16 a[202:203], v236 offset:20992\n"
    "v_mfma_f32_16x16x32_bf16 a[32:35], a[208:211], v[160:163], a[32:35]\n"
    "buffer_load_dwordx4 v[20:23], v240, s[28:31], s58 offen offset:3328\n"
    "v_mfma_f32_16x16x32_bf16 a[80:83], a[208:211], v[164:167], a[80:83]\n"
    "ds_read_b64_tr_b16 a[204:205], v241 offset:14848\n"
    "v_mfma_f32_16x16x32_bf16 a[128:131], a[208:211], v[168:171], a[128:131]\n"
    "ds_read_b64_tr_b16 a[206:207], v241 offset:20992\n"
    "v_mfma_f32_16x16x32_bf16 a[176:179], a[208:211], v[172:175], a[176:179]\n"
    "v_exp_f32 v221, v137\n"
    "v_mfma_f32_16x16x32_bf16 a[36:39], a[212:215], v[160:163], a[36:39]\n"
    "v_exp_f32 v222, v138\n"
    "v_mfma_f32_16x16x32_bf16 a[84:87], a[212:215], v[164:167], a[84:87]\n"
    "v_exp_f32 v223, v139\n"
    "v_mfma_f32_16x16x32_bf16 a[132:135], a[212:215], v[168:171], a[132:135]\n"
    "buffer_load_dwordx4 v[24:27], v240, s[28:31], s59 offen offset:0\n"
    "v_mfma_f32_16x16x32_bf16 a[180:183], a[212:215], v[172:175], a[180:183]\n"
    "v_exp_f32 v224, v144\n"
    "s_waitcnt lgkmcnt(4)\n"
    "v_mfma_f32_16x16x32_bf16 a[40:43], a[216:219], v[160:163], a[40:43]\n"
    "v_exp_f32 v225, v145\n"
    "v_mfma_f32_16x16x32_bf16 a[88:91], a[216:219], v[164:167], a[88:91]\n"
    "v_exp_f32 v226, v146\n"
    "v_mfma_f32_16x16x32_bf16 a[136:139], a[216:219], v[168:171], a[136:139]\n"
    "buffer_load_dword v254, v253, s[92:95], s62 offen\n"
    "v_mfma_f32_16x16x32_bf16 a[184:187], a[216:219], v[172:175], a[184:187]\n"
    "v_exp_f32 v227, v147\n"
    "v_mfma_f32_16x16x32_bf16 a[44:47], a[220:223], v[160:163], a[44:47]\n"
    "buffer_load_dwordx4 v[28:31], v240, s[28:31], s59 offen offset:3072\n"
    "v_mfma_f32_16x16x32_bf16 a[92:95], a[220:223], v[164:167], a[92:95]\n"
    "v_exp_f32 v228, v152\n"
    "v_mfma_f32_16x16x32_bf16 a[140:143], a[220:223], v[168:171], a[140:143]\n"
    "v_exp_f32 v229, v153\n"
    "v_mfma_f32_16x16x32_bf16 a[188:191], a[220:223], v[172:175], a[188:191]\n"
    "v_cvt_pk_bf16_f32 v180, v220, v221\n"
    "v_add_f32 v213, v220, v221\n"
    "v_exp_f32 v230, v154\n"
    "v_exp_f32 v231, v155\n"
    "v_cvt_pk_bf16_f32 v181, v222, v223\n"
    "v_add_f32 v213, v213, v222\n"
    "v_add_f32 v213, v213, v223\n"
    "v_cvt_pk_bf16_f32 v184, v224, v225\n"
    "v_exp_f32 v216, v132\n"
    "v_exp_f32 v217, v133\n"
    "v_add_f32 v214, v224, v225\n"
    "v_cvt_pk_bf16_f32 v185, v226, v227\n"
    "v_exp_f32 v218, v134\n"
    "v_exp_f32 v219, v135\n"
    "v_exp_f32 v220, v140\n"
    "v_add_f32 v214, v214, v226\n"
    "v_add_f32 v214, v214, v227\n"
    "v_cvt_pk_bf16_f32 v188, v228, v229\n"
    "v_add_f32 v215, v228, v229\n"
    "v_exp_f32 v221, v141\n"
    "v_exp_f32 v222, v142\n"
    "v_cvt_pk_bf16_f32 v189, v230, v231\n"
    "v_add_f32 v215, v215, v230\n"
    "v_exp_f32 v223, v143\n"
    "v_add_f32 v215, v215, v231\n"
    "v_cvt_pk_bf16_f32 v178, v216, v217\n"
    "v_add_f32 v212, v212, v216\n"
    "v_add_f32 v212, v212, v217\n"
    "v_exp_f32 v224, v148\n"
    "v_exp_f32 v225, v149\n"
    "v_cvt_pk_bf16_f32 v179, v218, v219\n"
    "v_add_f32 v212, v212, v218\n"
    "v_exp_f32 v226, v150\n"
    "v_exp_f32 v227, v151\n"
    "v_exp_f32 v228, v156\n"
    "v_add_f32 v212, v212, v219\n"
    "v_cvt_pk_bf16_f32 v182, v220, v221\n"
    "v_add_f32 v213, v213, v220\n"
    "v_add_f32 v213, v213, v221\n"
    "v_exp_f32 v229, v157\n"
    "v_exp_f32 v230, v158\n"
    "v_cvt_pk_bf16_f32 v183, v222, v223\n"
    "v_add_f32 v213, v213, v222\n"
    "v_exp_f32 v231, v159\n"
    "v_add_f32 v213, v213, v223\n"
    "v_cvt_pk_bf16_f32 v186, v224, v225\n"
    "v_add_f32 v214, v214, v224\n"
    "v_add_f32 v214, v214, v225\n"
    "v_cvt_pk_bf16_f32 v187, v226, v227\n"
    "v_add_f32 v214, v214, v226\n"
    "v_add_f32 v214, v214, v227\n"
    "v_cvt_pk_bf16_f32 v190, v228, v229\n"
    "v_add_f32 v215, v215, v228\n"
    "v_add_f32 v215, v215, v229\n"
    "v_cvt_pk_bf16_f32 v191, v230, v231\n"
    "v_add_f32 v215, v215, v230\n"
    "v_add_f32 v215, v215, v231\n"
    "v_max3_f32 v232, v212, v213, v214\n"
    "v_max_f32 v232, v232, v215\n"
    "v_cmp_nge_f32 vcc, s49, v232\n"
    "s_cbranch_vccnz LRARE_o_4_%=\n"
    "LRET_o_4_F0_%=:\n"
    "v_mfma_f32_16x16x32_bf16 a[0:3], a[224:227], v[176:179], a[0:3]\n"
    "v_add_f32 v208, v208, v212\n"
    "v_add_f32 v209, v209, v213\n"
    "v_add_f32 v210, v210, v214\n"
    "v_mfma_f32_16x16x32_bf16 a[4:7], a[228:231], v[176:179], a[4:7]\n"
    "v_add_f32 v211, v211, v215\n"
    "s_mul_i32 s62, s53, 6144\n"
    "v_mov_b32 v245, v208\n"
    "v_mov_b32 v247, v209\n"
    "v_mfma_f32_16x16x32_bf16 a[8:11], a[232:235], v[176:179], a[8:11]\n"
    "v_mov_b32 v249, v210\n"
    "v_mov_b32 v251, v211\n"
    "s_add_u32 s62, s62, s42\n"
    "v_permlane32_swap_b32 v208, v245\n"
    "v_mfma_f32_16x16x32_bf16 a[12:15], a[236:239], v[176:179], a[12:15]\n"
    "v_permlane32_swap_b32 v209, v247\n"
    "v_permlane32_swap_b32 v210, v249\n"
    "v_permlane32_swap_b32 v211, v251\n"
    "v_mfma_f32_16x16x32_bf16 a[16:19], a[240:243], v[176:179], a[16:19]\n"
    "s_add_u32 s62, s62, s56\n"
    "v_add_f32 v208, v208, v245\n"
    "v_add_f32 v209, v209, v247\n"
    "v_add_f32 v210, v210, v249\n"
    "v_mfma_f32_16x16x32_bf16 a[20:23], a[244:247], v[176:179], a[20:23]\n"
    "v_add_f32 v211, v211, v251\n"
    "s_add_u32 s62, s62, 73728\n"
    "v_mov_b32 v245, v208\n"
    "v_mov_b32 v247, v209\n"
    "v_mfma_f32_16x16x32_bf16 a[24:27], a[248:251], v[176:179], a[24:27]\n"
    "v_mov_b32 v249, v210\n"
    "v_mov_b32 v251, v211\n"
    "v_lshrrev_b32 v216, 7, v253\n"
    "v_permlane16_swap_b32 v208, v245\n"
    "v_mfma_f32_16x16x32_bf16 a[28:31], a[252:255], v[176:179], a[28:31]\n"
    "v_permlane16_swap_b32 v209, v247\n"
    "v_permlane16_swap_b32 v210, v249\n"
    "v_permlane16_swap_b32 v211, v251\n"
    "v_mfma_f32_16x16x32_bf16 a[32:35], a[192:195], v[176:179], a[32:35]\n"
    "v_and_b32 v217, 15, v216\n"
    "v_add_f32 v208, v208, v245\n"
    "v_add_f32 v209, v209, v247\n"
    "v_add_f32 v210, v210, v249\n"
    "v_mfma_f32_16x16x32_bf16 a[36:39], a[196:199], v[176:179], a[36:39]\n"
    "v_add_f32 v211, v211, v251\n"
    "v_mul_u32_u24 v217, 144, v217\n"
    "v_rcp_f32 v244, v208\n"
    "v_rcp_f32 v246, v209\n"
    "s_waitcnt lgkmcnt(0)\n"
    "v_mfma_f32_16x16x32_bf16 a[40:43], a[200:203], v[176:179], a[40:43]\n"
    "v_rcp_f32 v248, v210\n"
    "v_rcp_f32 v250, v211\n"
    "v_lshrrev_b32 v218, 4, v216\n"
    "v_mfma_f32_16x16x32_bf16 a[44:47], a[204:207], v[176:179], a[44:47]\n"
    "v_lshl_add_u32 v217, v218, 3, v217\n"
    "v_add_u32 v192, s62, v217\n"
    "v_add_u32 v193, 2304, v192\n"
    "v_lshrrev_b32 v217, 3, v216\n"
    "v_mul_u32_u24 v217, 144, v217\n"
    "v_and_b32 v218, 7, v216\n"
    "v_lshl_add_u32 v217, v218, 4, v217\n"
    "v_add_u32 v194, s62, v217\n"
    "s_add_u32 s80, s54, 0\n"
    "s_add_u32 s81, s54, 6144\n"
    "s_add_u32 s82, s54, 12288\n"
    "s_add_u32 s83, s54, 18432\n"
    "v_mfma_f32_16x16x32_bf16 a[48:51], a[224:227], v[180:183], a[48:51]\n"
    "v_accvgpr_read_b32 v96, a0\n"
    "v_accvgpr_read_b32 v97, a1\n"
    "buffer_load_dwordx4 v[32:35], v240, s[28:31], s59 offen offset:128\n"
    "v_accvgpr_read_b32 v98, a2\n"
    "v_accvgpr_read_b32 v99, a3\n"
    "v_accvgpr_read_b32 v100, a4\n"
    "v_mfma_f32_16x16x32_bf16 a[52:55], a[228:231], v[180:183], a[52:55]\n"
    "v_accvgpr_read_b32 v101, a5\n"
    "v_accvgpr_read_b32 v102, a6\n"
    "v_accvgpr_read_b32 v103, a7\n"
    "v_accvgpr_read_b32 v104, a8\n"
    "v_accvgpr_read_b32 v105, a9\n"
    "v_accvgpr_read_b32 v106, a10\n"
    "v_accvgpr_read_b32 v107, a11\n"
    "v_accvgpr_read_b32 v108, a12\n"
    "v_accvgpr_read_b32 v109, a13\n"
    "v_accvgpr_read_b32 v110, a14\n"
    "v_accvgpr_read_b32 v111, a15\n"
    "v_pk_mul_f32 v[96:97], v[96:97], v[244:245] op_sel_hi:[1,0]\n"
    "v_pk_mul_f32 v[98:99], v[98:99], v[244:245] op_sel_hi:[1,0]\n"
    "v_pk_mul_f32 v[100:101], v[100:101], v[244:245] op_sel_hi:[1,0]\n"
    "v_pk_mul_f32 v[102:103], v[102:103], v[244:245] op_sel_hi:[1,0]\n"
    "v_pk_mul_f32 v[104:105], v[104:105], v[244:245] op_sel_hi:[1,0]\n"
    "v_pk_mul_f32 v[106:107], v[106:107], v[244:245] op_sel_hi:[1,0]\n"
    "v_pk_mul_f32 v[108:109], v[108:109], v[244:245] op_sel_hi:[1,0]\n"
    "v_pk_mul_f32 v[110:111], v[110:111], v[244:245] op_sel_hi:[1,0]\n"
    "v_mfma_f32_16x16x32_bf16 a[56:59], a[232:235], v[180:183], a[56:59]\n"
    "v_cvt_pk_bf16_f32 v128, v96, v97\n"
    "v_cvt_pk_bf16_f32 v129, v98, v99\n"
    "v_cvt_pk_bf16_f32 v130, v100, v101\n"
    "v_cvt_pk_bf16_f32 v131, v102, v103\n"
    "v_mfma_f32_16x16x32_bf16 a[60:63], a[236:239], v[180:183], a[60:63]\n"
    "v_cvt_pk_bf16_f32 v132, v104, v105\n"
    "v_cvt_pk_bf16_f32 v133, v106, v107\n"
    "v_cvt_pk_bf16_f32 v134, v108, v109\n"
    "v_cvt_pk_bf16_f32 v135, v110, v111\n"
    "ds_write2_b64 v192, v[128:129], v[130:131] offset0:0 offset1:4\n"
    "ds_write2_b64 v192, v[132:133], v[134:135] offset0:8 offset1:12\n"
    "v_mfma_f32_16x16x32_bf16 a[64:67], a[240:243], v[180:183], a[64:67]\n"
    "v_accvgpr_read_b32 v112, a16\n"
    "v_accvgpr_read_b32 v113, a17\n"
    "buffer_load_dwordx4 v[36:39], v240, s[28:31], s59 offen offset:3200\n"
    "v_accvgpr_read_b32 v114, a18\n"
    "v_accvgpr_read_b32 v115, a19\n"
    "v_accvgpr_read_b32 v116, a20\n"
    "v_mfma_f32_16x16x32_bf16 a[68:71], a[244:247], v[180:183], a[68:71]\n"
    "v_accvgpr_read_b32 v117, a21\n"
    "v_accvgpr_read_b32 v118, a22\n"
    "v_accvgpr_read_b32 v119, a23\n"
    "v_accvgpr_read_b32 v120, a24\n"
    "v_accvgpr_read_b32 v121, a25\n"
    "v_accvgpr_read_b32 v122, a26\n"
    "v_accvgpr_read_b32 v123, a27\n"
    "v_accvgpr_read_b32 v124, a28\n"
    "v_accvgpr_read_b32 v125, a29\n"
    "v_accvgpr_read_b32 v126, a30\n"
    "v_accvgpr_read_b32 v127, a31\n"
    "v_pk_mul_f32 v[112:113], v[112:113], v[244:245] op_sel_hi:[1,0]\n"
    "v_pk_mul_f32 v[114:115], v[114:115], v[244:245] op_sel_hi:[1,0]\n"
    "v_pk_mul_f32 v[116:117], v[116:117], v[244:245] op_sel_hi:[1,0]\n"
    "v_pk_mul_f32 v[118:119], v[118:119], v[244:245] op_sel_hi:[1,0]\n"
    "v_pk_mul_f32 v[120:121], v[120:121], v[244:245] op_sel_hi:[1,0]\n"
    "v_pk_mul_f32 v[122:123], v[122:123], v[244:245] op_sel_hi:[1,0]\n"
    "v_pk_mul_f32 v[124:125], v[124:125], v[244:245] op_sel_hi:[1,0]\n"
    "v_pk_mul_f32 v[126:127], v[126:127], v[244:245] op_sel_hi:[1,0]\n"
    "v_mfma_f32_16x16x32_bf16 a[72:75], a[248:251], v[180:183], a[72:75]\n"
    "v_cvt_pk_bf16_f32 v136, v112, v113\n"
    "v_cvt_pk_bf16_f32 v137, v114, v115\n"
    "v_cvt_pk_bf16_f32 v138, v116, v117\n"
    "v_cvt_pk_bf16_f32 v139, v118, v119\n"
    "v_mfma_f32_16x16x32_bf16 a[76:79], a[252:255], v[180:183], a[76:79]\n"
    "v_cvt_pk_bf16_f32 v140, v120, v121\n"
    "v_cvt_pk_bf16_f32 v141, v122, v123\n"
    "v_cvt_pk_bf16_f32 v142, v124, v125\n"
    "v_cvt_pk_bf16_f32 v143, v126, v127\n"
    "ds_read_b128 v[144:147], v194 offset:0\n"
    "ds_read_b128 v[148:151], v194 offset:1152\n"
    "ds_write2_b64 v193, v[136:137], v[138:139] offset0:0 offset1:4\n"
    "ds_write2_b64 v193, v[140:141], v[142:143] offset0:8 offset1:12\n"
    "v_mfma_f32_16x16x32_bf16 a[80:83], a[192:195], v[180:183], a[80:83]\n"
    "v_accvgpr_read_b32 v96, a32\n"
    "v_accvgpr_read_b32 v97, a33\n"
    "buffer_load_dwordx4 v[40:43], v240, s[28:31], s59 offen offset:256\n"
    "v_accvgpr_read_b32 v98, a34\n"
    "v_accvgpr_read_b32 v99, a35\n"
    "v_accvgpr_read_b32 v100, a36\n"
    "v_mfma_f32_16x16x32_bf16 a[84:87], a[196:199], v[180:183], a[84:87]\n"
    "v_accvgpr_read_b32 v101, a37\n"
    "v_accvgpr_read_b32 v102, a38\n"
    "s_waitcnt lgkmcnt(2)\n"
    "buffer_store_dwordx4 v[144:147], v240, s[36:39], s80 offen offset:0\n"
    "buffer_store_dwordx4 v[148:151], v240, s[36:39], s80 offen offset:3072\n"
    "v_accvgpr_read_b32 v103, a39\n"
    "v_accvgpr_read_b32 v104, a40\n"
    "v_accvgpr_read_b32 v105, a41\n"
    "v_accvgpr_read_b32 v106, a42\n"
    "v_accvgpr_read_b32 v107, a43\n"
    "v_accvgpr_read_b32 v108, a44\n"
    "v_accvgpr_read_b32 v109, a45\n"
    "v_accvgpr_read_b32 v110, a46\n"
    "v_accvgpr_read_b32 v111, a47\n"
    "v_pk_mul_f32 v[96:97], v[96:97], v[244:245] op_sel_hi:[1,0]\n"
    "v_pk_mul_f32 v[98:99], v[98:99], v[244:245] op_sel_hi:[1,0]\n"
    "v_pk_mul_f32 v[100:101], v[100:101], v[244:245] op_sel_hi:[1,0]\n"
    "v_pk_mul_f32 v[102:103], v[102:103], v[244:245] op_sel_hi:[1,0]\n"
    "v_pk_mul_f32 v[104:105], v[104:105], v[244:245] op_sel_hi:[1,0]\n"
    "v_pk_mul_f32 v[106:107], v[106:107], v[244:245] op_sel_hi:[1,0]\n"
    "v_pk_mul_f32 v[108:109], v[108:109], v[244:245] op_sel_hi:[1,0]\n"
    "v_pk_mul_f32 v[110:111], v[110:111], v[244:245] op_sel_hi:[1,0]\n"
    "v_mfma_f32_16x16x32_bf16 a[88:91], a[200:203], v[180:183], a[88:91]\n"
    "v_cvt_pk_bf16_f32 v128, v96, v97\n"
    "v_cvt_pk_bf16_f32 v129, v98, v99\n"
    "v_cvt_pk_bf16_f32 v130, v100, v101\n"
    "v_cvt_pk_bf16_f32 v131, v102, v103\n"
    "v_mfma_f32_16x16x32_bf16 a[92:95], a[204:207], v[180:183], a[92:95]\n"
    "v_cvt_pk_bf16_f32 v132, v104, v105\n"
    "v_cvt_pk_bf16_f32 v133, v106, v107\n"
    "v_cvt_pk_bf16_f32 v134, v108, v109\n"
    "v_cvt_pk_bf16_f32 v135, v110, v111\n"
    "ds_read_b128 v[152:155], v194 offset:2304\n"
    "ds_read_b128 v[156:159], v194 offset:3456\n"
    "ds_write2_b64 v192, v[128:129], v[130:131] offset0:0 offset1:4\n"
    "ds_write2_b64 v192, v[132:133], v[134:135] offset0:8 offset1:12\n"
    "v_mfma_f32_16x16x32_bf16 a[96:99], a[224:227], v[184:187], a[96:99]\n"
    "v_accvgpr_read_b32 v112, a48\n"
    "v_accvgpr_read_b32 v113, a49\n"
    "buffer_load_dwordx4 v[44:47], v240, s[28:31], s59 offen offset:3328\n"
    "v_accvgpr_read_b32 v114, a50\n"
    "v_accvgpr_read_b32 v115, a51\n"
    "v_accvgpr_read_b32 v116, a52\n"
    "v_mfma_f32_16x16x32_bf16 a[100:103], a[228:231], v[184:187], a[100:103]\n"
    "v_accvgpr_read_b32 v117, a53\n"
    "v_accvgpr_read_b32 v118, a54\n"
    "s_waitcnt lgkmcnt(2)\n"
    "buffer_store_dwordx4 v[152:155], v240, s[36:39], s80 offen offset:128\n"
    "buffer_store_dwordx4 v[156:159], v240, s[36:39], s80 offen offset:3200\n"
    "v_accvgpr_read_b32 v119, a55\n"
    "v_accvgpr_read_b32 v120, a56\n"
    "v_accvgpr_read_b32 v121, a57\n"
    "v_accvgpr_read_b32 v122, a58\n"
    "v_accvgpr_read_b32 v123, a59\n"
    "v_accvgpr_read_b32 v124, a60\n"
    "v_accvgpr_read_b32 v125, a61\n"
    "v_accvgpr_read_b32 v126, a62\n"
    "v_accvgpr_read_b32 v127, a63\n"
    "v_pk_mul_f32 v[112:113], v[112:113], v[246:247] op_sel_hi:[1,0]\n"
    "v_pk_mul_f32 v[114:115], v[114:115], v[246:247] op_sel_hi:[1,0]\n"
    "v_pk_mul_f32 v[116:117], v[116:117], v[246:247] op_sel_hi:[1,0]\n"
    "v_pk_mul_f32 v[118:119], v[118:119], v[246:247] op_sel_hi:[1,0]\n"
    "v_pk_mul_f32 v[120:121], v[120:121], v[246:247] op_sel_hi:[1,0]\n"
    "v_pk_mul_f32 v[122:123], v[122:123], v[246:247] op_sel_hi:[1,0]\n"
    "v_pk_mul_f32 v[124:125], v[124:125], v[246:247] op_sel_hi:[1,0]\n"
    "v_pk_mul_f32 v[126:127], v[126:127], v[246:247] op_sel_hi:[1,0]\n"
    "v_mfma_f32_16x16x32_bf16 a[104:107], a[232:235], v[184:187], a[104:107]\n"
    "v_cvt_pk_bf16_f32 v136, v112, v113\n"
    "v_cvt_pk_bf16_f32 v137, v114, v115\n"
    "v_cvt_pk_bf16_f32 v138, v116, v117\n"
    "v_cvt_pk_bf16_f32 v139, v118, v119\n"
    "v_mfma_f32_16x16x32_bf16 a[108:111], a[236:239], v[184:187], a[108:111]\n"
    "v_cvt_pk_bf16_f32 v140, v120, v121\n"
    "v_cvt_pk_bf16_f32 v141, v122, v123\n"
    "v_cvt_pk_bf16_f32 v142, v124, v125\n"
    "v_cvt_pk_bf16_f32 v143, v126, v127\n"
    "ds_read_b128 v[216:219], v194 offset:0\n"
    "ds_read_b128 v[220:223], v194 offset:1152\n"
    "ds_write2_b64 v193, v[136:137], v[138:139] offset0:0 offset1:4\n"
    "ds_write2_b64 v193, v[140:141], v[142:143] offset0:8 offset1:12\n"
    "v_mfma_f32_16x16x32_bf16 a[112:115], a[240:243], v[184:187], a[112:115]\n"
    "v_accvgpr_read_b32 v96, a64\n"
    "v_accvgpr_read_b32 v97, a65\n"
    "buffer_load_dwordx4 v[48:51], v240, s[28:31], s60 offen offset:0\n"
    "v_accvgpr_read_b32 v98, a66\n"
    "v_accvgpr_read_b32 v99, a67\n"
    "v_accvgpr_read_b32 v100, a68\n"
    "v_mfma_f32_16x16x32_bf16 a[116:119], a[244:247], v[184:187], a[116:119]\n"
    "v_accvgpr_read_b32 v101, a69\n"
    "v_accvgpr_read_b32 v102, a70\n"
    "s_waitcnt lgkmcnt(2)\n"
    "buffer_store_dwordx4 v[216:219], v240, s[36:39], s80 offen offset:256\n"
    "buffer_store_dwordx4 v[220:223], v240, s[36:39], s80 offen offset:3328\n"
    "v_accvgpr_read_b32 v103, a71\n"
    "v_accvgpr_read_b32 v104, a72\n"
    "v_accvgpr_read_b32 v105, a73\n"
    "v_accvgpr_read_b32 v106, a74\n"
    "v_accvgpr_read_b32 v107, a75\n"
    "v_accvgpr_read_b32 v108, a76\n"
    "v_accvgpr_read_b32 v109, a77\n"
    "v_accvgpr_read_b32 v110, a78\n"
    "v_accvgpr_read_b32 v111, a79\n"
    "v_pk_mul_f32 v[96:97], v[96:97], v[246:247] op_sel_hi:[1,0]\n"
    "v_pk_mul_f32 v[98:99], v[98:99], v[246:247] op_sel_hi:[1,0]\n"
    "v_pk_mul_f32 v[100:101], v[100:101], v[246:247] op_sel_hi:[1,0]\n"
    "v_pk_mul_f32 v[102:103], v[102:103], v[246:247] op_sel_hi:[1,0]\n"
    "v_pk_mul_f32 v[104:105], v[104:105], v[246:247] op_sel_hi:[1,0]\n"
    "v_pk_mul_f32 v[106:107], v[106:107], v[246:247] op_sel_hi:[1,0]\n"
    "v_pk_mul_f32 v[108:109], v[108:109], v[246:247] op_sel_hi:[1,0]\n"
    "v_pk_mul_f32 v[110:111], v[110:111], v[246:247] op_sel_hi:[1,0]\n"
    "v_mfma_f32_16x16x32_bf16 a[120:123], a[248:251], v[184:187], a[120:123]\n"
    "v_cvt_pk_bf16_f32 v128, v96, v97\n"
    "v_cvt_pk_bf16_f32 v129, v98, v99\n"
    "v_cvt_pk_bf16_f32 v130, v100, v101\n"
    "v_cvt_pk_bf16_f32 v131, v102, v103\n"
    "v_mfma_f32_16x16x32_bf16 a[124:127], a[252:255], v[184:187], a[124:127]\n"
    "v_cvt_pk_bf16_f32 v132, v104, v105\n"
    "v_cvt_pk_bf16_f32 v133, v106, v107\n"
    "v_cvt_pk_bf16_f32 v134, v108, v109\n"
    "v_cvt_pk_bf16_f32 v135, v110, v111\n"
    "ds_read_b128 v[144:147], v194 offset:2304\n"
    "ds_read_b128 v[148:151], v194 offset:3456\n"
    "ds_write2_b64 v192, v[128:129], v[130:131] offset0:0 offset1:4\n"
    "ds_write2_b64 v192, v[132:133], v[134:135] offset0:8 offset1:12\n"
    "v_mfma_f32_16x16x32_bf16 a[128:131], a[192:195], v[184:187], a[128:131]\n"
    "v_accvgpr_read_b32 v112, a80\n"
    "v_accvgpr_read_b32 v113, a81\n"
    "buffer_load_dwordx4 v[52:55], v240, s[28:31], s60 offen offset:3072\n"
    "v_accvgpr_read_b32 v114, a82\n"
    "v_accvgpr_read_b32 v115, a83\n"
    "v_accvgpr_read_b32 v116, a84\n"
    "v_mfma_f32_16x16x32_bf16 a[132:135], a[196:199], v[184:187], a[132:135]\n"
    "v_accvgpr_read_b32 v117, a85\n"
    "v_accvgpr_read_b32 v118, a86\n"
    "s_waitcnt lgkmcnt(2)\n"
    "buffer_store_dwordx4 v[144:147], v240, s[36:39], s81 offen offset:0\n"
    "buffer_store_dwordx4 v[148:151], v240, s[36:39], s81 offen offset:3072\n"
    "v_accvgpr_read_b32 v119, a87\n"
    "v_accvgpr_read_b32 v120, a88\n"
    "v_accvgpr_read_b32 v121, a89\n"
    "v_accvgpr_read_b32 v122, a90\n"
    "v_accvgpr_read_b32 v123, a91\n"
    "v_accvgpr_read_b32 v124, a92\n"
    "v_accvgpr_read_b32 v125, a93\n"
    "v_accvgpr_read_b32 v126, a94\n"
    "v_accvgpr_read_b32 v127, a95\n"
    "v_pk_mul_f32 v[112:113], v[112:113], v[246:247] op_sel_hi:[1,0]\n"
    "v_pk_mul_f32 v[114:115], v[114:115], v[246:247] op_sel_hi:[1,0]\n"
    "v_pk_mul_f32 v[116:117], v[116:117], v[246:247] op_sel_hi:[1,0]\n"
    "v_pk_mul_f32 v[118:119], v[118:119], v[246:247] op_sel_hi:[1,0]\n"
    "v_pk_mul_f32 v[120:121], v[120:121], v[246:247] op_sel_hi:[1,0]\n"
    "v_pk_mul_f32 v[122:123], v[122:123], v[246:247] op_sel_hi:[1,0]\n"
    "v_pk_mul_f32 v[124:125], v[124:125], v[246:247] op_sel_hi:[1,0]\n"
    "v_pk_mul_f32 v[126:127], v[126:127], v[246:247] op_sel_hi:[1,0]\n"
    "v_mfma_f32_16x16x32_bf16 a[136:139], a[200:203], v[184:187], a[136:139]\n"
    "v_cvt_pk_bf16_f32 v136, v112, v113\n"
    "v_cvt_pk_bf16_f32 v137, v114, v115\n"
    "v_cvt_pk_bf16_f32 v138, v116, v117\n"
    "v_cvt_pk_bf16_f32 v139, v118, v119\n"
    "v_mfma_f32_16x16x32_bf16 a[140:143], a[204:207], v[184:187], a[140:143]\n"
    "v_cvt_pk_bf16_f32 v140, v120, v121\n"
    "v_cvt_pk_bf16_f32 v141, v122, v123\n"
    "v_cvt_pk_bf16_f32 v142, v124, v125\n"
    "v_cvt_pk_bf16_f32 v143, v126, v127\n"
    "ds_read_b128 v[152:155], v194 offset:0\n"
    "ds_read_b128 v[156:159], v194 offset:1152\n"
    "ds_write2_b64 v193, v[136:137], v[138:139] offset0:0 offset1:4\n"
    "ds_write2_b64 v193, v[140:141], v[142:143] offset0:8 offset1:12\n"
    "v_mfma_f32_16x16x32_bf16 a[144:147], a[224:227], v[188:191], a[144:147]\n"
    "v_accvgpr_read_b32 v96, a96\n"
    "v_accvgpr_read_b32 v97, a97\n"
    "buffer_load_dwordx4 v[56:59], v240, s[28:31], s60 offen offset:128\n"
    "v_accvgpr_read_b32 v98, a98\n"
    "v_accvgpr_read_b32 v99, a99\n"
    "v_accvgpr_read_b32 v100, a100\n"
    "v_mfma_f32_16x16x32_bf16 a[148:151], a[228:231], v[188:191], a[148:151]\n"
    "v_accvgpr_read_b32 v101, a101\n"
    "v_accvgpr_read_b32 v102, a102\n"
    "s_waitcnt lgkmcnt(2)\n"
    "buffer_store_dwordx4 v[152:155], v240, s[36:39], s81 offen offset:128\n"
    "buffer_store_dwordx4 v[156:159], v240, s[36:39], s81 offen offset:3200\n"
    "v_accvgpr_read_b32 v103, a103\n"
    "v_accvgpr_read_b32 v104, a104\n"
    "v_accvgpr_read_b32 v105, a105\n"
    "v_accvgpr_read_b32 v106, a106\n"
    "v_accvgpr_read_b32 v107, a107\n"
    "v_accvgpr_read_b32 v108, a108\n"
    "v_accvgpr_read_b32 v109, a109\n"
    "v_accvgpr_read_b32 v110, a110\n"
    "v_accvgpr_read_b32 v111, a111\n"
    "v_pk_mul_f32 v[96:97], v[96:97], v[248:249] op_sel_hi:[1,0]\n"
    "v_pk_mul_f32 v[98:99], v[98:99], v[248:249] op_sel_hi:[1,0]\n"
    "v_pk_mul_f32 v[100:101], v[100:101], v[248:249] op_sel_hi:[1,0]\n"
    "v_pk_mul_f32 v[102:103], v[102:103], v[248:249] op_sel_hi:[1,0]\n"
    "v_pk_mul_f32 v[104:105], v[104:105], v[248:249] op_sel_hi:[1,0]\n"
    "v_pk_mul_f32 v[106:107], v[106:107], v[248:249] op_sel_hi:[1,0]\n"
    "v_pk_mul_f32 v[108:109], v[108:109], v[248:249] op_sel_hi:[1,0]\n"
    "v_pk_mul_f32 v[110:111], v[110:111], v[248:249] op_sel_hi:[1,0]\n"
    "v_mfma_f32_16x16x32_bf16 a[152:155], a[232:235], v[188:191], a[152:155]\n"
    "v_cvt_pk_bf16_f32 v128, v96, v97\n"
    "v_cvt_pk_bf16_f32 v129, v98, v99\n"
    "v_cvt_pk_bf16_f32 v130, v100, v101\n"
    "v_cvt_pk_bf16_f32 v131, v102, v103\n"
    "v_mfma_f32_16x16x32_bf16 a[156:159], a[236:239], v[188:191], a[156:159]\n"
    "v_cvt_pk_bf16_f32 v132, v104, v105\n"
    "v_cvt_pk_bf16_f32 v133, v106, v107\n"
    "v_cvt_pk_bf16_f32 v134, v108, v109\n"
    "v_cvt_pk_bf16_f32 v135, v110, v111\n"
    "ds_read_b128 v[216:219], v194 offset:2304\n"
    "ds_read_b128 v[220:223], v194 offset:3456\n"
    "ds_write2_b64 v192, v[128:129], v[130:131] offset0:0 offset1:4\n"
    "ds_write2_b64 v192, v[132:133], v[134:135] offset0:8 offset1:12\n"
    "v_mfma_f32_16x16x32_bf16 a[160:163], a[240:243], v[188:191], a[160:163]\n"
    "v_accvgpr_read_b32 v112, a112\n"
    "v_accvgpr_read_b32 v113, a113\n"
    "buffer_load_dwordx4 v[60:63], v240, s[28:31], s60 offen offset:3200\n"
    "v_accvgpr_read_b32 v114, a114\n"
    "v_accvgpr_read_b32 v115, a115\n"
    "v_accvgpr_read_b32 v116, a116\n"
    "v_mfma_f32_16x16x32_bf16 a[164:167], a[244:247], v[188:191], a[164:167]\n"
    "v_accvgpr_read_b32 v117, a117\n"
    "v_accvgpr_read_b32 v118, a118\n"
    "s_waitcnt lgkmcnt(2)\n"
    "buffer_store_dwordx4 v[216:219], v240, s[36:39], s81 offen offset:256\n"
    "buffer_store_dwordx4 v[220:223], v240, s[36:39], s81 offen offset:3328\n"
    "v_accvgpr_read_b32 v119, a119\n"
    "v_accvgpr_read_b32 v120, a120\n"
    "v_accvgpr_read_b32 v121, a121\n"
    "v_accvgpr_read_b32 v122, a122\n"
    "v_accvgpr_read_b32 v123, a123\n"
    "v_accvgpr_read_b32 v124, a124\n"
    "v_accvgpr_read_b32 v125, a125\n"
    "v_accvgpr_read_b32 v126, a126\n"
    "v_accvgpr_read_b32 v127, a127\n"
    "v_pk_mul_f32 v[112:113], v[112:113], v[248:249] op_sel_hi:[1,0]\n"
    "v_pk_mul_f32 v[114:115], v[114:115], v[248:249] op_sel_hi:[1,0]\n"
    "v_pk_mul_f32 v[116:117], v[116:117], v[248:249] op_sel_hi:[1,0]\n"
    "v_pk_mul_f32 v[118:119], v[118:119], v[248:249] op_sel_hi:[1,0]\n"
    "v_pk_mul_f32 v[120:121], v[120:121], v[248:249] op_sel_hi:[1,0]\n"
    "v_pk_mul_f32 v[122:123], v[122:123], v[248:249] op_sel_hi:[1,0]\n"
    "v_pk_mul_f32 v[124:125], v[124:125], v[248:249] op_sel_hi:[1,0]\n"
    "v_pk_mul_f32 v[126:127], v[126:127], v[248:249] op_sel_hi:[1,0]\n"
    "v_mfma_f32_16x16x32_bf16 a[168:171], a[248:251], v[188:191], a[168:171]\n"
    "v_cvt_pk_bf16_f32 v136, v112, v113\n"
    "v_cvt_pk_bf16_f32 v137, v114, v115\n"
    "v_cvt_pk_bf16_f32 v138, v116, v117\n"
    "v_cvt_pk_bf16_f32 v139, v118, v119\n"
    "v_mfma_f32_16x16x32_bf16 a[172:175], a[252:255], v[188:191], a[172:175]\n"
    "v_cvt_pk_bf16_f32 v140, v120, v121\n"
    "v_cvt_pk_bf16_f32 v141, v122, v123\n"
    "v_cvt_pk_bf16_f32 v142, v124, v125\n"
    "v_cvt_pk_bf16_f32 v143, v126, v127\n"
    "ds_read_b128 v[144:147], v194 offset:0\n"
    "ds_read_b128 v[148:151], v194 offset:1152\n"
    "ds_write2_b64 v193, v[136:137], v[138:139] offset0:0 offset1:4\n"
    "ds_write2_b64 v193, v[140:141], v[142:143] offset0:8 offset1:12\n"
    "v_mfma_f32_16x16x32_bf16 a[176:179], a[192:195], v[188:191], a[176:179]\n"
    "v_accvgpr_read_b32 v96, a128\n"
    "v_accvgpr_read_b32 v97, a129\n"
    "buffer_load_dwordx4 v[64:67], v240, s[28:31], s60 offen offset:256\n"
    "v_accvgpr_read_b32 v98, a130\n"
    "v_accvgpr_read_b32 v99, a131\n"
    "v_accvgpr_read_b32 v100, a132\n"
    "v_mfma_f32_16x16x32_bf16 a[180:183], a[196:199], v[188:191], a[180:183]\n"
    "v_accvgpr_read_b32 v101, a133\n"
    "v_accvgpr_read_b32 v102, a134\n"
    "s_waitcnt lgkmcnt(2)\n"
    "buffer_store_dwordx4 v[144:147], v240, s[36:39], s82 offen offset:0\n"
    "buffer_store_dwordx4 v[148:151], v240, s[36:39], s82 offen offset:3072\n"
    "v_accvgpr_read_b32 v103, a135\n"
    "v_accvgpr_read_b32 v104, a136\n"
    "v_accvgpr_read_b32 v105, a137\n"
    "v_accvgpr_read_b32 v106, a138\n"
    "v_accvgpr_read_b32 v107, a139\n"
    "v_accvgpr_read_b32 v108, a140\n"
    "v_accvgpr_read_b32 v109, a141\n"
    "v_accvgpr_read_b32 v110, a142\n"
    "v_accvgpr_read_b32 v111, a143\n"
    "v_pk_mul_f32 v[96:97], v[96:97], v[248:249] op_sel_hi:[1,0]\n"
    "v_pk_mul_f32 v[98:99], v[98:99], v[248:249] op_sel_hi:[1,0]\n"
    "v_pk_mul_f32 v[100:101], v[100:101], v[248:249] op_sel_hi:[1,0]\n"
    "v_pk_mul_f32 v[102:103], v[102:103], v[248:249] op_sel_hi:[1,0]\n"
    "v_pk_mul_f32 v[104:105], v[104:105], v[248:249] op_sel_hi:[1,0]\n"
    "v_pk_mul_f32 v[106:107], v[106:107], v[248:249] op_sel_hi:[1,0]\n"
    "v_pk_mul_f32 v[108:109], v[108:109], v[248:249] op_sel_hi:[1,0]\n"
    "v_pk_mul_f32 v[110:111], v[110:111], v[248:249] op_sel_hi:[1,0]\n"
    "v_mfma_f32_16x16x32_bf16 a[184:187], a[200:203], v[188:191], a[184:187]\n"
    "v_cvt_pk_bf16_f32 v128, v96, v97\n"
    "v_cvt_pk_bf16_f32 v129, v98, v99\n"
    "v_cvt_pk_bf16_f32 v130, v100, v101\n"
    "v_cvt_pk_bf16_f32 v131, v102, v103\n"
    "v_mfma_f32_16x16x32_bf16 a[188:191], a[204:207], v[188:191], a[188:191]\n"
    "v_cvt_pk_bf16_f32 v132, v104, v105\n"
    "v_cvt_pk_bf16_f32 v133, v106, v107\n"
    "v_cvt_pk_bf16_f32 v134, v108, v109\n"
    "v_cvt_pk_bf16_f32 v135, v110, v111\n"
    "ds_read_b128 v[152:155], v194 offset:2304\n"
    "ds_read_b128 v[156:159], v194 offset:3456\n"
    "ds_write2_b64 v192, v[128:129], v[130:131] offset0:0 offset1:4\n"
    "ds_write2_b64 v192, v[132:133], v[134:135] offset0:8 offset1:12\n"
    "v_accvgpr_read_b32 v112, a144\n"
    "v_accvgpr_read_b32 v113, a145\n"
    "v_accvgpr_read_b32 v114, a146\n"
    "buffer_load_dwordx4 v[68:71], v240, s[28:31], s60 offen offset:3328\n"
    "v_accvgpr_read_b32 v115, a147\n"
    "v_accvgpr_read_b32 v116, a148\n"
    "v_accvgpr_read_b32 v117, a149\n"
    "v_accvgpr_read_b32 v118, a150\n"
    "v_accvgpr_read_b32 v119, a151\n"
    "v_accvgpr_read_b32 v120, a152\n"
    "s_waitcnt lgkmcnt(2)\n"
    "buffer_store_dwordx4 v[152:155], v240, s[36:39], s82 offen offset:128\n"
    "buffer_store_dwordx4 v[156:159], v240, s[36:39], s82 offen offset:3200\n"
    "v_accvgpr_read_b32 v121, a153\n"
    "v_accvgpr_read_b32 v122, a154\n"
    "v_accvgpr_read_b32 v123, a155\n"
    "v_accvgpr_read_b32 v124, a156\n"
    "v_accvgpr_read_b32 v125, a157\n"
    "v_accvgpr_read_b32 v126, a158\n"
    "v_accvgpr_read_b32 v127, a159\n"
    "v_pk_mul_f32 v[112:113], v[112:113], v[250:251] op_sel_hi:[1,0]\n"
    "v_pk_mul_f32 v[114:115], v[114:115], v[250:251] op_sel_hi:[1,0]\n"
    "v_pk_mul_f32 v[116:117], v[116:117], v[250:251] op_sel_hi:[1,0]\n"
    "v_pk_mul_f32 v[118:119], v[118:119], v[250:251] op_sel_hi:[1,0]\n"
    "v_pk_mul_f32 v[120:121], v[120:121], v[250:251] op_sel_hi:[1,0]\n"
    "v_pk_mul_f32 v[122:123], v[122:123], v[250:251] op_sel_hi:[1,0]\n"
    "v_pk_mul_f32 v[124:125], v[124:125], v[250:251] op_sel_hi:[1,0]\n"
    "v_pk_mul_f32 v[126:127], v[126:127], v[250:251] op_sel_hi:[1,0]\n"
    "v_cvt_pk_bf16_f32 v136, v112, v113\n"
    "v_cvt_pk_bf16_f32 v137, v114, v115\n"
    "v_cvt_pk_bf16_f32 v138, v116, v117\n"
    "v_cvt_pk_bf16_f32 v139, v118, v119\n"
    "v_cvt_pk_bf16_f32 v140, v120, v121\n"
    "v_cvt_pk_bf16_f32 v141, v122, v123\n"
    "v_cvt_pk_bf16_f32 v142, v124, v125\n"
    "v_cvt_pk_bf16_f32 v143, v126, v127\n"
    "ds_read_b128 v[216:219], v194 offset:0\n"
    "ds_read_b128 v[220:223], v194 offset:1152\n"
    "ds_write2_b64 v193, v[136:137], v[138:139] offset0:0 offset1:4\n"
    "ds_write2_b64 v193, v[140:141], v[142:143] offset0:8 offset1:12\n"
    "v_accvgpr_read_b32 v96, a160\n"
    "v_accvgpr_read_b32 v97, a161\n"
    "v_accvgpr_read_b32 v98, a162\n"
    "v_accvgpr_read_b32 v99, a163\n"
    "v_accvgpr_read_b32 v100, a164\n"
    "v_accvgpr_read_b32 v101, a165\n"
    "v_accvgpr_read_b32 v102, a166\n"
    "v_accvgpr_read_b32 v103, a167\n"
    "v_accvgpr_read_b32 v104, a168\n"
    "s_waitcnt lgkmcnt(2)\n"
    "buffer_store_dwordx4 v[216:219], v240, s[36:39], s82 offen offset:256\n"
    "buffer_store_dwordx4 v[220:223], v240, s[36:39], s82 offen offset:3328\n"
    "v_accvgpr_read_b32 v105, a169\n"
    "v_accvgpr_read_b32 v106, a170\n"
    "v_accvgpr_read_b32 v107, a171\n"
    "v_accvgpr_read_b32 v108, a172\n"
    "v_accvgpr_read_b32 v109, a173\n"
    "v_accvgpr_read_b32 v110, a174\n"
    "v_accvgpr_read_b32 v111, a175\n"
    "v_pk_mul_f32 v[96:97], v[96:97], v[250:251] op_sel_hi:[1,0]\n"
    "v_pk_mul_f32 v[98:99], v[98:99], v[250:251] op_sel_hi:[1,0]\n"
    "v_pk_mul_f32 v[100:101], v[100:101], v[250:251] op_sel_hi:[1,0]\n"
    "v_pk_mul_f32 v[102:103], v[102:103], v[250:251] op_sel_hi:[1,0]\n"
    "v_pk_mul_f32 v[104:105], v[104:105], v[250:251] op_sel_hi:[1,0]\n"
    "v_pk_mul_f32 v[106:107], v[106:107], v[250:251] op_sel_hi:[1,0]\n"
    "v_pk_mul_f32 v[108:109], v[108:109], v[250:251] op_sel_hi:[1,0]\n"
    "v_pk_mul_f32 v[110:111], v[110:111], v[250:251] op_sel_hi:[1,0]\n"
    "v_cvt_pk_bf16_f32 v128, v96, v97\n"
    "v_cvt_pk_bf16_f32 v129, v98, v99\n"
    "v_cvt_pk_bf16_f32 v130, v100, v101\n"
    "v_cvt_pk_bf16_f32 v131, v102, v103\n"
    "v_cvt_pk_bf16_f32 v132, v104, v105\n"
    "v_cvt_pk_bf16_f32 v133, v106, v107\n"
    "v_cvt_pk_bf16_f32 v134, v108, v109\n"
    "v_cvt_pk_bf16_f32 v135, v110, v111\n"
    "ds_read_b128 v[144:147], v194 offset:2304\n"
    "ds_read_b128 v[148:151], v194 offset:3456\n"
    "ds_write2_b64 v192, v[128:129], v[130:131] offset0:0 offset1:4\n"
    "ds_write2_b64 v192, v[132:133], v[134:135] offset0:8 offset1:12\n"
    "v_accvgpr_read_b32 v112, a176\n"
    "v_accvgpr_read_b32 v113, a177\n"
    "v_accvgpr_read_b32 v114, a178\n"
    "v_accvgpr_read_b32 v115, a179\n"
    "v_accvgpr_read_b32 v116, a180\n"
    "v_accvgpr_read_b32 v117, a181\n"
    "v_accvgpr_read_b32 v118, a182\n"
    "v_accvgpr_read_b32 v119, a183\n"
    "v_accvgpr_read_b32 v120, a184\n"
    "s_waitcnt lgkmcnt(2)\n"
    "buffer_store_dwordx4 v[144:147], v240, s[36:39], s83 offen offset:0\n"
    "buffer_store_dwordx4 v[148:151], v240, s[36:39], s83 offen offset:3072\n"
    "v_accvgpr_read_b32 v121, a185\n"
    "v_accvgpr_read_b32 v122, a186\n"
    "v_accvgpr_read_b32 v123, a187\n"
    "v_accvgpr_read_b32 v124, a188\n"
    "v_accvgpr_read_b32 v125, a189\n"
    "v_accvgpr_read_b32 v126, a190\n"
    "v_accvgpr_read_b32 v127, a191\n"
    "v_pk_mul_f32 v[112:113], v[112:113], v[250:251] op_sel_hi:[1,0]\n"
    "v_pk_mul_f32 v[114:115], v[114:115], v[250:251] op_sel_hi:[1,0]\n"
    "v_pk_mul_f32 v[116:117], v[116:117], v[250:251] op_sel_hi:[1,0]\n"
    "v_pk_mul_f32 v[118:119], v[118:119], v[250:251] op_sel_hi:[1,0]\n"
    "v_pk_mul_f32 v[120:121], v[120:121], v[250:251] op_sel_hi:[1,0]\n"
    "v_pk_mul_f32 v[122:123], v[122:123], v[250:251] op_sel_hi:[1,0]\n"
    "v_pk_mul_f32 v[124:125], v[124:125], v[250:251] op_sel_hi:[1,0]\n"
    "v_pk_mul_f32 v[126:127], v[126:127], v[250:251] op_sel_hi:[1,0]\n"
    "v_cvt_pk_bf16_f32 v136, v112, v113\n"
    "v_cvt_pk_bf16_f32 v137, v114, v115\n"
    "v_cvt_pk_bf16_f32 v138, v116, v117\n"
    "v_cvt_pk_bf16_f32 v139, v118, v119\n"
    "v_cvt_pk_bf16_f32 v140, v120, v121\n"
    "v_cvt_pk_bf16_f32 v141, v122, v123\n"
    "v_cvt_pk_bf16_f32 v142, v124, v125\n"
    "v_cvt_pk_bf16_f32 v143, v126, v127\n"
    "ds_read_b128 v[152:155], v194 offset:0\n"
    "ds_read_b128 v[156:159], v194 offset:1152\n"
    "ds_write2_b64 v193, v[136:137], v[138:139] offset0:0 offset1:4\n"
    "ds_write2_b64 v193, v[140:141], v[142:143] offset0:8 offset1:12\n"
    "s_waitcnt lgkmcnt(2)\n"
    "buffer_store_dwordx4 v[152:155], v240, s[36:39], s83 offen offset:128\n"
    "buffer_store_dwordx4 v[156:159], v240, s[36:39], s83 offen offset:3200\n"
    "ds_read_b128 v[216:219], v194 offset:2304\n"
    "ds_read_b128 v[220:223], v194 offset:3456\n"
    "s_waitcnt lgkmcnt(0)\n"
    "buffer_store_dwordx4 v[216:219], v240, s[36:39], s83 offen offset:256\n"
    "buffer_store_dwordx4 v[220:223], v240, s[36:39], s83 offen offset:3328\n"
    "s_branch LNEXT_%=\n"
    "LFIN1_4_%=:\n"
    "s_mov_b32 s57, 2\n"
    "s_waitcnt lgkmcnt(4)\n"
    "v_mfma_f32_16x16x32_bf16 a[0:3], a[240:243], v[160:163], a[0:3]\n"
    "ds_read_b64_tr_b16 a[224:225], v236 offset:12288\n"
    "v_add_f32 v208, v208, v212\n"
    "v_mfma_f32_16x16x32_bf16 a[48:51], a[240:243], v[164:167], a[48:51]\n"
    "ds_read_b64_tr_b16 a[226:227], v236 offset:18432\n"
    "v_add_f32 v209, v209, v213\n"
    "v_mfma_f32_16x16x32_bf16 a[96:99], a[240:243], v[168:171], a[96:99]\n"
    "v_exp_f32 v216, v128\n"
    "v_mfma_f32_16x16x32_bf16 a[144:147], a[240:243], v[172:175], a[144:147]\n"
    "ds_read_b64_tr_b16 a[228:229], v241 offset:12288\n"
    "v_add_f32 v210, v210, v214\n"
    "v_mfma_f32_16x16x32_bf16 a[4:7], a[244:247], v[160:163], a[4:7]\n"
    "ds_read_b64_tr_b16 a[230:231], v241 offset:18432\n"
    "v_add_f32 v211, v211, v215\n"
    "v_mfma_f32_16x16x32_bf16 a[52:55], a[244:247], v[164:167], a[52:55]\n"
    "v_exp_f32 v217, v129\n"
    "v_mfma_f32_16x16x32_bf16 a[100:103], a[244:247], v[168:171], a[100:103]\n"
    "ds_read_b64_tr_b16 a[232:233], v236 offset:12800\n"
    "v_cvt_pk_bf16_f32 v176, v216, v217\n"
    "v_mfma_f32_16x16x32_bf16 a[148:151], a[244:247], v[172:175], a[148:151]\n"
    "ds_read_b64_tr_b16 a[234:235], v236 offset:18944\n"
    "v_add_f32 v212, v216, v217\n"
    "v_mfma_f32_16x16x32_bf16 a[8:11], a[248:251], v[160:163], a[8:11]\n"
    "v_exp_f32 v218, v130\n"
    "v_mfma_f32_16x16x32_bf16 a[56:59], a[248:251], v[164:167], a[56:59]\n"
    "ds_read_b64_tr_b16 a[236:237], v241 offset:12800\n"
    "v_mfma_f32_16x16x32_bf16 a[104:107], a[248:251], v[168:171], a[104:107]\n"
    "ds_read_b64_tr_b16 a[238:239], v241 offset:18944\n"
    "v_mfma_f32_16x16x32_bf16 a[152:155], a[248:251], v[172:175], a[152:155]\n"
    "v_exp_f32 v219, v131\n"
    "v_mfma_f32_16x16x32_bf16 a[12:15], a[252:255], v[160:163], a[12:15]\n"
    "ds_read_b64_tr_b16 a[240:241], v236 offset:13312\n"
    "v_cvt_pk_bf16_f32 v177, v218, v219\n"
    "v_mfma_f32_16x16x32_bf16 a[60:63], a[252:255], v[164:167], a[60:63]\n"
    "ds_read_b64_tr_b16 a[242:243], v236 offset:19456\n"
    "v_add_f32 v212, v212, v218\n"
    "v_mfma_f32_16x16x32_bf16 a[108:111], a[252:255], v[168:171], a[108:111]\n"
    "v_exp_f32 v220, v136\n"
    "v_mfma_f32_16x16x32_bf16 a[156:159], a[252:255], v[172:175], a[156:159]\n"
    "ds_read_b64_tr_b16 a[244:245], v241 offset:13312\n"
    "v_add_f32 v212, v212, v219\n"
    "v_mfma_f32_16x16x32_bf16 a[16:19], a[192:195], v[160:163], a[16:19]\n"
    "ds_read_b64_tr_b16 a[246:247], v241 offset:19456\n"
    "v_mfma_f32_16x16x32_bf16 a[64:67], a[192:195], v[164:167], a[64:67]\n"
    "v_exp_f32 v221, v137\n"
    "v_mfma_f32_16x16x32_bf16 a[112:115], a[192:195], v[168:171], a[112:115]\n"
    "ds_read_b64_tr_b16 a[248:249], v236 offset:13824\n"
    "v_cvt_pk_bf16_f32 v180, v220, v221\n"
    "v_mfma_f32_16x16x32_bf16 a[160:163], a[192:195], v[172:175], a[160:163]\n"
    "ds_read_b64_tr_b16 a[250:251], v236 offset:19968\n"
    "v_add_f32 v213, v220, v221\n"
    "v_mfma_f32_16x16x32_bf16 a[20:23], a[196:199], v[160:163], a[20:23]\n"
    "v_exp_f32 v222, v138\n"
    "v_mfma_f32_16x16x32_bf16 a[68:71], a[196:199], v[164:167], a[68:71]\n"
    "ds_read_b64_tr_b16 a[252:253], v241 offset:13824\n"
    "v_mfma_f32_16x16x32_bf16 a[116:119], a[196:199], v[168:171], a[116:119]\n"
    "ds_read_b64_tr_b16 a[254:255], v241 offset:19968\n"
    "v_mfma_f32_16x16x32_bf16 a[164:167], a[196:199], v[172:175], a[164:167]\n"
    "v_exp_f32 v223, v139\n"
    "v_mfma_f32_16x16x32_bf16 a[24:27], a[200:203], v[160:163], a[24:27]\n"
    "ds_read_b64_tr_b16 a[192:193], v236 offset:14336\n"
    "v_cvt_pk_bf16_f32 v181, v222, v223\n"
    "v_mfma_f32_16x16x32_bf16 a[72:75], a[200:203], v[164:167], a[72:75]\n"
    "ds_read_b64_tr_b16 a[194:195], v236 offset:20480\n"
    "v_add_f32 v213, v213, v222\n"
    "v_mfma_f32_16x16x32_bf16 a[120:123], a[200:203], v[168:171], a[120:123]\n"
    "v_exp_f32 v224, v144\n"
    "v_mfma_f32_16x16x32_bf16 a[168:171], a[200:203], v[172:175], a[168:171]\n"
    "ds_read_b64_tr_b16 a[196:197], v241 offset:14336\n"
    "v_add_f32 v213, v213, v223\n"
    "v_mfma_f32_16x16x32_bf16 a[28:31], a[204:207], v[160:163], a[28:31]\n"
    "ds_read_b64_tr_b16 a[198:199], v241 offset:20480\n"
    "v_mfma_f32_16x16x32_bf16 a[76:79], a[204:207], v[164:167], a[76:79]\n"
    "v_exp_f32 v225, v145\n"
    "v_mfma_f32_16x16x32_bf16 a[124:127], a[204:207], v[168:171], a[124:127]\n"
    "ds_read_b64_tr_b16 a[200:201], v236 offset:14848\n"
    "v_cvt_pk_bf16_f32 v184, v224, v225\n"
    "v_mfma_f32_16x16x32_bf16 a[172:175], a[204:207], v[172:175], a[172:175]\n"
    "ds_read_b64_tr_b16 a[202:203], v236 offset:20992\n"
    "v_add_f32 v214, v224, v225\n"
    "v_mfma_f32_16x16x32_bf16 a[32:35], a[208:211], v[160:163], a[32:35]\n"
    "v_exp_f32 v226, v146\n"
    "v_mfma_f32_16x16x32_bf16 a[80:83], a[208:211], v[164:167], a[80:83]\n"
    "ds_read_b64_tr_b16 a[204:205], v241 offset:14848\n"
    "v_mfma_f32_16x16x32_bf16 a[128:131], a[208:211], v[168:171], a[128:131]\n"
    "ds_read_b64_tr_b16 a[206:207], v241 offset:20992\n"
    "v_mfma_f32_16x16x32_bf16 a[176:179], a[208:211], v[172:175], a[176:179]\n"
    "v_exp_f32 v227, v147\n"
    "v_mfma_f32_16x16x32_bf16 a[36:39], a[212:215], v[160:163], a[36:39]\n"
    "v_exp_f32 v228, v152\n"
    "v_mfma_f32_16x16x32_bf16 a[84:87], a[212:215], v[164:167], a[84:87]\n"
    "v_exp_f32 v229, v153\n"
    "v_mfma_f32_16x16x32_bf16 a[132:135], a[212:215], v[168:171], a[132:135]\n"
    "v_exp_f32 v230, v154\n"
    "v_mfma_f32_16x16x32_bf16 a[180:183], a[212:215], v[172:175], a[180:183]\n"
    "v_exp_f32 v231, v155\n"
    "s_waitcnt lgkmcnt(4)\n"
    "v_mfma_f32_16x16x32_bf16 a[40:43], a[216:219], v[160:163], a[40:43]\n"
    "v_exp_f32 v216, v132\n"
    "v_mfma_f32_16x16x32_bf16 a[88:91], a[216:219], v[164:167], a[88:91]\n"
    "v_exp_f32 v217, v133\n"
    "v_mfma_f32_16x16x32_bf16 a[136:139], a[216:219], v[168:171], a[136:139]\n"
    "v_cvt_pk_bf16_f32 v185, v226, v227\n"
    "v_add_f32 v214, v214, v226\n"
    "v_mfma_f32_16x16x32_bf16 a[184:187], a[216:219], v[172:175], a[184:187]\n"
    "v_exp_f32 v218, v134\n"
    "v_mfma_f32_16x16x32_bf16 a[44:47], a[220:223], v[160:163], a[44:47]\n"
    "v_exp_f32 v219, v135\n"
    "v_mfma_f32_16x16x32_bf16 a[92:95], a[220:223], v[164:167], a[92:95]\n"
    "v_add_f32 v214, v214, v227\n"
    "v_cvt_pk_bf16_f32 v188, v228, v229\n"
    "v_mfma_f32_16x16x32_bf16 a[140:143], a[220:223], v[168:171], a[140:143]\n"
    "v_exp_f32 v220, v140\n"
    "v_mfma_f32_16x16x32_bf16 a[188:191], a[220:223], v[172:175], a[188:191]\n"
    "v_exp_f32 v221, v141\n"
    "v_add_f32 v215, v228, v229\n"
    "v_cvt_pk_bf16_f32 v189, v230, v231\n"
    "v_add_f32 v215, v215, v230\n"
    "v_add_f32 v215, v215, v231\n"
    "v_exp_f32 v222, v142\n"
    "v_exp_f32 v223, v143\n"
    "v_cvt_pk_bf16_f32 v178, v216, v217\n"
    "v_add_f32 v212, v212, v216\n"
    "v_exp_f32 v224, v148\n"
    "v_exp_f32 v225, v149\n"
    "v_exp_f32 v226, v150\n"
    "v_add_f32 v212, v212, v217\n"
    "v_cvt_pk_bf16_f32 v179, v218, v219\n"
    "v_add_f32 v212, v212, v218\n"
    "v_add_f32 v212, v212, v219\n"
    "v_exp_f32 v227, v151\n"
    "v_exp_f32 v228, v156\n"
    "v_cvt_pk_bf16_f32 v182, v220, v221\n"
    "v_add_f32 v213, v213, v220\n"
    "v_exp_f32 v229, v157\n"
    "v_add_f32 v213, v213, v221\n"
    "v_cvt_pk_bf16_f32 v183, v222, v223\n"
    "v_add_f32 v213, v213, v222\n"
    "v_add_f32 v213, v213, v223\n"
    "v_exp_f32 v230, v158\n"
    "v_exp_f32 v231, v159\n"
    "v_cvt_pk_bf16_f32 v186, v224, v225\n"
    "v_add_f32 v214, v214, v224\n"
    "v_add_f32 v214, v214, v225\n"
    "v_cvt_pk_bf16_f32 v187, v226, v227\n"
    "v_add_f32 v214, v214, v226\n"
    "v_add_f32 v214, v214, v227\n"
    "v_cvt_pk_bf16_f32 v190, v228, v229\n"
    "v_add_f32 v215, v215, v228\n"
    "v_add_f32 v215, v215, v229\n"
    "v_cvt_pk_bf16_f32 v191, v230, v231\n"
    "v_add_f32 v215, v215, v230\n"
    "v_add_f32 v215, v215, v231\n"
    "v_max3_f32 v232, v212, v213, v214\n"
    "v_max_f32 v232, v232, v215\n"
    "v_cmp_nge_f32 vcc, s49, v232\n"
    "s_cbranch_vccnz LRARE_o_4_%=\n"
    "LRET_o_4_F1_%=:\n"
    "v_mfma_f32_16x16x32_bf16 a[0:3], a[224:227], v[176:179], a[0:3]\n"
    "v_add_f32 v208, v208, v212\n"
    "v_add_f32 v209, v209, v213\n"
    "v_add_f32 v210, v210, v214\n"
    "v_mfma_f32_16x16x32_bf16 a[4:7], a[228:231], v[176:179], a[4:7]\n"
    "v_add_f32 v211, v211, v215\n"
    "s_mul_i32 s62, s53, 6144\n"
    "v_mov_b32 v245, v208\n"
    "v_mov_b32 v247, v209\n"
    "v_mfma_f32_16x16x32_bf16 a[8:11], a[232:235], v[176:179], a[8:11]\n"
    "v_mov_b32 v249, v210\n"
    "v_mov_b32 v251, v211\n"
    "s_add_u32 s62, s62, s42\n"
    "v_permlane32_swap_b32 v208, v245\n"
    "v_mfma_f32_16x16x32_bf16 a[12:15], a[236:239], v[176:179], a[12:15]\n"
    "v_permlane32_swap_b32 v209, v247\n"
    "v_permlane32_swap_b32 v210, v249\n"
    "v_permlane32_swap_b32 v211, v251\n"
    "v_mfma_f32_16x16x32_bf16 a[16:19], a[240:243], v[176:179], a[16:19]\n"
    "s_add_u32 s62, s62, s56\n"
    "v_add_f32 v208, v208, v245\n"
    "v_add_f32 v209, v209, v247\n"
    "v_add_f32 v210, v210, v249\n"
    "v_mfma_f32_16x16x32_bf16 a[20:23], a[244:247], v[176:179], a[20:23]\n"
    "v_add_f32 v211, v211, v251\n"
    "s_add_u32 s62, s62, 73728\n"
    "v_mov_b32 v245, v208\n"
    "v_mov_b32 v247, v209\n"
    "v_mfma_f32_16x16x32_bf16 a[24:27], a[248:251], v[176:179], a[24:27]\n"
    "v_mov_b32 v249, v210\n"
    "v_mov_b32 v251, v211\n"
    "v_lshrrev_b32 v216, 7, v253\n"
    "v_permlane16_swap_b32 v208, v245\n"
    "v_mfma_f32_16x16x32_bf16 a[28:31], a[252:255], v[176:179], a[28:31]\n"
    "v_permlane16_swap_b32 v209, v247\n"
    "v_permlane16_swap_b32 v210, v249\n"
    "v_permlane16_swap_b32 v211, v251\n"
    "v_mfma_f32_16x16x32_bf16 a[32:35], a[192:195], v[176:179], a[32:35]\n"
    "v_and_b32 v217, 15, v216\n"
    "v_add_f32 v208, v208, v245\n"
    "v_add_f32 v209, v209, v247\n"
    "v_add_f32 v210, v210, v249\n"
    "v_mfma_f32_16x16x32_bf16 a[36:39], a[196:199], v[176:179], a[36:39]\n"
    "v_add_f32 v211, v211, v251\n"
    "v_mul_u32_u24 v217, 144, v217\n"
    "v_rcp_f32 v244, v208\n"
    "v_rcp_f32 v246, v209\n"
    "s_waitcnt lgkmcnt(0)\n"
    "v_mfma_f32_16x16x32_bf16 a[40:43], a[200:203], v[176:179], a[40:43]\n"
    "v_rcp_f32 v248, v210\n"
    "v_rcp_f32 v250, v211\n"
    "v_lshrrev_b32 v218, 4, v216\n"
    "v_mfma_f32_16x16x32_bf16 a[44:47], a[204:207], v[176:179], a[44:47]\n"
    "v_lshl_add_u32 v217, v218, 3, v217\n"
    "v_add_u32 v192, s62, v217\n"
    "v_add_u32 v193, 2304, v192\n"
    "v_lshrrev_b32 v217, 3, v216\n"
    "v_mul_u32_u24 v217, 144, v217\n"
    "v_and_b32 v218, 7, v216\n"
    "v_lshl_add_u32 v217, v218, 4, v217\n"
    "v_add_u32 v194, s62, v217\n"
    "s_add_u32 s80, s54, 0\n"
    "s_add_u32 s81, s54, 6144\n"
    "s_add_u32 s82, s54, 12288\n"
    "s_add_u32 s83, s54, 18432\n"
    "v_mfma_f32_16x16x32_bf16 a[48:51], a[224:227], v[180:183], a[48:51]\n"
    "v_accvgpr_read_b32 v96, a0\n"
    "v_accvgpr_read_b32 v97, a1\n"
    "v_accvgpr_read_b32 v98, a2\n"
    "v_accvgpr_read_b32 v99, a3\n"
    "v_accvgpr_read_b32 v100, a4\n"
    "v_mfma_f32_16x16x32_bf16 a[52:55], a[228:231], v[180:183], a[52:55]\n"
    "v_accvgpr_read_b32 v101, a5\n"
    "v_accvgpr_read_b32 v102, a6\n"
    "v_accvgpr_read_b32 v103, a7\n"
    "v_accvgpr_read_b32 v104, a8\n"
    "v_accvgpr_read_b32 v105, a9\n"
    "v_accvgpr_read_b32 v106, a10\n"
    "v_accvgpr_read_b32 v107, a11\n"
    "v_accvgpr_read_b32 v108, a12\n"
    "v_accvgpr_read_b32 v109, a13\n"
    "v_accvgpr_read_b32 v110, a14\n"
    "v_accvgpr_read_b32 v111, a15\n"
    "v_pk_mul_f32 v[96:97], v[96:97], v[244:245] op_sel_hi:[1,0]\n"
    "v_pk_mul_f32 v[98:99], v[98:99], v[244:245] op_sel_hi:[1,0]\n"
    "v_pk_mul_f32 v[100:101], v[100:101], v[244:245] op_sel_hi:[1,0]\n"
    "v_pk_mul_f32 v[102:103], v[102:103], v[244:245] op_sel_hi:[1,0]\n"
    "v_pk_mul_f32 v[104:105], v[104:105], v[244:245] op_sel_hi:[1,0]\n"
    "v_pk_mul_f32 v[106:107], v[106:107], v[244:245] op_sel_hi:[1,0]\n"
    "v_pk_mul_f32 v[108:109], v[108:109], v[244:245] op_sel_hi:[1,0]\n"
    "v_pk_mul_f32 v[110:111], v[110:111], v[244:245] op_sel_hi:[1,0]\n"
    "v_mfma_f32_16x16x32_bf16 a[56:59], a[232:235], v[180:183], a[56:59]\n"
    "v_cvt_pk_bf16_f32 v128, v96, v97\n"
    "v_cvt_pk_bf16_f32 v129, v98, v99\n"
    "v_cvt_pk_bf16_f32 v130, v100, v101\n"
    "v_cvt_pk_bf16_f32 v131, v102, v103\n"
    "v_mfma_f32_16x16x32_bf16 a[60:63], a[236:239], v[180:183], a[60:63]\n"
    "v_cvt_pk_bf16_f32 v132, v104, v105\n"
    "v_cvt_pk_bf16_f32 v133, v106, v107\n"
    "v_cvt_pk_bf16_f32 v134, v108, v109\n"
    "v_cvt_pk_bf16_f32 v135, v110, v111\n"
    "ds_write2_b64 v192, v[128:129], v[130:131] offset0:0 offset1:4\n"
    "ds_write2_b64 v192, v[132:133], v[134:135] offset0:8 offset1:12\n"
    "v_mfma_f32_16x16x32_bf16 a[64:67], a[240:243], v[180:183], a[64:67]\n"
    "v_accvgpr_read_b32 v112, a16\n"
    "v_accvgpr_read_b32 v113, a17\n"
    "v_accvgpr_read_b32 v114, a18\n"
    "v_accvgpr_read_b32 v115, a19\n"
    "v_accvgpr_read_b32 v116, a20\n"
    "v_mfma_f32_16x16x32_bf16 a[68:71], a[244:247], v[180:183], a[68:71]\n"
    "v_accvgpr_read_b32 v117, a21\n"
    "v_accvgpr_read_b32 v118, a22\n"
    "v_accvgpr_read_b32 v119, a23\n"
    "v_accvgpr_read_b32 v120, a24\n"
    "v_accvgpr_read_b32 v121, a25\n"
    "v_accvgpr_read_b32 v122, a26\n"
    "v_accvgpr_read_b32 v123, a27\n"
    "v_accvgpr_read_b32 v124, a28\n"
    "v_accvgpr_read_b32 v125, a29\n"
    "v_accvgpr_read_b32 v126, a30\n"
    "v_accvgpr_read_b32 v127, a31\n"
    "v_pk_mul_f32 v[112:113], v[112:113], v[244:245] op_sel_hi:[1,0]\n"
    "v_pk_mul_f32 v[114:115], v[114:115], v[244:245] op_sel_hi:[1,0]\n"
    "v_pk_mul_f32 v[116:117], v[116:117], v[244:245] op_sel_hi:[1,0]\n"
    "v_pk_mul_f32 v[118:119], v[118:119], v[244:245] op_sel_hi:[1,0]\n"
    "v_pk_mul_f32 v[120:121], v[120:121], v[244:245] op_sel_hi:[1,0]\n"
    "v_pk_mul_f32 v[122:123], v[122:123], v[244:245] op_sel_hi:[1,0]\n"
    "v_pk_mul_f32 v[124:125], v[124:125], v[244:245] op_sel_hi:[1,0]\n"
    "v_pk_mul_f32 v[126:127], v[126:127], v[244:245] op_sel_hi:[1,0]\n"
    "v_mfma_f32_16x16x32_bf16 a[72:75], a[248:251], v[180:183], a[72:75]\n"
    "v_cvt_pk_bf16_f32 v136, v112, v113\n"
    "v_cvt_pk_bf16_f32 v137, v114, v115\n"
    "v_cvt_pk_bf16_f32 v138, v116, v117\n"
    "v_cvt_pk_bf16_f32 v139, v118, v119\n"
    "v_mfma_f32_16x16x32_bf16 a[76:79], a[252:255], v[180:183], a[76:79]\n"
    "v_cvt_pk_bf16_f32 v140, v120, v121\n"
    "v_cvt_pk_bf16_f32 v141, v122, v123\n"
    "v_cvt_pk_bf16_f32 v142, v124, v125\n"
    "v_cvt_pk_bf16_f32 v143, v126, v127\n"
    "ds_read_b128 v[144:147], v194 offset:0\n"
    "ds_read_b128 v[148:151], v194 offset:1152\n"
    "ds_write2_b64 v193, v[136:137], v[138:139] offset0:0 offset1:4\n"
    "ds_write2_b64 v193, v[140:141], v[142:143] offset0:8 offset1:12\n"
    "v_mfma_f32_16x16x32_bf16 a[80:83], a[192:195], v[180:183], a[80:83]\n"
    "v_accvgpr_read_b32 v96, a32\n"
    "v_accvgpr_read_b32 v97, a33\n"
    "v_accvgpr_read_b32 v98, a34\n"
    "v_accvgpr_read_b32 v99, a35\n"
    "v_accvgpr_read_b32 v100, a36\n"
    "v_mfma_f32_16x16x32_bf16 a[84:87], a[196:199], v[180:183], a[84:87]\n"
    "v_accvgpr_read_b32 v101, a37\n"
    "v_accvgpr_read_b32 v102, a38\n"
    "s_waitcnt lgkmcnt(2)\n"
    "buffer_store_dwordx4 v[144:147], v240, s[36:39], s80 offen offset:0 sc0 sc1\n"
    "buffer_store_dwordx4 v[148:151], v240, s[36:39], s80 offen offset:3072 sc0 sc1\n"
    "v_accvgpr_read_b32 v103, a39\n"
    "v_accvgpr_read_b32 v104, a40\n"
    "v_accvgpr_read_b32 v105, a41\n"
    "v_accvgpr_read_b32 v106, a42\n"
    "v_accvgpr_read_b32 v107, a43\n"
    "v_accvgpr_read_b32 v108, a44\n"
    "v_accvgpr_read_b32 v109, a45\n"
    "v_accvgpr_read_b32 v110, a46\n"
    "v_accvgpr_read_b32 v111, a47\n"
    "v_pk_mul_f32 v[96:97], v[96:97], v[244:245] op_sel_hi:[1,0]\n"
    "v_pk_mul_f32 v[98:99], v[98:99], v[244:245] op_sel_hi:[1,0]\n"
    "v_pk_mul_f32 v[100:101], v[100:101], v[244:245] op_sel_hi:[1,0]\n"
    "v_pk_mul_f32 v[102:103], v[102:103], v[244:245] op_sel_hi:[1,0]\n"
    "v_pk_mul_f32 v[104:105], v[104:105], v[244:245] op_sel_hi:[1,0]\n"
    "v_pk_mul_f32 v[106:107], v[106:107], v[244:245] op_sel_hi:[1,0]\n"
    "v_pk_mul_f32 v[108:109], v[108:109], v[244:245] op_sel_hi:[1,0]\n"
    "v_pk_mul_f32 v[110:111], v[110:111], v[244:245] op_sel_hi:[1,0]\n"
    "v_mfma_f32_16x16x32_bf16 a[88:91], a[200:203], v[180:183], a[88:91]\n"
    "v_cvt_pk_bf16_f32 v128, v96, v97\n"
    "v_cvt_pk_bf16_f32 v129, v98, v99\n"
    "v_cvt_pk_bf16_f32 v130, v100, v101\n"
    "v_cvt_pk_bf16_f32 v131, v102, v103\n"
    "v_mfma_f32_16x16x32_bf16 a[92:95], a[204:207], v[180:183], a[92:95]\n"
    "v_cvt_pk_bf16_f32 v132, v104, v105\n"
    "v_cvt_pk_bf16_f32 v133, v106, v107\n"
    "v_cvt_pk_bf16_f32 v134, v108, v109\n"
    "v_cvt_pk_bf16_f32 v135, v110, v111\n"
    "ds_read_b128 v[152:155], v194 offset:2304\n"
    "ds_read_b128 v[156:159], v194 offset:3456\n"
    "ds_write2_b64 v192, v[128:129], v[130:131] offset0:0 offset1:4\n"
    "ds_write2_b64 v192, v[132:133], v[134:135] offset0:8 offset1:12\n"
    "v_mfma_f32_16x16x32_bf16 a[96:99], a[224:227], v[184:187], a[96:99]\n"
    "v_accvgpr_read_b32 v112, a48\n"
    "v_accvgpr_read_b32 v113, a49\n"
    "v_accvgpr_read_b32 v114, a50\n"
    "v_accvgpr_read_b32 v115, a51\n"
    "v_accvgpr_read_b32 v116, a52\n"
    "v_mfma_f32_16x16x32_bf16 a[100:103], a[228:231], v[184:187], a[100:103]\n"
    "v_accvgpr_read_b32 v117, a53\n"
    "v_accvgpr_read_b32 v118, a54\n"
    "s_waitcnt lgkmcnt(2)\n"
    "buffer_store_dwordx4 v[152:155], v240, s[36:39], s80 offen offset:128 sc0 sc1\n"
    "buffer_store_dwordx4 v[156:159], v240, s[36:39], s80 offen offset:3200 sc0 sc1\n"
    "v_accvgpr_read_b32 v119, a55\n"
    "v_accvgpr_read_b32 v120, a56\n"
    "v_accvgpr_read_b32 v121, a57\n"
    "v_accvgpr_read_b32 v122, a58\n"
    "v_accvgpr_read_b32 v123, a59\n"
    "v_accvgpr_read_b32 v124, a60\n"
    "v_accvgpr_read_b32 v125, a61\n"
    "v_accvgpr_read_b32 v126, a62\n"
    "v_accvgpr_read_b32 v127, a63\n"
    "v_pk_mul_f32 v[112:113], v[112:113], v[246:247] op_sel_hi:[1,0]\n"
    "v_pk_mul_f32 v[114:115], v[114:115], v[246:247] op_sel_hi:[1,0]\n"
    "v_pk_mul_f32 v[116:117], v[116:117], v[246:247] op_sel_hi:[1,0]\n"
    "v_pk_mul_f32 v[118:119], v[118:119], v[246:247] op_sel_hi:[1,0]\n"
    "v_pk_mul_f32 v[120:121], v[120:121], v[246:247] op_sel_hi:[1,0]\n"
    "v_pk_mul_f32 v[122:123], v[122:123], v[246:247] op_sel_hi:[1,0]\n"
    "v_pk_mul_f32 v[124:125], v[124:125], v[246:247] op_sel_hi:[1,0]\n"
    "v_pk_mul_f32 v[126:127], v[126:127], v[246:247] op_sel_hi:[1,0]\n"
    "v_mfma_f32_16x16x32_bf16 a[104:107], a[232:235], v[184:187], a[104:107]\n"
    "v_cvt_pk_bf16_f32 v136, v112, v113\n"
    "v_cvt_pk_bf16_f32 v137, v114, v115\n"
    "v_cvt_pk_bf16_f32 v138, v116, v117\n"
    "v_cvt_pk_bf16_f32 v139, v118, v119\n"
    "v_mfma_f32_16x16x32_bf16 a[108:111], a[236:239], v[184:187], a[108:111]\n"
    "v_cvt_pk_bf16_f32 v140, v120, v121\n"
    "v_cvt_pk_bf16_f32 v141, v122, v123\n"
    "v_cvt_pk_bf16_f32 v142, v124, v125\n"
    "v_cvt_pk_bf16_f32 v143, v126, v127\n"
    "ds_read_b128 v[216:219], v194 offset:0\n"
    "ds_read_b128 v[220:223], v194 offset:1152\n"
    "ds_write2_b64 v193, v[136:137], v[138:139] offset0:0 offset1:4\n"
    "ds_write2_b64 v193, v[140:141], v[142:143] offset0:8 offset1:12\n"
    "v_mfma_f32_16x16x32_bf16 a[112:115], a[240:243], v[184:187], a[112:115]\n"
    "v_accvgpr_read_b32 v96, a64\n"
    "v_accvgpr_read_b32 v97, a65\n"
    "v_accvgpr_read_b32 v98, a66\n"
    "v_accvgpr_read_b32 v99, a67\n"
    "v_accvgpr_read_b32 v100, a68\n"
    "v_mfma_f32_16x16x32_bf16 a[116:119], a[244:247], v[184:187], a[116:119]\n"
    "v_accvgpr_read_b32 v101, a69\n"
    "v_accvgpr_read_b32 v102, a70\n"
    "s_waitcnt lgkmcnt(2)\n"
    "buffer_store_dwordx4 v[216:219], v240, s[36:39], s80 offen offset:256 sc0 sc1\n"
    "buffer_store_dwordx4 v[220:223], v240, s[36:39], s80 offen offset:3328 sc0 sc1\n"
    "v_accvgpr_read_b32 v103, a71\n"
    "v_accvgpr_read_b32 v104, a72\n"
    "v_accvgpr_read_b32 v105, a73\n"
    "v_accvgpr_read_b32 v106, a74\n"
    "v_accvgpr_read_b32 v107, a75\n"
    "v_accvgpr_read_b32 v108, a76\n"
    "v_accvgpr_read_b32 v109, a77\n"
    "v_accvgpr_read_b32 v110, a78\n"
    "v_accvgpr_read_b32 v111, a79\n"
    "v_pk_mul_f32 v[96:97], v[96:97], v[246:247] op_sel_hi:[1,0]\n"
    "v_pk_mul_f32 v[98:99], v[98:99], v[246:247] op_sel_hi:[1,0]\n"
    "v_pk_mul_f32 v[100:101], v[100:101], v[246:247] op_sel_hi:[1,0]\n"
    "v_pk_mul_f32 v[102:103], v[102:103], v[246:247] op_sel_hi:[1,0]\n"
    "v_pk_mul_f32 v[104:105], v[104:105], v[246:247] op_sel_hi:[1,0]\n"
    "v_pk_mul_f32 v[106:107], v[106:107], v[246:247] op_sel_hi:[1,0]\n"
    "v_pk_mul_f32 v[108:109], v[108:109], v[246:247] op_sel_hi:[1,0]\n"
    "v_pk_mul_f32 v[110:111], v[110:111], v[246:247] op_sel_hi:[1,0]\n"
    "v_mfma_f32_16x16x32_bf16 a[120:123], a[248:251], v[184:187], a[120:123]\n"
    "v_cvt_pk_bf16_f32 v128, v96, v97\n"
    "v_cvt_pk_bf16_f32 v129, v98, v99\n"
    "v_cvt_pk_bf16_f32 v130, v100, v101\n"
    "v_cvt_pk_bf16_f32 v131, v102, v103\n"
    "v_mfma_f32_16x16x32_bf16 a[124:127], a[252:255], v[184:187], a[124:127]\n"
    "v_cvt_pk_bf16_f32 v132, v104, v105\n"
    "v_cvt_pk_bf16_f32 v133, v106, v107\n"
    "v_cvt_pk_bf16_f32 v134, v108, v109\n"
    "v_cvt_pk_bf16_f32 v135, v110, v111\n"
    "ds_read_b128 v[144:147], v194 offset:2304\n"
    "ds_read_b128 v[148:151], v194 offset:3456\n"
    "ds_write2_b64 v192, v[128:129], v[130:131] offset0:0 offset1:4\n"
    "ds_write2_b64 v192, v[132:133], v[134:135] offset0:8 offset1:12\n"
    "v_mfma_f32_16x16x32_bf16 a[128:131], a[192:195], v[184:187], a[128:131]\n"
    "v_accvgpr_read_b32 v112, a80\n"
    "v_accvgpr_read_b32 v113, a81\n"
    "v_accvgpr_read_b32 v114, a82\n"
    "v_accvgpr_read_b32 v115, a83\n"
    "v_accvgpr_read_b32 v116, a84\n"
    "v_mfma_f32_16x16x32_bf16 a[132:135], a[196:199], v[184:187], a[132:135]\n"
    "v_accvgpr_read_b32 v117, a85\n"
    "v_accvgpr_read_b32 v118, a86\n"
    "s_waitcnt lgkmcnt(2)\n"
    "buffer_store_dwordx4 v[144:147], v240, s[36:39], s81 offen offset:0 sc0 sc1\n"
    "buffer_store_dwordx4 v[148:151], v240, s[36:39], s81 offen offset:3072 sc0 sc1\n"
    "v_accvgpr_read_b32 v119, a87\n"
    "v_accvgpr_read_b32 v120, a88\n"
    "v_accvgpr_read_b32 v121, a89\n"
    "v_accvgpr_read_b32 v122, a90\n"
    "v_accvgpr_read_b32 v123, a91\n"
    "v_accvgpr_read_b32 v124, a92\n"
    "v_accvgpr_read_b32 v125, a93\n"
    "v_accvgpr_read_b32 v126, a94\n"
    "v_accvgpr_read_b32 v127, a95\n"
    "v_pk_mul_f32 v[112:113], v[112:113], v[246:247] op_sel_hi:[1,0]\n"
    "v_pk_mul_f32 v[114:115], v[114:115], v[246:247] op_sel_hi:[1,0]\n"
    "v_pk_mul_f32 v[116:117], v[116:117], v[246:247] op_sel_hi:[1,0]\n"
    "v_pk_mul_f32 v[118:119], v[118:119], v[246:247] op_sel_hi:[1,0]\n"
    "v_pk_mul_f32 v[120:121], v[120:121], v[246:247] op_sel_hi:[1,0]\n"
    "v_pk_mul_f32 v[122:123], v[122:123], v[246:247] op_sel_hi:[1,0]\n"
    "v_pk_mul_f32 v[124:125], v[124:125], v[246:247] op_sel_hi:[1,0]\n"
    "v_pk_mul_f32 v[126:127], v[126:127], v[246:247] op_sel_hi:[1,0]\n"
    "v_mfma_f32_16x16x32_bf16 a[136:139], a[200:203], v[184:187], a[136:139]\n"
    "v_cvt_pk_bf16_f32 v136, v112, v113\n"
    "v_cvt_pk_bf16_f32 v137, v114, v115\n"
    "v_cvt_pk_bf16_f32 v138, v116, v117\n"
    "v_cvt_pk_bf16_f32 v139, v118, v119\n"
    "v_mfma_f32_16x16x32_bf16 a[140:143], a[204:207], v[184:187], a[140:143]\n"
    "v_cvt_pk_bf16_f32 v140, v120, v121\n"
    "v_cvt_pk_bf16_f32 v141, v122, v123\n"
    "v_cvt_pk_bf16_f32 v142, v124, v125\n"
    "v_cvt_pk_bf16_f32 v143, v126, v127\n"
    "ds_read_b128 v[152:155], v194 offset:0\n"
    "ds_read_b128 v[156:159], v194 offset:1152\n"
    "ds_write2_b64 v193, v[136:137], v[138:139] offset0:0 offset1:4\n"
    "ds_write2_b64 v193, v[140:141], v[142:143] offset0:8 offset1:12\n"
    "v_mfma_f32_16x16x32_bf16 a[144:147], a[224:227], v[188:191], a[144:147]\n"
    "v_accvgpr_read_b32 v96, a96\n"
    "v_accvgpr_read_b32 v97, a97\n"
    "v_accvgpr_read_b32 v98, a98\n"
    "v_accvgpr_read_b32 v99, a99\n"
    "v_accvgpr_read_b32 v100, a100\n"
    "v_mfma_f32_16x16x32_bf16 a[148:151], a[228:231], v[188:191], a[148:151]\n"
    "v_accvgpr_read_b32 v101, a101\n"
    "v_accvgpr_read_b32 v102, a102\n"
    "s_waitcnt lgkmcnt(2)\n"
    "buffer_store_dwordx4 v[152:155], v240, s[36:39], s81 offen offset:128 sc0 sc1\n"
    "buffer_store_dwordx4 v[156:159], v240, s[36:39], s81 offen offset:3200 sc0 sc1\n"
    "v_accvgpr_read_b32 v103, a103\n"
    "v_accvgpr_read_b32 v104, a104\n"
    "v_accvgpr_read_b32 v105, a105\n"
    "v_accvgpr_read_b32 v106, a106\n"
    "v_accvgpr_read_b32 v107, a107\n"
    "v_accvgpr_read_b32 v108, a108\n"
    "v_accvgpr_read_b32 v109, a109\n"
    "v_accvgpr_read_b32 v110, a110\n"
    "v_accvgpr_read_b32 v111, a111\n"
    "v_pk_mul_f32 v[96:97], v[96:97], v[248:249] op_sel_hi:[1,0]\n"
    "v_pk_mul_f32 v[98:99], v[98:99], v[248:249] op_sel_hi:[1,0]\n"
    "v_pk_mul_f32 v[100:101], v[100:101], v[248:249] op_sel_hi:[1,0]\n"
    "v_pk_mul_f32 v[102:103], v[102:103], v[248:249] op_sel_hi:[1,0]\n"
    "v_pk_mul_f32 v[104:105], v[104:105], v[248:249] op_sel_hi:[1,0]\n"
    "v_pk_mul_f32 v[106:107], v[106:107], v[248:249] op_sel_hi:[1,0]\n"
    "v_pk_mul_f32 v[108:109], v[108:109], v[248:249] op_sel_hi:[1,0]\n"
    "v_pk_mul_f32 v[110:111], v[110:111], v[248:249] op_sel_hi:[1,0]\n"
    "v_mfma_f32_16x16x32_bf16 a[152:155], a[232:235], v[188:191], a[152:155]\n"
    "v_cvt_pk_bf16_f32 v128, v96, v97\n"
    "v_cvt_pk_bf16_f32 v129, v98, v99\n"
    "v_cvt_pk_bf16_f32 v130, v100, v101\n"
    "v_cvt_pk_bf16_f32 v131, v102, v103\n"
    "v_mfma_f32_16x16x32_bf16 a[156:159], a[236:239], v[188:191], a[156:159]\n"
    "v_cvt_pk_bf16_f32 v132, v104, v105\n"
    "v_cvt_pk_bf16_f32 v133, v106, v107\n"
    "v_cvt_pk_bf16_f32 v134, v108, v109\n"
    "v_cvt_pk_bf16_f32 v135, v110, v111\n"
    "ds_read_b128 v[216:219], v194 offset:2304\n"
    "ds_read_b128 v[220:223], v194 offset:3456\n"
    "ds_write2_b64 v192, v[128:129], v[130:131] offset0:0 offset1:4\n"
    "ds_write2_b64 v192, v[132:133], v[134:135] offset0:8 offset1:12\n"
    "v_mfma_f32_16x16x32_bf16 a[160:163], a[240:243], v[188:191], a[160:163]\n"
    "v_accvgpr_read_b32 v112, a112\n"
    "v_accvgpr_read_b32 v113, a113\n"
    "v_accvgpr_read_b32 v114, a114\n"
    "v_accvgpr_read_b32 v115, a115\n"
    "v_accvgpr_read_b32 v116, a116\n"
    "v_mfma_f32_16x16x32_bf16 a[164:167], a[244:247], v[188:191], a[164:167]\n"
    "v_accvgpr_read_b32 v117, a117\n"
    "v_accvgpr_read_b32 v118, a118\n"
    "s_waitcnt lgkmcnt(2)\n"
    "buffer_store_dwordx4 v[216:219], v240, s[36:39], s81 offen offset:256 sc0 sc1\n"
    "buffer_store_dwordx4 v[220:223], v240, s[36:39], s81 offen offset:3328 sc0 sc1\n"
    "v_accvgpr_read_b32 v119, a119\n"
    "v_accvgpr_read_b32 v120, a120\n"
    "v_accvgpr_read_b32 v121, a121\n"
    "v_accvgpr_read_b32 v122, a122\n"
    "v_accvgpr_read_b32 v123, a123\n"
    "v_accvgpr_read_b32 v124, a124\n"
    "v_accvgpr_read_b32 v125, a125\n"
    "v_accvgpr_read_b32 v126, a126\n"
    "v_accvgpr_read_b32 v127, a127\n"
    "v_pk_mul_f32 v[112:113], v[112:113], v[248:249] op_sel_hi:[1,0]\n"
    "v_pk_mul_f32 v[114:115], v[114:115], v[248:249] op_sel_hi:[1,0]\n"
    "v_pk_mul_f32 v[116:117], v[116:117], v[248:249] op_sel_hi:[1,0]\n"
    "v_pk_mul_f32 v[118:119], v[118:119], v[248:249] op_sel_hi:[1,0]\n"
    "v_pk_mul_f32 v[120:121], v[120:121], v[248:249] op_sel_hi:[1,0]\n"
    "v_pk_mul_f32 v[122:123], v[122:123], v[248:249] op_sel_hi:[1,0]\n"
    "v_pk_mul_f32 v[124:125], v[124:125], v[248:249] op_sel_hi:[1,0]\n"
    "v_pk_mul_f32 v[126:127], v[126:127], v[248:249] op_sel_hi:[1,0]\n"
    "v_mfma_f32_16x16x32_bf16 a[168:171], a[248:251], v[188:191], a[168:171]\n"
    "v_cvt_pk_bf16_f32 v136, v112, v113\n"
    "v_cvt_pk_bf16_f32 v137, v114, v115\n"
    "v_cvt_pk_bf16_f32 v138, v116, v117\n"
    "v_cvt_pk_bf16_f32 v139, v118, v119\n"
    "v_mfma_f32_16x16x32_bf16 a[172:175], a[252:255], v[188:191], a[172:175]\n"
    "v_cvt_pk_bf16_f32 v140, v120, v121\n"
    "v_cvt_pk_bf16_f32 v141, v122, v123\n"
    "v_cvt_pk_bf16_f32 v142, v124, v125\n"
    "v_cvt_pk_bf16_f32 v143, v126, v127\n"
    "ds_read_b128 v[144:147], v194 offset:0\n"
    "ds_read_b128 v[148:151], v194 offset:1152\n"
    "ds_write2_b64 v193, v[136:137], v[138:139] offset0:0 offset1:4\n"
    "ds_write2_b64 v193, v[140:141], v[142:143] offset0:8 offset1:12\n"
    "v_mfma_f32_16x16x32_bf16 a[176:179], a[192:195], v[188:191], a[176:179]\n"
    "v_accvgpr_read_b32 v96, a128\n"
    "v_accvgpr_read_b32 v97, a129\n"
    "v_accvgpr_read_b32 v98, a130\n"
    "v_accvgpr_read_b32 v99, a131\n"
    "v_accvgpr_read_b32 v100, a132\n"
    "v_mfma_f32_16x16x32_bf16 a[180:183], a[196:199], v[188:191], a[180:183]\n"
    "v_accvgpr_read_b32 v101, a133\n"
    "v_accvgpr_read_b32 v102, a134\n"
    "s_waitcnt lgkmcnt(2)\n"
    "buffer_store_dwordx4 v[144:147], v240, s[36:39], s82 offen offset:0 sc0 sc1\n"
    "buffer_store_dwordx4 v[148:151], v240, s[36:39], s82 offen offset:3072 sc0 sc1\n"
    "v_accvgpr_read_b32 v103, a135\n"
    "v_accvgpr_read_b32 v104, a136\n"
    "v_accvgpr_read_b32 v105, a137\n"
    "v_accvgpr_read_b32 v106, a138\n"
    "v_accvgpr_read_b32 v107, a139\n"
    "v_accvgpr_read_b32 v108, a140\n"
    "v_accvgpr_read_b32 v109, a141\n"
    "v_accvgpr_read_b32 v110, a142\n"
    "v_accvgpr_read_b32 v111, a143\n"
    "v_pk_mul_f32 v[96:97], v[96:97], v[248:249] op_sel_hi:[1,0]\n"
    "v_pk_mul_f32 v[98:99], v[98:99], v[248:249] op_sel_hi:[1,0]\n"
    "v_pk_mul_f32 v[100:101], v[100:101], v[248:249] op_sel_hi:[1,0]\n"
    "v_pk_mul_f32 v[102:103], v[102:103], v[248:249] op_sel_hi:[1,0]\n"
    "v_pk_mul_f32 v[104:105], v[104:105], v[248:249] op_sel_hi:[1,0]\n"
    "v_pk_mul_f32 v[106:107], v[106:107], v[248:249] op_sel_hi:[1,0]\n"
    "v_pk_mul_f32 v[108:109], v[108:109], v[248:249] op_sel_hi:[1,0]\n"
    "v_pk_mul_f32 v[110:111], v[110:111], v[248:249] op_sel_hi:[1,0]\n"
    "v_mfma_f32_16x16x32_bf16 a[184:187], a[200:203], v[188:191], a[184:187]\n"
    "v_cvt_pk_bf16_f32 v128, v96, v97\n"
    "v_cvt_pk_bf16_f32 v129, v98, v99\n"
    "v_cvt_pk_bf16_f32 v130, v100, v101\n"
    "v_cvt_pk_bf16_f32 v131, v102, v103\n"
    "v_mfma_f32_16x16x32_bf16 a[188:191], a[204:207], v[188:191], a[188:191]\n"
    "v_cvt_pk_bf16_f32 v132, v104, v105\n"
    "v_cvt_pk_bf16_f32 v133, v106, v107\n"
    "v_cvt_pk_bf16_f32 v134, v108, v109\n"
    "v_cvt_pk_bf16_f32 v135, v110, v111\n"
    "ds_read_b128 v[152:155], v194 offset:2304\n"
    "ds_read_b128 v[156:159], v194 offset:3456\n"
    "ds_write2_b64 v192, v[128:129], v[130:131] offset0:0 offset1:4\n"
    "ds_write2_b64 v192, v[132:133], v[134:135] offset0:8 offset1:12\n"
    "v_accvgpr_read_b32 v112, a144\n"
    "v_accvgpr_read_b32 v113, a145\n"
    "v_accvgpr_read_b32 v114, a146\n"
    "v_accvgpr_read_b32 v115, a147\n"
    "v_accvgpr_read_b32 v116, a148\n"
    "v_accvgpr_read_b32 v117, a149\n"
    "v_accvgpr_read_b32 v118, a150\n"
    "v_accvgpr_read_b32 v119, a151\n"
    "v_accvgpr_read_b32 v120, a152\n"
    "s_waitcnt lgkmcnt(2)\n"
    "buffer_store_dwordx4 v[152:155], v240, s[36:39], s82 offen offset:128 sc0 sc1\n"
    "buffer_store_dwordx4 v[156:159], v240, s[36:39], s82 offen offset:3200 sc0 sc1\n"
    "v_accvgpr_read_b32 v121, a153\n"
    "v_accvgpr_read_b32 v122, a154\n"
    "v_accvgpr_read_b32 v123, a155\n"
    "v_accvgpr_read_b32 v124, a156\n"
    "v_accvgpr_read_b32 v125, a157\n"
    "v_accvgpr_read_b32 v126, a158\n"
    "v_accvgpr_read_b32 v127, a159\n"
    "v_pk_mul_f32 v[112:113], v[112:113], v[250:251] op_sel_hi:[1,0]\n"
    "v_pk_mul_f32 v[114:115], v[114:115], v[250:251] op_sel_hi:[1,0]\n"
    "v_pk_mul_f32 v[116:117], v[116:117], v[250:251] op_sel_hi:[1,0]\n"
    "v_pk_mul_f32 v[118:119], v[118:119], v[250:251] op_sel_hi:[1,0]\n"
    "v_pk_mul_f32 v[120:121], v[120:121], v[250:251] op_sel_hi:[1,0]\n"
    "v_pk_mul_f32 v[122:123], v[122:123], v[250:251] op_sel_hi:[1,0]\n"
    "v_pk_mul_f32 v[124:125], v[124:125], v[250:251] op_sel_hi:[1,0]\n"
    "v_pk_mul_f32 v[126:127], v[126:127], v[250:251] op_sel_hi:[1,0]\n"
    "v_cvt_pk_bf16_f32 v136, v112, v113\n"
    "v_cvt_pk_bf16_f32 v137, v114, v115\n"
    "v_cvt_pk_bf16_f32 v138, v116, v117\n"
    "v_cvt_pk_bf16_f32 v139, v118, v119\n"
    "v_cvt_pk_bf16_f32 v140, v120, v121\n"
    "v_cvt_pk_bf16_f32 v141, v122, v123\n"
    "v_cvt_pk_bf16_f32 v142, v124, v125\n"
    "v_cvt_pk_bf16_f32 v143, v126, v127\n"
    "ds_read_b128 v[216:219], v194 offset:0\n"
    "ds_read_b128 v[220:223], v194 offset:1152\n"
    "ds_write2_b64 v193, v[136:137], v[138:139] offset0:0 offset1:4\n"
    "ds_write2_b64 v193, v[140:141], v[142:143] offset0:8 offset1:12\n"
    "v_accvgpr_read_b32 v96, a160\n"
    "v_accvgpr_read_b32 v97, a161\n"
    "v_accvgpr_read_b32 v98, a162\n"
    "v_accvgpr_read_b32 v99, a163\n"
    "v_accvgpr_read_b32 v100, a164\n"
    "v_accvgpr_read_b32 v101, a165\n"
    "v_accvgpr_read_b32 v102, a166\n"
    "v_accvgpr_read_b32 v103, a167\n"
    "v_accvgpr_read_b32 v104, a168\n"
    "s_waitcnt lgkmcnt(2)\n"
    "buffer_store_dwordx4 v[216:219], v240, s[36:39], s82 offen offset:256 sc0 sc1\n"
    "buffer_store_dwordx4 v[220:223], v240, s[36:39], s82 offen offset:3328 sc0 sc1\n"
    "v_accvgpr_read_b32 v105, a169\n"
    "v_accvgpr_read_b32 v106, a170\n"
    "v_accvgpr_read_b32 v107, a171\n"
    "v_accvgpr_read_b32 v108, a172\n"
    "v_accvgpr_read_b32 v109, a173\n"
    "v_accvgpr_read_b32 v110, a174\n"
    "v_accvgpr_read_b32 v111, a175\n"
    "v_pk_mul_f32 v[96:97], v[96:97], v[250:251] op_sel_hi:[1,0]\n"
    "v_pk_mul_f32 v[98:99], v[98:99], v[250:251] op_sel_hi:[1,0]\n"
    "v_pk_mul_f32 v[100:101], v[100:101], v[250:251] op_sel_hi:[1,0]\n"
    "v_pk_mul_f32 v[102:103], v[102:103], v[250:251] op_sel_hi:[1,0]\n"
    "v_pk_mul_f32 v[104:105], v[104:105], v[250:251] op_sel_hi:[1,0]\n"
    "v_pk_mul_f32 v[106:107], v[106:107], v[250:251] op_sel_hi:[1,0]\n"
    "v_pk_mul_f32 v[108:109], v[108:109], v[250:251] op_sel_hi:[1,0]\n"
    "v_pk_mul_f32 v[110:111], v[110:111], v[250:251] op_sel_hi:[1,0]\n"
    "v_cvt_pk_bf16_f32 v128, v96, v97\n"
    "v_cvt_pk_bf16_f32 v129, v98, v99\n"
    "v_cvt_pk_bf16_f32 v130, v100, v101\n"
    "v_cvt_pk_bf16_f32 v131, v102, v103\n"
    "v_cvt_pk_bf16_f32 v132, v104, v105\n"
    "v_cvt_pk_bf16_f32 v133, v106, v107\n"
    "v_cvt_pk_bf16_f32 v134, v108, v109\n"
    "v_cvt_pk_bf16_f32 v135, v110, v111\n"
    "ds_read_b128 v[144:147], v194 offset:2304\n"
    "ds_read_b128 v[148:151], v194 offset:3456\n"
    "ds_write2_b64 v192, v[128:129], v[130:131] offset0:0 offset1:4\n"
    "ds_write2_b64 v192, v[132:133], v[134:135] offset0:8 offset1:12\n"
    "v_accvgpr_read_b32 v112, a176\n"
    "v_accvgpr_read_b32 v113, a177\n"
    "v_accvgpr_read_b32 v114, a178\n"
    "v_accvgpr_read_b32 v115, a179\n"
    "v_accvgpr_read_b32 v116, a180\n"
    "v_accvgpr_read_b32 v117, a181\n"
    "v_accvgpr_read_b32 v118, a182\n"
    "v_accvgpr_read_b32 v119, a183\n"
    "v_accvgpr_read_b32 v120, a184\n"
    "s_waitcnt lgkmcnt(2)\n"
    "buffer_store_dwordx4 v[144:147], v240, s[36:39], s83 offen offset:0 sc0 sc1\n"
    "buffer_store_dwordx4 v[148:151], v240, s[36:39], s83 offen offset:3072 sc0 sc1\n"
    "v_accvgpr_read_b32 v121, a185\n"
    "v_accvgpr_read_b32 v122, a186\n"
    "v_accvgpr_read_b32 v123, a187\n"
    "v_accvgpr_read_b32 v124, a188\n"
    "v_accvgpr_read_b32 v125, a189\n"
    "v_accvgpr_read_b32 v126, a190\n"
    "v_accvgpr_read_b32 v127, a191\n"
    "v_pk_mul_f32 v[112:113], v[112:113], v[250:251] op_sel_hi:[1,0]\n"
    "v_pk_mul_f32 v[114:115], v[114:115], v[250:251] op_sel_hi:[1,0]\n"
    "v_pk_mul_f32 v[116:117], v[116:117], v[250:251] op_sel_hi:[1,0]\n"
    "v_pk_mul_f32 v[118:119], v[118:119], v[250:251] op_sel_hi:[1,0]\n"
    "v_pk_mul_f32 v[120:121], v[120:121], v[250:251] op_sel_hi:[1,0]\n"
    "v_pk_mul_f32 v[122:123], v[122:123], v[250:251] op_sel_hi:[1,0]\n"
    "v_pk_mul_f32 v[124:125], v[124:125], v[250:251] op_sel_hi:[1,0]\n"
    "v_pk_mul_f32 v[126:127], v[126:127], v[250:251] op_sel_hi:[1,0]\n"
    "v_cvt_pk_bf16_f32 v136, v112, v113\n"
    "v_cvt_pk_bf16_f32 v137, v114, v115\n"
    "v_cvt_pk_bf16_f32 v138, v116, v117\n"
    "v_cvt_pk_bf16_f32 v139, v118, v119\n"
    "v_cvt_pk_bf16_f32 v140, v120, v121\n"
    "v_cvt_pk_bf16_f32 v141, v122, v123\n"
    "v_cvt_pk_bf16_f32 v142, v124, v125\n"
    "v_cvt_pk_bf16_f32 v143, v126, v127\n"
    "ds_read_b128 v[152:155], v194 offset:0\n"
    "ds_read_b128 v[156:159], v194 offset:1152\n"
    "ds_write2_b64 v193, v[136:137], v[138:139] offset0:0 offset1:4\n"
    "ds_write2_b64 v193, v[140:141], v[142:143] offset0:8 offset1:12\n"
    "s_waitcnt lgkmcnt(2)\n"
    "buffer_store_dwordx4 v[152:155], v240, s[36:39], s83 offen offset:128 sc0 sc1\n"
    "buffer_store_dwordx4 v[156:159], v240, s[36:39], s83 offen offset:3200 sc0 sc1\n"
    "ds_read_b128 v[216:219], v194 offset:2304\n"
    "ds_read_b128 v[220:223], v194 offset:3456\n"
    "s_waitcnt lgkmcnt(0)\n"
    "buffer_store_dwordx4 v[216:219], v240, s[36:39], s83 offen offset:256 sc0 sc1\n"
    "buffer_store_dwordx4 v[220:223], v240, s[36:39], s83 offen offset:3328 sc0 sc1\n"
    "s_branch LNEXT_%=\n"
    "LITEM_3_%=:\n"
    "s_cmp_eq_u32 s51, 0\n"
    "s_cbranch_scc0 LPRO1_3_%=\n"
    "LPRO0_3_%=:\n"
    "s_mov_b32 s84, s44\n"
    "s_add_u32 s87, s87, 1\n"
    "v_readlane_b32 s44, v252, s87\n"
    "v_xor_b32 v232, 64, v243\n"
    "v_add_u32 v235, s41, v233\n"
    "s_waitcnt vmcnt(18)\n"
    "v_lshlrev_b32 v216, 16, v0\n"
    "v_and_b32 v217, s17, v0\n"
    "v_lshlrev_b32 v218, 16, v1\n"
    "v_and_b32 v219, s17, v1\n"
    "v_lshlrev_b32 v220, 16, v2\n"
    "v_and_b32 v221, s17, v2\n"
    "v_lshlrev_b32 v222, 16, v3\n"
    "v_and_b32 v223, s17, v3\n"
    "v_pk_mul_f32 v[216:217], v[216:217], s[50:51] op_sel_hi:[1,0]\n"
    "v_pk_mul_f32 v[218:219], v[218:219], s[50:51] op_sel_hi:[1,0]\n"
    "v_pk_mul_f32 v[220:221], v[220:221], s[50:51] op_sel_hi:[1,0]\n"
    "v_pk_mul_f32 v[222:223], v[222:223], s[50:51] op_sel_hi:[1,0]\n"
    "v_cvt_pk_bf16_f32 v0, v216, v217\n"
    "v_cvt_pk_bf16_f32 v1, v218, v219\n"
    "v_cvt_pk_bf16_f32 v2, v220, v221\n"
    "v_cvt_pk_bf16_f32 v3, v222, v223\n"
    "v_lshlrev_b32 v224, 16, v4\n"
    "v_and_b32 v225, s17, v4\n"
    "v_lshlrev_b32 v226, 16, v5\n"
    "v_and_b32 v227, s17, v5\n"
    "v_lshlrev_b32 v228, 16, v6\n"
    "v_and_b32 v229, s17, v6\n"
    "v_lshlrev_b32 v230, 16, v7\n"
    "v_and_b32 v231, s17, v7\n"
    "v_pk_mul_f32 v[224:225], v[224:225], s[50:51] op_sel_hi:[1,0]\n"
    "v_pk_mul_f32 v[226:227], v[226:227], s[50:51] op_sel_hi:[1,0]\n"
    "v_pk_mul_f32 v[228:229], v[228:229], s[50:51] op_sel_hi:[1,0]\n"
    "v_pk_mul_f32 v[230:231], v[230:231], s[50:51] op_sel_hi:[1,0]\n"
    "v_cvt_pk_bf16_f32 v4, v224, v225\n"
    "v_cvt_pk_bf16_f32 v5, v226, v227\n"
    "v_cvt_pk_bf16_f32 v6, v228, v229\n"
    "v_cvt_pk_bf16_f32 v7, v230, v231\n"
    "ds_write_b128 v242, v[0:3] offset:0\n"
    "ds_write_b128 v242, v[4:7] offset:1024\n"
    "v_lshlrev_b32 v216, 16, v8\n"
    "v_and_b32 v217, s17, v8\n"
    "v_lshlrev_b32 v218, 16, v9\n"
    "v_and_b32 v219, s17, v9\n"
    "v_lshlrev_b32 v220, 16, v10\n"
    "v_and_b32 v221, s17, v10\n"
    "v_lshlrev_b32 v222, 16, v11\n"
    "v_and_b32 v223, s17, v11\n"
    "v_pk_mul_f32 v[216:217], v[216:217], s[50:51] op_sel_hi:[1,0]\n"
    "v_pk_mul_f32 v[218:219], v[218:219], s[50:51] op_sel_hi:[1,0]\n"
    "v_pk_mul_f32 v[220:221], v[220:221], s[50:51] op_sel_hi:[1,0]\n"
    "v_pk_mul_f32 v[222:223], v[222:223], s[50:51] op_sel_hi:[1,0]\n"
    "v_cvt_pk_bf16_f32 v8, v216, v217\n"
    "v_cvt_pk_bf16_f32 v9, v218, v219\n"
    "v_cvt_pk_bf16_f32 v10, v220, v221\n"
    "v_cvt_pk_bf16_f32 v11, v222, v223\n"
    "v_lshlrev_b32 v224, 16, v12\n"
    "v_and_b32 v225, s17, v12\n"
    "v_lshlrev_b32 v226, 16, v13\n"
    "v_and_b32 v227, s17, v13\n"
    "v_lshlrev_b32 v228, 16, v14\n"
    "v_and_b32 v229, s17, v14\n"
    "v_lshlrev_b32 v230, 16, v15\n"
    "v_and_b32 v231, s17, v15\n"
    "v_pk_mul_f32 v[224:225], v[224:225], s[50:51] op_sel_hi:[1,0]\n"
    "v_pk_mul_f32 v[226:227], v[226:227], s[50:51] op_sel_hi:[1,0]\n"
    "v_pk_mul_f32 v[228:229], v[228:229], s[50:51] op_sel_hi:[1,0]\n"
    "v_pk_mul_f32 v[230:231], v[230:231], s[50:51] op_sel_hi:[1,0]\n"
    "v_cvt_pk_bf16_f32 v12, v224, v225\n"
    "v_cvt_pk_bf16_f32 v13, v226, v227\n"
    "v_cvt_pk_bf16_f32 v14, v228, v229\n"
    "v_cvt_pk_bf16_f32 v15, v230, v231\n"
    "ds_read_b128 v[0:3], v243 offset:0\n"
    "ds_read_b128 v[4:7], v232 offset:0\n"
    "ds_write_b128 v242, v[8:11] offset:2048\n"
    "ds_write_b128 v242, v[12:15] offset:3072\n"
    "v_lshlrev_b32 v216, 16, v16\n"
    "v_and_b32 v217, s17, v16\n"
    "v_lshlrev_b32 v218, 16, v17\n"
    "v_and_b32 v219, s17, v17\n"
    "v_lshlrev_b32 v220, 16, v18\n"
    "v_and_b32 v221, s17, v18\n"
    "v_lshlrev_b32 v222, 16, v19\n"
    "v_and_b32 v223, s17, v19\n"
    "v_pk_mul_f32 v[216:217], v[216:217], s[50:51] op_sel_hi:[1,0]\n"
    "v_pk_mul_f32 v[218:219], v[218:219], s[50:51] op_sel_hi:[1,0]\n"
    "v_pk_mul_f32 v[220:221], v[220:221], s[50:51] op_sel_hi:[1,0]\n"
    "v_pk_mul_f32 v[222:223], v[222:223], s[50:51] op_sel_hi:[1,0]\n"
    "v_cvt_pk_bf16_f32 v16, v216, v217\n"
    "v_cvt_pk_bf16_f32 v17, v218, v219\n"
    "v_cvt_pk_bf16_f32 v18, v220, v221\n"
    "v_cvt_pk_bf16_f32 v19, v222, v223\n"
    "v_lshlrev_b32 v224, 16, v20\n"
    "v_and_b32 v225, s17, v20\n"
    "v_lshlrev_b32 v226, 16, v21\n"
    "v_and_b32 v227, s17, v21\n"
    "v_lshlrev_b32 v228, 16, v22\n"
    "v_and_b32 v229, s17, v22\n"
    "v_lshlrev_b32 v230, 16, v23\n"
    "v_and_b32 v231, s17, v23\n"
    "v_pk_mul_f32 v[224:225], v[224:225], s[50:51] op_sel_hi:[1,0]\n"
    "v_pk_mul_f32 v[226:227], v[226:227], s[50:51] op_sel_hi:[1,0]\n"
    "v_pk_mul_f32 v[228:229], v[228:229], s[50:51] op_sel_hi:[1,0]\n"
    "v_pk_mul_f32 v[230:231], v[230:231], s[50:51] op_sel_hi:[1,0]\n"
    "v_cvt_pk_bf16_f32 v20, v224, v225\n"
    "v_cvt_pk_bf16_f32 v21, v226, v227\n"
    "v_cvt_pk_bf16_f32 v22, v228, v229\n"
    "v_cvt_pk_bf16_f32 v23, v230, v231\n"
    "ds_read_b128 v[8:11], v243 offset:2048\n"
    "ds_read_b128 v[12:15], v232 offset:2048\n"
    "ds_write_b128 v242, v[16:19] offset:0\n"
    "ds_write_b128 v242, v[20:23] offset:1024\n"
    "s_waitcnt vmcnt(6)\n"
    "s_barrier\n"
    "ds_read_b128 a[224:227], v235 offset:0\n"
    "ds_read_b128 a[228:231], v235 offset:512\n"
    "ds_read_b128 a[232:235], v235 offset:1024\n"
    "ds_read_b128 a[236:239], v235 offset:1536\n"
    "ds_read_b128 a[240:243], v235 offset:2048\n"
    "ds_read_b128 a[244:247], v235 offset:2560\n"
    "ds_read_b128 a[248:251], v235 offset:6144\n"
    "ds_read_b128 a[252:255], v235 offset:6656\n"
    "ds_read_b128 a[192:195], v235 offset:7168\n"
    "ds_read_b128 a[196:199], v235 offset:7680\n"
    "ds_read_b128 a[200:203], v235 offset:8192\n"
    "ds_read_b128 a[204:207], v235 offset:8704\n"
    "s_mul_i32 s62, s53, 3072\n"
    "s_sub_u32 s63, s62, s85\n"
    "s_add_u32 s62, s62, s56\n"
    "v_add_u32 v128, s63, v237\n"
    "v_add_u32 v129, s63, v238\n"
    "v_add_u32 v130, s63, v239\n"
    "s_add_u32 m0, s62, 73728\n"
    "s_add_u32 s45, s84, 0\n"
    "buffer_load_dwordx4 v128, s[24:27], s45 offen offset:0 lds\n"
    "v_lshlrev_b32 v216, 16, v24\n"
    "v_and_b32 v217, s17, v24\n"
    "v_lshlrev_b32 v218, 16, v25\n"
    "v_and_b32 v219, s17, v25\n"
    "v_lshlrev_b32 v220, 16, v26\n"
    "v_and_b32 v221, s17, v26\n"
    "v_lshlrev_b32 v222, 16, v27\n"
    "v_and_b32 v223, s17, v27\n"
    "v_pk_mul_f32 v[216:217], v[216:217], s[50:51] op_sel_hi:[1,0]\n"
    "v_pk_mul_f32 v[218:219], v[218:219], s[50:51] op_sel_hi:[1,0]\n"
    "v_pk_mul_f32 v[220:221], v[220:221], s[50:51] op_sel_hi:[1,0]\n"
    "v_pk_mul_f32 v[222:223], v[222:223], s[50:51] op_sel_hi:[1,0]\n"
    "v_cvt_pk_bf16_f32 v24, v216, v217\n"
    "v_cvt_pk_bf16_f32 v25, v218, v219\n"
    "v_cvt_pk_bf16_f32 v26, v220, v221\n"
    "v_cvt_pk_bf16_f32 v27, v222, v223\n"
    "v_lshlrev_b32 v224, 16, v28\n"
    "v_and_b32 v225, s17, v28\n"
    "v_lshlrev_b32 v226, 16, v29\n"
    "v_and_b32 v227, s17, v29\n"
    "v_lshlrev_b32 v228, 16, v30\n"
    "v_and_b32 v229, s17, v30\n"
    "v_lshlrev_b32 v230, 16, v31\n"
    "v_and_b32 v231, s17, v31\n"
    "v_pk_mul_f32 v[224:225], v[224:225], s[50:51] op_sel_hi:[1,0]\n"
    "v_pk_mul_f32 v[226:227], v[226:227], s[50:51] op_sel_hi:[1,0]\n"
    "v_pk_mul_f32 v[228:229], v[228:229], s[50:51] op_sel_hi:[1,0]\n"
    "v_pk_mul_f32 v[230:231], v[230:231], s[50:51] op_sel_hi:[1,0]\n"
    "v_cvt_pk_bf16_f32 v28, v224, v225\n"
    "v_cvt_pk_bf16_f32 v29, v226, v227\n"
    "v_cvt_pk_bf16_f32 v30, v228, v229\n"
    "v_cvt_pk_bf16_f32 v31, v230, v231\n"
    "ds_read_b128 v[16:19], v243 offset:0\n"
    "ds_read_b128 v[20:23], v232 offset:0\n"
    "ds_write_b128 v242, v[24:27] offset:2048\n"
    "ds_write_b128 v242, v[28:31] offset:3072\n"
    "buffer_load_dwordx4 v129, s[24:27], s45 offen offset:1024 lds\n"
    "v_lshlrev_b32 v216, 16, v32\n"
    "v_and_b32 v217, s17, v32\n"
    "v_lshlrev_b32 v218, 16, v33\n"
    "v_and_b32 v219, s17, v33\n"
    "v_lshlrev_b32 v220, 16, v34\n"
    "v_and_b32 v221, s17, v34\n"
    "v_lshlrev_b32 v222, 16, v35\n"
    "v_and_b32 v223, s17, v35\n"
    "v_pk_mul_f32 v[216:217], v[216:217], s[50:51] op_sel_hi:[1,0]\n"
    "v_pk_mul_f32 v[218:219], v[218:219], s[50:51] op_sel_hi:[1,0]\n"
    "v_pk_mul_f32 v[220:221], v[220:221], s[50:51] op_sel_hi:[1,0]\n"
    "v_pk_mul_f32 v[222:223], v[222:223], s[50:51] op_sel_hi:[1,0]\n"
    "s_waitcnt lgkmcnt(4)\n"
    "v_mfma_f32_16x16x32_bf16 v[96:99], a[224:227], v[0:3], 0\n"
    "v_cvt_pk_bf16_f32 v32, v216, v217\n"
    "v_cvt_pk_bf16_f32 v33, v218, v219\n"
    "v_mfma_f32_16x16x32_bf16 v[96:99], a[228:231], v[4:7], v[96:99]\n"
    "v_cvt_pk_bf16_f32 v34, v220, v221\n"
    "v_cvt_pk_bf16_f32 v35, v222, v223\n"
    "v_mfma_f32_16x16x32_bf16 v[96:99], a[232:235], v[8:11], v[96:99]\n"
    "v_lshlrev_b32 v224, 16, v36\n"
    "v_and_b32 v225, s17, v36\n"
    "v_mfma_f32_16x16x32_bf16 v[96:99], a[236:239], v[12:15], v[96:99]\n"
    "v_lshlrev_b32 v226, 16, v37\n"
    "v_and_b32 v227, s17, v37\n"
    "v_lshlrev_b32 v228, 16, v38\n"
    "v_and_b32 v229, s17, v38\n"
    "v_lshlrev_b32 v230, 16, v39\n"
    "v_and_b32 v231, s17, v39\n"
    "v_pk_mul_f32 v[224:225], v[224:225], s[50:51] op_sel_hi:[1,0]\n"
    "v_pk_mul_f32 v[226:227], v[226:227], s[50:51] op_sel_hi:[1,0]\n"
    "v_pk_mul_f32 v[228:229], v[228:229], s[50:51] op_sel_hi:[1,0]\n"
    "v_pk_mul_f32 v[230:231], v[230:231], s[50:51] op_sel_hi:[1,0]\n"
    "s_waitcnt lgkmcnt(1)\n"
    "v_mfma_f32_16x16x32_bf16 v[96:99], a[240:243], v[16:19], v[96:99]\n"
    "v_cvt_pk_bf16_f32 v36, v224, v225\n"
    "v_cvt_pk_bf16_f32 v37, v226, v227\n"
    "v_mfma_f32_16x16x32_bf16 v[96:99], a[244:247], v[20:23], v[96:99]\n"
    "v_cvt_pk_bf16_f32 v38, v228, v229\n"
    "v_cvt_pk_bf16_f32 v39, v230, v231\n"
    "ds_read_b128 v[24:27], v243 offset:2048\n"
    "ds_read_b128 v[28:31], v232 offset:2048\n"
    "ds_write_b128 v242, v[32:35] offset:0\n"
    "ds_write_b128 v242, v[36:39] offset:1024\n"
    "buffer_load_dwordx4 v130, s[24:27], s45 offen offset:2048 lds\n"
    "v_lshlrev_b32 v216, 16, v40\n"
    "v_and_b32 v217, s17, v40\n"
    "v_lshlrev_b32 v218, 16, v41\n"
    "v_and_b32 v219, s17, v41\n"
    "v_lshlrev_b32 v220, 16, v42\n"
    "v_and_b32 v221, s17, v42\n"
    "v_lshlrev_b32 v222, 16, v43\n"
    "v_and_b32 v223, s17, v43\n"
    "v_pk_mul_f32 v[216:217], v[216:217], s[50:51] op_sel_hi:[1,0]\n"
    "v_pk_mul_f32 v[218:219], v[218:219], s[50:51] op_sel_hi:[1,0]\n"
    "v_pk_mul_f32 v[220:221], v[220:221], s[50:51] op_sel_hi:[1,0]\n"
    "v_pk_mul_f32 v[222:223], v[222:223], s[50:51] op_sel_hi:[1,0]\n"
    "v_mfma_f32_16x16x32_bf16 v[100:103], a[248:251], v[0:3], 0\n"
    "v_cvt_pk_bf16_f32 v40, v216, v217\n"
    "v_cvt_pk_bf16_f32 v41, v218, v219\n"
    "v_mfma_f32_16x16x32_bf16 v[100:103], a[252:255], v[4:7], v[100:103]\n"
    "v_cvt_pk_bf16_f32 v42, v220, v221\n"
    "v_cvt_pk_bf16_f32 v43, v222, v223\n"
    "v_mfma_f32_16x16x32_bf16 v[100:103], a[192:195], v[8:11], v[100:103]\n"
    "v_lshlrev_b32 v224, 16, v44\n"
    "v_and_b32 v225, s17, v44\n"
    "v_mfma_f32_16x16x32_bf16 v[100:103], a[196:199], v[12:15], v[100:103]\n"
    "v_lshlrev_b32 v226, 16, v45\n"
    "v_and_b32 v227, s17, v45\n"
    "v_lshlrev_b32 v228, 16, v46\n"
    "v_and_b32 v229, s17, v46\n"
    "v_lshlrev_b32 v230, 16, v47\n"
    "v_and_b32 v231, s17, v47\n"
    "v_pk_mul_f32 v[224:225], v[224:225], s[50:51] op_sel_hi:[1,0]\n"
    "v_pk_mul_f32 v[226:227], v[226:227], s[50:51] op_sel_hi:[1,0]\n"
    "v_pk_mul_f32 v[228:229], v[228:229], s[50:51] op_sel_hi:[1,0]\n"
    "v_pk_mul_f32 v[230:231], v[230:231], s[50:51] op_sel_hi:[1,0]\n"
    "v_mfma_f32_16x16x32_bf16 v[100:103], a[200:203], v[16:19], v[100:103]\n"
    "v_cvt_pk_bf16_f32 v44, v224, v225\n"
    "v_cvt_pk_bf16_f32 v45, v226, v227\n"
    "v_mfma_f32_16x16x32_bf16 v[100:103], a[204:207], v[20:23], v[100:103]\n"
    "v_cvt_pk_bf16_f32 v46, v228, v229\n"
    "v_cvt_pk_bf16_f32 v47, v230, v231\n"
    "ds_read_b128 v[32:35], v243 offset:0\n"
    "ds_read_b128 v[36:39], v232 offset:0\n"
    "ds_write_b128 v242, v[40:43] offset:2048\n"
    "ds_write_b128 v242, v[44:47] offset:3072\n"
    "ds_read_b128 v[40:43], v243 offset:2048\n"
    "ds_read_b128 v[44:47], v232 offset:2048\n"
    "v_max3_f32 v244, v96, v97, v98\n"
    "v_max3_f32 v244, v244, v99, v100\n"
    "v_max3_f32 v244, v244, v101, v102\n"
    "v_max_f32 v244, v244, v103\n"
    "v_mov_b32 v245, v244\n"
    "s_nop 1\n"
    "v_permlane32_swap_b32 v244, v245\n"
    "v_max_f32 v244, v244, v245\n"
    "v_mov_b32 v245, v244\n"
    "s_waitcnt lgkmcnt(6)\n"
    "v_mfma_f32_16x16x32_bf16 v[104:107], a[224:227], v[24:27], 0\n"
    "v_permlane16_swap_b32 v244, v245\n"
    "v_max_f32 v244, v244, v245\n"
    "v_sub_f32 v192, 0, v244\n"
    "v_mfma_f32_16x16x32_bf16 v[104:107], a[228:231], v[28:31], v[104:107]\n"
    "v_sub_f32 v193, 0, v244\n"
    "v_sub_f32 v194, 0, v244\n"
    "v_sub_f32 v195, 0, v244\n"
    "s_waitcnt lgkmcnt(5)\n"
    "v_mfma_f32_16x16x32_bf16 v[104:107], a[232:235], v[32:35], v[104:107]\n"
    "v_sub_f32 v96, v96, v244\n"
    "v_sub_f32 v97, v97, v244\n"
    "v_sub_f32 v98, v98, v244\n"
    "s_waitcnt lgkmcnt(4)\n"
    "v_mfma_f32_16x16x32_bf16 v[104:107], a[236:239], v[36:39], v[104:107]\n"
    "v_sub_f32 v99, v99, v244\n"
    "v_sub_f32 v100, v100, v244\n"
    "v_sub_f32 v101, v101, v244\n"
    "s_waitcnt lgkmcnt(1)\n"
    "v_mfma_f32_16x16x32_bf16 v[104:107], a[240:243], v[40:43], v[104:107]\n"
    "v_sub_f32 v102, v102, v244\n"
    "v_sub_f32 v103, v103, v244\n"
    "v_exp_f32 v216, v96\n"
    "s_waitcnt lgkmcnt(0)\n"
    "v_mfma_f32_16x16x32_bf16 v[104:107], a[244:247], v[44:47], v[104:107]\n"
    "v_exp_f32 v217, v97\n"
    "v_exp_f32 v218, v98\n"
    "v_exp_f32 v219, v99\n"
    "v_mfma_f32_16x16x32_bf16 v[108:111], a[248:251], v[24:27], 0\n"
    "v_cvt_pk_bf16_f32 v160, v216, v217\n"
    "v_cvt_pk_bf16_f32 v161, v218, v219\n"
    "v_add_f32 v212, v216, v217\n"
    "v_mfma_f32_16x16x32_bf16 v[108:111], a[252:255], v[28:31], v[108:111]\n"
    "v_add_f32 v212, v212, v218\n"
    "v_add_f32 v212, v212, v219\n"
    "v_exp_f32 v216, v100\n"
    "v_mfma_f32_16x16x32_bf16 v[108:111], a[192:195], v[32:35], v[108:111]\n"
    "v_exp_f32 v217, v101\n"
    "v_exp_f32 v218, v102\n"
    "v_exp_f32 v219, v103\n"
    "v_mfma_f32_16x16x32_bf16 v[108:111], a[196:199], v[36:39], v[108:111]\n"
    "v_cvt_pk_bf16_f32 v162, v216, v217\n"
    "v_cvt_pk_bf16_f32 v163, v218, v219\n"
    "v_add_f32 v212, v212, v216\n"
    "v_mfma_f32_16x16x32_bf16 v[108:111], a[200:203], v[40:43], v[108:111]\n"
    "v_add_f32 v212, v212, v217\n"
    "v_add_f32 v212, v212, v218\n"
    "v_add_f32 v212, v212, v219\n"
    "v_mfma_f32_16x16x32_bf16 v[108:111], a[204:207], v[44:47], v[108:111]\n"
    "v_max3_f32 v246, v104, v105, v106\n"
    "s_nop 6\n"
    "v_max3_f32 v246, v246, v107, v108\n"
    "v_max3_f32 v246, v246, v109, v110\n"
    "v_max_f32 v246, v246, v111\n"
    "v_mov_b32 v247, v246\n"
    "s_nop 1\n"
    "v_permlane32_swap_b32 v246, v247\n"
    "v_max_f32 v246, v246, v247\n"
    "v_mov_b32 v247, v246\n"
    "s_nop 1\n"
    "v_permlane16_swap_b32 v246, v247\n"
    "v_max_f32 v246, v246, v247\n"
    "v_sub_f32 v196, 0, v246\n"
    "v_sub_f32 v197, 0, v246\n"
    "v_sub_f32 v198, 0, v246\n"
    "v_sub_f32 v199, 0, v246\n"
    "v_sub_f32 v104, v104, v246\n"
    "v_sub_f32 v105, v105, v246\n"
    "v_sub_f32 v106, v106, v246\n"
    "v_sub_f32 v107, v107, v246\n"
    "v_sub_f32 v108, v108, v246\n"
    "v_sub_f32 v109, v109, v246\n"
    "v_sub_f32 v110, v110, v246\n"
    "v_sub_f32 v111, v111, v246\n"
    "v_exp_f32 v220, v104\n"
    "v_exp_f32 v221, v105\n"
    "v_exp_f32 v222, v106\n"
    "v_exp_f32 v223, v107\n"
    "v_cvt_pk_bf16_f32 v164, v220, v221\n"
    "v_cvt_pk_bf16_f32 v165, v222, v223\n"
    "v_add_f32 v213, v220, v221\n"
    "v_add_f32 v213, v213, v222\n"
    "v_add_f32 v213, v213, v223\n"
    "v_exp_f32 v220, v108\n"
    "v_exp_f32 v221, v109\n"
    "v_exp_f32 v222, v110\n"
    "v_exp_f32 v223, v111\n"
    "v_cvt_pk_bf16_f32 v166, v220, v221\n"
    "v_cvt_pk_bf16_f32 v167, v222, v223\n"
    "v_add_f32 v213, v213, v220\n"
    "v_add_f32 v213, v213, v221\n"
    "v_add_f32 v213, v213, v222\n"
    "v_add_f32 v213, v213, v223\n"
    "s_waitcnt vmcnt(3)\n"
    "s_add_u32 m0, s62, 24576\n"
    "s_add_u32 s45, s44, 0\n"
    "buffer_load_dwordx4 v128, s[20:23], s45 offen offset:0 lds\n"
    "v_lshlrev_b32 v216, 16, v48\n"
    "v_and_b32 v217, s17, v48\n"
    "v_lshlrev_b32 v218, 16, v49\n"
    "v_and_b32 v219, s17, v49\n"
    "v_lshlrev_b32 v220, 16, v50\n"
    "v_and_b32 v221, s17, v50\n"
    "v_lshlrev_b32 v222, 16, v51\n"
    "v_and_b32 v223, s17, v51\n"
    "v_pk_mul_f32 v[216:217], v[216:217], s[50:51] op_sel_hi:[1,0]\n"
    "v_pk_mul_f32 v[218:219], v[218:219], s[50:51] op_sel_hi:[1,0]\n"
    "v_pk_mul_f32 v[220:221], v[220:221], s[50:51] op_sel_hi:[1,0]\n"
    "buffer_load_dwordx4 v129, s[20:23], s45 offen offset:1024 lds\n"
    "v_pk_mul_f32 v[222:223], v[222:223], s[50:51] op_sel_hi:[1,0]\n"
    "v_cvt_pk_bf16_f32 v48, v216, v217\n"
    "v_cvt_pk_bf16_f32 v49, v218, v219\n"
    "v_cvt_pk_bf16_f32 v50, v220, v221\n"
    "v_cvt_pk_bf16_f32 v51, v222, v223\n"
    "v_lshlrev_b32 v224, 16, v52\n"
    "v_and_b32 v225, s17, v52\n"
    "v_lshlrev_b32 v226, 16, v53\n"
    "v_and_b32 v227, s17, v53\n"
    "v_lshlrev_b32 v228, 16, v54\n"
    "v_and_b32 v229, s17, v54\n"
    "v_lshlrev_b32 v230, 16, v55\n"
    "v_and_b32 v231, s17, v55\n"
    "buffer_load_dwordx4 v130, s[20:23], s45 offen offset:2048 lds\n"
    "v_pk_mul_f32 v[224:225], v[224:225], s[50:51] op_sel_hi:[1,0]\n"
    "v_pk_mul_f32 v[226:227], v[226:227], s[50:51] op_sel_hi:[1,0]\n"
    "v_pk_mul_f32 v[228:229], v[228:229], s[50:51] op_sel_hi:[1,0]\n"
    "v_pk_mul_f32 v[230:231], v[230:231], s[50:51] op_sel_hi:[1,0]\n"
    "v_cvt_pk_bf16_f32 v52, v224, v225\n"
    "v_cvt_pk_bf16_f32 v53, v226, v227\n"
    "v_cvt_pk_bf16_f32 v54, v228, v229\n"
    "v_cvt_pk_bf16_f32 v55, v230, v231\n"
    "ds_write_b128 v242, v[48:51] offset:0\n"
    "ds_write_b128 v242, v[52:55] offset:1024\n"
    "s_add_u32 m0, s62, 86016\n"
    "s_add_u32 s45, s84, 12288\n"
    "buffer_load_dwordx4 v128, s[24:27], s45 offen offset:0 lds\n"
    "v_lshlrev_b32 v216, 16, v56\n"
    "v_and_b32 v217, s17, v56\n"
    "v_lshlrev_b32 v218, 16, v57\n"
    "v_and_b32 v219, s17, v57\n"
    "v_lshlrev_b32 v220, 16, v58\n"
    "v_and_b32 v221, s17, v58\n"
    "v_lshlrev_b32 v222, 16, v59\n"
    "v_and_b32 v223, s17, v59\n"
    "v_pk_mul_f32 v[216:217], v[216:217], s[50:51] op_sel_hi:[1,0]\n"
    "v_pk_mul_f32 v[218:219], v[218:219], s[50:51] op_sel_hi:[1,0]\n"
    "v_pk_mul_f32 v[220:221], v[220:221], s[50:51] op_sel_hi:[1,0]\n"
    "buffer_load_dwordx4 v129, s[24:27], s45 offen offset:1024 lds\n"
    "v_pk_mul_f32 v[222:223], v[222:223], s[50:51] op_sel_hi:[1,0]\n"
    "v_cvt_pk_bf16_f32 v56, v216, v217\n"
    "v_cvt_pk_bf16_f32 v57, v218, v219\n"
    "v_cvt_pk_bf16_f32 v58, v220, v221\n"
    "v_cvt_pk_bf16_f32 v59, v222, v223\n"
    "v_lshlrev_b32 v224, 16, v60\n"
    "v_and_b32 v225, s17, v60\n"
    "v_lshlrev_b32 v226, 16, v61\n"
    "v_and_b32 v227, s17, v61\n"
    "v_lshlrev_b32 v228, 16, v62\n"
    "v_and_b32 v229, s17, v62\n"
    "v_lshlrev_b32 v230, 16, v63\n"
    "v_and_b32 v231, s17, v63\n"
    "buffer_load_dwordx4 v130, s[24:27], s45 offen offset:2048 lds\n"
    "v_pk_mul_f32 v[224:225], v[224:225], s[50:51] op_sel_hi:[1,0]\n"
    "v_pk_mul_f32 v[226:227], v[226:227], s[50:51] op_sel_hi:[1,0]\n"
    "v_pk_mul_f32 v[228:229], v[228:229], s[50:51] op_sel_hi:[1,0]\n"
    "v_pk_mul_f32 v[230:231], v[230:231], s[50:51] op_sel_hi:[1,0]\n"
    "v_cvt_pk_bf16_f32 v60, v224, v225\n"
    "v_cvt_pk_bf16_f32 v61, v226, v227\n"
    "v_cvt_pk_bf16_f32 v62, v228, v229\n"
    "v_cvt_pk_bf16_f32 v63, v230, v231\n"
    "ds_read_b128 v[48:51], v243 offset:0\n"
    "ds_read_b128 v[52:55], v232 offset:0\n"
    "ds_write_b128 v242, v[56:59] offset:2048\n"
    "ds_write_b128 v242, v[60:63] offset:3072\n"
    "s_add_u32 m0, s62, 36864\n"
    "s_add_u32 s45, s44, 12288\n"
    "buffer_load_dwordx4 v128, s[20:23], s45 offen offset:0 lds\n"
    "v_lshlrev_b32 v216, 16, v64\n"
    "v_and_b32 v217, s17, v64\n"
    "v_lshlrev_b32 v218, 16, v65\n"
    "v_and_b32 v219, s17, v65\n"
    "v_lshlrev_b32 v220, 16, v66\n"
    "v_and_b32 v221, s17, v66\n"
    "v_lshlrev_b32 v222, 16, v67\n"
    "v_and_b32 v223, s17, v67\n"
    "v_pk_mul_f32 v[216:217], v[216:217], s[50:51] op_sel_hi:[1,0]\n"
    "v_pk_mul_f32 v[218:219], v[218:219], s[50:51] op_sel_hi:[1,0]\n"
    "v_pk_mul_f32 v[220:221], v[220:221], s[50:51] op_sel_hi:[1,0]\n"
    "buffer_load_dwordx4 v129, s[20:23], s45 offen offset:1024 lds\n"
    "v_pk_mul_f32 v[222:223], v[222:223], s[50:51] op_sel_hi:[1,0]\n"
    "v_cvt_pk_bf16_f32 v64, v216, v217\n"
    "v_cvt_pk_bf16_f32 v65, v218, v219\n"
    "v_cvt_pk_bf16_f32 v66, v220, v221\n"
    "v_cvt_pk_bf16_f32 v67, v222, v223\n"
    "v_lshlrev_b32 v224, 16, v68\n"
    "v_and_b32 v225, s17, v68\n"
    "v_lshlrev_b32 v226, 16, v69\n"
    "v_and_b32 v227, s17, v69\n"
    "v_lshlrev_b32 v228, 16, v70\n"
    "v_and_b32 v229, s17, v70\n"
    "v_lshlrev_b32 v230, 16, v71\n"
    "v_and_b32 v231, s17, v71\n"
    "buffer_load_dwordx4 v130, s[20:23], s45 offen offset:2048 lds\n"
    "s_mov_b32 s84, s44\n"
    "s_add_u32 s87, s87, 1\n"
    "v_readlane_b32 s44, v252, s87\n"
    "s_mov_b32 s40, 49152\n"
    "s_mov_b32 s42, 24576\n"
    "v_pk_mul_f32 v[224:225], v[224:225], s[50:51] op_sel_hi:[1,0]\n"
    "v_pk_mul_f32 v[226:227], v[226:227], s[50:51] op_sel_hi:[1,0]\n"
    "v_pk_mul_f32 v[228:229], v[228:229], s[50:51] op_sel_hi:[1,0]\n"
    "v_pk_mul_f32 v[230:231], v[230:231], s[50:51] op_sel_hi:[1,0]\n"
    "v_cvt_pk_bf16_f32 v68, v224, v225\n"
    "v_cvt_pk_bf16_f32 v69, v226, v227\n"
    "v_cvt_pk_bf16_f32 v70, v228, v229\n"
    "v_cvt_pk_bf16_f32 v71, v230, v231\n"
    "ds_read_b128 v[56:59], v243 offset:2048\n"
    "ds_read_b128 v[60:63], v232 offset:2048\n"
    "ds_write_b128 v242, v[64:67] offset:0\n"
    "ds_write_b128 v242, v[68:71] offset:1024\n"
    "s_add_u32 m0, s40, s47\n"
    "s_nop 0\n"
    "buffer_load_dwordx4 v237, s[20:23], s44 offen offset:0 lds\n"
    "ds_read_b128 a[208:211], v235 offset:14336\n"
    "ds_read_b128 a[212:215], v235 offset:14848\n"
    "ds_read_b128 a[216:219], v235 offset:18432\n"
    "ds_read_b128 a[220:223], v235 offset:18944\n"
    "v_add_u32 v236, s41, v234\n"
    "v_xor_b32 v241, 32, v236\n"
    "s_mov_b32 s43, s40\n"
    "s_mov_b32 s40, s41\n"
    "s_mov_b32 s41, s42\n"
    "s_mov_b32 s42, s43\n"
    "ds_read_b128 v[64:67], v243 offset:0\n"
    "ds_read_b128 v[68:71], v232 offset:0\n"
    "s_waitcnt lgkmcnt(10)\n"
    "v_mfma_f32_16x16x32_bf16 v[112:115], a[224:227], v[48:51], 0\n"
    "v_mfma_f32_16x16x32_bf16 v[112:115], a[228:231], v[52:55], v[112:115]\n"
    "buffer_load_dwordx4 v238, s[20:23], s44 offen offset:1024 lds\n"
    "s_waitcnt lgkmcnt(9)\n"
    "v_mfma_f32_16x16x32_bf16 v[112:115], a[232:235], v[56:59], v[112:115]\n"
    "s_waitcnt lgkmcnt(8)\n"
    "v_mfma_f32_16x16x32_bf16 v[112:115], a[236:239], v[60:63], v[112:115]\n"
    "s_waitcnt lgkmcnt(1)\n"
    "v_mfma_f32_16x16x32_bf16 v[112:115], a[240:243], v[64:67], v[112:115]\n"
    "s_waitcnt lgkmcnt(0)\n"
    "v_mfma_f32_16x16x32_bf16 v[112:115], a[244:247], v[68:71], v[112:115]\n"
    "v_mfma_f32_16x16x32_bf16 v[116:119], a[248:251], v[48:51], 0\n"
    "v_mfma_f32_16x16x32_bf16 v[116:119], a[252:255], v[52:55], v[116:119]\n"
    "v_mfma_f32_16x16x32_bf16 v[116:119], a[192:195], v[56:59], v[116:119]\n"
    "v_mfma_f32_16x16x32_bf16 v[116:119], a[196:199], v[60:63], v[116:119]\n"
    "v_mfma_f32_16x16x32_bf16 v[116:119], a[200:203], v[64:67], v[116:119]\n"
    "v_mfma_f32_16x16x32_bf16 v[116:119], a[204:207], v[68:71], v[116:119]\n"
    "ds_read_b128 a[224:227], v235 offset:19456\n"
    "ds_read_b128 a[228:231], v235 offset:19968\n"
    "ds_read_b128 a[232:235], v235 offset:20480\n"
    "ds_read_b128 a[236:239], v235 offset:20992\n"
    "ds_read_b128 a[192:195], v235 offset:12288\n"
    "ds_read_b128 a[196:199], v235 offset:12800\n"
    "buffer_load_dwordx4 v239, s[20:23], s44 offen offset:2048 lds\n"
    "ds_read_b128 a[200:203], v235 offset:13312\n"
    "ds_read_b128 a[204:207], v235 offset:13824\n"
    "v_max3_f32 v248, v112, v113, v114\n"
    "v_max3_f32 v248, v248, v115, v116\n"
    "v_max3_f32 v248, v248, v117, v118\n"
    "v_max_f32 v248, v248, v119\n"
    "v_mov_b32 v249, v248\n"
    "s_nop 1\n"
    "v_permlane32_swap_b32 v248, v249\n"
    "v_max_f32 v248, v248, v249\n"
    "v_mov_b32 v249, v248\n"
    "s_nop 1\n"
    "v_permlane16_swap_b32 v248, v249\n"
    "v_max_f32 v248, v248, v249\n"
    "s_waitcnt vmcnt(12)\n"
    "s_waitcnt lgkmcnt(0)\n"
    "s_branch LPEEL_3_%=\n"
    "LPRO1_3_%=:\n"
    "v_xor_b32 v232, 64, v243\n"
    "v_add_u32 v235, s41, v233\n"
    "s_waitcnt vmcnt(37)\n"
    "s_barrier\n"
    "ds_read_b128 a[224:227], v235 offset:0\n"
    "ds_read_b128 a[228:231], v235 offset:512\n"
    "ds_read_b128 a[232:235], v235 offset:1024\n"
    "ds_read_b128 a[236:239], v235 offset:1536\n"
    "ds_read_b128 a[240:243], v235 offset:2048\n"
    "ds_read_b128 a[244:247], v235 offset:2560\n"
    "ds_read_b128 a[248:251], v235 offset:6144\n"
    "ds_read_b128 a[252:255], v235 offset:6656\n"
    "ds_read_b128 a[192:195], v235 offset:7168\n"
    "ds_read_b128 a[196:199], v235 offset:7680\n"
    "ds_read_b128 a[200:203], v235 offset:8192\n"
    "ds_read_b128 a[204:207], v235 offset:8704\n"
    "v_lshlrev_b32 v216, 16, v0\n"
    "v_and_b32 v217, s17, v0\n"
    "v_lshlrev_b32 v218, 16, v1\n"
    "v_and_b32 v219, s17, v1\n"
    "v_lshlrev_b32 v220, 16, v2\n"
    "v_and_b32 v221, s17, v2\n"
    "v_lshlrev_b32 v222, 16, v3\n"
    "v_and_b32 v223, s17, v3\n"
    "v_pk_mul_f32 v[216:217], v[216:217], s[50:51] op_sel_hi:[1,0]\n"
    "v_pk_mul_f32 v[218:219], v[218:219], s[50:51] op_sel_hi:[1,0]\n"
    "v_pk_mul_f32 v[220:221], v[220:221], s[50:51] op_sel_hi:[1,0]\n"
    "v_pk_mul_f32 v[222:223], v[222:223], s[50:51] op_sel_hi:[1,0]\n"
    "v_cvt_pk_bf16_f32 v0, v216, v217\n"
    "v_cvt_pk_bf16_f32 v1, v218, v219\n"
    "v_cvt_pk_bf16_f32 v2, v220, v221\n"
    "v_cvt_pk_bf16_f32 v3, v222, v223\n"
    "v_lshlrev_b32 v224, 16, v4\n"
    "v_and_b32 v225, s17, v4\n"
    "v_lshlrev_b32 v226, 16, v5\n"
    "v_and_b32 v227, s17, v5\n"
    "v_lshlrev_b32 v228, 16, v6\n"
    "v_and_b32 v229, s17, v6\n"
    "v_lshlrev_b32 v230, 16, v7\n"
    "v_and_b32 v231, s17, v7\n"
    "v_pk_mul_f32 v[224:225], v[224:225], s[50:51] op_sel_hi:[1,0]\n"
    "v_pk_mul_f32 v[226:227], v[226:227], s[50:51] op_sel_hi:[1,0]\n"
    "v_pk_mul_f32 v[228:229], v[228:229], s[50:51] op_sel_hi:[1,0]\n"
    "v_pk_mul_f32 v[230:231], v[230:231], s[50:51] op_sel_hi:[1,0]\n"
    "v_cvt_pk_bf16_f32 v4, v224, v225\n"
    "v_cvt_pk_bf16_f32 v5, v226, v227\n"
    "v_cvt_pk_bf16_f32 v6, v228, v229\n"
    "v_cvt_pk_bf16_f32 v7, v230, v231\n"
    "ds_write_b128 v242, v[0:3] offset:0\n"
    "ds_write_b128 v242, v[4:7] offset:1024\n"
    "v_lshlrev_b32 v216, 16, v8\n"
    "v_and_b32 v217, s17, v8\n"
    "v_lshlrev_b32 v218, 16, v9\n"
    "v_and_b32 v219, s17, v9\n"
    "v_lshlrev_b32 v220, 16, v10\n"
    "v_and_b32 v221, s17, v10\n"
    "v_lshlrev_b32 v222, 16, v11\n"
    "v_and_b32 v223, s17, v11\n"
    "v_pk_mul_f32 v[216:217], v[216:217], s[50:51] op_sel_hi:[1,0]\n"
    "v_pk_mul_f32 v[218:219], v[218:219], s[50:51] op_sel_hi:[1,0]\n"
    "v_pk_mul_f32 v[220:221], v[220:221], s[50:51] op_sel_hi:[1,0]\n"
    "v_pk_mul_f32 v[222:223], v[222:223], s[50:51] op_sel_hi:[1,0]\n"
    "v_cvt_pk_bf16_f32 v8, v216, v217\n"
    "v_cvt_pk_bf16_f32 v9, v218, v219\n"
    "v_cvt_pk_bf16_f32 v10, v220, v221\n"
    "v_cvt_pk_bf16_f32 v11, v222, v223\n"
    "v_lshlrev_b32 v224, 16, v12\n"
    "v_and_b32 v225, s17, v12\n"
    "v_lshlrev_b32 v226, 16, v13\n"
    "v_and_b32 v227, s17, v13\n"
    "v_lshlrev_b32 v228, 16, v14\n"
    "v_and_b32 v229, s17, v14\n"
    "v_lshlrev_b32 v230, 16, v15\n"
    "v_and_b32 v231, s17, v15\n"
    "v_pk_mul_f32 v[224:225], v[224:225], s[50:51] op_sel_hi:[1,0]\n"
    "v_pk_mul_f32 v[226:227], v[226:227], s[50:51] op_sel_hi:[1,0]\n"
    "v_pk_mul_f32 v[228:229], v[228:229], s[50:51] op_sel_hi:[1,0]\n"
    "v_pk_mul_f32 v[230:231], v[230:231], s[50:51] op_sel_hi:[1,0]\n"
    "v_cvt_pk_bf16_f32 v12, v224, v225\n"
    "v_cvt_pk_bf16_f32 v13, v226, v227\n"
    "v_cvt_pk_bf16_f32 v14, v228, v229\n"
    "v_cvt_pk_bf16_f32 v15, v230, v231\n"
    "ds_read_b128 v[0:3], v243 offset:0\n"
    "ds_read_b128 v[4:7], v232 offset:0\n"
    "ds_write_b128 v242, v[8:11] offset:2048\n"
    "ds_write_b128 v242, v[12:15] offset:3072\n"
    "v_lshlrev_b32 v216, 16, v16\n"
    "v_and_b32 v217, s17, v16\n"
    "v_lshlrev_b32 v218, 16, v17\n"
    "v_and_b32 v219, s17, v17\n"
    "v_lshlrev_b32 v220, 16, v18\n"
    "v_and_b32 v221, s17, v18\n"
    "v_lshlrev_b32 v222, 16, v19\n"
    "v_and_b32 v223, s17, v19\n"
    "v_pk_mul_f32 v[216:217], v[216:217], s[50:51] op_sel_hi:[1,0]\n"
    "v_pk_mul_f32 v[218:219], v[218:219], s[50:51] op_sel_hi:[1,0]\n"
    "v_pk_mul_f32 v[220:221], v[220:221], s[50:51] op_sel_hi:[1,0]\n"
    "v_pk_mul_f32 v[222:223], v[222:223], s[50:51] op_sel_hi:[1,0]\n"
    "v_cvt_pk_bf16_f32 v16, v216, v217\n"
    "v_cvt_pk_bf16_f32 v17, v218, v219\n"
    "v_cvt_pk_bf16_f32 v18, v220, v221\n"
    "v_cvt_pk_bf16_f32 v19, v222, v223\n"
    "v_lshlrev_b32 v224, 16, v20\n"
    "v_and_b32 v225, s17, v20\n"
    "v_lshlrev_b32 v226, 16, v21\n"
    "v_and_b32 v227, s17, v21\n"
    "v_lshlrev_b32 v228, 16, v22\n"
    "v_and_b32 v229, s17, v22\n"
    "v_lshlrev_b32 v230, 16, v23\n"
    "v_and_b32 v231, s17, v23\n"
    "v_pk_mul_f32 v[224:225], v[224:225], s[50:51] op_sel_hi:[1,0]\n"
    "v_pk_mul_f32 v[226:227], v[226:227], s[50:51] op_sel_hi:[1,0]\n"
    "v_pk_mul_f32 v[228:229], v[228:229], s[50:51] op_sel_hi:[1,0]\n"
    "v_pk_mul_f32 v[230:231], v[230:231], s[50:51] op_sel_hi:[1,0]\n"
    "v_cvt_pk_bf16_f32 v20, v224, v225\n"
    "v_cvt_pk_bf16_f32 v21, v226, v227\n"
    "v_cvt_pk_bf16_f32 v22, v228, v229\n"
    "v_cvt_pk_bf16_f32 v23, v230, v231\n"
    "ds_read_b128 v[8:11], v243 offset:2048\n"
    "ds_read_b128 v[12:15], v232 offset:2048\n"
    "ds_write_b128 v242, v[16:19] offset:0\n"
    "ds_write_b128 v242, v[20:23] offset:1024\n"
    "s_waitcnt vmcnt(28)\n"
    "v_lshlrev_b32 v216, 16, v24\n"
    "v_and_b32 v217, s17, v24\n"
    "v_lshlrev_b32 v218, 16, v25\n"
    "v_and_b32 v219, s17, v25\n"
    "v_lshlrev_b32 v220, 16, v26\n"
    "v_and_b32 v221, s17, v26\n"
    "v_lshlrev_b32 v222, 16, v27\n"
    "v_and_b32 v223, s17, v27\n"
    "v_pk_mul_f32 v[216:217], v[216:217], s[50:51] op_sel_hi:[1,0]\n"
    "v_pk_mul_f32 v[218:219], v[218:219], s[50:51] op_sel_hi:[1,0]\n"
    "v_pk_mul_f32 v[220:221], v[220:221], s[50:51] op_sel_hi:[1,0]\n"
    "v_pk_mul_f32 v[222:223], v[222:223], s[50:51] op_sel_hi:[1,0]\n"
    "v_cvt_pk_bf16_f32 v24, v216, v217\n"
    "v_cvt_pk_bf16_f32 v25, v218, v219\n"
    "v_cvt_pk_bf16_f32 v26, v220, v221\n"
    "v_cvt_pk_bf16_f32 v27, v222, v223\n"
    "v_lshlrev_b32 v224, 16, v28\n"
    "v_and_b32 v225, s17, v28\n"
    "v_lshlrev_b32 v226, 16, v29\n"
    "v_and_b32 v227, s17, v29\n"
    "v_lshlrev_b32 v228, 16, v30\n"
    "v_and_b32 v229, s17, v30\n"
    "v_lshlrev_b32 v230, 16, v31\n"
    "v_and_b32 v231, s17, v31\n"
    "v_pk_mul_f32 v[224:225], v[224:225], s[50:51] op_sel_hi:[1,0]\n"
    "v_pk_mul_f32 v[226:227], v[226:227], s[50:51] op_sel_hi:[1,0]\n"
    "v_pk_mul_f32 v[228:229], v[228:229], s[50:51] op_sel_hi:[1,0]\n"
    "v_pk_mul_f32 v[230:231], v[230:231], s[50:51] op_sel_hi:[1,0]\n"
    "v_cvt_pk_bf16_f32 v28, v224, v225\n"
    "v_cvt_pk_bf16_f32 v29, v226, v227\n"
    "v_cvt_pk_bf16_f32 v30, v228, v229\n"
    "v_cvt_pk_bf16_f32 v31, v230, v231\n"
    "ds_read_b128 v[16:19], v243 offset:0\n"
    "ds_read_b128 v[20:23], v232 offset:0\n"
    "ds_write_b128 v242, v[24:27] offset:2048\n"
    "ds_write_b128 v242, v[28:31] offset:3072\n"
    "v_lshlrev_b32 v216, 16, v32\n"
    "v_and_b32 v217, s17, v32\n"
    "v_lshlrev_b32 v218, 16, v33\n"
    "v_and_b32 v219, s17, v33\n"
    "v_lshlrev_b32 v220, 16, v34\n"
    "v_and_b32 v221, s17, v34\n"
    "v_lshlrev_b32 v222, 16, v35\n"
    "v_and_b32 v223, s17, v35\n"
    "v_pk_mul_f32 v[216:217], v[216:217], s[50:51] op_sel_hi:[1,0]\n"
    "v_pk_mul_f32 v[218:219], v[218:219], s[50:51] op_sel_hi:[1,0]\n"
    "v_pk_mul_f32 v[220:221], v[220:221], s[50:51] op_sel_hi:[1,0]\n"
    "v_pk_mul_f32 v[222:223], v[222:223], s[50:51] op_sel_hi:[1,0]\n"
    "s_waitcnt lgkmcnt(4)\n"
    "v_mfma_f32_16x16x32_bf16 v[96:99], a[224:227], v[0:3], 0\n"
    "v_cvt_pk_bf16_f32 v32, v216, v217\n"
    "v_cvt_pk_bf16_f32 v33, v218, v219\n"
    "v_mfma_f32_16x16x32_bf16 v[96:99], a[228:231], v[4:7], v[96:99]\n"
    "v_cvt_pk_bf16_f32 v34, v220, v221\n"
    "v_cvt_pk_bf16_f32 v35, v222, v223\n"
    "v_mfma_f32_16x16x32_bf16 v[96:99], a[232:235], v[8:11], v[96:99]\n"
    "v_lshlrev_b32 v224, 16, v36\n"
    "v_and_b32 v225, s17, v36\n"
    "v_mfma_f32_16x16x32_bf16 v[96:99], a[236:239], v[12:15], v[96:99]\n"
    "v_lshlrev_b32 v226, 16, v37\n"
    "v_and_b32 v227, s17, v37\n"
    "v_lshlrev_b32 v228, 16, v38\n"
    "v_and_b32 v229, s17, v38\n"
    "v_lshlrev_b32 v230, 16, v39\n"
    "v_and_b32 v231, s17, v39\n"
    "v_pk_mul_f32 v[224:225], v[224:225], s[50:51] op_sel_hi:[1,0]\n"
    "v_pk_mul_f32 v[226:227], v[226:227], s[50:51] op_sel_hi:[1,0]\n"
    "v_pk_mul_f32 v[228:229], v[228:229], s[50:51] op_sel_hi:[1,0]\n"
    "v_pk_mul_f32 v[230:231], v[230:231], s[50:51] op_sel_hi:[1,0]\n"
    "s_waitcnt lgkmcnt(2)\n"
    "v_mfma_f32_16x16x32_bf16 v[96:99], a[240:243], v[16:19], v[96:99]\n"
    "v_cvt_pk_bf16_f32 v36, v224, v225\n"
    "v_cvt_pk_bf16_f32 v37, v226, v227\n"
    "v_mfma_f32_16x16x32_bf16 v[96:99], a[244:247], v[20:23], v[96:99]\n"
    "v_cvt_pk_bf16_f32 v38, v228, v229\n"
    "v_cvt_pk_bf16_f32 v39, v230, v231\n"
    "ds_read_b128 v[24:27], v243 offset:2048\n"
    "ds_read_b128 v[28:31], v232 offset:2048\n"
    "ds_write_b128 v242, v[32:35] offset:0\n"
    "ds_write_b128 v242, v[36:39] offset:1024\n"
    "v_lshlrev_b32 v216, 16, v40\n"
    "v_and_b32 v217, s17, v40\n"
    "v_lshlrev_b32 v218, 16, v41\n"
    "v_and_b32 v219, s17, v41\n"
    "v_lshlrev_b32 v220, 16, v42\n"
    "v_and_b32 v221, s17, v42\n"
    "v_lshlrev_b32 v222, 16, v43\n"
    "v_and_b32 v223, s17, v43\n"
    "v_pk_mul_f32 v[216:217], v[216:217], s[50:51] op_sel_hi:[1,0]\n"
    "v_pk_mul_f32 v[218:219], v[218:219], s[50:51] op_sel_hi:[1,0]\n"
    "v_pk_mul_f32 v[220:221], v[220:221], s[50:51] op_sel_hi:[1,0]\n"
    "v_pk_mul_f32 v[222:223], v[222:223], s[50:51] op_sel_hi:[1,0]\n"
    "v_mfma_f32_16x16x32_bf16 v[100:103], a[248:251], v[0:3], 0\n"
    "v_cvt_pk_bf16_f32 v40, v216, v217\n"
    "v_cvt_pk_bf16_f32 v41, v218, v219\n"
    "v_mfma_f32_16x16x32_bf16 v[100:103], a[252:255], v[4:7], v[100:103]\n"
    "v_cvt_pk_bf16_f32 v42, v220, v221\n"
    "v_cvt_pk_bf16_f32 v43, v222, v223\n"
    "v_mfma_f32_16x16x32_bf16 v[100:103], a[192:195], v[8:11], v[100:103]\n"
    "v_lshlrev_b32 v224, 16, v44\n"
    "v_and_b32 v225, s17, v44\n"
    "v_mfma_f32_16x16x32_bf16 v[100:103], a[196:199], v[12:15], v[100:103]\n"
    "v_lshlrev_b32 v226, 16, v45\n"
    "v_and_b32 v227, s17, v45\n"
    "v_lshlrev_b32 v228, 16, v46\n"
    "v_and_b32 v229, s17, v46\n"
    "v_lshlrev_b32 v230, 16, v47\n"
    "v_and_b32 v231, s17, v47\n"
    "v_pk_mul_f32 v[224:225], v[224:225], s[50:51] op_sel_hi:[1,0]\n"
    "v_pk_mul_f32 v[226:227], v[226:227], s[50:51] op_sel_hi:[1,0]\n"
    "v_pk_mul_f32 v[228:229], v[228:229], s[50:51] op_sel_hi:[1,0]\n"
    "v_pk_mul_f32 v[230:231], v[230:231], s[50:51] op_sel_hi:[1,0]\n"
    "v_mfma_f32_16x16x32_bf16 v[100:103], a[200:203], v[16:19], v[100:103]\n"
    "v_cvt_pk_bf16_f32 v44, v224, v225\n"
    "v_cvt_pk_bf16_f32 v45, v226, v227\n"
    "v_mfma_f32_16x16x32_bf16 v[100:103], a[204:207], v[20:23], v[100:103]\n"
    "v_cvt_pk_bf16_f32 v46, v228, v229\n"
    "v_cvt_pk_bf16_f32 v47, v230, v231\n"
    "ds_read_b128 v[32:35], v243 offset:0\n"
    "ds_read_b128 v[36:39], v232 offset:0\n"
    "ds_write_b128 v242, v[40:43] offset:2048\n"
    "ds_write_b128 v242, v[44:47] offset:3072\n"
    "ds_read_b128 v[40:43], v243 offset:2048\n"
    "ds_read_b128 v[44:47], v232 offset:2048\n"
    "v_max3_f32 v244, v96, v97, v98\n"
    "v_max3_f32 v244, v244, v99, v100\n"
    "v_max3_f32 v244, v244, v101, v102\n"
    "v_max_f32 v244, v244, v103\n"
    "v_mov_b32 v245, v244\n"
    "s_nop 1\n"
    "v_permlane32_swap_b32 v244, v245\n"
    "v_max_f32 v244, v244, v245\n"
    "v_mov_b32 v245, v244\n"
    "s_waitcnt lgkmcnt(6)\n"
    "v_mfma_f32_16x16x32_bf16 v[104:107], a[224:227], v[24:27], 0\n"
    "v_permlane16_swap_b32 v244, v245\n"
    "v_max_f32 v244, v244, v245\n"
    "v_sub_f32 v192, 0, v244\n"
    "v_mfma_f32_16x16x32_bf16 v[104:107], a[228:231], v[28:31], v[104:107]\n"
    "v_sub_f32 v193, 0, v244\n"
    "v_sub_f32 v194, 0, v244\n"
    "v_sub_f32 v195, 0, v244\n"
    "s_waitcnt lgkmcnt(5)\n"
    "v_mfma_f32_16x16x32_bf16 v[104:107], a[232:235], v[32:35], v[104:107]\n"
    "v_sub_f32 v96, v96, v244\n"
    "v_sub_f32 v97, v97, v244\n"
    "v_sub_f32 v98, v98, v244\n"
    "s_waitcnt lgkmcnt(4)\n"
    "v_mfma_f32_16x16x32_bf16 v[104:107], a[236:239], v[36:39], v[104:107]\n"
    "v_sub_f32 v99, v99, v244\n"
    "v_sub_f32 v100, v100, v244\n"
    "v_sub_f32 v101, v101, v244\n"
    "s_waitcnt lgkmcnt(1)\n"
    "v_mfma_f32_16x16x32_bf16 v[104:107], a[240:243], v[40:43], v[104:107]\n"
    "v_sub_f32 v102, v102, v244\n"
    "v_sub_f32 v103, v103, v244\n"
    "v_exp_f32 v216, v96\n"
    "s_waitcnt lgkmcnt(0)\n"
    "v_mfma_f32_16x16x32_bf16 v[104:107], a[244:247], v[44:47], v[104:107]\n"
    "v_exp_f32 v217, v97\n"
    "v_exp_f32 v218, v98\n"
    "v_exp_f32 v219, v99\n"
    "v_mfma_f32_16x16x32_bf16 v[108:111], a[248:251], v[24:27], 0\n"
    "v_cvt_pk_bf16_f32 v160, v216, v217\n"
    "v_cvt_pk_bf16_f32 v161, v218, v219\n"
    "v_add_f32 v212, v216, v217\n"
    "v_mfma_f32_16x16x32_bf16 v[108:111], a[252:255], v[28:31], v[108:111]\n"
    "v_add_f32 v212, v212, v218\n"
    "v_add_f32 v212, v212, v219\n"
    "v_exp_f32 v216, v100\n"
    "v_mfma_f32_16x16x32_bf16 v[108:111], a[192:195], v[32:35], v[108:111]\n"
    "v_exp_f32 v217, v101\n"
    "v_exp_f32 v218, v102\n"
    "v_exp_f32 v219, v103\n"
    "v_mfma_f32_16x16x32_bf16 v[108:111], a[196:199], v[36:39], v[108:111]\n"
    "v_cvt_pk_bf16_f32 v162, v216, v217\n"
    "v_cvt_pk_bf16_f32 v163, v218, v219\n"
    "v_add_f32 v212, v212, v216\n"
    "v_mfma_f32_16x16x32_bf16 v[108:111], a[200:203], v[40:43], v[108:111]\n"
    "v_add_f32 v212, v212, v217\n"
    "v_add_f32 v212, v212, v218\n"
    "v_add_f32 v212, v212, v219\n"
    "v_mfma_f32_16x16x32_bf16 v[108:111], a[204:207], v[44:47], v[108:111]\n"
    "v_max3_f32 v246, v104, v105, v106\n"
    "s_nop 6\n"
    "v_max3_f32 v246, v246, v107, v108\n"
    "v_max3_f32 v246, v246, v109, v110\n"
    "v_max_f32 v246, v246, v111\n"
    "v_mov_b32 v247, v246\n"
    "s_nop 1\n"
    "v_permlane32_swap_b32 v246, v247\n"
    "v_max_f32 v246, v246, v247\n"
    "v_mov_b32 v247, v246\n"
    "s_nop 1\n"
    "v_permlane16_swap_b32 v246, v247\n"
    "v_max_f32 v246, v246, v247\n"
    "v_sub_f32 v196, 0, v246\n"
    "v_sub_f32 v197, 0, v246\n"
    "v_sub_f32 v198, 0, v246\n"
    "v_sub_f32 v199, 0, v246\n"
    "v_sub_f32 v104, v104, v246\n"
    "v_sub_f32 v105, v105, v246\n"
    "v_sub_f32 v106, v106, v246\n"
    "v_sub_f32 v107, v107, v246\n"
    "v_sub_f32 v108, v108, v246\n"
    "v_sub_f32 v109, v109, v246\n"
    "v_sub_f32 v110, v110, v246\n"
    "v_sub_f32 v111, v111, v246\n"
    "v_exp_f32 v220, v104\n"
    "v_exp_f32 v221, v105\n"
    "v_exp_f32 v222, v106\n"
    "v_exp_f32 v223, v107\n"
    "v_cvt_pk_bf16_f32 v164, v220, v221\n"
    "v_cvt_pk_bf16_f32 v165, v222, v223\n"
    "v_add_f32 v213, v220, v221\n"
    "v_add_f32 v213, v213, v222\n"
    "v_add_f32 v213, v213, v223\n"
    "v_exp_f32 v220, v108\n"
    "v_exp_f32 v221, v109\n"
    "v_exp_f32 v222, v110\n"
    "v_exp_f32 v223, v111\n"
    "v_cvt_pk_bf16_f32 v166, v220, v221\n"
    "v_cvt_pk_bf16_f32 v167, v222, v223\n"
    "v_add_f32 v213, v213, v220\n"
    "v_add_f32 v213, v213, v221\n"
    "v_add_f32 v213, v213, v222\n"
    "v_add_f32 v213, v213, v223\n"
    "s_waitcnt vmcnt(10)\n"
    "v_lshlrev_b32 v216, 16, v48\n"
    "v_and_b32 v217, s17, v48\n"
    "v_lshlrev_b32 v218, 16, v49\n"
    "v_and_b32 v219, s17, v49\n"
    "v_lshlrev_b32 v220, 16, v50\n"
    "v_and_b32 v221, s17, v50\n"
    "v_lshlrev_b32 v222, 16, v51\n"
    "v_and_b32 v223, s17, v51\n"
    "v_pk_mul_f32 v[216:217], v[216:217], s[50:51] op_sel_hi:[1,0]\n"
    "v_pk_mul_f32 v[218:219], v[218:219], s[50:51] op_sel_hi:[1,0]\n"
    "v_pk_mul_f32 v[220:221], v[220:221], s[50:51] op_sel_hi:[1,0]\n"
    "v_pk_mul_f32 v[222:223], v[222:223], s[50:51] op_sel_hi:[1,0]\n"
    "v_cvt_pk_bf16_f32 v48, v216, v217\n"
    "v_cvt_pk_bf16_f32 v49, v218, v219\n"
    "v_cvt_pk_bf16_f32 v50, v220, v221\n"
    "v_cvt_pk_bf16_f32 v51, v222, v223\n"
    "v_lshlrev_b32 v224, 16, v52\n"
    "v_and_b32 v225, s17, v52\n"
    "v_lshlrev_b32 v226, 16, v53\n"
    "v_and_b32 v227, s17, v53\n"
    "v_lshlrev_b32 v228, 16, v54\n"
    "v_and_b32 v229, s17, v54\n"
    "v_lshlrev_b32 v230, 16, v55\n"
    "v_and_b32 v231, s17, v55\n"
    "v_pk_mul_f32 v[224:225], v[224:225], s[50:51] op_sel_hi:[1,0]\n"
    "v_pk_mul_f32 v[226:227], v[226:227], s[50:51] op_sel_hi:[1,0]\n"
    "v_pk_mul_f32 v[228:229], v[228:229], s[50:51] op_sel_hi:[1,0]\n"
    "v_pk_mul_f32 v[230:231], v[230:231], s[50:51] op_sel_hi:[1,0]\n"
    "v_cvt_pk_bf16_f32 v52, v224, v225\n"
    "v_cvt_pk_bf16_f32 v53, v226, v227\n"
    "v_cvt_pk_bf16_f32 v54, v228, v229\n"
    "v_cvt_pk_bf16_f32 v55, v230, v231\n"
    "ds_write_b128 v242, v[48:51] offset:0\n"
    "ds_write_b128 v242, v[52:55] offset:1024\n"
    "v_lshlrev_b32 v216, 16, v56\n"
    "v_and_b32 v217, s17, v56\n"
    "v_lshlrev_b32 v218, 16, v57\n"
    "v_and_b32 v219, s17, v57\n"
    "v_lshlrev_b32 v220, 16, v58\n"
    "v_and_b32 v221, s17, v58\n"
    "v_lshlrev_b32 v222, 16, v59\n"
    "v_and_b32 v223, s17, v59\n"
    "v_pk_mul_f32 v[216:217], v[216:217], s[50:51] op_sel_hi:[1,0]\n"
    "v_pk_mul_f32 v[218:219], v[218:219], s[50:51] op_sel_hi:[1,0]\n"
    "v_pk_mul_f32 v[220:221], v[220:221], s[50:51] op_sel_hi:[1,0]\n"
    "v_pk_mul_f32 v[222:223], v[222:223], s[50:51] op_sel_hi:[1,0]\n"
    "v_cvt_pk_bf16_f32 v56, v216, v217\n"
    "v_cvt_pk_bf16_f32 v57, v218, v219\n"
    "v_cvt_pk_bf16_f32 v58, v220, v221\n"
    "v_cvt_pk_bf16_f32 v59, v222, v223\n"
    "v_lshlrev_b32 v224, 16, v60\n"
    "v_and_b32 v225, s17, v60\n"
    "v_lshlrev_b32 v226, 16, v61\n"
    "v_and_b32 v227, s17, v61\n"
    "v_lshlrev_b32 v228, 16, v62\n"
    "v_and_b32 v229, s17, v62\n"
    "v_lshlrev_b32 v230, 16, v63\n"
    "v_and_b32 v231, s17, v63\n"
    "v_pk_mul_f32 v[224:225], v[224:225], s[50:51] op_sel_hi:[1,0]\n"
    "v_pk_mul_f32 v[226:227], v[226:227], s[50:51] op_sel_hi:[1,0]\n"
    "v_pk_mul_f32 v[228:229], v[228:229], s[50:51] op_sel_hi:[1,0]\n"
    "v_pk_mul_f32 v[230:231], v[230:231], s[50:51] op_sel_hi:[1,0]\n"
    "v_cvt_pk_bf16_f32 v60, v224, v225\n"
    "v_cvt_pk_bf16_f32 v61, v226, v227\n"
    "v_cvt_pk_bf16_f32 v62, v228, v229\n"
    "v_cvt_pk_bf16_f32 v63, v230, v231\n"
    "ds_read_b128 v[48:51], v243 offset:0\n"
    "ds_read_b128 v[52:55], v232 offset:0\n"
    "ds_write_b128 v242, v[56:59] offset:2048\n"
    "ds_write_b128 v242, v[60:63] offset:3072\n"
    "v_lshlrev_b32 v216, 16, v64\n"
    "v_and_b32 v217, s17, v64\n"
    "v_lshlrev_b32 v218, 16, v65\n"
    "v_and_b32 v219, s17, v65\n"
    "v_lshlrev_b32 v220, 16, v66\n"
    "v_and_b32 v221, s17, v66\n"
    "v_lshlrev_b32 v222, 16, v67\n"
    "v_and_b32 v223, s17, v67\n"
    "v_pk_mul_f32 v[216:217], v[216:217], s[50:51] op_sel_hi:[1,0]\n"
    "v_pk_mul_f32 v[218:219], v[218:219], s[50:51] op_sel_hi:[1,0]\n"
    "v_pk_mul_f32 v[220:221], v[220:221], s[50:51] op_sel_hi:[1,0]\n"
    "v_pk_mul_f32 v[222:223], v[222:223], s[50:51] op_sel_hi:[1,0]\n"
    "v_cvt_pk_bf16_f32 v64, v216, v217\n"
    "v_cvt_pk_bf16_f32 v65, v218, v219\n"
    "v_cvt_pk_bf16_f32 v66, v220, v221\n"
    "v_cvt_pk_bf16_f32 v67, v222, v223\n"
    "v_lshlrev_b32 v224, 16, v68\n"
    "v_and_b32 v225, s17, v68\n"
    "v_lshlrev_b32 v226, 16, v69\n"
    "v_and_b32 v227, s17, v69\n"
    "v_lshlrev_b32 v228, 16, v70\n"
    "v_and_b32 v229, s17, v70\n"
    "v_lshlrev_b32 v230, 16, v71\n"
    "v_and_b32 v231, s17, v71\n"
    "v_pk_mul_f32 v[224:225], v[224:225], s[50:51] op_sel_hi:[1,0]\n"
    "v_pk_mul_f32 v[226:227], v[226:227], s[50:51] op_sel_hi:[1,0]\n"
    "v_pk_mul_f32 v[228:229], v[228:229], s[50:51] op_sel_hi:[1,0]\n"
    "v_pk_mul_f32 v[230:231], v[230:231], s[50:51] op_sel_hi:[1,0]\n"
    "v_cvt_pk_bf16_f32 v68, v224, v225\n"
    "v_cvt_pk_bf16_f32 v69, v226, v227\n"
    "v_cvt_pk_bf16_f32 v70, v228, v229\n"
    "v_cvt_pk_bf16_f32 v71, v230, v231\n"
    "ds_read_b128 v[56:59], v243 offset:2048\n"
    "ds_read_b128 v[60:63], v232 offset:2048\n"
    "ds_write_b128 v242, v[64:67] offset:0\n"
    "ds_write_b128 v242, v[68:71] offset:1024\n"
    "ds_read_b128 a[208:211], v235 offset:14336\n"
    "ds_read_b128 a[212:215], v235 offset:14848\n"
    "ds_read_b128 a[216:219], v235 offset:18432\n"
    "ds_read_b128 a[220:223], v235 offset:18944\n"
    "v_add_u32 v236, s41, v234\n"
    "v_xor_b32 v241, 32, v236\n"
    "s_mov_b32 s43, s40\n"
    "s_mov_b32 s40, s41\n"
    "s_mov_b32 s41, s42\n"
    "s_mov_b32 s42, s43\n"
    "ds_read_b128 v[64:67], v243 offset:0\n"
    "ds_read_b128 v[68:71], v232 offset:0\n"
    "s_waitcnt lgkmcnt(10)\n"
    "v_mfma_f32_16x16x32_bf16 v[112:115], a[224:227], v[48:51], 0\n"
    "v_mfma_f32_16x16x32_bf16 v[112:115], a[228:231], v[52:55], v[112:115]\n"
    "s_waitcnt lgkmcnt(9)\n"
    "v_mfma_f32_16x16x32_bf16 v[112:115], a[232:235], v[56:59], v[112:115]\n"
    "s_waitcnt lgkmcnt(8)\n"
    "v_mfma_f32_16x16x32_bf16 v[112:115], a[236:239], v[60:63], v[112:115]\n"
    "s_waitcnt lgkmcnt(1)\n"
    "v_mfma_f32_16x16x32_bf16 v[112:115], a[240:243], v[64:67], v[112:115]\n"
    "s_waitcnt lgkmcnt(0)\n"
    "v_mfma_f32_16x16x32_bf16 v[112:115], a[244:247], v[68:71], v[112:115]\n"
    "v_mfma_f32_16x16x32_bf16 v[116:119], a[248:251], v[48:51], 0\n"
    "v_mfma_f32_16x16x32_bf16 v[116:119], a[252:255], v[52:55], v[116:119]\n"
    "v_mfma_f32_16x16x32_bf16 v[116:119], a[192:195], v[56:59], v[116:119]\n"
    "v_mfma_f32_16x16x32_bf16 v[116:119], a[196:199], v[60:63], v[116:119]\n"
    "v_mfma_f32_16x16x32_bf16 v[116:119], a[200:203], v[64:67], v[116:119]\n"
    "v_mfma_f32_16x16x32_bf16 v[116:119], a[204:207], v[68:71], v[116:119]\n"
    "ds_read_b128 a[224:227], v235 offset:19456\n"
    "ds_read_b128 a[228:231], v235 offset:19968\n"
    "ds_read_b128 a[232:235], v235 offset:20480\n"
    "ds_read_b128 a[236:239], v235 offset:20992\n"
    "ds_read_b128 a[192:195], v235 offset:12288\n"
    "ds_read_b128 a[196:199], v235 offset:12800\n"
    "ds_read_b128 a[200:203], v235 offset:13312\n"
    "ds_read_b128 a[204:207], v235 offset:13824\n"
    "v_max3_f32 v248, v112, v113, v114\n"
    "v_max3_f32 v248, v248, v115, v116\n"
    "v_max3_f32 v248, v248, v117, v118\n"
    "v_max_f32 v248, v248, v119\n"
    "v_mov_b32 v249, v248\n"
    "s_nop 1\n"
    "v_permlane32_swap_b32 v248, v249\n"
    "v_max_f32 v248, v248, v249\n"
    "v_mov_b32 v249, v248\n"
    "s_nop 1\n"
    "v_permlane16_swap_b32 v248, v249\n"
    "v_max_f32 v248, v248, v249\n"
    "s_waitcnt lgkmcnt(0)\n"
    "LPEEL_3_%=:\n"
    "s_barrier\n"
    "s_mov_b32 s57, 3\n"
    "v_mfma_f32_16x16x32_bf16 v[128:131], a[192:195], v[0:3], v[192:195]\n"
    "ds_read_b64_tr_b16 a[240:241], v236 offset:0\n"
    "v_add_u32 v235, s41, v233\n"
    "v_mfma_f32_16x16x32_bf16 v[128:131], a[196:199], v[4:7], v[128:131]\n"
    "ds_read_b64_tr_b16 a[242:243], v236 offset:6144\n"
    "v_sub_f32 v200, 0, v248\n"
    "v_mfma_f32_16x16x32_bf16 v[128:131], a[200:203], v[8:11], v[128:131]\n"
    "v_sub_f32 v201, 0, v248\n"
    "v_sub_f32 v202, 0, v248\n"
    "v_mfma_f32_16x16x32_bf16 v[128:131], a[204:207], v[12:15], v[128:131]\n"
    "ds_read_b64_tr_b16 a[244:245], v241 offset:0\n"
    "s_add_u32 m0, s41, s48\n"
    "v_mfma_f32_16x16x32_bf16 v[128:131], a[208:211], v[16:19], v[128:131]\n"
    "ds_read_b64_tr_b16 a[246:247], v241 offset:6144\n"
    "v_sub_f32 v203, 0, v248\n"
    "v_mfma_f32_16x16x32_bf16 v[128:131], a[212:215], v[20:23], v[128:131]\n"
    "buffer_load_dwordx4 v237, s[24:27], s84 offen offset:0 lds\n"
    "v_mfma_f32_16x16x32_bf16 v[136:139], a[192:195], v[24:27], v[196:199]\n"
    "ds_read_b64_tr_b16 a[248:249], v236 offset:512\n"
    "v_sub_f32 v112, v112, v248\n"
    "v_mfma_f32_16x16x32_bf16 v[136:139], a[196:199], v[28:31], v[136:139]\n"
    "ds_read_b64_tr_b16 a[250:251], v236 offset:6656\n"
    "v_sub_f32 v113, v113, v248\n"
    "v_mfma_f32_16x16x32_bf16 v[136:139], a[200:203], v[32:35], v[136:139]\n"
    "v_sub_f32 v114, v114, v248\n"
    "v_sub_f32 v115, v115, v248\n"
    "v_mfma_f32_16x16x32_bf16 v[136:139], a[204:207], v[36:39], v[136:139]\n"
    "ds_read_b64_tr_b16 a[252:253], v241 offset:512\n"
    "v_sub_f32 v116, v116, v248\n"
    "v_mfma_f32_16x16x32_bf16 v[136:139], a[208:211], v[40:43], v[136:139]\n"
    "ds_read_b64_tr_b16 a[254:255], v241 offset:6656\n"
    "v_sub_f32 v117, v117, v248\n"
    "v_mfma_f32_16x16x32_bf16 v[136:139], a[212:215], v[44:47], v[136:139]\n"
    "v_sub_f32 v118, v118, v248\n"
    "v_sub_f32 v119, v119, v248\n"
    "v_mfma_f32_16x16x32_bf16 v[144:147], a[192:195], v[48:51], v[200:203]\n"
    "ds_read_b64_tr_b16 a[192:193], v236 offset:1024\n"
    "v_mfma_f32_16x16x32_bf16 v[144:147], a[196:199], v[52:55], v[144:147]\n"
    "ds_read_b64_tr_b16 a[194:195], v236 offset:7168\n"
    "v_mfma_f32_16x16x32_bf16 v[144:147], a[200:203], v[56:59], v[144:147]\n"
    "buffer_load_dwordx4 v238, s[24:27], s84 offen offset:1024 lds\n"
    "v_mfma_f32_16x16x32_bf16 v[144:147], a[204:207], v[60:63], v[144:147]\n"
    "ds_read_b64_tr_b16 a[196:197], v241 offset:1024\n"
    "v_mfma_f32_16x16x32_bf16 v[144:147], a[208:211], v[64:67], v[144:147]\n"
    "ds_read_b64_tr_b16 a[198:199], v241 offset:7168\n"
    "v_mfma_f32_16x16x32_bf16 v[144:147], a[212:215], v[68:71], v[144:147]\n"
    "v_exp_f32 v224, v112\n"
    "v_mfma_f32_16x16x32_bf16 v[132:135], a[216:219], v[0:3], v[192:195]\n"
    "ds_read_b64_tr_b16 a[200:201], v236 offset:1536\n"
    "v_mfma_f32_16x16x32_bf16 v[132:135], a[220:223], v[4:7], v[132:135]\n"
    "ds_read_b64_tr_b16 a[202:203], v236 offset:7680\n"
    "v_mfma_f32_16x16x32_bf16 v[132:135], a[224:227], v[8:11], v[132:135]\n"
    "v_exp_f32 v225, v113\n"
    "v_mfma_f32_16x16x32_bf16 v[132:135], a[228:231], v[12:15], v[132:135]\n"
    "ds_read_b64_tr_b16 a[204:205], v241 offset:1536\n"
    "v_cvt_pk_bf16_f32 v168, v224, v225\n"
    "v_mfma_f32_16x16x32_bf16 v[132:135], a[232:235], v[16:19], v[132:135]\n"
    "ds_read_b64_tr_b16 a[206:207], v241 offset:7680\n"
    "v_add_f32 v214, v224, v225\n"
    "v_mfma_f32_16x16x32_bf16 v[132:135], a[236:239], v[20:23], v[132:135]\n"
    "buffer_load_dwordx4 v239, s[24:27], s84 offen offset:2048 lds\n"
    "v_mfma_f32_16x16x32_bf16 v[140:143], a[216:219], v[24:27], v[196:199]\n"
    "ds_read_b64_tr_b16 a[208:209], v236 offset:2048\n"
    "s_mov_b32 s84, s44\n"
    "v_mfma_f32_16x16x32_bf16 v[140:143], a[220:223], v[28:31], v[140:143]\n"
    "ds_read_b64_tr_b16 a[210:211], v236 offset:8192\n"
    "s_add_u32 s87, s87, 1\n"
    "v_mfma_f32_16x16x32_bf16 v[140:143], a[224:227], v[32:35], v[140:143]\n"
    "v_readlane_b32 s44, v252, s87\n"
    "v_mfma_f32_16x16x32_bf16 v[140:143], a[228:231], v[36:39], v[140:143]\n"
    "ds_read_b64_tr_b16 a[212:213], v241 offset:2048\n"
    "v_mfma_f32_16x16x32_bf16 v[140:143], a[232:235], v[40:43], v[140:143]\n"
    "ds_read_b64_tr_b16 a[214:215], v241 offset:8192\n"
    "v_mfma_f32_16x16x32_bf16 v[140:143], a[236:239], v[44:47], v[140:143]\n"
    "v_exp_f32 v226, v114\n"
    "v_mfma_f32_16x16x32_bf16 v[148:151], a[216:219], v[48:51], v[200:203]\n"
    "v_exp_f32 v227, v115\n"
    "v_mfma_f32_16x16x32_bf16 v[148:151], a[220:223], v[52:55], v[148:151]\n"
    "ds_read_b64_tr_b16 a[216:217], v236 offset:2560\n"
    "v_cvt_pk_bf16_f32 v169, v226, v227\n"
    "v_mfma_f32_16x16x32_bf16 v[148:151], a[224:227], v[56:59], v[148:151]\n"
    "ds_read_b64_tr_b16 a[218:219], v236 offset:8704\n"
    "v_add_f32 v214, v214, v226\n"
    "v_mfma_f32_16x16x32_bf16 v[148:151], a[228:231], v[60:63], v[148:151]\n"
    "ds_read_b64_tr_b16 a[220:221], v241 offset:2560\n"
    "v_add_f32 v214, v214, v227\n"
    "v_mfma_f32_16x16x32_bf16 v[148:151], a[232:235], v[64:67], v[148:151]\n"
    "v_exp_f32 v228, v116\n"
    "v_mfma_f32_16x16x32_bf16 v[148:151], a[236:239], v[68:71], v[148:151]\n"
    "ds_read_b64_tr_b16 a[222:223], v241 offset:8704\n"
    "v_exp_f32 v229, v117\n"
    "v_exp_f32 v230, v118\n"
    "v_exp_f32 v231, v119\n"
    "v_cvt_pk_bf16_f32 v170, v228, v229\n"
    "v_add_f32 v214, v214, v228\n"
    "v_add_f32 v214, v214, v229\n"
    "v_cvt_pk_bf16_f32 v171, v230, v231\n"
    "v_add_f32 v214, v214, v230\n"
    "v_add_f32 v214, v214, v231\n"
    "s_waitcnt vmcnt(12)\n"
    "s_barrier\n"
    "s_waitcnt lgkmcnt(4)\n"
    "v_mfma_f32_16x16x32_bf16 a[0:3], a[240:243], v[160:163], 0\n"
    "v_mov_b32 v208, v212\n"
    "s_add_u32 m0, s42, s48\n"
    "v_mfma_f32_16x16x32_bf16 a[48:51], a[240:243], v[164:167], 0\n"
    "ds_read_b128 a[224:227], v235 offset:0\n"
    "v_mov_b32 v209, v213\n"
    "v_mfma_f32_16x16x32_bf16 a[96:99], a[240:243], v[168:171], 0\n"
    "buffer_load_dwordx4 v237, s[24:27], s84 offen offset:0 lds\n"
    "v_mfma_f32_16x16x32_bf16 a[4:7], a[244:247], v[160:163], 0\n"
    "ds_read_b128 a[228:231], v235 offset:512\n"
    "v_mov_b32 v210, v214\n"
    "v_mfma_f32_16x16x32_bf16 a[52:55], a[244:247], v[164:167], 0\n"
    "v_exp_f32 v216, v128\n"
    "v_mfma_f32_16x16x32_bf16 a[100:103], a[244:247], v[168:171], 0\n"
    "ds_read_b128 a[232:235], v235 offset:1024\n"
    "v_mfma_f32_16x16x32_bf16 a[8:11], a[248:251], v[160:163], 0\n"
    "buffer_load_dwordx4 v238, s[24:27], s84 offen offset:1024 lds\n"
    "v_mfma_f32_16x16x32_bf16 a[56:59], a[248:251], v[164:167], 0\n"
    "ds_read_b128 a[236:239], v235 offset:1536\n"
    "v_mfma_f32_16x16x32_bf16 a[104:107], a[248:251], v[168:171], 0\n"
    "v_exp_f32 v217, v129\n"
    "v_mfma_f32_16x16x32_bf16 a[12:15], a[252:255], v[160:163], 0\n"
    "ds_read_b128 a[240:243], v235 offset:2048\n"
    "v_cvt_pk_bf16_f32 v176, v216, v217\n"
    "v_mfma_f32_16x16x32_bf16 a[60:63], a[252:255], v[164:167], 0\n"
    "v_exp_f32 v218, v130\n"
    "v_mfma_f32_16x16x32_bf16 a[108:111], a[252:255], v[168:171], 0\n"
    "ds_read_b128 a[244:247], v235 offset:2560\n"
    "v_add_f32 v212, v216, v217\n"
    "v_mfma_f32_16x16x32_bf16 a[16:19], a[192:195], v[160:163], 0\n"
    "buffer_load_dwordx4 v239, s[24:27], s84 offen offset:2048 lds\n"
    "v_mfma_f32_16x16x32_bf16 a[64:67], a[192:195], v[164:167], 0\n"
    "ds_read_b128 a[248:251], v235 offset:6144\n"
    "s_add_u32 m0, s40, s47\n"
    "v_mfma_f32_16x16x32_bf16 a[112:115], a[192:195], v[168:171], 0\n"
    "v_exp_f32 v219, v131\n"
    "v_mfma_f32_16x16x32_bf16 a[20:23], a[196:199], v[160:163], 0\n"
    "ds_read_b128 a[252:255], v235 offset:6656\n"
    "v_cvt_pk_bf16_f32 v177, v218, v219\n"
    "v_mfma_f32_16x16x32_bf16 a[68:71], a[196:199], v[164:167], 0\n"
    "buffer_load_dwordx4 v237, s[20:23], s44 offen offset:0 lds\n"
    "v_mfma_f32_16x16x32_bf16 a[116:119], a[196:199], v[168:171], 0\n"
    "ds_read_b128 a[192:195], v235 offset:7168\n"
    "v_add_f32 v212, v212, v218\n"
    "v_mfma_f32_16x16x32_bf16 a[24:27], a[200:203], v[160:163], 0\n"
    "v_exp_f32 v220, v136\n"
    "v_mfma_f32_16x16x32_bf16 a[72:75], a[200:203], v[164:167], 0\n"
    "ds_read_b128 a[196:199], v235 offset:7680\n"
    "v_add_f32 v212, v212, v219\n"
    "v_mfma_f32_16x16x32_bf16 a[120:123], a[200:203], v[168:171], 0\n"
    "v_exp_f32 v221, v137\n"
    "v_mfma_f32_16x16x32_bf16 a[28:31], a[204:207], v[160:163], 0\n"
    "ds_read_b128 a[200:203], v235 offset:8192\n"
    "v_cvt_pk_bf16_f32 v180, v220, v221\n"
    "v_mfma_f32_16x16x32_bf16 a[76:79], a[204:207], v[164:167], 0\n"
    "buffer_load_dwordx4 v238, s[20:23], s44 offen offset:1024 lds\n"
    "v_mfma_f32_16x16x32_bf16 a[124:127], a[204:207], v[168:171], 0\n"
    "ds_read_b128 a[204:207], v235 offset:8704\n"
    "v_mfma_f32_16x16x32_bf16 a[32:35], a[208:211], v[160:163], 0\n"
    "v_exp_f32 v222, v138\n"
    "v_mfma_f32_16x16x32_bf16 a[80:83], a[208:211], v[164:167], 0\n"
    "v_exp_f32 v223, v139\n"
    "v_mfma_f32_16x16x32_bf16 a[128:131], a[208:211], v[168:171], 0\n"
    "buffer_load_dwordx4 v239, s[20:23], s44 offen offset:2048 lds\n"
    "v_mfma_f32_16x16x32_bf16 a[36:39], a[212:215], v[160:163], 0\n"
    "v_exp_f32 v224, v144\n"
    "v_mfma_f32_16x16x32_bf16 a[84:87], a[212:215], v[164:167], 0\n"
    "v_exp_f32 v225, v145\n"
    "v_mfma_f32_16x16x32_bf16 a[132:135], a[212:215], v[168:171], 0\n"
    "v_exp_f32 v226, v146\n"
    "s_waitcnt lgkmcnt(2)\n"
    "v_mfma_f32_16x16x32_bf16 a[40:43], a[216:219], v[160:163], 0\n"
    "v_exp_f32 v227, v147\n"
    "v_mfma_f32_16x16x32_bf16 a[88:91], a[216:219], v[164:167], 0\n"
    "v_exp_f32 v228, v132\n"
    "v_mfma_f32_16x16x32_bf16 a[136:139], a[216:219], v[168:171], 0\n"
    "v_exp_f32 v229, v133\n"
    "v_mfma_f32_16x16x32_bf16 a[44:47], a[220:223], v[160:163], 0\n"
    "v_exp_f32 v230, v134\n"
    "v_mfma_f32_16x16x32_bf16 a[92:95], a[220:223], v[164:167], 0\n"
    "v_exp_f32 v231, v135\n"
    "v_mfma_f32_16x16x32_bf16 a[140:143], a[220:223], v[168:171], 0\n"
    "v_add_f32 v213, v220, v221\n"
    "v_cvt_pk_bf16_f32 v181, v222, v223\n"
    "s_waitcnt vmcnt(15)\n"
    "s_barrier\n"
    "v_mfma_f32_16x16x32_bf16 v[96:99], a[224:227], v[0:3], v[192:195]\n"
    "ds_read_b64_tr_b16 a[208:209], v236 offset:12288\n"
    "v_add_f32 v213, v213, v222\n"
    "v_mfma_f32_16x16x32_bf16 v[96:99], a[228:231], v[4:7], v[96:99]\n"
    "ds_read_b64_tr_b16 a[210:211], v236 offset:18432\n"
    "v_add_f32 v213, v213, v223\n"
    "v_mfma_f32_16x16x32_bf16 v[96:99], a[232:235], v[8:11], v[96:99]\n"
    "v_exp_f32 v216, v140\n"
    "v_mfma_f32_16x16x32_bf16 v[96:99], a[236:239], v[12:15], v[96:99]\n"
    "ds_read_b64_tr_b16 a[212:213], v241 offset:12288\n"
    "v_cvt_pk_bf16_f32 v184, v224, v225\n"
    "v_mfma_f32_16x16x32_bf16 v[96:99], a[240:243], v[16:19], v[96:99]\n"
    "ds_read_b64_tr_b16 a[214:215], v241 offset:18432\n"
    "v_add_f32 v214, v224, v225\n"
    "v_mfma_f32_16x16x32_bf16 v[96:99], a[244:247], v[20:23], v[96:99]\n"
    "v_exp_f32 v217, v141\n"
    "v_mfma_f32_16x16x32_bf16 v[104:107], a[224:227], v[24:27], v[196:199]\n"
    "ds_read_b64_tr_b16 a[216:217], v236 offset:12800\n"
    "v_cvt_pk_bf16_f32 v185, v226, v227\n"
    "v_mfma_f32_16x16x32_bf16 v[104:107], a[228:231], v[28:31], v[104:107]\n"
    "ds_read_b64_tr_b16 a[218:219], v236 offset:18944\n"
    "v_add_f32 v214, v214, v226\n"
    "v_mfma_f32_16x16x32_bf16 v[104:107], a[232:235], v[32:35], v[104:107]\n"
    "v_exp_f32 v218, v142\n"
    "v_mfma_f32_16x16x32_bf16 v[104:107], a[236:239], v[36:39], v[104:107]\n"
    "ds_read_b64_tr_b16 a[220:221], v241 offset:12800\n"
    "v_add_f32 v214, v214, v227\n"
    "v_mfma_f32_16x16x32_bf16 v[104:107], a[240:243], v[40:43], v[104:107]\n"
    "ds_read_b64_tr_b16 a[222:223], v241 offset:18944\n"
    "v_cvt_pk_bf16_f32 v178, v228, v229\n"
    "v_mfma_f32_16x16x32_bf16 v[104:107], a[244:247], v[44:47], v[104:107]\n"
    "v_exp_f32 v219, v143\n"
    "v_mfma_f32_16x16x32_bf16 v[112:115], a[224:227], v[48:51], v[200:203]\n"
    "ds_read_b64_tr_b16 a[224:225], v236 offset:13312\n"
    "v_add_f32 v212, v212, v228\n"
    "v_mfma_f32_16x16x32_bf16 v[112:115], a[228:231], v[52:55], v[112:115]\n"
    "ds_read_b64_tr_b16 a[226:227], v236 offset:19456\n"
    "v_add_f32 v212, v212, v229\n"
    "v_mfma_f32_16x16x32_bf16 v[112:115], a[232:235], v[56:59], v[112:115]\n"
    "v_exp_f32 v220, v148\n"
    "v_mfma_f32_16x16x32_bf16 v[112:115], a[236:239], v[60:63], v[112:115]\n"
    "ds_read_b64_tr_b16 a[228:229], v241 offset:13312\n"
    "v_cvt_pk_bf16_f32 v179, v230, v231\n"
    "v_mfma_f32_16x16x32_bf16 v[112:115], a[240:243], v[64:67], v[112:115]\n"
    "ds_read_b64_tr_b16 a[230:231], v241 offset:19456\n"
    "v_add_f32 v212, v212, v230\n"
    "v_mfma_f32_16x16x32_bf16 v[112:115], a[244:247], v[68:71], v[112:115]\n"
    "v_exp_f32 v221, v149\n"
    "v_mfma_f32_16x16x32_bf16 v[100:103], a[248:251], v[0:3], v[192:195]\n"
    "ds_read_b64_tr_b16 a[232:233], v236 offset:13824\n"
    "v_mfma_f32_16x16x32_bf16 v[100:103], a[252:255], v[4:7], v[100:103]\n"
    "ds_read_b64_tr_b16 a[234:235], v236 offset:19968\n"
    "v_add_f32 v212, v212, v231\n"
    "v_mfma_f32_16x16x32_bf16 v[100:103], a[192:195], v[8:11], v[100:103]\n"
    "v_exp_f32 v222, v150\n"
    "v_mfma_f32_16x16x32_bf16 v[100:103], a[196:199], v[12:15], v[100:103]\n"
    "ds_read_b64_tr_b16 a[236:237], v241 offset:13824\n"
    "v_cvt_pk_bf16_f32 v182, v216, v217\n"
    "s_waitcnt lgkmcnt(7)\n"
    "v_mfma_f32_16x16x32_bf16 v[100:103], a[200:203], v[16:19], v[100:103]\n"
    "ds_read_b64_tr_b16 a[238:239], v241 offset:19968\n"
    "v_add_f32 v213, v213, v216\n"
    "v_mfma_f32_16x16x32_bf16 v[100:103], a[204:207], v[20:23], v[100:103]\n"
    "v_exp_f32 v223, v151\n"
    "v_mfma_f32_16x16x32_bf16 v[108:111], a[248:251], v[24:27], v[196:199]\n"
    "ds_read_b64_tr_b16 a[240:241], v236 offset:14336\n"
    "v_add_f32 v213, v213, v217\n"
    "v_mfma_f32_16x16x32_bf16 v[108:111], a[252:255], v[28:31], v[108:111]\n"
    "ds_read_b64_tr_b16 a[242:243], v236 offset:20480\n"
    "v_cvt_pk_bf16_f32 v183, v218, v219\n"
    "v_mfma_f32_16x16x32_bf16 v[108:111], a[192:195], v[32:35], v[108:111]\n"
    "v_add_f32 v213, v213, v218\n"
    "v_add_f32 v213, v213, v219\n"
    "v_mfma_f32_16x16x32_bf16 v[108:111], a[196:199], v[36:39], v[108:111]\n"
    "ds_read_b64_tr_b16 a[244:245], v241 offset:14336\n"
    "v_cvt_pk_bf16_f32 v186, v220, v221\n"
    "v_mfma_f32_16x16x32_bf16 v[108:111], a[200:203], v[40:43], v[108:111]\n"
    "ds_read_b64_tr_b16 a[246:247], v241 offset:20480\n"
    "v_add_f32 v214, v214, v220\n"
    "v_mfma_f32_16x16x32_bf16 v[108:111], a[204:207], v[44:47], v[108:111]\n"
    "v_add_f32 v214, v214, v221\n"
    "v_cvt_pk_bf16_f32 v187, v222, v223\n"
    "v_mfma_f32_16x16x32_bf16 v[116:119], a[248:251], v[48:51], v[200:203]\n"
    "v_add_f32 v214, v214, v222\n"
    "v_add_f32 v214, v214, v223\n"
    "v_mfma_f32_16x16x32_bf16 v[116:119], a[252:255], v[52:55], v[116:119]\n"
    "ds_read_b64_tr_b16 a[248:249], v236 offset:14848\n"
    "v_max3_f32 v232, v212, v213, v214\n"
    "v_mfma_f32_16x16x32_bf16 v[116:119], a[192:195], v[56:59], v[116:119]\n"
    "ds_read_b64_tr_b16 a[250:251], v236 offset:20992\n"
    "v_cmp_nge_f32 vcc, s49, v232\n"
    "v_mfma_f32_16x16x32_bf16 v[116:119], a[196:199], v[60:63], v[116:119]\n"
    "ds_read_b64_tr_b16 a[252:253], v241 offset:14848\n"
    "v_mfma_f32_16x16x32_bf16 v[116:119], a[200:203], v[64:67], v[116:119]\n"
    "v_mfma_f32_16x16x32_bf16 v[116:119], a[204:207], v[68:71], v[116:119]\n"
    "ds_read_b64_tr_b16 a[254:255], v241 offset:20992\n"
    "s_cbranch_vccnz LRARE_o_3_%=\n"
    "LRET_o_3_P0_%=:\n"
    "s_waitcnt vmcnt(12)\n"
    "s_barrier\n"
    "v_mfma_f32_16x16x32_bf16 a[0:3], a[208:211], v[176:179], a[0:3]\n"
    "v_exp_f32 v224, v96\n"
    "v_mfma_f32_16x16x32_bf16 a[48:51], a[208:211], v[180:183], a[48:51]\n"
    "ds_read_b128 a[192:195], v235 offset:12288\n"
    "v_add_u32 v236, s41, v234\n"
    "v_mfma_f32_16x16x32_bf16 a[96:99], a[208:211], v[184:187], a[96:99]\n"
    "v_exp_f32 v225, v97\n"
    "v_mfma_f32_16x16x32_bf16 a[4:7], a[212:215], v[176:179], a[4:7]\n"
    "ds_read_b128 a[196:199], v235 offset:12800\n"
    "v_xor_b32 v241, 32, v236\n"
    "v_mfma_f32_16x16x32_bf16 a[52:55], a[212:215], v[180:183], a[52:55]\n"
    "v_exp_f32 v226, v98\n"
    "v_mfma_f32_16x16x32_bf16 a[100:103], a[212:215], v[184:187], a[100:103]\n"
    "ds_read_b128 a[200:203], v235 offset:13312\n"
    "s_mov_b32 s43, s40\n"
    "v_mfma_f32_16x16x32_bf16 a[8:11], a[216:219], v[176:179], a[8:11]\n"
    "v_exp_f32 v227, v99\n"
    "v_mfma_f32_16x16x32_bf16 a[56:59], a[216:219], v[180:183], a[56:59]\n"
    "ds_read_b128 a[204:207], v235 offset:13824\n"
    "s_mov_b32 s40, s41\n"
    "v_mfma_f32_16x16x32_bf16 a[104:107], a[216:219], v[184:187], a[104:107]\n"
    "v_add_f32 v208, v208, v212\n"
    "v_mfma_f32_16x16x32_bf16 a[12:15], a[220:223], v[176:179], a[12:15]\n"
    "ds_read_b128 a[208:211], v235 offset:14336\n"
    "s_mov_b32 s41, s42\n"
    "v_mfma_f32_16x16x32_bf16 a[60:63], a[220:223], v[180:183], a[60:63]\n"
    "v_exp_f32 v228, v104\n"
    "v_mfma_f32_16x16x32_bf16 a[108:111], a[220:223], v[184:187], a[108:111]\n"
    "ds_read_b128 a[212:215], v235 offset:14848\n"
    "s_mov_b32 s42, s43\n"
    "s_waitcnt lgkmcnt(5)\n"
    "v_mfma_f32_16x16x32_bf16 a[16:19], a[224:227], v[176:179], a[16:19]\n"
    "v_exp_f32 v229, v105\n"
    "v_mfma_f32_16x16x32_bf16 a[64:67], a[224:227], v[180:183], a[64:67]\n"
    "ds_read_b128 a[216:219], v235 offset:18432\n"
    "s_mov_b32 s84, s44\n"
    "v_mfma_f32_16x16x32_bf16 a[112:115], a[224:227], v[184:187], a[112:115]\n"
    "v_exp_f32 v230, v106\n"
    "v_mfma_f32_16x16x32_bf16 a[20:23], a[228:231], v[176:179], a[20:23]\n"
    "ds_read_b128 a[220:223], v235 offset:18944\n"
    "s_add_u32 s87, s87, 1\n"
    "v_mfma_f32_16x16x32_bf16 a[68:71], a[228:231], v[180:183], a[68:71]\n"
    "v_exp_f32 v231, v107\n"
    "v_mfma_f32_16x16x32_bf16 a[116:119], a[228:231], v[184:187], a[116:119]\n"
    "ds_read_b128 a[224:227], v235 offset:19456\n"
    "v_readlane_b32 s44, v252, s87\n"
    "v_mfma_f32_16x16x32_bf16 a[24:27], a[232:235], v[176:179], a[24:27]\n"
    "v_exp_f32 v216, v112\n"
    "v_mfma_f32_16x16x32_bf16 a[72:75], a[232:235], v[180:183], a[72:75]\n"
    "ds_read_b128 a[228:231], v235 offset:19968\n"
    "v_add_f32 v209, v209, v213\n"
    "v_mfma_f32_16x16x32_bf16 a[120:123], a[232:235], v[184:187], a[120:123]\n"
    "v_exp_f32 v217, v113\n"
    "v_mfma_f32_16x16x32_bf16 a[28:31], a[236:239], v[176:179], a[28:31]\n"
    "ds_read_b128 a[232:235], v235 offset:20480\n"
    "v_add_f32 v210, v210, v214\n"
    "v_mfma_f32_16x16x32_bf16 a[76:79], a[236:239], v[180:183], a[76:79]\n"
    "v_exp_f32 v218, v114\n"
    "v_mfma_f32_16x16x32_bf16 a[124:127], a[236:239], v[184:187], a[124:127]\n"
    "ds_read_b128 a[236:239], v235 offset:20992\n"
    "v_cvt_pk_bf16_f32 v160, v224, v225\n"
    "v_mfma_f32_16x16x32_bf16 a[32:35], a[240:243], v[176:179], a[32:35]\n"
    "v_exp_f32 v219, v115\n"
    "v_mfma_f32_16x16x32_bf16 a[80:83], a[240:243], v[180:183], a[80:83]\n"
    "v_add_f32 v212, v224, v225\n"
    "v_cvt_pk_bf16_f32 v161, v226, v227\n"
    "v_mfma_f32_16x16x32_bf16 a[128:131], a[240:243], v[184:187], a[128:131]\n"
    "v_exp_f32 v220, v100\n"
    "v_mfma_f32_16x16x32_bf16 a[36:39], a[244:247], v[176:179], a[36:39]\n"
    "v_exp_f32 v221, v101\n"
    "v_mfma_f32_16x16x32_bf16 a[84:87], a[244:247], v[180:183], a[84:87]\n"
    "v_add_f32 v212, v212, v226\n"
    "v_add_f32 v212, v212, v227\n"
    "v_mfma_f32_16x16x32_bf16 a[132:135], a[244:247], v[184:187], a[132:135]\n"
    "v_cvt_pk_bf16_f32 v164, v228, v229\n"
    "v_add_f32 v213, v228, v229\n"
    "v_mfma_f32_16x16x32_bf16 a[40:43], a[248:251], v[176:179], a[40:43]\n"
    "v_exp_f32 v222, v102\n"
    "v_mfma_f32_16x16x32_bf16 a[88:91], a[248:251], v[180:183], a[88:91]\n"
    "v_exp_f32 v223, v103\n"
    "v_mfma_f32_16x16x32_bf16 a[136:139], a[248:251], v[184:187], a[136:139]\n"
    "v_cvt_pk_bf16_f32 v165, v230, v231\n"
    "v_mfma_f32_16x16x32_bf16 a[44:47], a[252:255], v[176:179], a[44:47]\n"
    "v_add_f32 v213, v213, v230\n"
    "v_add_f32 v213, v213, v231\n"
    "v_mfma_f32_16x16x32_bf16 a[92:95], a[252:255], v[180:183], a[92:95]\n"
    "s_waitcnt vmcnt(6) lgkmcnt(0)\n"
    "s_barrier\n"
    "v_mfma_f32_16x16x32_bf16 a[140:143], a[252:255], v[184:187], a[140:143]\n"
    "s_cmp_gt_u32 s87, s46\n"
    "s_cselect_b32 s71, -1.0, s49\n"
    "s_mov_b32 s57, 0\n"
    "s_branch LLOOPA_3_%=\n"
    ".p2align 6\n"
    "LLOOP_3_%=:\n"
    "v_mfma_f32_16x16x32_bf16 a[0:3], a[240:243], v[160:163], a[0:3]\n"
    "v_add_f32 v208, v208, v212\n"
    "s_add_u32 m0, s42, s48\n"
    "v_mfma_f32_16x16x32_bf16 a[48:51], a[240:243], v[164:167], a[48:51]\n"
    "ds_read_b128 a[224:227], v235 offset:0\n"
    "v_add_f32 v209, v209, v213\n"
    "v_mfma_f32_16x16x32_bf16 a[96:99], a[240:243], v[168:171], a[96:99]\n"
    "buffer_load_dwordx4 v237, s[24:27], s84 offen offset:0 lds\n"
    "v_mfma_f32_16x16x32_bf16 a[4:7], a[244:247], v[160:163], a[4:7]\n"
    "ds_read_b128 a[228:231], v235 offset:512\n"
    "v_add_f32 v210, v210, v214\n"
    "v_mfma_f32_16x16x32_bf16 a[52:55], a[244:247], v[164:167], a[52:55]\n"
    "v_exp_f32 v216, v128\n"
    "v_mfma_f32_16x16x32_bf16 a[100:103], a[244:247], v[168:171], a[100:103]\n"
    "ds_read_b128 a[232:235], v235 offset:1024\n"
    "v_mfma_f32_16x16x32_bf16 a[8:11], a[248:251], v[160:163], a[8:11]\n"
    "buffer_load_dwordx4 v238, s[24:27], s84 offen offset:1024 lds\n"
    "v_mfma_f32_16x16x32_bf16 a[56:59], a[248:251], v[164:167], a[56:59]\n"
    "ds_read_b128 a[236:239], v235 offset:1536\n"
    "v_mfma_f32_16x16x32_bf16 a[104:107], a[248:251], v[168:171], a[104:107]\n"
    "v_exp_f32 v217, v129\n"
    "v_mfma_f32_16x16x32_bf16 a[12:15], a[252:255], v[160:163], a[12:15]\n"
    "ds_read_b128 a[240:243], v235 offset:2048\n"
    "v_cvt_pk_bf16_f32 v176, v216, v217\n"
    "v_mfma_f32_16x16x32_bf16 a[60:63], a[252:255], v[164:167], a[60:63]\n"
    "v_exp_f32 v218, v130\n"
    "v_mfma_f32_16x16x32_bf16 a[108:111], a[252:255], v[168:171], a[108:111]\n"
    "ds_read_b128 a[244:247], v235 offset:2560\n"
    "v_add_f32 v212, v216, v217\n"
    "v_mfma_f32_16x16x32_bf16 a[16:19], a[192:195], v[160:163], a[16:19]\n"
    "buffer_load_dwordx4 v239, s[24:27], s84 offen offset:2048 lds\n"
    "v_mfma_f32_16x16x32_bf16 a[64:67], a[192:195], v[164:167], a[64:67]\n"
    "ds_read_b128 a[248:251], v235 offset:6144\n"
    "s_add_u32 m0, s40, s47\n"
    "v_mfma_f32_16x16x32_bf16 a[112:115], a[192:195], v[168:171], a[112:115]\n"
    "v_exp_f32 v219, v131\n"
    "v_mfma_f32_16x16x32_bf16 a[20:23], a[196:199], v[160:163], a[20:23]\n"
    "ds_read_b128 a[252:255], v235 offset:6656\n"
    "v_cvt_pk_bf16_f32 v177, v218, v219\n"
    "v_mfma_f32_16x16x32_bf16 a[68:71], a[196:199], v[164:167], a[68:71]\n"
    "buffer_load_dwordx4 v237, s[20:23], s44 offen offset:0 lds\n"
    "v_mfma_f32_16x16x32_bf16 a[116:119], a[196:199], v[168:171], a[116:119]\n"
    "ds_read_b128 a[192:195], v235 offset:7168\n"
    "v_add_f32 v212, v212, v218\n"
    "v_mfma_f32_16x16x32_bf16 a[24:27], a[200:203], v[160:163], a[24:27]\n"
    "v_exp_f32 v220, v136\n"
    "v_mfma_f32_16x16x32_bf16 a[72:75], a[200:203], v[164:167], a[72:75]\n"
    "ds_read_b128 a[196:199], v235 offset:7680\n"
    "v_add_f32 v212, v212, v219\n"
    "v_mfma_f32_16x16x32_bf16 a[120:123], a[200:203], v[168:171], a[120:123]\n"
    "v_exp_f32 v221, v137\n"
    "v_mfma_f32_16x16x32_bf16 a[28:31], a[204:207], v[160:163], a[28:31]\n"
    "ds_read_b128 a[200:203], v235 offset:8192\n"
    "v_cvt_pk_bf16_f32 v180, v220, v221\n"
    "v_mfma_f32_16x16x32_bf16 a[76:79], a[204:207], v[164:167], a[76:79]\n"
    "buffer_load_dwordx4 v238, s[20:23], s44 offen offset:1024 lds\n"
    "v_mfma_f32_16x16x32_bf16 a[124:127], a[204:207], v[168:171], a[124:127]\n"
    "ds_read_b128 a[204:207], v235 offset:8704\n"
    "s_waitcnt lgkmcnt(5)\n"
    "v_mfma_f32_16x16x32_bf16 a[32:35], a[208:211], v[160:163], a[32:35]\n"
    "v_exp_f32 v222, v138\n"
    "v_mfma_f32_16x16x32_bf16 a[80:83], a[208:211], v[164:167], a[80:83]\n"
    "v_exp_f32 v223, v139\n"
    "v_mfma_f32_16x16x32_bf16 a[128:131], a[208:211], v[168:171], a[128:131]\n"
    "buffer_load_dwordx4 v239, s[20:23], s44 offen offset:2048 lds\n"
    "v_mfma_f32_16x16x32_bf16 a[36:39], a[212:215], v[160:163], a[36:39]\n"
    "v_exp_f32 v224, v144\n"
    "v_mfma_f32_16x16x32_bf16 a[84:87], a[212:215], v[164:167], a[84:87]\n"
    "v_exp_f32 v225, v145\n"
    "v_mfma_f32_16x16x32_bf16 a[132:135], a[212:215], v[168:171], a[132:135]\n"
    "v_exp_f32 v226, v146\n"
    "v_mfma_f32_16x16x32_bf16 a[40:43], a[216:219], v[160:163], a[40:43]\n"
    "v_exp_f32 v227, v147\n"
    "v_mfma_f32_16x16x32_bf16 a[88:91], a[216:219], v[164:167], a[88:91]\n"
    "v_exp_f32 v228, v132\n"
    "v_mfma_f32_16x16x32_bf16 a[136:139], a[216:219], v[168:171], a[136:139]\n"
    "v_exp_f32 v229, v133\n"
    "v_mfma_f32_16x16x32_bf16 a[44:47], a[220:223], v[160:163], a[44:47]\n"
    "v_exp_f32 v230, v134\n"
    "v_mfma_f32_16x16x32_bf16 a[92:95], a[220:223], v[164:167], a[92:95]\n"
    "v_exp_f32 v231, v135\n"
    "v_mfma_f32_16x16x32_bf16 a[140:143], a[220:223], v[168:171], a[140:143]\n"
    "v_add_f32 v213, v220, v221\n"
    "v_cvt_pk_bf16_f32 v181, v222, v223\n"
    "v_mfma_f32_16x16x32_bf16 v[96:99], a[224:227], v[0:3], v[192:195]\n"
    "ds_read_b64_tr_b16 a[208:209], v236 offset:12288\n"
    "v_add_f32 v213, v213, v222\n"
    "v_mfma_f32_16x16x32_bf16 v[96:99], a[228:231], v[4:7], v[96:99]\n"
    "ds_read_b64_tr_b16 a[210:211], v236 offset:18432\n"
    "v_add_f32 v213, v213, v223\n"
    "v_mfma_f32_16x16x32_bf16 v[96:99], a[232:235], v[8:11], v[96:99]\n"
    "v_exp_f32 v216, v140\n"
    "v_mfma_f32_16x16x32_bf16 v[96:99], a[236:239], v[12:15], v[96:99]\n"
    "ds_read_b64_tr_b16 a[212:213], v241 offset:12288\n"
    "v_cvt_pk_bf16_f32 v184, v224, v225\n"
    "v_mfma_f32_16x16x32_bf16 v[96:99], a[240:243], v[16:19], v[96:99]\n"
    "ds_read_b64_tr_b16 a[214:215], v241 offset:18432\n"
    "v_add_f32 v214, v224, v225\n"
    "v_mfma_f32_16x16x32_bf16 v[96:99], a[244:247], v[20:23], v[96:99]\n"
    "v_exp_f32 v217, v141\n"
    "v_mfma_f32_16x16x32_bf16 v[104:107], a[224:227], v[24:27], v[196:199]\n"
    "ds_read_b64_tr_b16 a[216:217], v236 offset:12800\n"
    "v_cvt_pk_bf16_f32 v185, v226, v227\n"
    "v_mfma_f32_16x16x32_bf16 v[104:107], a[228:231], v[28:31], v[104:107]\n"
    "ds_read_b64_tr_b16 a[218:219], v236 offset:18944\n"
    "v_add_f32 v214, v214, v226\n"
    "v_mfma_f32_16x16x32_bf16 v[104:107], a[232:235], v[32:35], v[104:107]\n"
    "v_exp_f32 v218, v142\n"
    "v_mfma_f32_16x16x32_bf16 v[104:107], a[236:239], v[36:39], v[104:107]\n"
    "ds_read_b64_tr_b16 a[220:221], v241 offset:12800\n"
    "v_add_f32 v214, v214, v227\n"
    "v_mfma_f32_16x16x32_bf16 v[104:107], a[240:243], v[40:43], v[104:107]\n"
    "ds_read_b64_tr_b16 a[222:223], v241 offset:18944\n"
    "v_cvt_pk_bf16_f32 v178, v228, v229\n"
    "v_mfma_f32_16x16x32_bf16 v[104:107], a[244:247], v[44:47], v[104:107]\n"
    "v_exp_f32 v219, v143\n"
    "v_mfma_f32_16x16x32_bf16 v[112:115], a[224:227], v[48:51], v[200:203]\n"
    "ds_read_b64_tr_b16 a[224:225], v236 offset:13312\n"
    "v_add_f32 v212, v212, v228\n"
    "v_mfma_f32_16x16x32_bf16 v[112:115], a[228:231], v[52:55], v[112:115]\n"
    "ds_read_b64_tr_b16 a[226:227], v236 offset:19456\n"
    "v_add_f32 v212, v212, v229\n"
    "v_mfma_f32_16x16x32_bf16 v[112:115], a[232:235], v[56:59], v[112:115]\n"
    "v_exp_f32 v220, v148\n"
    "v_mfma_f32_16x16x32_bf16 v[112:115], a[236:239], v[60:63], v[112:115]\n"
    "ds_read_b64_tr_b16 a[228:229], v241 offset:13312\n"
    "v_cvt_pk_bf16_f32 v179, v230, v231\n"
    "v_mfma_f32_16x16x32_bf16 v[112:115], a[240:243], v[64:67], v[112:115]\n"
    "ds_read_b64_tr_b16 a[230:231], v241 offset:19456\n"
    "v_add_f32 v212, v212, v230\n"
    "v_mfma_f32_16x16x32_bf16 v[112:115], a[244:247], v[68:71], v[112:115]\n"
    "v_exp_f32 v221, v149\n"
    "v_mfma_f32_16x16x32_bf16 v[100:103], a[248:251], v[0:3], v[192:195]\n"
    "ds_read_b64_tr_b16 a[232:233], v236 offset:13824\n"
    "s_waitcnt lgkmcnt(7)\n"
    "v_mfma_f32_16x16x32_bf16 v[100:103], a[252:255], v[4:7], v[100:103]\n"
    "ds_read_b64_tr_b16 a[234:235], v236 offset:19968\n"
    "v_add_f32 v212, v212, v231\n"
    "v_mfma_f32_16x16x32_bf16 v[100:103], a[192:195], v[8:11], v[100:103]\n"
    "v_exp_f32 v222, v150\n"
    "v_mfma_f32_16x16x32_bf16 v[100:103], a[196:199], v[12:15], v[100:103]\n"
    "ds_read_b64_tr_b16 a[236:237], v241 offset:13824\n"
    "v_cvt_pk_bf16_f32 v182, v216, v217\n"
    "v_mfma_f32_16x16x32_bf16 v[100:103], a[200:203], v[16:19], v[100:103]\n"
    "ds_read_b64_tr_b16 a[238:239], v241 offset:19968\n"
    "v_add_f32 v213, v213, v216\n"
    "v_mfma_f32_16x16x32_bf16 v[100:103], a[204:207], v[20:23], v[100:103]\n"
    "v_exp_f32 v223, v151\n"
    "v_mfma_f32_16x16x32_bf16 v[108:111], a[248:251], v[24:27], v[196:199]\n"
    "ds_read_b64_tr_b16 a[240:241], v236 offset:14336\n"
    "v_add_f32 v213, v213, v217\n"
    "v_mfma_f32_16x16x32_bf16 v[108:111], a[252:255], v[28:31], v[108:111]\n"
    "ds_read_b64_tr_b16 a[242:243], v236 offset:20480\n"
    "v_cvt_pk_bf16_f32 v183, v218, v219\n"
    "v_mfma_f32_16x16x32_bf16 v[108:111], a[192:195], v[32:35], v[108:111]\n"
    "v_add_f32 v213, v213, v218\n"
    "v_add_f32 v213, v213, v219\n"
    "v_mfma_f32_16x16x32_bf16 v[108:111], a[196:199], v[36:39], v[108:111]\n"
    "ds_read_b64_tr_b16 a[244:245], v241 offset:14336\n"
    "v_cvt_pk_bf16_f32 v186, v220, v221\n"
    "v_mfma_f32_16x16x32_bf16 v[108:111], a[200:203], v[40:43], v[108:111]\n"
    "ds_read_b64_tr_b16 a[246:247], v241 offset:20480\n"
    "v_add_f32 v214, v214, v220\n"
    "v_mfma_f32_16x16x32_bf16 v[108:111], a[204:207], v[44:47], v[108:111]\n"
    "v_add_f32 v214, v214, v221\n"
    "v_cvt_pk_bf16_f32 v187, v222, v223\n"
    "v_mfma_f32_16x16x32_bf16 v[116:119], a[248:251], v[48:51], v[200:203]\n"
    "v_add_f32 v214, v214, v222\n"
    "v_add_f32 v214, v214, v223\n"
    "v_mfma_f32_16x16x32_bf16 v[116:119], a[252:255], v[52:55], v[116:119]\n"
    "ds_read_b64_tr_b16 a[248:249], v236 offset:14848\n"
    "v_max3_f32 v232, v212, v213, v214\n"
    "v_mfma_f32_16x16x32_bf16 v[116:119], a[192:195], v[56:59], v[116:119]\n"
    "ds_read_b64_tr_b16 a[250:251], v236 offset:20992\n"
    "v_cmp_nge_f32 vcc, s49, v232\n"
    "v_mfma_f32_16x16x32_bf16 v[116:119], a[196:199], v[60:63], v[116:119]\n"
    "ds_read_b64_tr_b16 a[252:253], v241 offset:14848\n"
    "v_mfma_f32_16x16x32_bf16 v[116:119], a[200:203], v[64:67], v[116:119]\n"
    "v_mfma_f32_16x16x32_bf16 v[116:119], a[204:207], v[68:71], v[116:119]\n"
    "ds_read_b64_tr_b16 a[254:255], v241 offset:20992\n"
    "s_cbranch_vccnz LRARE_o_3_%=\n"
    "LRET_o_3_L_%=:\n"
    "v_mfma_f32_16x16x32_bf16 a[0:3], a[208:211], v[176:179], a[0:3]\n"
    "v_exp_f32 v224, v96\n"
    "v_mfma_f32_16x16x32_bf16 a[48:51], a[208:211], v[180:183], a[48:51]\n"
    "ds_read_b128 a[192:195], v235 offset:12288\n"
    "v_add_u32 v236, s41, v234\n"
    "v_mfma_f32_16x16x32_bf16 a[96:99], a[208:211], v[184:187], a[96:99]\n"
    "v_exp_f32 v225, v97\n"
    "v_mfma_f32_16x16x32_bf16 a[4:7], a[212:215], v[176:179], a[4:7]\n"
    "ds_read_b128 a[196:199], v235 offset:12800\n"
    "v_xor_b32 v241, 32, v236\n"
    "v_mfma_f32_16x16x32_bf16 a[52:55], a[212:215], v[180:183], a[52:55]\n"
    "v_exp_f32 v226, v98\n"
    "v_mfma_f32_16x16x32_bf16 a[100:103], a[212:215], v[184:187], a[100:103]\n"
    "ds_read_b128 a[200:203], v235 offset:13312\n"
    "s_mov_b32 s43, s40\n"
    "v_mfma_f32_16x16x32_bf16 a[8:11], a[216:219], v[176:179], a[8:11]\n"
    "v_exp_f32 v227, v99\n"
    "v_mfma_f32_16x16x32_bf16 a[56:59], a[216:219], v[180:183], a[56:59]\n"
    "ds_read_b128 a[204:207], v235 offset:13824\n"
    "s_mov_b32 s40, s41\n"
    "v_mfma_f32_16x16x32_bf16 a[104:107], a[216:219], v[184:187], a[104:107]\n"
    "v_add_f32 v208, v208, v212\n"
    "s_waitcnt lgkmcnt(5)\n"
    "v_mfma_f32_16x16x32_bf16 a[12:15], a[220:223], v[176:179], a[12:15]\n"
    "ds_read_b128 a[208:211], v235 offset:14336\n"
    "s_mov_b32 s41, s42\n"
    "v_mfma_f32_16x16x32_bf16 a[60:63], a[220:223], v[180:183], a[60:63]\n"
    "v_exp_f32 v228, v104\n"
    "v_mfma_f32_16x16x32_bf16 a[108:111], a[220:223], v[184:187], a[108:111]\n"
    "ds_read_b128 a[212:215], v235 offset:14848\n"
    "s_mov_b32 s42, s43\n"
    "v_mfma_f32_16x16x32_bf16 a[16:19], a[224:227], v[176:179], a[16:19]\n"
    "v_exp_f32 v229, v105\n"
    "v_mfma_f32_16x16x32_bf16 a[64:67], a[224:227], v[180:183], a[64:67]\n"
    "ds_read_b128 a[216:219], v235 offset:18432\n"
    "s_mov_b32 s84, s44\n"
    "v_mfma_f32_16x16x32_bf16 a[112:115], a[224:227], v[184:187], a[112:115]\n"
    "v_exp_f32 v230, v106\n"
    "v_mfma_f32_16x16x32_bf16 a[20:23], a[228:231], v[176:179], a[20:23]\n"
    "ds_read_b128 a[220:223], v235 offset:18944\n"
    "s_add_u32 s87, s87, 1\n"
    "v_mfma_f32_16x16x32_bf16 a[68:71], a[228:231], v[180:183], a[68:71]\n"
    "v_exp_f32 v231, v107\n"
    "v_mfma_f32_16x16x32_bf16 a[116:119], a[228:231], v[184:187], a[116:119]\n"
    "ds_read_b128 a[224:227], v235 offset:19456\n"
    "v_readlane_b32 s44, v252, s87\n"
    "v_mfma_f32_16x16x32_bf16 a[24:27], a[232:235], v[176:179], a[24:27]\n"
    "v_exp_f32 v216, v112\n"
    "v_mfma_f32_16x16x32_bf16 a[72:75], a[232:235], v[180:183], a[72:75]\n"
    "ds_read_b128 a[228:231], v235 offset:19968\n"
    "v_add_f32 v209, v209, v213\n"
    "v_mfma_f32_16x16x32_bf16 a[120:123], a[232:235], v[184:187], a[120:123]\n"
    "v_exp_f32 v217, v113\n"
    "v_mfma_f32_16x16x32_bf16 a[28:31], a[236:239], v[176:179], a[28:31]\n"
    "ds_read_b128 a[232:235], v235 offset:20480\n"
    "v_add_f32 v210, v210, v214\n"
    "v_mfma_f32_16x16x32_bf16 a[76:79], a[236:239], v[180:183], a[76:79]\n"
    "v_exp_f32 v218, v114\n"
    "v_mfma_f32_16x16x32_bf16 a[124:127], a[236:239], v[184:187], a[124:127]\n"
    "ds_read_b128 a[236:239], v235 offset:20992\n"
    "v_cvt_pk_bf16_f32 v160, v224, v225\n"
    "v_mfma_f32_16x16x32_bf16 a[32:35], a[240:243], v[176:179], a[32:35]\n"
    "v_exp_f32 v219, v115\n"
    "v_mfma_f32_16x16x32_bf16 a[80:83], a[240:243], v[180:183], a[80:83]\n"
    "v_add_f32 v212, v224, v225\n"
    "v_cvt_pk_bf16_f32 v161, v226, v227\n"
    "v_mfma_f32_16x16x32_bf16 a[128:131], a[240:243], v[184:187], a[128:131]\n"
    "v_exp_f32 v220, v100\n"
    "v_mfma_f32_16x16x32_bf16 a[36:39], a[244:247], v[176:179], a[36:39]\n"
    "v_exp_f32 v221, v101\n"
    "v_mfma_f32_16x16x32_bf16 a[84:87], a[244:247], v[180:183], a[84:87]\n"
    "v_add_f32 v212, v212, v226\n"
    "v_add_f32 v212, v212, v227\n"
    "v_mfma_f32_16x16x32_bf16 a[132:135], a[244:247], v[184:187], a[132:135]\n"
    "v_cvt_pk_bf16_f32 v164, v228, v229\n"
    "v_add_f32 v213, v228, v229\n"
    "v_mfma_f32_16x16x32_bf16 a[40:43], a[248:251], v[176:179], a[40:43]\n"
    "v_exp_f32 v222, v102\n"
    "v_mfma_f32_16x16x32_bf16 a[88:91], a[248:251], v[180:183], a[88:91]\n"
    "v_exp_f32 v223, v103\n"
    "v_mfma_f32_16x16x32_bf16 a[136:139], a[248:251], v[184:187], a[136:139]\n"
    "v_cvt_pk_bf16_f32 v165, v230, v231\n"
    "s_waitcnt lgkmcnt(1)\n"
    "v_mfma_f32_16x16x32_bf16 a[44:47], a[252:255], v[176:179], a[44:47]\n"
    "v_add_f32 v213, v213, v230\n"
    "v_add_f32 v213, v213, v231\n"
    "v_mfma_f32_16x16x32_bf16 a[92:95], a[252:255], v[180:183], a[92:95]\n"
    "s_waitcnt vmcnt(6) lgkmcnt(0)\n"
    "s_barrier\n"
    "v_mfma_f32_16x16x32_bf16 a[140:143], a[252:255], v[184:187], a[140:143]\n"
    "s_cmp_gt_u32 s87, s46\n"
    "s_cselect_b32 s71, -1.0, s49\n"
    "LLOOPA_3_%=:\n"
    "v_mfma_f32_16x16x32_bf16 v[128:131], a[192:195], v[0:3], v[192:195]\n"
    "ds_read_b64_tr_b16 a[240:241], v236 offset:0\n"
    "v_add_u32 v235, s41, v233\n"
    "v_mfma_f32_16x16x32_bf16 v[128:131], a[196:199], v[4:7], v[128:131]\n"
    "ds_read_b64_tr_b16 a[242:243], v236 offset:6144\n"
    "v_cvt_pk_bf16_f32 v168, v216, v217\n"
    "v_mfma_f32_16x16x32_bf16 v[128:131], a[200:203], v[8:11], v[128:131]\n"
    "v_exp_f32 v224, v108\n"
    "v_mfma_f32_16x16x32_bf16 v[128:131], a[204:207], v[12:15], v[128:131]\n"
    "ds_read_b64_tr_b16 a[244:245], v241 offset:0\n"
    "v_add_f32 v214, v216, v217\n"
    "v_mfma_f32_16x16x32_bf16 v[128:131], a[208:211], v[16:19], v[128:131]\n"
    "ds_read_b64_tr_b16 a[246:247], v241 offset:6144\n"
    "v_cvt_pk_bf16_f32 v169, v218, v219\n"
    "v_mfma_f32_16x16x32_bf16 v[128:131], a[212:215], v[20:23], v[128:131]\n"
    "v_exp_f32 v225, v109\n"
    "v_mfma_f32_16x16x32_bf16 v[136:139], a[192:195], v[24:27], v[196:199]\n"
    "ds_read_b64_tr_b16 a[248:249], v236 offset:512\n"
    "v_add_f32 v214, v214, v218\n"
    "v_mfma_f32_16x16x32_bf16 v[136:139], a[196:199], v[28:31], v[136:139]\n"
    "ds_read_b64_tr_b16 a[250:251], v236 offset:6656\n"
    "v_add_f32 v214, v214, v219\n"
    "v_mfma_f32_16x16x32_bf16 v[136:139], a[200:203], v[32:35], v[136:139]\n"
    "v_exp_f32 v226, v110\n"
    "v_mfma_f32_16x16x32_bf16 v[136:139], a[204:207], v[36:39], v[136:139]\n"
    "ds_read_b64_tr_b16 a[252:253], v241 offset:512\n"
    "v_cvt_pk_bf16_f32 v162, v220, v221\n"
    "v_mfma_f32_16x16x32_bf16 v[136:139], a[208:211], v[40:43], v[136:139]\n"
    "ds_read_b64_tr_b16 a[254:255], v241 offset:6656\n"
    "v_add_f32 v212, v212, v220\n"
    "v_mfma_f32_16x16x32_bf16 v[136:139], a[212:215], v[44:47], v[136:139]\n"
    "v_exp_f32 v227, v111\n"
    "v_mfma_f32_16x16x32_bf16 v[144:147], a[192:195], v[48:51], v[200:203]\n"
    "ds_read_b64_tr_b16 a[192:193], v236 offset:1024\n"
    "v_add_f32 v212, v212, v221\n"
    "v_mfma_f32_16x16x32_bf16 v[144:147], a[196:199], v[52:55], v[144:147]\n"
    "ds_read_b64_tr_b16 a[194:195], v236 offset:7168\n"
    "v_cvt_pk_bf16_f32 v163, v222, v223\n"
    "v_mfma_f32_16x16x32_bf16 v[144:147], a[200:203], v[56:59], v[144:147]\n"
    "v_exp_f32 v228, v116\n"
    "v_mfma_f32_16x16x32_bf16 v[144:147], a[204:207], v[60:63], v[144:147]\n"
    "ds_read_b64_tr_b16 a[196:197], v241 offset:1024\n"
    "v_add_f32 v212, v212, v222\n"
    "v_mfma_f32_16x16x32_bf16 v[144:147], a[208:211], v[64:67], v[144:147]\n"
    "ds_read_b64_tr_b16 a[198:199], v241 offset:7168\n"
    "v_add_f32 v212, v212, v223\n"
    "v_mfma_f32_16x16x32_bf16 v[144:147], a[212:215], v[68:71], v[144:147]\n"
    "v_exp_f32 v229, v117\n"
    "v_mfma_f32_16x16x32_bf16 v[132:135], a[216:219], v[0:3], v[192:195]\n"
    "ds_read_b64_tr_b16 a[200:201], v236 offset:1536\n"
    "v_cvt_pk_bf16_f32 v166, v224, v225\n"
    "v_mfma_f32_16x16x32_bf16 v[132:135], a[220:223], v[4:7], v[132:135]\n"
    "ds_read_b64_tr_b16 a[202:203], v236 offset:7680\n"
    "v_add_f32 v213, v213, v224\n"
    "v_mfma_f32_16x16x32_bf16 v[132:135], a[224:227], v[8:11], v[132:135]\n"
    "v_exp_f32 v230, v118\n"
    "v_mfma_f32_16x16x32_bf16 v[132:135], a[228:231], v[12:15], v[132:135]\n"
    "ds_read_b64_tr_b16 a[204:205], v241 offset:1536\n"
    "v_add_f32 v213, v213, v225\n"
    "v_mfma_f32_16x16x32_bf16 v[132:135], a[232:235], v[16:19], v[132:135]\n"
    "ds_read_b64_tr_b16 a[206:207], v241 offset:7680\n"
    "v_cvt_pk_bf16_f32 v167, v226, v227\n"
    "v_mfma_f32_16x16x32_bf16 v[132:135], a[236:239], v[20:23], v[132:135]\n"
    "v_exp_f32 v231, v119\n"
    "v_mfma_f32_16x16x32_bf16 v[140:143], a[216:219], v[24:27], v[196:199]\n"
    "ds_read_b64_tr_b16 a[208:209], v236 offset:2048\n"
    "v_add_f32 v213, v213, v226\n"
    "v_mfma_f32_16x16x32_bf16 v[140:143], a[220:223], v[28:31], v[140:143]\n"
    "ds_read_b64_tr_b16 a[210:211], v236 offset:8192\n"
    "v_add_f32 v213, v213, v227\n"
    "v_mfma_f32_16x16x32_bf16 v[140:143], a[224:227], v[32:35], v[140:143]\n"
    "v_cvt_pk_bf16_f32 v170, v228, v229\n"
    "v_add_f32 v214, v214, v228\n"
    "v_mfma_f32_16x16x32_bf16 v[140:143], a[228:231], v[36:39], v[140:143]\n"
    "ds_read_b64_tr_b16 a[212:213], v241 offset:2048\n"
    "v_add_f32 v214, v214, v229\n"
    "v_mfma_f32_16x16x32_bf16 v[140:143], a[232:235], v[40:43], v[140:143]\n"
    "ds_read_b64_tr_b16 a[214:215], v241 offset:8192\n"
    "v_cvt_pk_bf16_f32 v171, v230, v231\n"
    "v_mfma_f32_16x16x32_bf16 v[140:143], a[236:239], v[44:47], v[140:143]\n"
    "v_add_f32 v214, v214, v230\n"
    "v_add_f32 v214, v214, v231\n"
    "v_mfma_f32_16x16x32_bf16 v[148:151], a[216:219], v[48:51], v[200:203]\n"
    "v_mfma_f32_16x16x32_bf16 v[148:151], a[220:223], v[52:55], v[148:151]\n"
    "ds_read_b64_tr_b16 a[216:217], v236 offset:2560\n"
    "v_max3_f32 v232, v212, v213, v214\n"
    "v_mfma_f32_16x16x32_bf16 v[148:151], a[224:227], v[56:59], v[148:151]\n"
    "ds_read_b64_tr_b16 a[218:219], v236 offset:8704\n"
    "v_cmp_nge_f32 vcc, s71, v232\n"
    "v_mfma_f32_16x16x32_bf16 v[148:151], a[228:231], v[60:63], v[148:151]\n"
    "ds_read_b64_tr_b16 a[220:221], v241 offset:2560\n"
    "s_waitcnt lgkmcnt(7)\n"
    "v_mfma_f32_16x16x32_bf16 v[148:151], a[232:235], v[64:67], v[148:151]\n"
    "v_mfma_f32_16x16x32_bf16 v[148:151], a[236:239], v[68:71], v[148:151]\n"
    "ds_read_b64_tr_b16 a[222:223], v241 offset:8704\n"
    "s_cbranch_vccz LLOOP_3_%=\n"
    "v_cmp_nge_f32 vcc, s49, v232\n"
    "s_cmp_le_u32 s87, s46\n"
    "s_cbranch_vccnz LRARE_e_3_%=\n"
    "s_cbranch_scc1 LLOOP_3_%=\n"
    "s_branch LFINX_3_%=\n"
    "LRET_e_3_L_%=:\n"
    "s_cmp_le_u32 s87, s46\n"
    "s_cbranch_scc1 LLOOP_3_%=\n"
    "LFINX_3_%=:\n"
    "s_cmp_eq_u32 s51, 0\n"
    "s_cbranch_scc0 LFIN1_3_%=\n"
    "s_branch LFIN0_3_%=\n"
    "LITEM_2_%=:\n"
    "s_cmp_eq_u32 s51, 0\n"
    "s_cbranch_scc0 LPRO1_2_%=\n"
    "LPRO0_2_%=:\n"
    "s_mov_b32 s84, s44\n"
    "s_add_u32 s87, s87, 1\n"
    "v_readlane_b32 s44, v252, s87\n"
    "s_mov_b32 s86, LFIN0_3_%=-LSTART_%=+13631488\n"
    "v_xor_b32 v232, 64, v243\n"
    "v_add_u32 v235, s41, v233\n"
    "s_waitcnt vmcnt(12)\n"
    "s_mul_i32 s62, s53, 3072\n"
    "s_sub_u32 s63, s62, s85\n"
    "s_add_u32 s62, s62, s56\n"
    "v_add_u32 v128, s63, v237\n"
    "v_add_u32 v129, s63, v238\n"
    "v_add_u32 v130, s63, v239\n"
    "s_add_u32 m0, s62, 73728\n"
    "s_add_u32 s45, s84, 0\n"
    "buffer_load_dwordx4 v128, s[24:27], s45 offen offset:0 lds\n"
    "v_lshlrev_b32 v216, 16, v0\n"
    "v_and_b32 v217, s17, v0\n"
    "v_lshlrev_b32 v218, 16, v1\n"
    "v_and_b32 v219, s17, v1\n"
    "v_lshlrev_b32 v220, 16, v2\n"
    "v_and_b32 v221, s17, v2\n"
    "v_lshlrev_b32 v222, 16, v3\n"
    "v_and_b32 v223, s17, v3\n"
    "v_pk_mul_f32 v[216:217], v[216:217], s[50:51] op_sel_hi:[1,0]\n"
    "v_pk_mul_f32 v[218:219], v[218:219], s[50:51] op_sel_hi:[1,0]\n"
    "v_pk_mul_f32 v[220:221], v[220:221], s[50:51] op_sel_hi:[1,0]\n"
    "v_pk_mul_f32 v[222:223], v[222:223], s[50:51] op_sel_hi:[1,0]\n"
    "v_cvt_pk_bf16_f32 v0, v216, v217\n"
    "v_cvt_pk_bf16_f32 v1, v218, v219\n"
    "v_cvt_pk_bf16_f32 v2, v220, v221\n"
    "v_cvt_pk_bf16_f32 v3, v222, v223\n"
    "v_lshlrev_b32 v224, 16, v4\n"
    "v_and_b32 v225, s17, v4\n"
    "v_lshlrev_b32 v226, 16, v5\n"
    "v_and_b32 v227, s17, v5\n"
    "v_lshlrev_b32 v228, 16, v6\n"
    "v_and_b32 v229, s17, v6\n"
    "v_lshlrev_b32 v230, 16, v7\n"
    "v_and_b32 v231, s17, v7\n"
    "v_pk_mul_f32 v[224:225], v[224:225], s[50:51] op_sel_hi:[1,0]\n"
    "v_pk_mul_f32 v[226:227], v[226:227], s[50:51] op_sel_hi:[1,0]\n"
    "v_pk_mul_f32 v[228:229], v[228:229], s[50:51] op_sel_hi:[1,0]\n"
    "v_pk_mul_f32 v[230:231], v[230:231], s[50:51] op_sel_hi:[1,0]\n"
    "v_cvt_pk_bf16_f32 v4, v224, v225\n"
    "v_cvt_pk_bf16_f32 v5, v226, v227\n"
    "v_cvt_pk_bf16_f32 v6, v228, v229\n"
    "v_cvt_pk_bf16_f32 v7, v230, v231\n"
    "ds_write_b128 v242, v[0:3] offset:0\n"
    "ds_write_b128 v242, v[4:7] offset:1024\n"
    "buffer_load_dwordx4 v129, s[24:27], s45 offen offset:1024 lds\n"
    "v_lshlrev_b32 v216, 16, v8\n"
    "v_and_b32 v217, s17, v8\n"
    "v_lshlrev_b32 v218, 16, v9\n"
    "v_and_b32 v219, s17, v9\n"
    "v_lshlrev_b32 v220, 16, v10\n"
    "v_and_b32 v221, s17, v10\n"
    "v_lshlrev_b32 v222, 16, v11\n"
    "v_and_b32 v223, s17, v11\n"
    "v_pk_mul_f32 v[216:217], v[216:217], s[50:51] op_sel_hi:[1,0]\n"
    "v_pk_mul_f32 v[218:219], v[218:219], s[50:51] op_sel_hi:[1,0]\n"
    "v_pk_mul_f32 v[220:221], v[220:221], s[50:51] op_sel_hi:[1,0]\n"
    "v_pk_mul_f32 v[222:223], v[222:223], s[50:51] op_sel_hi:[1,0]\n"
    "v_cvt_pk_bf16_f32 v8, v216, v217\n"
    "v_cvt_pk_bf16_f32 v9, v218, v219\n"
    "v_cvt_pk_bf16_f32 v10, v220, v221\n"
    "v_cvt_pk_bf16_f32 v11, v222, v223\n"
    "v_lshlrev_b32 v224, 16, v12\n"
    "v_and_b32 v225, s17, v12\n"
    "v_lshlrev_b32 v226, 16, v13\n"
    "v_and_b32 v227, s17, v13\n"
    "v_lshlrev_b32 v228, 16, v14\n"
    "v_and_b32 v229, s17, v14\n"
    "v_lshlrev_b32 v230, 16, v15\n"
    "v_and_b32 v231, s17, v15\n"
    "v_pk_mul_f32 v[224:225], v[224:225], s[50:51] op_sel_hi:[1,0]\n"
    "v_pk_mul_f32 v[226:227], v[226:227], s[50:51] op_sel_hi:[1,0]\n"
    "v_pk_mul_f32 v[228:229], v[228:229], s[50:51] op_sel_hi:[1,0]\n"
    "v_pk_mul_f32 v[230:231], v[230:231], s[50:51] op_sel_hi:[1,0]\n"
    "v_cvt_pk_bf16_f32 v12, v224, v225\n"
    "v_cvt_pk_bf16_f32 v13, v226, v227\n"
    "v_cvt_pk_bf16_f32 v14, v228, v229\n"
    "v_cvt_pk_bf16_f32 v15, v230, v231\n"
    "ds_read_b128 v[0:3], v243 offset:0\n"
    "ds_read_b128 v[4:7], v232 offset:0\n"
    "ds_write_b128 v242, v[8:11] offset:2048\n"
    "ds_write_b128 v242, v[12:15] offset:3072\n"
    "buffer_load_dwordx4 v130, s[24:27], s45 offen offset:2048 lds\n"
    "v_lshlrev_b32 v216, 16, v16\n"
    "v_and_b32 v217, s17, v16\n"
    "v_lshlrev_b32 v218, 16, v17\n"
    "v_and_b32 v219, s17, v17\n"
    "v_lshlrev_b32 v220, 16, v18\n"
    "v_and_b32 v221, s17, v18\n"
    "v_lshlrev_b32 v222, 16, v19\n"
    "v_and_b32 v223, s17, v19\n"
    "v_pk_mul_f32 v[216:217], v[216:217], s[50:51] op_sel_hi:[1,0]\n"
    "v_pk_mul_f32 v[218:219], v[218:219], s[50:51] op_sel_hi:[1,0]\n"
    "v_pk_mul_f32 v[220:221], v[220:221], s[50:51] op_sel_hi:[1,0]\n"
    "v_pk_mul_f32 v[222:223], v[222:223], s[50:51] op_sel_hi:[1,0]\n"
    "v_cvt_pk_bf16_f32 v16, v216, v217\n"
    "v_cvt_pk_bf16_f32 v17, v218, v219\n"
    "v_cvt_pk_bf16_f32 v18, v220, v221\n"
    "v_cvt_pk_bf16_f32 v19, v222, v223\n"
    "v_lshlrev_b32 v224, 16, v20\n"
    "v_and_b32 v225, s17, v20\n"
    "v_lshlrev_b32 v226, 16, v21\n"
    "v_and_b32 v227, s17, v21\n"
    "v_lshlrev_b32 v228, 16, v22\n"
    "v_and_b32 v229, s17, v22\n"
    "v_lshlrev_b32 v230, 16, v23\n"
    "v_and_b32 v231, s17, v23\n"
    "v_pk_mul_f32 v[224:225], v[224:225], s[50:51] op_sel_hi:[1,0]\n"
    "v_pk_mul_f32 v[226:227], v[226:227], s[50:51] op_sel_hi:[1,0]\n"
    "v_pk_mul_f32 v[228:229], v[228:229], s[50:51] op_sel_hi:[1,0]\n"
    "v_pk_mul_f32 v[230:231], v[230:231], s[50:51] op_sel_hi:[1,0]\n"
    "v_cvt_pk_bf16_f32 v20, v224, v225\n"
    "v_cvt_pk_bf16_f32 v21, v226, v227\n"
    "v_cvt_pk_bf16_f32 v22, v228, v229\n"
    "v_cvt_pk_bf16_f32 v23, v230, v231\n"
    "ds_read_b128 v[8:11], v243 offset:2048\n"
    "ds_read_b128 v[12:15], v232 offset:2048\n"
    "ds_write_b128 v242, v[16:19] offset:0\n"
    "ds_write_b128 v242, v[20:23] offset:1024\n"
    "s_waitcnt vmcnt(9)\n"
    "s_barrier\n"
    "ds_read_b128 a[224:227], v235 offset:0\n"
    "ds_read_b128 a[228:231], v235 offset:512\n"
    "ds_read_b128 a[232:235], v235 offset:1024\n"
    "ds_read_b128 a[236:239], v235 offset:1536\n"
    "ds_read_b128 a[240:243], v235 offset:2048\n"
    "ds_read_b128 a[244:247], v235 offset:2560\n"
    "ds_read_b128 a[248:251], v235 offset:6144\n"
    "ds_read_b128 a[252:255], v235 offset:6656\n"
    "ds_read_b128 a[192:195], v235 offset:7168\n"
    "ds_read_b128 a[196:199], v235 offset:7680\n"
    "ds_read_b128 a[200:203], v235 offset:8192\n"
    "ds_read_b128 a[204:207], v235 offset:8704\n"
    "ds_read_b128 v[16:19], v243 offset:0\n"
    "ds_read_b128 v[20:23], v232 offset:0\n"
    "s_waitcnt lgkmcnt(13)\n"
    "v_mfma_f32_16x16x32_bf16 v[96:99], a[224:227], v[0:3], 0\n"
    "s_waitcnt lgkmcnt(12)\n"
    "v_mfma_f32_16x16x32_bf16 v[96:99], a[228:231], v[4:7], v[96:99]\n"
    "s_waitcnt lgkmcnt(11)\n"
    "v_mfma_f32_16x16x32_bf16 v[96:99], a[232:235], v[8:11], v[96:99]\n"
    "s_waitcnt lgkmcnt(10)\n"
    "v_mfma_f32_16x16x32_bf16 v[96:99], a[236:239], v[12:15], v[96:99]\n"
    "s_waitcnt lgkmcnt(1)\n"
    "v_mfma_f32_16x16x32_bf16 v[96:99], a[240:243], v[16:19], v[96:99]\n"
    "s_waitcnt lgkmcnt(0)\n"
    "v_mfma_f32_16x16x32_bf16 v[96:99], a[244:247], v[20:23], v[96:99]\n"
    "v_mfma_f32_16x16x32_bf16 v[100:103], a[248:251], v[0:3], 0\n"
    "v_mfma_f32_16x16x32_bf16 v[100:103], a[252:255], v[4:7], v[100:103]\n"
    "v_mfma_f32_16x16x32_bf16 v[100:103], a[192:195], v[8:11], v[100:103]\n"
    "v_mfma_f32_16x16x32_bf16 v[100:103], a[196:199], v[12:15], v[100:103]\n"
    "v_mfma_f32_16x16x32_bf16 v[100:103], a[200:203], v[16:19], v[100:103]\n"
    "v_mfma_f32_16x16x32_bf16 v[100:103], a[204:207], v[20:23], v[100:103]\n"
    "s_nop 1\n"
    "v_max3_f32 v244, v96, v97, v98\n"
    "s_nop 4\n"
    "v_max3_f32 v244, v244, v99, v100\n"
    "v_max3_f32 v244, v244, v101, v102\n"
    "v_max_f32 v244, v244, v103\n"
    "v_mov_b32 v245, v244\n"
    "s_nop 1\n"
    "v_permlane32_swap_b32 v244, v245\n"
    "v_max_f32 v244, v244, v245\n"
    "v_mov_b32 v245, v244\n"
    "s_nop 1\n"
    "v_permlane16_swap_b32 v244, v245\n"
    "v_max_f32 v244, v244, v245\n"
    "v_sub_f32 v192, 0, v244\n"
    "v_sub_f32 v193, 0, v244\n"
    "v_sub_f32 v194, 0, v244\n"
    "v_sub_f32 v195, 0, v244\n"
    "v_sub_f32 v96, v96, v244\n"
    "v_sub_f32 v97, v97, v244\n"
    "v_sub_f32 v98, v98, v244\n"
    "v_sub_f32 v99, v99, v244\n"
    "v_sub_f32 v100, v100, v244\n"
    "v_sub_f32 v101, v101, v244\n"
    "v_sub_f32 v102, v102, v244\n"
    "v_sub_f32 v103, v103, v244\n"
    "v_exp_f32 v216, v96\n"
    "v_exp_f32 v217, v97\n"
    "v_exp_f32 v218, v98\n"
    "v_exp_f32 v219, v99\n"
    "v_cvt_pk_bf16_f32 v160, v216, v217\n"
    "v_cvt_pk_bf16_f32 v161, v218, v219\n"
    "v_add_f32 v212, v216, v217\n"
    "v_add_f32 v212, v212, v218\n"
    "v_add_f32 v212, v212, v219\n"
    "v_exp_f32 v216, v100\n"
    "v_exp_f32 v217, v101\n"
    "v_exp_f32 v218, v102\n"
    "v_exp_f32 v219, v103\n"
    "v_cvt_pk_bf16_f32 v162, v216, v217\n"
    "v_cvt_pk_bf16_f32 v163, v218, v219\n"
    "v_add_f32 v212, v212, v216\n"
    "v_add_f32 v212, v212, v217\n"
    "v_add_f32 v212, v212, v218\n"
    "v_add_f32 v212, v212, v219\n"
    "s_waitcnt vmcnt(3)\n"
    "s_add_u32 m0, s62, 24576\n"
    "s_add_u32 s45, s44, 0\n"
    "buffer_load_dwordx4 v128, s[20:23], s45 offen offset:0 lds\n"
    "v_lshlrev_b32 v216, 16, v24\n"
    "v_and_b32 v217, s17, v24\n"
    "v_lshlrev_b32 v218, 16, v25\n"
    "v_and_b32 v219, s17, v25\n"
    "v_lshlrev_b32 v220, 16, v26\n"
    "v_and_b32 v221, s17, v26\n"
    "v_lshlrev_b32 v222, 16, v27\n"
    "v_and_b32 v223, s17, v27\n"
    "v_pk_mul_f32 v[216:217], v[216:217], s[50:51] op_sel_hi:[1,0]\n"
    "v_pk_mul_f32 v[218:219], v[218:219], s[50:51] op_sel_hi:[1,0]\n"
    "v_pk_mul_f32 v[220:221], v[220:221], s[50:51] op_sel_hi:[1,0]\n"
    "buffer_load_dwordx4 v129, s[20:23], s45 offen offset:1024 lds\n"
    "v_pk_mul_f32 v[222:223], v[222:223], s[50:51] op_sel_hi:[1,0]\n"
    "v_cvt_pk_bf16_f32 v24, v216, v217\n"
    "v_cvt_pk_bf16_f32 v25, v218, v219\n"
    "v_cvt_pk_bf16_f32 v26, v220, v221\n"
    "v_cvt_pk_bf16_f32 v27, v222, v223\n"
    "v_lshlrev_b32 v224, 16, v28\n"
    "v_and_b32 v225, s17, v28\n"
    "v_lshlrev_b32 v226, 16, v29\n"
    "v_and_b32 v227, s17, v29\n"
    "v_lshlrev_b32 v228, 16, v30\n"
    "v_and_b32 v229, s17, v30\n"
    "v_lshlrev_b32 v230, 16, v31\n"
    "v_and_b32 v231, s17, v31\n"
    "buffer_load_dwordx4 v130, s[20:23], s45 offen offset:2048 lds\n"
    "v_pk_mul_f32 v[224:225], v[224:225], s[50:51] op_sel_hi:[1,0]\n"
    "v_pk_mul_f32 v[226:227], v[226:227], s[50:51] op_sel_hi:[1,0]\n"
    "v_pk_mul_f32 v[228:229], v[228:229], s[50:51] op_sel_hi:[1,0]\n"
    "v_pk_mul_f32 v[230:231], v[230:231], s[50:51] op_sel_hi:[1,0]\n"
    "v_cvt_pk_bf16_f32 v28, v224, v225\n"
    "v_cvt_pk_bf16_f32 v29, v226, v227\n"
    "v_cvt_pk_bf16_f32 v30, v228, v229\n"
    "v_cvt_pk_bf16_f32 v31, v230, v231\n"
    "ds_write_b128 v242, v[24:27] offset:2048\n"
    "ds_write_b128 v242, v[28:31] offset:3072\n"
    "s_add_u32 m0, s62, 86016\n"
    "s_add_u32 s45, s84, 12288\n"
    "buffer_load_dwordx4 v128, s[24:27], s45 offen offset:0 lds\n"
    "v_lshlrev_b32 v216, 16, v32\n"
    "v_and_b32 v217, s17, v32\n"
    "v_lshlrev_b32 v218, 16, v33\n"
    "v_and_b32 v219, s17, v33\n"
    "v_lshlrev_b32 v220, 16, v34\n"
    "v_and_b32 v221, s17, v34\n"
    "v_lshlrev_b32 v222, 16, v35\n"
    "v_and_b32 v223, s17, v35\n"
    "v_pk_mul_f32 v[216:217], v[216:217], s[50:51] op_sel_hi:[1,0]\n"
    "v_pk_mul_f32 v[218:219], v[218:219], s[50:51] op_sel_hi:[1,0]\n"
    "v_pk_mul_f32 v[220:221], v[220:221], s[50:51] op_sel_hi:[1,0]\n"
    "buffer_load_dwordx4 v129, s[24:27], s45 offen offset:1024 lds\n"
    "v_pk_mul_f32 v[222:223], v[222:223], s[50:51] op_sel_hi:[1,0]\n"
    "v_cvt_pk_bf16_f32 v32, v216, v217\n"
    "v_cvt_pk_bf16_f32 v33, v218, v219\n"
    "v_cvt_pk_bf16_f32 v34, v220, v221\n"
    "v_cvt_pk_bf16_f32 v35, v222, v223\n"
    "v_lshlrev_b32 v224, 16, v36\n"
    "v_and_b32 v225, s17, v36\n"
    "v_lshlrev_b32 v226, 16, v37\n"
    "v_and_b32 v227, s17, v37\n"
    "v_lshlrev_b32 v228, 16, v38\n"
    "v_and_b32 v229, s17, v38\n"
    "v_lshlrev_b32 v230, 16, v39\n"
    "v_and_b32 v231, s17, v39\n"
    "buffer_load_dwordx4 v130, s[24:27], s45 offen offset:2048 lds\n"
    "v_pk_mul_f32 v[224:225], v[224:225], s[50:51] op_sel_hi:[1,0]\n"
    "v_pk_mul_f32 v[226:227], v[226:227], s[50:51] op_sel_hi:[1,0]\n"
    "v_pk_mul_f32 v[228:229], v[228:229], s[50:51] op_sel_hi:[1,0]\n"
    "v_pk_mul_f32 v[230:231], v[230:231], s[50:51] op_sel_hi:[1,0]\n"
    "v_cvt_pk_bf16_f32 v36, v224, v225\n"
    "v_cvt_pk_bf16_f32 v37, v226, v227\n"
    "v_cvt_pk_bf16_f32 v38, v228, v229\n"
    "v_cvt_pk_bf16_f32 v39, v230, v231\n"
    "ds_read_b128 v[24:27], v243 offset:2048\n"
    "ds_read_b128 v[28:31], v232 offset:2048\n"
    "ds_write_b128 v242, v[32:35] offset:0\n"
    "ds_write_b128 v242, v[36:39] offset:1024\n"
    "s_add_u32 m0, s62, 36864\n"
    "s_add_u32 s45, s44, 12288\n"
    "buffer_load_dwordx4 v128, s[20:23], s45 offen offset:0 lds\n"
    "v_lshlrev_b32 v216, 16, v40\n"
    "v_and_b32 v217, s17, v40\n"
    "v_lshlrev_b32 v218, 16, v41\n"
    "v_and_b32 v219, s17, v41\n"
    "v_lshlrev_b32 v220, 16, v42\n"
    "v_and_b32 v221, s17, v42\n"
    "v_lshlrev_b32 v222, 16, v43\n"
    "v_and_b32 v223, s17, v43\n"
    "v_pk_mul_f32 v[216:217], v[216:217], s[50:51] op_sel_hi:[1,0]\n"
    "v_pk_mul_f32 v[218:219], v[218:219], s[50:51] op_sel_hi:[1,0]\n"
    "v_pk_mul_f32 v[220:221], v[220:221], s[50:51] op_sel_hi:[1,0]\n"
    "buffer_load_dwordx4 v129, s[20:23], s45 offen offset:1024 lds\n"
    "v_pk_mul_f32 v[222:223], v[222:223], s[50:51] op_sel_hi:[1,0]\n"
    "v_cvt_pk_bf16_f32 v40, v216, v217\n"
    "v_cvt_pk_bf16_f32 v41, v218, v219\n"
    "v_cvt_pk_bf16_f32 v42, v220, v221\n"
    "v_cvt_pk_bf16_f32 v43, v222, v223\n"
    "v_lshlrev_b32 v224, 16, v44\n"
    "v_and_b32 v225, s17, v44\n"
    "v_lshlrev_b32 v226, 16, v45\n"
    "v_and_b32 v227, s17, v45\n"
    "v_lshlrev_b32 v228, 16, v46\n"
    "v_and_b32 v229, s17, v46\n"
    "v_lshlrev_b32 v230, 16, v47\n"
    "v_and_b32 v231, s17, v47\n"
    "buffer_load_dwordx4 v130, s[20:23], s45 offen offset:2048 lds\n"
    "s_mov_b32 s84, s44\n"
    "s_add_u32 s87, s87, 1\n"
    "v_readlane_b32 s44, v252, s87\n"
    "s_mov_b32 s40, 49152\n"
    "s_mov_b32 s42, 24576\n"
    "v_pk_mul_f32 v[224:225], v[224:225], s[50:51] op_sel_hi:[1,0]\n"
    "v_pk_mul_f32 v[226:227], v[226:227], s[50:51] op_sel_hi:[1,0]\n"
    "v_pk_mul_f32 v[228:229], v[228:229], s[50:51] op_sel_hi:[1,0]\n"
    "v_pk_mul_f32 v[230:231], v[230:231], s[50:51] op_sel_hi:[1,0]\n"
    "v_cvt_pk_bf16_f32 v44, v224, v225\n"
    "v_cvt_pk_bf16_f32 v45, v226, v227\n"
    "v_cvt_pk_bf16_f32 v46, v228, v229\n"
    "v_cvt_pk_bf16_f32 v47, v230, v231\n"
    "ds_read_b128 v[32:35], v243 offset:0\n"
    "ds_read_b128 v[36:39], v232 offset:0\n"
    "ds_write_b128 v242, v[40:43] offset:2048\n"
    "ds_write_b128 v242, v[44:47] offset:3072\n"
    "s_add_u32 m0, s40, s47\n"
    "s_nop 0\n"
    "buffer_load_dwordx4 v237, s[20:23], s44 offen offset:0 lds\n"
    "ds_read_b128 a[208:211], v235 offset:14336\n"
    "ds_read_b128 a[212:215], v235 offset:14848\n"
    "buffer_load_dwordx4 v238, s[20:23], s44 offen offset:1024 lds\n"
    "ds_read_b128 a[216:219], v235 offset:18432\n"
    "ds_read_b128 a[220:223], v235 offset:18944\n"
    "v_add_u32 v236, s41, v234\n"
    "v_xor_b32 v241, 32, v236\n"
    "buffer_load_dwordx4 v239, s[20:23], s44 offen offset:2048 lds\n"
    "s_mov_b32 s43, s40\n"
    "s_mov_b32 s40, s41\n"
    "s_mov_b32 s41, s42\n"
    "s_add_u32 m0, m0, 3072\n"
    "s_add_u32 s45, s44, 3072\n"
    "buffer_load_dwordx4 v237, s[20:23], s45 offen offset:0 lds\n"
    "s_mov_b32 s42, s43\n"
    "ds_read_b128 v[40:43], v243 offset:2048\n"
    "ds_read_b128 v[44:47], v232 offset:2048\n"
    "s_waitcnt lgkmcnt(10)\n"
    "v_mfma_f32_16x16x32_bf16 v[104:107], a[224:227], v[24:27], 0\n"
    "buffer_load_dwordx4 v238, s[20:23], s45 offen offset:1024 lds\n"
    "v_mfma_f32_16x16x32_bf16 v[104:107], a[228:231], v[28:31], v[104:107]\n"
    "s_waitcnt lgkmcnt(9)\n"
    "v_mfma_f32_16x16x32_bf16 v[104:107], a[232:235], v[32:35], v[104:107]\n"
    "s_waitcnt lgkmcnt(8)\n"
    "v_mfma_f32_16x16x32_bf16 v[104:107], a[236:239], v[36:39], v[104:107]\n"
    "buffer_load_dwordx4 v239, s[20:23], s45 offen offset:2048 lds\n"
    "s_waitcnt lgkmcnt(1)\n"
    "v_mfma_f32_16x16x32_bf16 v[104:107], a[240:243], v[40:43], v[104:107]\n"
    "s_waitcnt lgkmcnt(0)\n"
    "v_mfma_f32_16x16x32_bf16 v[104:107], a[244:247], v[44:47], v[104:107]\n"
    "v_mfma_f32_16x16x32_bf16 v[108:111], a[248:251], v[24:27], 0\n"
    "v_mfma_f32_16x16x32_bf16 v[108:111], a[252:255], v[28:31], v[108:111]\n"
    "s_add_u32 m0, m0, 3072\n"
    "s_add_u32 s45, s44, 6144\n"
    "buffer_load_dwordx4 v237, s[20:23], s45 offen offset:0 lds\n"
    "v_mfma_f32_16x16x32_bf16 v[108:111], a[192:195], v[32:35], v[108:111]\n"
    "v_mfma_f32_16x16x32_bf16 v[108:111], a[196:199], v[36:39], v[108:111]\n"
    "v_mfma_f32_16x16x32_bf16 v[108:111], a[200:203], v[40:43], v[108:111]\n"
    "v_mfma_f32_16x16x32_bf16 v[108:111], a[204:207], v[44:47], v[108:111]\n"
    "ds_read_b128 a[224:227], v235 offset:19456\n"
    "buffer_load_dwordx4 v238, s[20:23], s45 offen offset:1024 lds\n"
    "ds_read_b128 a[228:231], v235 offset:19968\n"
    "ds_read_b128 a[232:235], v235 offset:20480\n"
    "ds_read_b128 a[236:239], v235 offset:20992\n"
    "ds_read_b128 a[192:195], v235 offset:12288\n"
    "buffer_load_dwordx4 v239, s[20:23], s45 offen offset:2048 lds\n"
    "ds_read_b128 a[196:199], v235 offset:12800\n"
    "ds_read_b128 a[200:203], v235 offset:13312\n"
    "ds_read_b128 a[204:207], v235 offset:13824\n"
    "v_max3_f32 v246, v104, v105, v106\n"
    "s_add_u32 m0, m0, 3072\n"
    "s_add_u32 s45, s44, 9216\n"
    "buffer_load_dwordx4 v237, s[20:23], s45 offen offset:0 lds\n"
    "v_max3_f32 v246, v246, v107, v108\n"
    "v_max3_f32 v246, v246, v109, v110\n"
    "v_max_f32 v246, v246, v111\n"
    "v_mov_b32 v247, v246\n"
    "s_nop 1\n"
    "v_permlane32_swap_b32 v246, v247\n"
    "v_max_f32 v246, v246, v247\n"
    "buffer_load_dwordx4 v238, s[20:23], s45 offen offset:1024 lds\n"
    "v_mov_b32 v247, v246\n"
    "s_nop 1\n"
    "v_permlane16_swap_b32 v246, v247\n"
    "v_max_f32 v246, v246, v247\n"
    "buffer_load_dwordx4 v239, s[20:23], s45 offen offset:2048 lds\n"
    "s_add_u32 m0, m0, 3072\n"
    "s_add_u32 s45, s44, 12288\n"
    "buffer_load_dwordx4 v237, s[20:23], s45 offen offset:0 lds\n"
    "buffer_load_dwordx4 v238, s[20:23], s45 offen offset:1024 lds\n"
    "buffer_load_dwordx4 v239, s[20:23], s45 offen offset:2048 lds\n"
    "s_waitcnt vmcnt(24)\n"
    "s_waitcnt lgkmcnt(0)\n"
    "s_branch LPEEL_2_%=\n"
    "LPRO1_2_%=:\n"
    "s_mov_b32 s86, LFIN1_3_%=-LSTART_%=+13631488\n"
    "v_xor_b32 v232, 64, v243\n"
    "v_add_u32 v235, s41, v233\n"
    "s_waitcnt vmcnt(37)\n"
    "s_barrier\n"
    "ds_read_b128 a[224:227], v235 offset:0\n"
    "ds_read_b128 a[228:231], v235 offset:512\n"
    "ds_read_b128 a[232:235], v235 offset:1024\n"
    "ds_read_b128 a[236:239], v235 offset:1536\n"
    "ds_read_b128 a[240:243], v235 offset:2048\n"
    "ds_read_b128 a[244:247], v235 offset:2560\n"
    "ds_read_b128 a[248:251], v235 offset:6144\n"
    "ds_read_b128 a[252:255], v235 offset:6656\n"
    "ds_read_b128 a[192:195], v235 offset:7168\n"
    "ds_read_b128 a[196:199], v235 offset:7680\n"
    "ds_read_b128 a[200:203], v235 offset:8192\n"
    "ds_read_b128 a[204:207], v235 offset:8704\n"
    "v_lshlrev_b32 v216, 16, v0\n"
    "v_and_b32 v217, s17, v0\n"
    "v_lshlrev_b32 v218, 16, v1\n"
    "v_and_b32 v219, s17, v1\n"
    "v_lshlrev_b32 v220, 16, v2\n"
    "v_and_b32 v221, s17, v2\n"
    "v_lshlrev_b32 v222, 16, v3\n"
    "v_and_b32 v223, s17, v3\n"
    "v_pk_mul_f32 v[216:217], v[216:217], s[50:51] op_sel_hi:[1,0]\n"
    "v_pk_mul_f32 v[218:219], v[218:219], s[50:51] op_sel_hi:[1,0]\n"
    "v_pk_mul_f32 v[220:221], v[220:221], s[50:51] op_sel_hi:[1,0]\n"
    "v_pk_mul_f32 v[222:223], v[222:223], s[50:51] op_sel_hi:[1,0]\n"
    "v_cvt_pk_bf16_f32 v0, v216, v217\n"
    "v_cvt_pk_bf16_f32 v1, v218, v219\n"
    "v_cvt_pk_bf16_f32 v2, v220, v221\n"
    "v_cvt_pk_bf16_f32 v3, v222, v223\n"
    "v_lshlrev_b32 v224, 16, v4\n"
    "v_and_b32 v225, s17, v4\n"
    "v_lshlrev_b32 v226, 16, v5\n"
    "v_and_b32 v227, s17, v5\n"
    "v_lshlrev_b32 v228, 16, v6\n"
    "v_and_b32 v229, s17, v6\n"
    "v_lshlrev_b32 v230, 16, v7\n"
    "v_and_b32 v231, s17, v7\n"
    "v_pk_mul_f32 v[224:225], v[224:225], s[50:51] op_sel_hi:[1,0]\n"
    "v_pk_mul_f32 v[226:227], v[226:227], s[50:51] op_sel_hi:[1,0]\n"
    "v_pk_mul_f32 v[228:229], v[228:229], s[50:51] op_sel_hi:[1,0]\n"
    "v_pk_mul_f32 v[230:231], v[230:231], s[50:51] op_sel_hi:[1,0]\n"
    "v_cvt_pk_bf16_f32 v4, v224, v225\n"
    "v_cvt_pk_bf16_f32 v5, v226, v227\n"
    "v_cvt_pk_bf16_f32 v6, v228, v229\n"
    "v_cvt_pk_bf16_f32 v7, v230, v231\n"
    "ds_write_b128 v242, v[0:3] offset:0\n"
    "ds_write_b128 v242, v[4:7] offset:1024\n"
    "v_lshlrev_b32 v216, 16, v8\n"
    "v_and_b32 v217, s17, v8\n"
    "v_lshlrev_b32 v218, 16, v9\n"
    "v_and_b32 v219, s17, v9\n"
    "v_lshlrev_b32 v220, 16, v10\n"
    "v_and_b32 v221, s17, v10\n"
    "v_lshlrev_b32 v222, 16, v11\n"
    "v_and_b32 v223, s17, v11\n"
    "v_pk_mul_f32 v[216:217], v[216:217], s[50:51] op_sel_hi:[1,0]\n"
    "v_pk_mul_f32 v[218:219], v[218:219], s[50:51] op_sel_hi:[1,0]\n"
    "v_pk_mul_f32 v[220:221], v[220:221], s[50:51] op_sel_hi:[1,0]\n"
    "v_pk_mul_f32 v[222:223], v[222:223], s[50:51] op_sel_hi:[1,0]\n"
    "v_cvt_pk_bf16_f32 v8, v216, v217\n"
    "v_cvt_pk_bf16_f32 v9, v218, v219\n"
    "v_cvt_pk_bf16_f32 v10, v220, v221\n"
    "v_cvt_pk_bf16_f32 v11, v222, v223\n"
    "v_lshlrev_b32 v224, 16, v12\n"
    "v_and_b32 v225, s17, v12\n"
    "v_lshlrev_b32 v226, 16, v13\n"
    "v_and_b32 v227, s17, v13\n"
    "v_lshlrev_b32 v228, 16, v14\n"
    "v_and_b32 v229, s17, v14\n"
    "v_lshlrev_b32 v230, 16, v15\n"
    "v_and_b32 v231, s17, v15\n"
    "v_pk_mul_f32 v[224:225], v[224:225], s[50:51] op_sel_hi:[1,0]\n"
    "v_pk_mul_f32 v[226:227], v[226:227], s[50:51] op_sel_hi:[1,0]\n"
    "v_pk_mul_f32 v[228:229], v[228:229], s[50:51] op_sel_hi:[1,0]\n"
    "v_pk_mul_f32 v[230:231], v[230:231], s[50:51] op_sel_hi:[1,0]\n"
    "v_cvt_pk_bf16_f32 v12, v224, v225\n"
    "v_cvt_pk_bf16_f32 v13, v226, v227\n"
    "v_cvt_pk_bf16_f32 v14, v228, v229\n"
    "v_cvt_pk_bf16_f32 v15, v230, v231\n"
    "ds_read_b128 v[0:3], v243 offset:0\n"
    "ds_read_b128 v[4:7], v232 offset:0\n"
    "ds_write_b128 v242, v[8:11] offset:2048\n"
    "ds_write_b128 v242, v[12:15] offset:3072\n"
    "v_lshlrev_b32 v216, 16, v16\n"
    "v_and_b32 v217, s17, v16\n"
    "v_lshlrev_b32 v218, 16, v17\n"
    "v_and_b32 v219, s17, v17\n"
    "v_lshlrev_b32 v220, 16, v18\n"
    "v_and_b32 v221, s17, v18\n"
    "v_lshlrev_b32 v222, 16, v19\n"
    "v_and_b32 v223, s17, v19\n"
    "v_pk_mul_f32 v[216:217], v[216:217], s[50:51] op_sel_hi:[1,0]\n"
    "v_pk_mul_f32 v[218:219], v[218:219], s[50:51] op_sel_hi:[1,0]\n"
    "v_pk_mul_f32 v[220:221], v[220:221], s[50:51] op_sel_hi:[1,0]\n"
    "v_pk_mul_f32 v[222:223], v[222:223], s[50:51] op_sel_hi:[1,0]\n"
    "v_cvt_pk_bf16_f32 v16, v216, v217\n"
    "v_cvt_pk_bf16_f32 v17, v218, v219\n"
    "v_cvt_pk_bf16_f32 v18, v220, v221\n"
    "v_cvt_pk_bf16_f32 v19, v222, v223\n"
    "v_lshlrev_b32 v224, 16, v20\n"
    "v_and_b32 v225, s17, v20\n"
    "v_lshlrev_b32 v226, 16, v21\n"
    "v_and_b32 v227, s17, v21\n"
    "v_lshlrev_b32 v228, 16, v22\n"
    "v_and_b32 v229, s17, v22\n"
    "v_lshlrev_b32 v230, 16, v23\n"
    "v_and_b32 v231, s17, v23\n"
    "v_pk_mul_f32 v[224:225], v[224:225], s[50:51] op_sel_hi:[1,0]\n"
    "v_pk_mul_f32 v[226:227], v[226:227], s[50:51] op_sel_hi:[1,0]\n"
    "v_pk_mul_f32 v[228:229], v[228:229], s[50:51] op_sel_hi:[1,0]\n"
    "v_pk_mul_f32 v[230:231], v[230:231], s[50:51] op_sel_hi:[1,0]\n"
    "v_cvt_pk_bf16_f32 v20, v224, v225\n"
    "v_cvt_pk_bf16_f32 v21, v226, v227\n"
    "v_cvt_pk_bf16_f32 v22, v228, v229\n"
    "v_cvt_pk_bf16_f32 v23, v230, v231\n"
    "ds_read_b128 v[8:11], v243 offset:2048\n"
    "ds_read_b128 v[12:15], v232 offset:2048\n"
    "ds_write_b128 v242, v[16:19] offset:0\n"
    "ds_write_b128 v242, v[20:23] offset:1024\n"
    "ds_read_b128 v[16:19], v243 offset:0\n"
    "ds_read_b128 v[20:23], v232 offset:0\n"
    "s_waitcnt lgkmcnt(7)\n"
    "v_mfma_f32_16x16x32_bf16 v[96:99], a[224:227], v[0:3], 0\n"
    "v_mfma_f32_16x16x32_bf16 v[96:99], a[228:231], v[4:7], v[96:99]\n"
    "s_waitcnt lgkmcnt(5)\n"
    "v_mfma_f32_16x16x32_bf16 v[96:99], a[232:235], v[8:11], v[96:99]\n"
    "s_waitcnt lgkmcnt(4)\n"
    "v_mfma_f32_16x16x32_bf16 v[96:99], a[236:239], v[12:15], v[96:99]\n"
    "s_waitcnt lgkmcnt(1)\n"
    "v_mfma_f32_16x16x32_bf16 v[96:99], a[240:243], v[16:19], v[96:99]\n"
    "s_waitcnt lgkmcnt(0)\n"
    "v_mfma_f32_16x16x32_bf16 v[96:99], a[244:247], v[20:23], v[96:99]\n"
    "v_mfma_f32_16x16x32_bf16 v[100:103], a[248:251], v[0:3], 0\n"
    "v_mfma_f32_16x16x32_bf16 v[100:103], a[252:255], v[4:7], v[100:103]\n"
    "v_mfma_f32_16x16x32_bf16 v[100:103], a[192:195], v[8:11], v[100:103]\n"
    "v_mfma_f32_16x16x32_bf16 v[100:103], a[196:199], v[12:15], v[100:103]\n"
    "v_mfma_f32_16x16x32_bf16 v[100:103], a[200:203], v[16:19], v[100:103]\n"
    "v_mfma_f32_16x16x32_bf16 v[100:103], a[204:207], v[20:23], v[100:103]\n"
    "s_nop 1\n"
    "v_max3_f32 v244, v96, v97, v98\n"
    "s_nop 4\n"
    "v_max3_f32 v244, v244, v99, v100\n"
    "v_max3_f32 v244, v244, v101, v102\n"
    "v_max_f32 v244, v244, v103\n"
    "v_mov_b32 v245, v244\n"
    "s_nop 1\n"
    "v_permlane32_swap_b32 v244, v245\n"
    "v_max_f32 v244, v244, v245\n"
    "v_mov_b32 v245, v244\n"
    "s_nop 1\n"
    "v_permlane16_swap_b32 v244, v245\n"
    "v_max_f32 v244, v244, v245\n"
    "v_sub_f32 v192, 0, v244\n"
    "v_sub_f32 v193, 0, v244\n"
    "v_sub_f32 v194, 0, v244\n"
    "v_sub_f32 v195, 0, v244\n"
    "v_sub_f32 v96, v96, v244\n"
    "v_sub_f32 v97, v97, v244\n"
    "v_sub_f32 v98, v98, v244\n"
    "v_sub_f32 v99, v99, v244\n"
    "v_sub_f32 v100, v100, v244\n"
    "v_sub_f32 v101, v101, v244\n"
    "v_sub_f32 v102, v102, v244\n"
    "v_sub_f32 v103, v103, v244\n"
    "v_exp_f32 v216, v96\n"
    "v_exp_f32 v217, v97\n"
    "v_exp_f32 v218, v98\n"
    "v_exp_f32 v219, v99\n"
    "v_cvt_pk_bf16_f32 v160, v216, v217\n"
    "v_cvt_pk_bf16_f32 v161, v218, v219\n"
    "v_add_f32 v212, v216, v217\n"
    "v_add_f32 v212, v212, v218\n"
    "v_add_f32 v212, v212, v219\n"
    "v_exp_f32 v216, v100\n"
    "v_exp_f32 v217, v101\n"
    "v_exp_f32 v218, v102\n"
    "v_exp_f32 v219, v103\n"
    "v_cvt_pk_bf16_f32 v162, v216, v217\n"
    "v_cvt_pk_bf16_f32 v163, v218, v219\n"
    "v_add_f32 v212, v212, v216\n"
    "v_add_f32 v212, v212, v217\n"
    "v_add_f32 v212, v212, v218\n"
    "v_add_f32 v212, v212, v219\n"
    "s_waitcnt vmcnt(28)\n"
    "v_lshlrev_b32 v216, 16, v24\n"
    "v_and_b32 v217, s17, v24\n"
    "v_lshlrev_b32 v218, 16, v25\n"
    "v_and_b32 v219, s17, v25\n"
    "v_lshlrev_b32 v220, 16, v26\n"
    "v_and_b32 v221, s17, v26\n"
    "v_lshlrev_b32 v222, 16, v27\n"
    "v_and_b32 v223, s17, v27\n"
    "v_pk_mul_f32 v[216:217], v[216:217], s[50:51] op_sel_hi:[1,0]\n"
    "v_pk_mul_f32 v[218:219], v[218:219], s[50:51] op_sel_hi:[1,0]\n"
    "v_pk_mul_f32 v[220:221], v[220:221], s[50:51] op_sel_hi:[1,0]\n"
    "v_pk_mul_f32 v[222:223], v[222:223], s[50:51] op_sel_hi:[1,0]\n"
    "v_cvt_pk_bf16_f32 v24, v216, v217\n"
    "v_cvt_pk_bf16_f32 v25, v218, v219\n"
    "v_cvt_pk_bf16_f32 v26, v220, v221\n"
    "v_cvt_pk_bf16_f32 v27, v222, v223\n"
    "v_lshlrev_b32 v224, 16, v28\n"
    "v_and_b32 v225, s17, v28\n"
    "v_lshlrev_b32 v226, 16, v29\n"
    "v_and_b32 v227, s17, v29\n"
    "v_lshlrev_b32 v228, 16, v30\n"
    "v_and_b32 v229, s17, v30\n"
    "v_lshlrev_b32 v230, 16, v31\n"
    "v_and_b32 v231, s17, v31\n"
    "v_pk_mul_f32 v[224:225], v[224:225], s[50:51] op_sel_hi:[1,0]\n"
    "v_pk_mul_f32 v[226:227], v[226:227], s[50:51] op_sel_hi:[1,0]\n"
    "v_pk_mul_f32 v[228:229], v[228:229], s[50:51] op_sel_hi:[1,0]\n"
    "v_pk_mul_f32 v[230:231], v[230:231], s[50:51] op_sel_hi:[1,0]\n"
    "v_cvt_pk_bf16_f32 v28, v224, v225\n"
    "v_cvt_pk_bf16_f32 v29, v226, v227\n"
    "v_cvt_pk_bf16_f32 v30, v228, v229\n"
    "v_cvt_pk_bf16_f32 v31, v230, v231\n"
    "ds_write_b128 v242, v[24:27] offset:2048\n"
    "ds_write_b128 v242, v[28:31] offset:3072\n"
    "v_lshlrev_b32 v216, 16, v32\n"
    "v_and_b32 v217, s17, v32\n"
    "v_lshlrev_b32 v218, 16, v33\n"
    "v_and_b32 v219, s17, v33\n"
    "v_lshlrev_b32 v220, 16, v34\n"
    "v_and_b32 v221, s17, v34\n"
    "v_lshlrev_b32 v222, 16, v35\n"
    "v_and_b32 v223, s17, v35\n"
    "v_pk_mul_f32 v[216:217], v[216:217], s[50:51] op_sel_hi:[1,0]\n"
    "v_pk_mul_f32 v[218:219], v[218:219], s[50:51] op_sel_hi:[1,0]\n"
    "v_pk_mul_f32 v[220:221], v[220:221], s[50:51] op_sel_hi:[1,0]\n"
    "v_pk_mul_f32 v[222:223], v[222:223], s[50:51] op_sel_hi:[1,0]\n"
    "v_cvt_pk_bf16_f32 v32, v216, v217\n"
    "v_cvt_pk_bf16_f32 v33, v218, v219\n"
    "v_cvt_pk_bf16_f32 v34, v220, v221\n"
    "v_cvt_pk_bf16_f32 v35, v222, v223\n"
    "v_lshlrev_b32 v224, 16, v36\n"
    "v_and_b32 v225, s17, v36\n"
    "v_lshlrev_b32 v226, 16, v37\n"
    "v_and_b32 v227, s17, v37\n"
    "v_lshlrev_b32 v228, 16, v38\n"
    "v_and_b32 v229, s17, v38\n"
    "v_lshlrev_b32 v230, 16, v39\n"
    "v_and_b32 v231, s17, v39\n"
    "v_pk_mul_f32 v[224:225], v[224:225], s[50:51] op_sel_hi:[1,0]\n"
    "v_pk_mul_f32 v[226:227], v[226:227], s[50:51] op_sel_hi:[1,0]\n"
    "v_pk_mul_f32 v[228:229], v[228:229], s[50:51] op_sel_hi:[1,0]\n"
    "v_pk_mul_f32 v[230:231], v[230:231], s[50:51] op_sel_hi:[1,0]\n"
    "v_cvt_pk_bf16_f32 v36, v224, v225\n"
    "v_cvt_pk_bf16_f32 v37, v226, v227\n"
    "v_cvt_pk_bf16_f32 v38, v228, v229\n"
    "v_cvt_pk_bf16_f32 v39, v230, v231\n"
    "ds_read_b128 v[24:27], v243 offset:2048\n"
    "ds_read_b128 v[28:31], v232 offset:2048\n"
    "ds_write_b128 v242, v[32:35] offset:0\n"
    "ds_write_b128 v242, v[36:39] offset:1024\n"
    "v_lshlrev_b32 v216, 16, v40\n"
    "v_and_b32 v217, s17, v40\n"
    "v_lshlrev_b32 v218, 16, v41\n"
    "v_and_b32 v219, s17, v41\n"
    "v_lshlrev_b32 v220, 16, v42\n"
    "v_and_b32 v221, s17, v42\n"
    "v_lshlrev_b32 v222, 16, v43\n"
    "v_and_b32 v223, s17, v43\n"
    "v_pk_mul_f32 v[216:217], v[216:217], s[50:51] op_sel_hi:[1,0]\n"
    "v_pk_mul_f32 v[218:219], v[218:219], s[50:51] op_sel_hi:[1,0]\n"
    "v_pk_mul_f32 v[220:221], v[220:221], s[50:51] op_sel_hi:[1,0]\n"
    "v_pk_mul_f32 v[222:223], v[222:223], s[50:51] op_sel_hi:[1,0]\n"
    "v_cvt_pk_bf16_f32 v40, v216, v217\n"
    "v_cvt_pk_bf16_f32 v41, v218, v219\n"
    "v_cvt_pk_bf16_f32 v42, v220, v221\n"
    "v_cvt_pk_bf16_f32 v43, v222, v223\n"
    "v_lshlrev_b32 v224, 16, v44\n"
    "v_and_b32 v225, s17, v44\n"
    "v_lshlrev_b32 v226, 16, v45\n"
    "v_and_b32 v227, s17, v45\n"
    "v_lshlrev_b32 v228, 16, v46\n"
    "v_and_b32 v229, s17, v46\n"
    "v_lshlrev_b32 v230, 16, v47\n"
    "v_and_b32 v231, s17, v47\n"
    "v_pk_mul_f32 v[224:225], v[224:225], s[50:51] op_sel_hi:[1,0]\n"
    "v_pk_mul_f32 v[226:227], v[226:227], s[50:51] op_sel_hi:[1,0]\n"
    "v_pk_mul_f32 v[228:229], v[228:229], s[50:51] op_sel_hi:[1,0]\n"
    "v_pk_mul_f32 v[230:231], v[230:231], s[50:51] op_sel_hi:[1,0]\n"
    "v_cvt_pk_bf16_f32 v44, v224, v225\n"
    "v_cvt_pk_bf16_f32 v45, v226, v227\n"
    "v_cvt_pk_bf16_f32 v46, v228, v229\n"
    "v_cvt_pk_bf16_f32 v47, v230, v231\n"
    "ds_read_b128 v[32:35], v243 offset:0\n"
    "ds_read_b128 v[36:39], v232 offset:0\n"
    "ds_write_b128 v242, v[40:43] offset:2048\n"
    "ds_write_b128 v242, v[44:47] offset:3072\n"
    "ds_read_b128 a[208:211], v235 offset:14336\n"
    "ds_read_b128 a[212:215], v235 offset:14848\n"
    "ds_read_b128 a[216:219], v235 offset:18432\n"
    "ds_read_b128 a[220:223], v235 offset:18944\n"
    "v_add_u32 v236, s41, v234\n"
    "v_xor_b32 v241, 32, v236\n"
    "s_mov_b32 s43, s40\n"
    "s_mov_b32 s40, s41\n"
    "s_mov_b32 s41, s42\n"
    "s_mov_b32 s42, s43\n"
    "ds_read_b128 v[40:43], v243 offset:2048\n"
    "ds_read_b128 v[44:47], v232 offset:2048\n"
    "s_waitcnt lgkmcnt(10)\n"
    "v_mfma_f32_16x16x32_bf16 v[104:107], a[224:227], v[24:27], 0\n"
    "v_mfma_f32_16x16x32_bf16 v[104:107], a[228:231], v[28:31], v[104:107]\n"
    "s_waitcnt lgkmcnt(9)\n"
    "v_mfma_f32_16x16x32_bf16 v[104:107], a[232:235], v[32:35], v[104:107]\n"
    "s_waitcnt lgkmcnt(8)\n"
    "v_mfma_f32_16x16x32_bf16 v[104:107], a[236:239], v[36:39], v[104:107]\n"
    "s_waitcnt lgkmcnt(1)\n"
    "v_mfma_f32_16x16x32_bf16 v[104:107], a[240:243], v[40:43], v[104:107]\n"
    "s_waitcnt lgkmcnt(0)\n"
    "v_mfma_f32_16x16x32_bf16 v[104:107], a[244:247], v[44:47], v[104:107]\n"
    "v_mfma_f32_16x16x32_bf16 v[108:111], a[248:251], v[24:27], 0\n"
    "v_mfma_f32_16x16x32_bf16 v[108:111], a[252:255], v[28:31], v[108:111]\n"
    "v_mfma_f32_16x16x32_bf16 v[108:111], a[192:195], v[32:35], v[108:111]\n"
    "v_mfma_f32_16x16x32_bf16 v[108:111], a[196:199], v[36:39], v[108:111]\n"
    "v_mfma_f32_16x16x32_bf16 v[108:111], a[200:203], v[40:43], v[108:111]\n"
    "v_mfma_f32_16x16x32_bf16 v[108:111], a[204:207], v[44:47], v[108:111]\n"
    "ds_read_b128 a[224:227], v235 offset:19456\n"
    "ds_read_b128 a[228:231], v235 offset:19968\n"
    "ds_read_b128 a[232:235], v235 offset:20480\n"
    "ds_read_b128 a[236:239], v235 offset:20992\n"
    "ds_read_b128 a[192:195], v235 offset:12288\n"
    "ds_read_b128 a[196:199], v235 offset:12800\n"
    "ds_read_b128 a[200:203], v235 offset:13312\n"
    "ds_read_b128 a[204:207], v235 offset:13824\n"
    "v_max3_f32 v246, v104, v105, v106\n"
    "v_max3_f32 v246, v246, v107, v108\n"
    "v_max3_f32 v246, v246, v109, v110\n"
    "v_max_f32 v246, v246, v111\n"
    "v_mov_b32 v247, v246\n"
    "s_nop 1\n"
    "v_permlane32_swap_b32 v246, v247\n"
    "v_max_f32 v246, v246, v247\n"
    "v_mov_b32 v247, v246\n"
    "s_nop 1\n"
    "v_permlane16_swap_b32 v246, v247\n"
    "v_max_f32 v246, v246, v247\n"
    "s_waitcnt lgkmcnt(0)\n"
    "LPEEL_2_%=:\n"
    "s_barrier\n"
    "s_mov_b32 s57, 3\n"
    "v_mfma_f32_16x16x32_bf16 v[128:131], a[192:195], v[0:3], v[192:195]\n"
    "ds_read_b64_tr_b16 a[240:241], v236 offset:0\n"
    "v_add_u32 v235, s41, v233\n"
    "v_mfma_f32_16x16x32_bf16 v[128:131], a[196:199], v[4:7], v[128:131]\n"
    "ds_read_b64_tr_b16 a[242:243], v236 offset:6144\n"
    "v_sub_f32 v196, 0, v246\n"
    "ds_read_b64_tr_b16 a[244:245], v241 offset:0\n"
    "v_sub_f32 v197, 0, v246\n"
    "v_sub_f32 v198, 0, v246\n"
    "v_sub_f32 v199, 0, v246\n"
    "v_mfma_f32_16x16x32_bf16 v[128:131], a[200:203], v[8:11], v[128:131]\n"
    "ds_read_b64_tr_b16 a[246:247], v241 offset:6144\n"
    "s_add_u32 m0, s41, s48\n"
    "v_mfma_f32_16x16x32_bf16 v[128:131], a[204:207], v[12:15], v[128:131]\n"
    "ds_read_b64_tr_b16 a[248:249], v236 offset:512\n"
    "v_sub_f32 v104, v104, v246\n"
    "ds_read_b64_tr_b16 a[250:251], v236 offset:6656\n"
    "v_sub_f32 v105, v105, v246\n"
    "v_sub_f32 v106, v106, v246\n"
    "v_sub_f32 v107, v107, v246\n"
    "v_mfma_f32_16x16x32_bf16 v[128:131], a[208:211], v[16:19], v[128:131]\n"
    "ds_read_b64_tr_b16 a[252:253], v241 offset:512\n"
    "v_sub_f32 v108, v108, v246\n"
    "v_mfma_f32_16x16x32_bf16 v[128:131], a[212:215], v[20:23], v[128:131]\n"
    "ds_read_b64_tr_b16 a[254:255], v241 offset:6656\n"
    "v_sub_f32 v109, v109, v246\n"
    "buffer_load_dwordx4 v237, s[24:27], s84 offen offset:0 lds\n"
    "v_sub_f32 v110, v110, v246\n"
    "v_sub_f32 v111, v111, v246\n"
    "v_mfma_f32_16x16x32_bf16 v[136:139], a[192:195], v[24:27], v[196:199]\n"
    "ds_read_b64_tr_b16 a[192:193], v236 offset:1024\n"
    "v_mfma_f32_16x16x32_bf16 v[136:139], a[196:199], v[28:31], v[136:139]\n"
    "ds_read_b64_tr_b16 a[194:195], v236 offset:7168\n"
    "ds_read_b64_tr_b16 a[196:197], v241 offset:1024\n"
    "v_exp_f32 v216, v104\n"
    "v_mfma_f32_16x16x32_bf16 v[136:139], a[200:203], v[32:35], v[136:139]\n"
    "ds_read_b64_tr_b16 a[198:199], v241 offset:7168\n"
    "v_mfma_f32_16x16x32_bf16 v[136:139], a[204:207], v[36:39], v[136:139]\n"
    "ds_read_b64_tr_b16 a[200:201], v236 offset:1536\n"
    "ds_read_b64_tr_b16 a[202:203], v236 offset:7680\n"
    "v_exp_f32 v217, v105\n"
    "v_mfma_f32_16x16x32_bf16 v[136:139], a[208:211], v[40:43], v[136:139]\n"
    "ds_read_b64_tr_b16 a[204:205], v241 offset:1536\n"
    "v_cvt_pk_bf16_f32 v164, v216, v217\n"
    "v_mfma_f32_16x16x32_bf16 v[136:139], a[212:215], v[44:47], v[136:139]\n"
    "ds_read_b64_tr_b16 a[206:207], v241 offset:7680\n"
    "v_add_f32 v213, v216, v217\n"
    "ds_read_b64_tr_b16 a[208:209], v236 offset:2048\n"
    "v_exp_f32 v218, v106\n"
    "v_mfma_f32_16x16x32_bf16 v[132:135], a[216:219], v[0:3], v[192:195]\n"
    "ds_read_b64_tr_b16 a[210:211], v236 offset:8192\n"
    "v_mfma_f32_16x16x32_bf16 v[132:135], a[220:223], v[4:7], v[132:135]\n"
    "ds_read_b64_tr_b16 a[212:213], v241 offset:2048\n"
    "ds_read_b64_tr_b16 a[214:215], v241 offset:8192\n"
    "v_exp_f32 v219, v107\n"
    "v_mfma_f32_16x16x32_bf16 v[132:135], a[224:227], v[8:11], v[132:135]\n"
    "buffer_load_dword v254, v253, s[92:95], s86 offen\n"
    "v_mfma_f32_16x16x32_bf16 v[132:135], a[228:231], v[12:15], v[132:135]\n"
    "buffer_load_dwordx4 v238, s[24:27], s84 offen offset:1024 lds\n"
    "buffer_load_dwordx4 v239, s[24:27], s84 offen offset:2048 lds\n"
    "v_exp_f32 v220, v108\n"
    "v_mfma_f32_16x16x32_bf16 v[132:135], a[232:235], v[16:19], v[132:135]\n"
    "s_add_u32 m0, m0, 3072\n"
    "s_add_u32 s45, s84, 3072\n"
    "v_mfma_f32_16x16x32_bf16 v[132:135], a[236:239], v[20:23], v[132:135]\n"
    "buffer_load_dwordx4 v237, s[24:27], s45 offen offset:0 lds\n"
    "buffer_load_dwordx4 v238, s[24:27], s45 offen offset:1024 lds\n"
    "v_exp_f32 v221, v109\n"
    "v_mfma_f32_16x16x32_bf16 v[140:143], a[216:219], v[24:27], v[196:199]\n"
    "ds_read_b64_tr_b16 a[216:217], v236 offset:2560\n"
    "s_sub_u32 s86, s86, 1048576\n"
    "v_mfma_f32_16x16x32_bf16 v[140:143], a[220:223], v[28:31], v[140:143]\n"
    "ds_read_b64_tr_b16 a[218:219], v236 offset:8704\n"
    "v_cvt_pk_bf16_f32 v165, v218, v219\n"
    "ds_read_b64_tr_b16 a[220:221], v241 offset:2560\n"
    "v_exp_f32 v222, v110\n"
    "v_add_f32 v213, v213, v218\n"
    "v_mfma_f32_16x16x32_bf16 v[140:143], a[224:227], v[32:35], v[140:143]\n"
    "buffer_load_dwordx4 v239, s[24:27], s45 offen offset:2048 lds\n"
    "v_mfma_f32_16x16x32_bf16 v[140:143], a[228:231], v[36:39], v[140:143]\n"
    "ds_read_b64_tr_b16 a[222:223], v241 offset:8704\n"
    "s_add_u32 m0, m0, 3072\n"
    "v_exp_f32 v223, v111\n"
    "v_add_f32 v213, v213, v219\n"
    "s_add_u32 s45, s84, 6144\n"
    "v_mfma_f32_16x16x32_bf16 v[140:143], a[232:235], v[40:43], v[140:143]\n"
    "buffer_load_dwordx4 v237, s[24:27], s45 offen offset:0 lds\n"
    "v_mfma_f32_16x16x32_bf16 v[140:143], a[236:239], v[44:47], v[140:143]\n"
    "buffer_load_dwordx4 v238, s[24:27], s45 offen offset:1024 lds\n"
    "buffer_load_dwordx4 v239, s[24:27], s45 offen offset:2048 lds\n"
    "v_cvt_pk_bf16_f32 v166, v220, v221\n"
    "v_add_f32 v213, v213, v220\n"
    "v_add_f32 v213, v213, v221\n"
    "v_cvt_pk_bf16_f32 v167, v222, v223\n"
    "s_add_u32 m0, m0, 3072\n"
    "s_add_u32 s45, s84, 9216\n"
    "buffer_load_dwordx4 v237, s[24:27], s45 offen offset:0 lds\n"
    "v_add_f32 v213, v213, v222\n"
    "v_add_f32 v213, v213, v223\n"
    "buffer_load_dwordx4 v238, s[24:27], s45 offen offset:1024 lds\n"
    "buffer_load_dwordx4 v239, s[24:27], s45 offen offset:2048 lds\n"
    "s_add_u32 m0, m0, 3072\n"
    "s_add_u32 s45, s84, 12288\n"
    "buffer_load_dwordx4 v237, s[24:27], s45 offen offset:0 lds\n"
    "buffer_load_dwordx4 v238, s[24:27], s45 offen offset:1024 lds\n"
    "buffer_load_dwordx4 v239, s[24:27], s45 offen offset:2048 lds\n"
    "s_mov_b32 s84, s44\n"
    "s_add_u32 s87, s87, 1\n"
    "v_readlane_b32 s44, v252, s87\n"
    "s_waitcnt vmcnt(37)\n"
    "s_barrier\n"
    "s_waitcnt lgkmcnt(1)\n"
    "v_mfma_f32_16x16x32_bf16 a[0:3], a[240:243], v[160:163], 0\n"
    "v_mov_b32 v208, v212\n"
    "s_add_u32 m0, s42, s48\n"
    "v_mfma_f32_16x16x32_bf16 a[48:51], a[240:243], v[164:167], 0\n"
    "ds_read_b128 a[224:227], v235 offset:0\n"
    "v_mov_b32 v209, v213\n"
    "buffer_load_dwordx4 v237, s[24:27], s84 offen offset:0 lds\n"
    "v_exp_f32 v216, v128\n"
    "v_mfma_f32_16x16x32_bf16 a[4:7], a[244:247], v[160:163], 0\n"
    "ds_read_b128 a[228:231], v235 offset:512\n"
    "v_mfma_f32_16x16x32_bf16 a[52:55], a[244:247], v[164:167], 0\n"
    "buffer_load_dwordx4 v238, s[24:27], s84 offen offset:1024 lds\n"
    "ds_read_b128 a[232:235], v235 offset:1024\n"
    "v_exp_f32 v217, v129\n"
    "v_mfma_f32_16x16x32_bf16 a[8:11], a[248:251], v[160:163], 0\n"
    "buffer_load_dwordx4 v239, s[24:27], s84 offen offset:2048 lds\n"
    "v_mfma_f32_16x16x32_bf16 a[56:59], a[248:251], v[164:167], 0\n"
    "ds_read_b128 a[236:239], v235 offset:1536\n"
    "s_add_u32 m0, m0, 3072\n"
    "v_exp_f32 v218, v130\n"
    "v_cvt_pk_bf16_f32 v176, v216, v217\n"
    "s_add_u32 s45, s84, 3072\n"
    "v_mfma_f32_16x16x32_bf16 a[12:15], a[252:255], v[160:163], 0\n"
    "ds_read_b128 a[240:243], v235 offset:2048\n"
    "v_add_f32 v212, v216, v217\n"
    "v_mfma_f32_16x16x32_bf16 a[60:63], a[252:255], v[164:167], 0\n"
    "buffer_load_dwordx4 v237, s[24:27], s45 offen offset:0 lds\n"
    "ds_read_b128 a[244:247], v235 offset:2560\n"
    "v_exp_f32 v219, v131\n"
    "v_mfma_f32_16x16x32_bf16 a[16:19], a[192:195], v[160:163], 0\n"
    "buffer_load_dwordx4 v238, s[24:27], s45 offen offset:1024 lds\n"
    "v_mfma_f32_16x16x32_bf16 a[64:67], a[192:195], v[164:167], 0\n"
    "ds_read_b128 a[248:251], v235 offset:6144\n"
    "v_cvt_pk_bf16_f32 v177, v218, v219\n"
    "buffer_load_dwordx4 v239, s[24:27], s45 offen offset:2048 lds\n"
    "v_exp_f32 v220, v136\n"
    "v_mfma_f32_16x16x32_bf16 a[20:23], a[196:199], v[160:163], 0\n"
    "ds_read_b128 a[252:255], v235 offset:6656\n"
    "s_add_u32 m0, m0, 3072\n"
    "v_mfma_f32_16x16x32_bf16 a[68:71], a[196:199], v[164:167], 0\n"
    "v_add_f32 v212, v212, v218\n"
    "s_add_u32 s45, s84, 6144\n"
    "ds_read_b128 a[192:195], v235 offset:7168\n"
    "v_exp_f32 v221, v137\n"
    "v_add_f32 v212, v212, v219\n"
    "v_mfma_f32_16x16x32_bf16 a[24:27], a[200:203], v[160:163], 0\n"
    "buffer_load_dwordx4 v237, s[24:27], s45 offen offset:0 lds\n"
    "v_mfma_f32_16x16x32_bf16 a[72:75], a[200:203], v[164:167], 0\n"
    "ds_read_b128 a[196:199], v235 offset:7680\n"
    "v_cvt_pk_bf16_f32 v180, v220, v221\n"
    "buffer_load_dwordx4 v238, s[24:27], s45 offen offset:1024 lds\n"
    "v_exp_f32 v222, v138\n"
    "v_mfma_f32_16x16x32_bf16 a[28:31], a[204:207], v[160:163], 0\n"
    "ds_read_b128 a[200:203], v235 offset:8192\n"
    "v_add_f32 v213, v220, v221\n"
    "v_mfma_f32_16x16x32_bf16 a[76:79], a[204:207], v[164:167], 0\n"
    "buffer_load_dwordx4 v239, s[24:27], s45 offen offset:2048 lds\n"
    "ds_read_b128 a[204:207], v235 offset:8704\n"
    "s_add_u32 m0, m0, 3072\n"
    "s_add_u32 s45, s84, 9216\n"
    "v_mfma_f32_16x16x32_bf16 a[32:35], a[208:211], v[160:163], 0\n"
    "v_exp_f32 v223, v139\n"
    "v_mfma_f32_16x16x32_bf16 a[80:83], a[208:211], v[164:167], 0\n"
    "buffer_load_dwordx4 v237, s[24:27], s45 offen offset:0 lds\n"
    "buffer_load_dwordx4 v238, s[24:27], s45 offen offset:1024 lds\n"
    "v_exp_f32 v224, v132\n"
    "v_mfma_f32_16x16x32_bf16 a[36:39], a[212:215], v[160:163], 0\n"
    "buffer_load_dwordx4 v239, s[24:27], s45 offen offset:2048 lds\n"
    "v_mfma_f32_16x16x32_bf16 a[84:87], a[212:215], v[164:167], 0\n"
    "s_add_u32 m0, m0, 3072\n"
    "s_add_u32 s45, s84, 12288\n"
    "v_exp_f32 v225, v133\n"
    "v_cvt_pk_bf16_f32 v181, v222, v223\n"
    "v_mfma_f32_16x16x32_bf16 a[40:43], a[216:219], v[160:163], 0\n"
    "buffer_load_dwordx4 v237, s[24:27], s45 offen offset:0 lds\n"
    "v_mfma_f32_16x16x32_bf16 a[88:91], a[216:219], v[164:167], 0\n"
    "v_exp_f32 v226, v134\n"
    "buffer_load_dwordx4 v238, s[24:27], s45 offen offset:1024 lds\n"
    "v_exp_f32 v227, v135\n"
    "s_waitcnt lgkmcnt(1)\n"
    "v_mfma_f32_16x16x32_bf16 a[44:47], a[220:223], v[160:163], 0\n"
    "v_exp_f32 v228, v140\n"
    "v_mfma_f32_16x16x32_bf16 a[92:95], a[220:223], v[164:167], 0\n"
    "buffer_load_dwordx4 v239, s[24:27], s45 offen offset:2048 lds\n"
    "v_exp_f32 v229, v141\n"
    "v_exp_f32 v230, v142\n"
    "s_waitcnt vmcnt(49)\n"
    "s_barrier\n"
    "v_mfma_f32_16x16x32_bf16 v[96:99], a[224:227], v[0:3], v[192:195]\n"
    "ds_read_b64_tr_b16 a[208:209], v236 offset:12288\n"
    "s_add_u32 m0, s40, s47\n"
    "v_mfma_f32_16x16x32_bf16 v[96:99], a[228:231], v[4:7], v[96:99]\n"
    "ds_read_b64_tr_b16 a[210:211], v236 offset:18432\n"
    "v_add_f32 v213, v213, v222\n"
    "ds_read_b64_tr_b16 a[212:213], v241 offset:12288\n"
    "v_exp_f32 v231, v143\n"
    "v_add_f32 v213, v213, v223\n"
    "v_mfma_f32_16x16x32_bf16 v[96:99], a[232:235], v[8:11], v[96:99]\n"
    "ds_read_b64_tr_b16 a[214:215], v241 offset:18432\n"
    "v_cvt_pk_bf16_f32 v178, v224, v225\n"
    "v_mfma_f32_16x16x32_bf16 v[96:99], a[236:239], v[12:15], v[96:99]\n"
    "ds_read_b64_tr_b16 a[216:217], v236 offset:12800\n"
    "v_add_f32 v212, v212, v224\n"
    "ds_read_b64_tr_b16 a[218:219], v236 offset:18944\n"
    "v_add_f32 v212, v212, v225\n"
    "v_cvt_pk_bf16_f32 v179, v226, v227\n"
    "v_add_f32 v212, v212, v226\n"
    "v_mfma_f32_16x16x32_bf16 v[96:99], a[240:243], v[16:19], v[96:99]\n"
    "ds_read_b64_tr_b16 a[220:221], v241 offset:12800\n"
    "v_add_f32 v212, v212, v227\n"
    "v_mfma_f32_16x16x32_bf16 v[96:99], a[244:247], v[20:23], v[96:99]\n"
    "ds_read_b64_tr_b16 a[222:223], v241 offset:18944\n"
    "v_cvt_pk_bf16_f32 v182, v228, v229\n"
    "buffer_load_dwordx4 v237, s[20:23], s44 offen offset:0 lds\n"
    "v_add_f32 v213, v213, v228\n"
    "v_add_f32 v213, v213, v229\n"
    "v_mfma_f32_16x16x32_bf16 v[104:107], a[224:227], v[24:27], v[196:199]\n"
    "ds_read_b64_tr_b16 a[224:225], v236 offset:13312\n"
    "v_cvt_pk_bf16_f32 v183, v230, v231\n"
    "v_mfma_f32_16x16x32_bf16 v[104:107], a[228:231], v[28:31], v[104:107]\n"
    "ds_read_b64_tr_b16 a[226:227], v236 offset:19456\n"
    "v_add_f32 v213, v213, v230\n"
    "ds_read_b64_tr_b16 a[228:229], v241 offset:13312\n"
    "v_add_f32 v213, v213, v231\n"
    "v_mfma_f32_16x16x32_bf16 v[104:107], a[232:235], v[32:35], v[104:107]\n"
    "ds_read_b64_tr_b16 a[230:231], v241 offset:19456\n"
    "v_mfma_f32_16x16x32_bf16 v[104:107], a[236:239], v[36:39], v[104:107]\n"
    "ds_read_b64_tr_b16 a[232:233], v236 offset:13824\n"
    "ds_read_b64_tr_b16 a[234:235], v236 offset:19968\n"
    "v_mfma_f32_16x16x32_bf16 v[104:107], a[240:243], v[40:43], v[104:107]\n"
    "ds_read_b64_tr_b16 a[236:237], v241 offset:13824\n"
    "v_mfma_f32_16x16x32_bf16 v[104:107], a[244:247], v[44:47], v[104:107]\n"
    "ds_read_b64_tr_b16 a[238:239], v241 offset:19968\n"
    "ds_read_b64_tr_b16 a[240:241], v236 offset:14336\n"
    "v_mfma_f32_16x16x32_bf16 v[100:103], a[248:251], v[0:3], v[192:195]\n"
    "ds_read_b64_tr_b16 a[242:243], v236 offset:20480\n"
    "v_mfma_f32_16x16x32_bf16 v[100:103], a[252:255], v[4:7], v[100:103]\n"
    "ds_read_b64_tr_b16 a[244:245], v241 offset:14336\n"
    "ds_read_b64_tr_b16 a[246:247], v241 offset:20480\n"
    "v_mfma_f32_16x16x32_bf16 v[100:103], a[192:195], v[8:11], v[100:103]\n"
    "buffer_load_dwordx4 v238, s[20:23], s44 offen offset:1024 lds\n"
    "v_mfma_f32_16x16x32_bf16 v[100:103], a[196:199], v[12:15], v[100:103]\n"
    "buffer_load_dwordx4 v239, s[20:23], s44 offen offset:2048 lds\n"
    "s_add_u32 m0, m0, 3072\n"
    "s_add_u32 s45, s44, 3072\n"
    "v_mfma_f32_16x16x32_bf16 v[100:103], a[200:203], v[16:19], v[100:103]\n"
    "s_waitcnt lgkmcnt(10)\n"
    "v_mfma_f32_16x16x32_bf16 v[100:103], a[204:207], v[20:23], v[100:103]\n"
    "buffer_load_dwordx4 v237, s[20:23], s45 offen offset:0 lds\n"
    "buffer_load_dwordx4 v238, s[20:23], s45 offen offset:1024 lds\n"
    "v_mfma_f32_16x16x32_bf16 v[108:111], a[248:251], v[24:27], v[196:199]\n"
    "ds_read_b64_tr_b16 a[248:249], v236 offset:14848\n"
    "v_mfma_f32_16x16x32_bf16 v[108:111], a[252:255], v[28:31], v[108:111]\n"
    "ds_read_b64_tr_b16 a[250:251], v236 offset:20992\n"
    "ds_read_b64_tr_b16 a[252:253], v241 offset:14848\n"
    "v_mfma_f32_16x16x32_bf16 v[108:111], a[192:195], v[32:35], v[108:111]\n"
    "buffer_load_dwordx4 v239, s[20:23], s45 offen offset:2048 lds\n"
    "v_mfma_f32_16x16x32_bf16 v[108:111], a[196:199], v[36:39], v[108:111]\n"
    "ds_read_b64_tr_b16 a[254:255], v241 offset:20992\n"
    "v_max_f32 v232, v212, v213\n"
    "v_cmp_nge_f32 vcc, s49, v232\n"
    "s_add_u32 m0, m0, 3072\n"
    "s_add_u32 s45, s44, 6144\n"
    "v_mfma_f32_16x16x32_bf16 v[108:111], a[200:203], v[40:43], v[108:111]\n"
    "buffer_load_dwordx4 v237, s[20:23], s45 offen offset:0 lds\n"
    "v_mfma_f32_16x16x32_bf16 v[108:111], a[204:207], v[44:47], v[108:111]\n"
    "buffer_load_dwordx4 v238, s[20:23], s45 offen offset:1024 lds\n"
    "buffer_load_dwordx4 v239, s[20:23], s45 offen offset:2048 lds\n"
    "s_add_u32 m0, m0, 3072\n"
    "s_cbranch_vccnz LRARE_o_2_%=\n"
    "LRET_o_2_P0_%=:\n"
    "s_waitcnt vmcnt(55)\n"
    "s_barrier\n"
    "v_mfma_f32_16x16x32_bf16 a[0:3], a[208:211], v[176:179], a[0:3]\n"
    "v_exp_f32 v216, v96\n"
    "v_mfma_f32_16x16x32_bf16 a[48:51], a[208:211], v[180:183], a[48:51]\n"
    "ds_read_b128 a[192:195], v235 offset:12288\n"
    "s_add_u32 s45, s44, 9216\n"
    "buffer_load_dwordx4 v237, s[20:23], s45 offen offset:0 lds\n"
    "v_exp_f32 v217, v97\n"
    "v_mfma_f32_16x16x32_bf16 a[4:7], a[212:215], v[176:179], a[4:7]\n"
    "ds_read_b128 a[196:199], v235 offset:12800\n"
    "v_add_f32 v208, v208, v212\n"
    "v_mfma_f32_16x16x32_bf16 a[52:55], a[212:215], v[180:183], a[52:55]\n"
    "buffer_load_dwordx4 v238, s[20:23], s45 offen offset:1024 lds\n"
    "ds_read_b128 a[200:203], v235 offset:13312\n"
    "v_exp_f32 v218, v98\n"
    "v_add_f32 v209, v209, v213\n"
    "v_mfma_f32_16x16x32_bf16 a[8:11], a[216:219], v[176:179], a[8:11]\n"
    "buffer_load_dwordx4 v239, s[20:23], s45 offen offset:2048 lds\n"
    "v_mfma_f32_16x16x32_bf16 a[56:59], a[216:219], v[180:183], a[56:59]\n"
    "ds_read_b128 a[204:207], v235 offset:13824\n"
    "s_add_u32 m0, m0, 3072\n"
    "v_exp_f32 v219, v99\n"
    "v_cvt_pk_bf16_f32 v160, v216, v217\n"
    "s_add_u32 s45, s44, 12288\n"
    "v_mfma_f32_16x16x32_bf16 a[12:15], a[220:223], v[176:179], a[12:15]\n"
    "ds_read_b128 a[208:211], v235 offset:14336\n"
    "v_add_f32 v212, v216, v217\n"
    "v_mfma_f32_16x16x32_bf16 a[60:63], a[220:223], v[180:183], a[60:63]\n"
    "buffer_load_dwordx4 v237, s[20:23], s45 offen offset:0 lds\n"
    "ds_read_b128 a[212:215], v235 offset:14848\n"
    "v_exp_f32 v220, v104\n"
    "v_cvt_pk_bf16_f32 v161, v218, v219\n"
    "v_mfma_f32_16x16x32_bf16 a[16:19], a[224:227], v[176:179], a[16:19]\n"
    "buffer_load_dwordx4 v238, s[20:23], s45 offen offset:1024 lds\n"
    "v_mfma_f32_16x16x32_bf16 a[64:67], a[224:227], v[180:183], a[64:67]\n"
    "ds_read_b128 a[216:219], v235 offset:18432\n"
    "buffer_load_dwordx4 v239, s[20:23], s45 offen offset:2048 lds\n"
    "v_exp_f32 v221, v105\n"
    "s_waitcnt lgkmcnt(5)\n"
    "v_mfma_f32_16x16x32_bf16 a[20:23], a[228:231], v[176:179], a[20:23]\n"
    "ds_read_b128 a[220:223], v235 offset:18944\n"
    "v_add_u32 v236, s41, v234\n"
    "v_mfma_f32_16x16x32_bf16 a[68:71], a[228:231], v[180:183], a[68:71]\n"
    "v_exp_f32 v222, v106\n"
    "ds_read_b128 a[224:227], v235 offset:19456\n"
    "v_xor_b32 v241, 32, v236\n"
    "v_exp_f32 v223, v107\n"
    "v_mfma_f32_16x16x32_bf16 a[24:27], a[232:235], v[176:179], a[24:27]\n"
    "v_exp_f32 v224, v100\n"
    "v_mfma_f32_16x16x32_bf16 a[72:75], a[232:235], v[180:183], a[72:75]\n"
    "ds_read_b128 a[228:231], v235 offset:19968\n"
    "s_mov_b32 s43, s40\n"
    "v_exp_f32 v225, v101\n"
    "v_exp_f32 v226, v102\n"
    "v_mfma_f32_16x16x32_bf16 a[28:31], a[236:239], v[176:179], a[28:31]\n"
    "ds_read_b128 a[232:235], v235 offset:20480\n"
    "s_mov_b32 s40, s41\n"
    "v_mfma_f32_16x16x32_bf16 a[76:79], a[236:239], v[180:183], a[76:79]\n"
    "v_exp_f32 v227, v103\n"
    "ds_read_b128 a[236:239], v235 offset:20992\n"
    "s_mov_b32 s41, s42\n"
    "s_mov_b32 s42, s43\n"
    "s_mov_b32 s84, s44\n"
    "v_mfma_f32_16x16x32_bf16 a[32:35], a[240:243], v[176:179], a[32:35]\n"
    "v_add_f32 v212, v212, v218\n"
    "s_add_u32 s87, s87, 1\n"
    "v_mfma_f32_16x16x32_bf16 a[80:83], a[240:243], v[180:183], a[80:83]\n"
    "v_readlane_b32 s44, v252, s87\n"
    "v_add_f32 v212, v212, v219\n"
    "v_exp_f32 v228, v108\n"
    "v_exp_f32 v229, v109\n"
    "v_mfma_f32_16x16x32_bf16 a[36:39], a[244:247], v[176:179], a[36:39]\n"
    "v_cvt_pk_bf16_f32 v164, v220, v221\n"
    "v_add_f32 v213, v220, v221\n"
    "v_mfma_f32_16x16x32_bf16 a[84:87], a[244:247], v[180:183], a[84:87]\n"
    "v_exp_f32 v230, v110\n"
    "v_exp_f32 v231, v111\n"
    "v_cvt_pk_bf16_f32 v165, v222, v223\n"
    "v_add_f32 v213, v213, v222\n"
    "v_mfma_f32_16x16x32_bf16 a[40:43], a[248:251], v[176:179], a[40:43]\n"
    "v_add_f32 v213, v213, v223\n"
    "v_cvt_pk_bf16_f32 v162, v224, v225\n"
    "v_mfma_f32_16x16x32_bf16 a[88:91], a[248:251], v[180:183], a[88:91]\n"
    "v_add_f32 v212, v212, v224\n"
    "v_add_f32 v212, v212, v225\n"
    "v_cvt_pk_bf16_f32 v163, v226, v227\n"
    "v_add_f32 v212, v212, v226\n"
    "v_add_f32 v212, v212, v227\n"
    "v_cvt_pk_bf16_f32 v166, v228, v229\n"
    "v_mfma_f32_16x16x32_bf16 a[44:47], a[252:255], v[176:179], a[44:47]\n"
    "v_add_f32 v213, v213, v228\n"
    "s_waitcnt vmcnt(30) lgkmcnt(0)\n"
    "v_mfma_f32_16x16x32_bf16 a[92:95], a[252:255], v[180:183], a[92:95]\n"
    "s_barrier\n"
    "v_add_f32 v213, v213, v229\n"
    "v_cvt_pk_bf16_f32 v167, v230, v231\n"
    "s_cmp_gt_u32 s87, s46\n"
    "s_cselect_b32 s71, -1.0, s49\n"
    "s_mov_b32 s57, 0\n"
    "s_branch LLOOPA_2_%=\n"
    ".p2align 6\n"
    "LLOOP_2_%=:\n"
    "v_mfma_f32_16x16x32_bf16 a[0:3], a[240:243], v[160:163], a[0:3]\n"
    "v_add_f32 v208, v208, v212\n"
    "s_add_u32 m0, s42, s48\n"
    "v_mfma_f32_16x16x32_bf16 a[48:51], a[240:243], v[164:167], a[48:51]\n"
    "ds_read_b128 a[224:227], v235 offset:0\n"
    "v_add_f32 v209, v209, v213\n"
    "buffer_load_dwordx4 v237, s[24:27], s84 offen offset:0 lds\n"
    "v_exp_f32 v216, v128\n"
    "v_mfma_f32_16x16x32_bf16 a[4:7], a[244:247], v[160:163], a[4:7]\n"
    "ds_read_b128 a[228:231], v235 offset:512\n"
    "v_mfma_f32_16x16x32_bf16 a[52:55], a[244:247], v[164:167], a[52:55]\n"
    "buffer_load_dwordx4 v238, s[24:27], s84 offen offset:1024 lds\n"
    "ds_read_b128 a[232:235], v235 offset:1024\n"
    "v_exp_f32 v217, v129\n"
    "v_mfma_f32_16x16x32_bf16 a[8:11], a[248:251], v[160:163], a[8:11]\n"
    "buffer_load_dwordx4 v239, s[24:27], s84 offen offset:2048 lds\n"
    "v_mfma_f32_16x16x32_bf16 a[56:59], a[248:251], v[164:167], a[56:59]\n"
    "ds_read_b128 a[236:239], v235 offset:1536\n"
    "s_add_u32 m0, m0, 3072\n"
    "v_exp_f32 v218, v130\n"
    "v_cvt_pk_bf16_f32 v176, v216, v217\n"
    "s_add_u32 s45, s84, 3072\n"
    "v_mfma_f32_16x16x32_bf16 a[12:15], a[252:255], v[160:163], a[12:15]\n"
    "ds_read_b128 a[240:243], v235 offset:2048\n"
    "v_add_f32 v212, v216, v217\n"
    "v_mfma_f32_16x16x32_bf16 a[60:63], a[252:255], v[164:167], a[60:63]\n"
    "buffer_load_dwordx4 v237, s[24:27], s45 offen offset:0 lds\n"
    "ds_read_b128 a[244:247], v235 offset:2560\n"
    "v_exp_f32 v219, v131\n"
    "v_mfma_f32_16x16x32_bf16 a[16:19], a[192:195], v[160:163], a[16:19]\n"
    "buffer_load_dwordx4 v238, s[24:27], s45 offen offset:1024 lds\n"
    "v_mfma_f32_16x16x32_bf16 a[64:67], a[192:195], v[164:167], a[64:67]\n"
    "ds_read_b128 a[248:251], v235 offset:6144\n"
    "v_cvt_pk_bf16_f32 v177, v218, v219\n"
    "buffer_load_dwordx4 v239, s[24:27], s45 offen offset:2048 lds\n"
    "v_exp_f32 v220, v136\n"
    "v_mfma_f32_16x16x32_bf16 a[20:23], a[196:199], v[160:163], a[20:23]\n"
    "ds_read_b128 a[252:255], v235 offset:6656\n"
    "s_add_u32 m0, m0, 3072\n"
    "v_mfma_f32_16x16x32_bf16 a[68:71], a[196:199], v[164:167], a[68:71]\n"
    "v_add_f32 v212, v212, v218\n"
    "s_add_u32 s45, s84, 6144\n"
    "ds_read_b128 a[192:195], v235 offset:7168\n"
    "v_exp_f32 v221, v137\n"
    "v_add_f32 v212, v212, v219\n"
    "v_mfma_f32_16x16x32_bf16 a[24:27], a[200:203], v[160:163], a[24:27]\n"
    "buffer_load_dwordx4 v237, s[24:27], s45 offen offset:0 lds\n"
    "v_mfma_f32_16x16x32_bf16 a[72:75], a[200:203], v[164:167], a[72:75]\n"
    "ds_read_b128 a[196:199], v235 offset:7680\n"
    "v_cvt_pk_bf16_f32 v180, v220, v221\n"
    "buffer_load_dwordx4 v238, s[24:27], s45 offen offset:1024 lds\n"
    "v_exp_f32 v222, v138\n"
    "v_mfma_f32_16x16x32_bf16 a[28:31], a[204:207], v[160:163], a[28:31]\n"
    "ds_read_b128 a[200:203], v235 offset:8192\n"
    "v_add_f32 v213, v220, v221\n"
    "v_mfma_f32_16x16x32_bf16 a[76:79], a[204:207], v[164:167], a[76:79]\n"
    "buffer_load_dwordx4 v239, s[24:27], s45 offen offset:2048 lds\n"
    "ds_read_b128 a[204:207], v235 offset:8704\n"
    "s_add_u32 m0, m0, 3072\n"
    "s_add_u32 s45, s84, 9216\n"
    "v_mfma_f32_16x16x32_bf16 a[32:35], a[208:211], v[160:163], a[32:35]\n"
    "v_exp_f32 v223, v139\n"
    "v_mfma_f32_16x16x32_bf16 a[80:83], a[208:211], v[164:167], a[80:83]\n"
    "buffer_load_dwordx4 v237, s[24:27], s45 offen offset:0 lds\n"
    "buffer_load_dwordx4 v238, s[24:27], s45 offen offset:1024 lds\n"
    "v_exp_f32 v224, v132\n"
    "v_mfma_f32_16x16x32_bf16 a[36:39], a[212:215], v[160:163], a[36:39]\n"
    "buffer_load_dwordx4 v239, s[24:27], s45 offen offset:2048 lds\n"
    "v_mfma_f32_16x16x32_bf16 a[84:87], a[212:215], v[164:167], a[84:87]\n"
    "s_add_u32 m0, m0, 3072\n"
    "s_add_u32 s45, s84, 12288\n"
    "v_exp_f32 v225, v133\n"
    "v_cvt_pk_bf16_f32 v181, v222, v223\n"
    "s_waitcnt lgkmcnt(2)\n"
    "v_mfma_f32_16x16x32_bf16 a[40:43], a[216:219], v[160:163], a[40:43]\n"
    "buffer_load_dwordx4 v237, s[24:27], s45 offen offset:0 lds\n"
    "v_mfma_f32_16x16x32_bf16 a[88:91], a[216:219], v[164:167], a[88:91]\n"
    "v_exp_f32 v226, v134\n"
    "buffer_load_dwordx4 v238, s[24:27], s45 offen offset:1024 lds\n"
    "v_exp_f32 v227, v135\n"
    "v_mfma_f32_16x16x32_bf16 a[44:47], a[220:223], v[160:163], a[44:47]\n"
    "v_exp_f32 v228, v140\n"
    "v_mfma_f32_16x16x32_bf16 a[92:95], a[220:223], v[164:167], a[92:95]\n"
    "buffer_load_dwordx4 v239, s[24:27], s45 offen offset:2048 lds\n"
    "v_exp_f32 v229, v141\n"
    "v_exp_f32 v230, v142\n"
    "v_mfma_f32_16x16x32_bf16 v[96:99], a[224:227], v[0:3], v[192:195]\n"
    "ds_read_b64_tr_b16 a[208:209], v236 offset:12288\n"
    "s_add_u32 m0, s40, s47\n"
    "v_mfma_f32_16x16x32_bf16 v[96:99], a[228:231], v[4:7], v[96:99]\n"
    "ds_read_b64_tr_b16 a[210:211], v236 offset:18432\n"
    "v_add_f32 v213, v213, v222\n"
    "ds_read_b64_tr_b16 a[212:213], v241 offset:12288\n"
    "v_exp_f32 v231, v143\n"
    "v_add_f32 v213, v213, v223\n"
    "v_mfma_f32_16x16x32_bf16 v[96:99], a[232:235], v[8:11], v[96:99]\n"
    "ds_read_b64_tr_b16 a[214:215], v241 offset:18432\n"
    "v_cvt_pk_bf16_f32 v178, v224, v225\n"
    "v_mfma_f32_16x16x32_bf16 v[96:99], a[236:239], v[12:15], v[96:99]\n"
    "ds_read_b64_tr_b16 a[216:217], v236 offset:12800\n"
    "v_add_f32 v212, v212, v224\n"
    "ds_read_b64_tr_b16 a[218:219], v236 offset:18944\n"
    "v_add_f32 v212, v212, v225\n"
    "v_cvt_pk_bf16_f32 v179, v226, v227\n"
    "v_add_f32 v212, v212, v226\n"
    "v_mfma_f32_16x16x32_bf16 v[96:99], a[240:243], v[16:19], v[96:99]\n"
    "ds_read_b64_tr_b16 a[220:221], v241 offset:12800\n"
    "v_add_f32 v212, v212, v227\n"
    "v_mfma_f32_16x16x32_bf16 v[96:99], a[244:247], v[20:23], v[96:99]\n"
    "ds_read_b64_tr_b16 a[222:223], v241 offset:18944\n"
    "v_cvt_pk_bf16_f32 v182, v228, v229\n"
    "buffer_load_dwordx4 v237, s[20:23], s44 offen offset:0 lds\n"
    "v_add_f32 v213, v213, v228\n"
    "v_add_f32 v213, v213, v229\n"
    "v_mfma_f32_16x16x32_bf16 v[104:107], a[224:227], v[24:27], v[196:199]\n"
    "ds_read_b64_tr_b16 a[224:225], v236 offset:13312\n"
    "v_cvt_pk_bf16_f32 v183, v230, v231\n"
    "v_mfma_f32_16x16x32_bf16 v[104:107], a[228:231], v[28:31], v[104:107]\n"
    "ds_read_b64_tr_b16 a[226:227], v236 offset:19456\n"
    "v_add_f32 v213, v213, v230\n"
    "ds_read_b64_tr_b16 a[228:229], v241 offset:13312\n"
    "v_add_f32 v213, v213, v231\n"
    "v_mfma_f32_16x16x32_bf16 v[104:107], a[232:235], v[32:35], v[104:107]\n"
    "ds_read_b64_tr_b16 a[230:231], v241 offset:19456\n"
    "v_mfma_f32_16x16x32_bf16 v[104:107], a[236:239], v[36:39], v[104:107]\n"
    "ds_read_b64_tr_b16 a[232:233], v236 offset:13824\n"
    "ds_read_b64_tr_b16 a[234:235], v236 offset:19968\n"
    "v_mfma_f32_16x16x32_bf16 v[104:107], a[240:243], v[40:43], v[104:107]\n"
    "ds_read_b64_tr_b16 a[236:237], v241 offset:13824\n"
    "v_mfma_f32_16x16x32_bf16 v[104:107], a[244:247], v[44:47], v[104:107]\n"
    "ds_read_b64_tr_b16 a[238:239], v241 offset:19968\n"
    "ds_read_b64_tr_b16 a[240:241], v236 offset:14336\n"
    "v_mfma_f32_16x16x32_bf16 v[100:103], a[248:251], v[0:3], v[192:195]\n"
    "ds_read_b64_tr_b16 a[242:243], v236 offset:20480\n"
    "v_mfma_f32_16x16x32_bf16 v[100:103], a[252:255], v[4:7], v[100:103]\n"
    "ds_read_b64_tr_b16 a[244:245], v241 offset:14336\n"
    "ds_read_b64_tr_b16 a[246:247], v241 offset:20480\n"
    "s_waitcnt lgkmcnt(10)\n"
    "v_mfma_f32_16x16x32_bf16 v[100:103], a[192:195], v[8:11], v[100:103]\n"
    "buffer_load_dwordx4 v238, s[20:23], s44 offen offset:1024 lds\n"
    "v_mfma_f32_16x16x32_bf16 v[100:103], a[196:199], v[12:15], v[100:103]\n"
    "buffer_load_dwordx4 v239, s[20:23], s44 offen offset:2048 lds\n"
    "s_add_u32 m0, m0, 3072\n"
    "s_add_u32 s45, s44, 3072\n"
    "v_mfma_f32_16x16x32_bf16 v[100:103], a[200:203], v[16:19], v[100:103]\n"
    "v_mfma_f32_16x16x32_bf16 v[100:103], a[204:207], v[20:23], v[100:103]\n"
    "buffer_load_dwordx4 v237, s[20:23], s45 offen offset:0 lds\n"
    "buffer_load_dwordx4 v238, s[20:23], s45 offen offset:1024 lds\n"
    "v_mfma_f32_16x16x32_bf16 v[108:111], a[248:251], v[24:27], v[196:199]\n"
    "ds_read_b64_tr_b16 a[248:249], v236 offset:14848\n"
    "v_mfma_f32_16x16x32_bf16 v[108:111], a[252:255], v[28:31], v[108:111]\n"
    "ds_read_b64_tr_b16 a[250:251], v236 offset:20992\n"
    "ds_read_b64_tr_b16 a[252:253], v241 offset:14848\n"
    "v_mfma_f32_16x16x32_bf16 v[108:111], a[192:195], v[32:35], v[108:111]\n"
    "buffer_load_dwordx4 v239, s[20:23], s45 offen offset:2048 lds\n"
    "v_mfma_f32_16x16x32_bf16 v[108:111], a[196:199], v[36:39], v[108:111]\n"
    "ds_read_b64_tr_b16 a[254:255], v241 offset:20992\n"
    "v_max_f32 v232, v212, v213\n"
    "v_cmp_nge_f32 vcc, s49, v232\n"
    "s_add_u32 m0, m0, 3072\n"
    "s_add_u32 s45, s44, 6144\n"
    "v_mfma_f32_16x16x32_bf16 v[108:111], a[200:203], v[40:43], v[108:111]\n"
    "buffer_load_dwordx4 v237, s[20:23], s45 offen offset:0 lds\n"
    "v_mfma_f32_16x16x32_bf16 v[108:111], a[204:207], v[44:47], v[108:111]\n"
    "buffer_load_dwordx4 v238, s[20:23], s45 offen offset:1024 lds\n"
    "buffer_load_dwordx4 v239, s[20:23], s45 offen offset:2048 lds\n"
    "s_add_u32 m0, m0, 3072\n"
    "s_cbranch_vccnz LRARE_o_2_%=\n"
    "LRET_o_2_L_%=:\n"
    "v_mfma_f32_16x16x32_bf16 a[0:3], a[208:211], v[176:179], a[0:3]\n"
    "v_exp_f32 v216, v96\n"
    "v_mfma_f32_16x16x32_bf16 a[48:51], a[208:211], v[180:183], a[48:51]\n"
    "ds_read_b128 a[192:195], v235 offset:12288\n"
    "s_add_u32 s45, s44, 9216\n"
    "buffer_load_dwordx4 v237, s[20:23], s45 offen offset:0 lds\n"
    "v_exp_f32 v217, v97\n"
    "v_mfma_f32_16x16x32_bf16 a[4:7], a[212:215], v[176:179], a[4:7]\n"
    "ds_read_b128 a[196:199], v235 offset:12800\n"
    "v_add_f32 v208, v208, v212\n"
    "v_mfma_f32_16x16x32_bf16 a[52:55], a[212:215], v[180:183], a[52:55]\n"
    "buffer_load_dwordx4 v238, s[20:23], s45 offen offset:1024 lds\n"
    "ds_read_b128 a[200:203], v235 offset:13312\n"
    "v_exp_f32 v218, v98\n"
    "v_add_f32 v209, v209, v213\n"
    "v_mfma_f32_16x16x32_bf16 a[8:11], a[216:219], v[176:179], a[8:11]\n"
    "buffer_load_dwordx4 v239, s[20:23], s45 offen offset:2048 lds\n"
    "v_mfma_f32_16x16x32_bf16 a[56:59], a[216:219], v[180:183], a[56:59]\n"
    "ds_read_b128 a[204:207], v235 offset:13824\n"
    "s_add_u32 m0, m0, 3072\n"
    "v_exp_f32 v219, v99\n"
    "v_cvt_pk_bf16_f32 v160, v216, v217\n"
    "s_add_u32 s45, s44, 12288\n"
    "v_mfma_f32_16x16x32_bf16 a[12:15], a[220:223], v[176:179], a[12:15]\n"
    "ds_read_b128 a[208:211], v235 offset:14336\n"
    "v_add_f32 v212, v216, v217\n"
    "v_mfma_f32_16x16x32_bf16 a[60:63], a[220:223], v[180:183], a[60:63]\n"
    "buffer_load_dwordx4 v237, s[20:23], s45 offen offset:0 lds\n"
    "ds_read_b128 a[212:215], v235 offset:14848\n"
    "v_exp_f32 v220, v104\n"
    "v_cvt_pk_bf16_f32 v161, v218, v219\n"
    "v_mfma_f32_16x16x32_bf16 a[16:19], a[224:227], v[176:179], a[16:19]\n"
    "buffer_load_dwordx4 v238, s[20:23], s45 offen offset:1024 lds\n"
    "v_mfma_f32_16x16x32_bf16 a[64:67], a[224:227], v[180:183], a[64:67]\n"
    "ds_read_b128 a[216:219], v235 offset:18432\n"
    "s_waitcnt lgkmcnt(5)\n"
    "buffer_load_dwordx4 v239, s[20:23], s45 offen offset:2048 lds\n"
    "v_exp_f32 v221, v105\n"
    "v_mfma_f32_16x16x32_bf16 a[20:23], a[228:231], v[176:179], a[20:23]\n"
    "ds_read_b128 a[220:223], v235 offset:18944\n"
    "v_add_u32 v236, s41, v234\n"
    "v_mfma_f32_16x16x32_bf16 a[68:71], a[228:231], v[180:183], a[68:71]\n"
    "v_exp_f32 v222, v106\n"
    "ds_read_b128 a[224:227], v235 offset:19456\n"
    "v_xor_b32 v241, 32, v236\n"
    "v_exp_f32 v223, v107\n"
    "v_mfma_f32_16x16x32_bf16 a[24:27], a[232:235], v[176:179], a[24:27]\n"
    "v_exp_f32 v224, v100\n"
    "v_mfma_f32_16x16x32_bf16 a[72:75], a[232:235], v[180:183], a[72:75]\n"
    "ds_read_b128 a[228:231], v235 offset:19968\n"
    "s_mov_b32 s43, s40\n"
    "v_exp_f32 v225, v101\n"
    "v_exp_f32 v226, v102\n"
    "v_mfma_f32_16x16x32_bf16 a[28:31], a[236:239], v[176:179], a[28:31]\n"
    "ds_read_b128 a[232:235], v235 offset:20480\n"
    "s_mov_b32 s40, s41\n"
    "v_mfma_f32_16x16x32_bf16 a[76:79], a[236:239], v[180:183], a[76:79]\n"
    "v_exp_f32 v227, v103\n"
    "ds_read_b128 a[236:239], v235 offset:20992\n"
    "s_mov_b32 s41, s42\n"
    "s_mov_b32 s42, s43\n"
    "s_mov_b32 s84, s44\n"
    "v_mfma_f32_16x16x32_bf16 a[32:35], a[240:243], v[176:179], a[32:35]\n"
    "v_add_f32 v212, v212, v218\n"
    "s_add_u32 s87, s87, 1\n"
    "v_mfma_f32_16x16x32_bf16 a[80:83], a[240:243], v[180:183], a[80:83]\n"
    "v_readlane_b32 s44, v252, s87\n"
    "v_add_f32 v212, v212, v219\n"
    "v_exp_f32 v228, v108\n"
    "v_exp_f32 v229, v109\n"
    "v_mfma_f32_16x16x32_bf16 a[36:39], a[244:247], v[176:179], a[36:39]\n"
    "v_cvt_pk_bf16_f32 v164, v220, v221\n"
    "v_add_f32 v213, v220, v221\n"
    "v_mfma_f32_16x16x32_bf16 a[84:87], a[244:247], v[180:183], a[84:87]\n"
    "v_exp_f32 v230, v110\n"
    "v_exp_f32 v231, v111\n"
    "v_cvt_pk_bf16_f32 v165, v222, v223\n"
    "v_add_f32 v213, v213, v222\n"
    "v_mfma_f32_16x16x32_bf16 a[40:43], a[248:251], v[176:179], a[40:43]\n"
    "v_add_f32 v213, v213, v223\n"
    "v_cvt_pk_bf16_f32 v162, v224, v225\n"
    "v_mfma_f32_16x16x32_bf16 a[88:91], a[248:251], v[180:183], a[88:91]\n"
    "v_add_f32 v212, v212, v224\n"
    "v_add_f32 v212, v212, v225\n"
    "v_cvt_pk_bf16_f32 v163, v226, v227\n"
    "v_add_f32 v212, v212, v226\n"
    "v_add_f32 v212, v212, v227\n"
    "v_cvt_pk_bf16_f32 v166, v228, v229\n"
    "v_mfma_f32_16x16x32_bf16 a[44:47], a[252:255], v[176:179], a[44:47]\n"
    "v_add_f32 v213, v213, v228\n"
    "s_waitcnt vmcnt(31) lgkmcnt(0)\n"
    "v_mfma_f32_16x16x32_bf16 a[92:95], a[252:255], v[180:183], a[92:95]\n"
    "s_barrier\n"
    "v_add_f32 v213, v213, v229\n"
    "v_cvt_pk_bf16_f32 v167, v230, v231\n"
    "s_cmp_gt_u32 s87, s46\n"
    "s_cselect_b32 s71, -1.0, s49\n"
    "LLOOPA_2_%=:\n"
    "v_mfma_f32_16x16x32_bf16 v[128:131], a[192:195], v[0:3], v[192:195]\n"
    "ds_read_b64_tr_b16 a[240:241], v236 offset:0\n"
    "v_add_u32 v235, s41, v233\n"
    "v_mfma_f32_16x16x32_bf16 v[128:131], a[196:199], v[4:7], v[128:131]\n"
    "ds_read_b64_tr_b16 a[242:243], v236 offset:6144\n"
    "v_add_f32 v213, v213, v230\n"
    "ds_read_b64_tr_b16 a[244:245], v241 offset:0\n"
    "v_add_f32 v213, v213, v231\n"
    "v_mfma_f32_16x16x32_bf16 v[128:131], a[200:203], v[8:11], v[128:131]\n"
    "ds_read_b64_tr_b16 a[246:247], v241 offset:6144\n"
    "v_mfma_f32_16x16x32_bf16 v[128:131], a[204:207], v[12:15], v[128:131]\n"
    "ds_read_b64_tr_b16 a[248:249], v236 offset:512\n"
    "ds_read_b64_tr_b16 a[250:251], v236 offset:6656\n"
    "v_mfma_f32_16x16x32_bf16 v[128:131], a[208:211], v[16:19], v[128:131]\n"
    "ds_read_b64_tr_b16 a[252:253], v241 offset:512\n"
    "v_mfma_f32_16x16x32_bf16 v[128:131], a[212:215], v[20:23], v[128:131]\n"
    "ds_read_b64_tr_b16 a[254:255], v241 offset:6656\n"
    "v_mfma_f32_16x16x32_bf16 v[136:139], a[192:195], v[24:27], v[196:199]\n"
    "ds_read_b64_tr_b16 a[192:193], v236 offset:1024\n"
    "v_mfma_f32_16x16x32_bf16 v[136:139], a[196:199], v[28:31], v[136:139]\n"
    "ds_read_b64_tr_b16 a[194:195], v236 offset:7168\n"
    "ds_read_b64_tr_b16 a[196:197], v241 offset:1024\n"
    "v_mfma_f32_16x16x32_bf16 v[136:139], a[200:203], v[32:35], v[136:139]\n"
    "ds_read_b64_tr_b16 a[198:199], v241 offset:7168\n"
    "v_mfma_f32_16x16x32_bf16 v[136:139], a[204:207], v[36:39], v[136:139]\n"
    "ds_read_b64_tr_b16 a[200:201], v236 offset:1536\n"
    "ds_read_b64_tr_b16 a[202:203], v236 offset:7680\n"
    "v_mfma_f32_16x16x32_bf16 v[136:139], a[208:211], v[40:43], v[136:139]\n"
    "ds_read_b64_tr_b16 a[204:205], v241 offset:1536\n"
    "v_mfma_f32_16x16x32_bf16 v[136:139], a[212:215], v[44:47], v[136:139]\n"
    "ds_read_b64_tr_b16 a[206:207], v241 offset:7680\n"
    "ds_read_b64_tr_b16 a[208:209], v236 offset:2048\n"
    "v_mfma_f32_16x16x32_bf16 v[132:135], a[216:219], v[0:3], v[192:195]\n"
    "ds_read_b64_tr_b16 a[210:211], v236 offset:8192\n"
    "v_mfma_f32_16x16x32_bf16 v[132:135], a[220:223], v[4:7], v[132:135]\n"
    "ds_read_b64_tr_b16 a[212:213], v241 offset:2048\n"
    "ds_read_b64_tr_b16 a[214:215], v241 offset:8192\n"
    "v_mfma_f32_16x16x32_bf16 v[132:135], a[224:227], v[8:11], v[132:135]\n"
    "buffer_load_dword v254, v253, s[92:95], s86 offen\n"
    "v_mfma_f32_16x16x32_bf16 v[132:135], a[228:231], v[12:15], v[132:135]\n"
    "v_mfma_f32_16x16x32_bf16 v[132:135], a[232:235], v[16:19], v[132:135]\n"
    "v_mfma_f32_16x16x32_bf16 v[132:135], a[236:239], v[20:23], v[132:135]\n"
    "v_mfma_f32_16x16x32_bf16 v[140:143], a[216:219], v[24:27], v[196:199]\n"
    "ds_read_b64_tr_b16 a[216:217], v236 offset:2560\n"
    "s_sub_u32 s86, s86, 1048576\n"
    "v_mfma_f32_16x16x32_bf16 v[140:143], a[220:223], v[28:31], v[140:143]\n"
    "ds_read_b64_tr_b16 a[218:219], v236 offset:8704\n"
    "ds_read_b64_tr_b16 a[220:221], v241 offset:2560\n"
    "v_mfma_f32_16x16x32_bf16 v[140:143], a[224:227], v[32:35], v[140:143]\n"
    "v_mfma_f32_16x16x32_bf16 v[140:143], a[228:231], v[36:39], v[140:143]\n"
    "ds_read_b64_tr_b16 a[222:223], v241 offset:8704\n"
    "v_max_f32 v232, v212, v213\n"
    "v_cmp_nge_f32 vcc, s71, v232\n"
    "v_mfma_f32_16x16x32_bf16 v[140:143], a[232:235], v[40:43], v[140:143]\n"
    "v_mfma_f32_16x16x32_bf16 v[140:143], a[236:239], v[44:47], v[140:143]\n"
    "s_waitcnt lgkmcnt(4)\n"
    "s_cbranch_vccz LLOOP_2_%=\n"
    "v_cmp_nge_f32 vcc, s49, v232\n"
    "s_cmp_le_u32 s87, s46\n"
    "s_cbranch_vccnz LRARE_e_2_%=\n"
    "s_cbranch_scc1 LLOOP_2_%=\n"
    "s_branch LFINX_2_%=\n"
    "LRET_e_2_L_%=:\n"
    "s_cmp_le_u32 s87, s46\n"
    "s_cbranch_scc1 LLOOP_2_%=\n"
    "LFINX_2_%=:\n"
    "s_cmp_eq_u32 s51, 0\n"
    "s_cbranch_scc0 LFIN1_2_%=\n"
    "s_branch LFIN0_2_%=\n"
    "LFIN0_3_%=:\n"
    "s_mov_b32 s57, 1\n"
    "s_add_u32 s58, s55, 0\n"
    "s_add_u32 s59, s55, 6144\n"
    "s_add_u32 s60, s55, 12288\n"
    "s_mov_b32 s62, LPRO1_4_%=-LSTART_%=\n"
    "s_bfe_u32 s63, s53, 65536\n"
    "s_lshl_b32 s63, s63, 13\n"
    "s_add_u32 s62, s62, s63\n"
    "s_waitcnt lgkmcnt(5)\n"
    "v_mfma_f32_16x16x32_bf16 a[0:3], a[240:243], v[160:163], a[0:3]\n"
    "ds_read_b64_tr_b16 a[224:225], v236 offset:12288\n"
    "v_add_f32 v208, v208, v212\n"
    "v_mfma_f32_16x16x32_bf16 a[48:51], a[240:243], v[164:167], a[48:51]\n"
    "ds_read_b64_tr_b16 a[226:227], v236 offset:18432\n"
    "v_add_f32 v209, v209, v213\n"
    "v_mfma_f32_16x16x32_bf16 a[96:99], a[240:243], v[168:171], a[96:99]\n"
    "ds_read_b64_tr_b16 a[228:229], v241 offset:12288\n"
    "v_add_f32 v210, v210, v214\n"
    "v_mfma_f32_16x16x32_bf16 a[4:7], a[244:247], v[160:163], a[4:7]\n"
    "ds_read_b64_tr_b16 a[230:231], v241 offset:18432\n"
    "v_mfma_f32_16x16x32_bf16 a[52:55], a[244:247], v[164:167], a[52:55]\n"
    "ds_read_b64_tr_b16 a[232:233], v236 offset:12800\n"
    "v_mfma_f32_16x16x32_bf16 a[100:103], a[244:247], v[168:171], a[100:103]\n"
    "buffer_load_dwordx4 v[0:3], v240, s[28:31], s58 offen offset:0\n"
    "v_mfma_f32_16x16x32_bf16 a[8:11], a[248:251], v[160:163], a[8:11]\n"
    "ds_read_b64_tr_b16 a[234:235], v236 offset:18944\n"
    "v_mfma_f32_16x16x32_bf16 a[56:59], a[248:251], v[164:167], a[56:59]\n"
    "ds_read_b64_tr_b16 a[236:237], v241 offset:12800\n"
    "v_mfma_f32_16x16x32_bf16 a[104:107], a[248:251], v[168:171], a[104:107]\n"
    "ds_read_b64_tr_b16 a[238:239], v241 offset:18944\n"
    "v_mfma_f32_16x16x32_bf16 a[12:15], a[252:255], v[160:163], a[12:15]\n"
    "ds_read_b64_tr_b16 a[240:241], v236 offset:13312\n"
    "v_mfma_f32_16x16x32_bf16 a[60:63], a[252:255], v[164:167], a[60:63]\n"
    "ds_read_b64_tr_b16 a[242:243], v236 offset:19456\n"
    "v_mfma_f32_16x16x32_bf16 a[108:111], a[252:255], v[168:171], a[108:111]\n"
    "buffer_load_dwordx4 v[4:7], v240, s[28:31], s58 offen offset:3072\n"
    "v_mfma_f32_16x16x32_bf16 a[16:19], a[192:195], v[160:163], a[16:19]\n"
    "ds_read_b64_tr_b16 a[244:245], v241 offset:13312\n"
    "v_mfma_f32_16x16x32_bf16 a[64:67], a[192:195], v[164:167], a[64:67]\n"
    "ds_read_b64_tr_b16 a[246:247], v241 offset:19456\n"
    "v_mfma_f32_16x16x32_bf16 a[112:115], a[192:195], v[168:171], a[112:115]\n"
    "ds_read_b64_tr_b16 a[248:249], v236 offset:13824\n"
    "v_mfma_f32_16x16x32_bf16 a[20:23], a[196:199], v[160:163], a[20:23]\n"
    "ds_read_b64_tr_b16 a[250:251], v236 offset:19968\n"
    "v_mfma_f32_16x16x32_bf16 a[68:71], a[196:199], v[164:167], a[68:71]\n"
    "ds_read_b64_tr_b16 a[252:253], v241 offset:13824\n"
    "v_mfma_f32_16x16x32_bf16 a[116:119], a[196:199], v[168:171], a[116:119]\n"
    "buffer_load_dwordx4 v[8:11], v240, s[28:31], s58 offen offset:128\n"
    "v_mfma_f32_16x16x32_bf16 a[24:27], a[200:203], v[160:163], a[24:27]\n"
    "ds_read_b64_tr_b16 a[254:255], v241 offset:19968\n"
    "v_mfma_f32_16x16x32_bf16 a[72:75], a[200:203], v[164:167], a[72:75]\n"
    "ds_read_b64_tr_b16 a[192:193], v236 offset:14336\n"
    "v_mfma_f32_16x16x32_bf16 a[120:123], a[200:203], v[168:171], a[120:123]\n"
    "ds_read_b64_tr_b16 a[194:195], v236 offset:20480\n"
    "v_mfma_f32_16x16x32_bf16 a[28:31], a[204:207], v[160:163], a[28:31]\n"
    "ds_read_b64_tr_b16 a[196:197], v241 offset:14336\n"
    "v_mfma_f32_16x16x32_bf16 a[76:79], a[204:207], v[164:167], a[76:79]\n"
    "ds_read_b64_tr_b16 a[198:199], v241 offset:20480\n"
    "v_mfma_f32_16x16x32_bf16 a[124:127], a[204:207], v[168:171], a[124:127]\n"
    "buffer_load_dwordx4 v[12:15], v240, s[28:31], s58 offen offset:3200\n"
    "v_mfma_f32_16x16x32_bf16 a[32:35], a[208:211], v[160:163], a[32:35]\n"
    "ds_read_b64_tr_b16 a[200:201], v236 offset:14848\n"
    "v_mfma_f32_16x16x32_bf16 a[80:83], a[208:211], v[164:167], a[80:83]\n"
    "ds_read_b64_tr_b16 a[202:203], v236 offset:20992\n"
    "v_mfma_f32_16x16x32_bf16 a[128:131], a[208:211], v[168:171], a[128:131]\n"
    "ds_read_b64_tr_b16 a[204:205], v241 offset:14848\n"
    "s_waitcnt lgkmcnt(8)\n"
    "v_mfma_f32_16x16x32_bf16 a[36:39], a[212:215], v[160:163], a[36:39]\n"
    "ds_read_b64_tr_b16 a[206:207], v241 offset:20992\n"
    "v_mfma_f32_16x16x32_bf16 a[84:87], a[212:215], v[164:167], a[84:87]\n"
    "buffer_load_dwordx4 v[16:19], v240, s[28:31], s58 offen offset:256\n"
    "v_mfma_f32_16x16x32_bf16 a[132:135], a[212:215], v[168:171], a[132:135]\n"
    "v_exp_f32 v216, v128\n"
    "v_mfma_f32_16x16x32_bf16 a[40:43], a[216:219], v[160:163], a[40:43]\n"
    "buffer_load_dword v254, v253, s[92:95], s62 offen\n"
    "v_mfma_f32_16x16x32_bf16 a[88:91], a[216:219], v[164:167], a[88:91]\n"
    "v_exp_f32 v217, v129\n"
    "v_mfma_f32_16x16x32_bf16 a[136:139], a[216:219], v[168:171], a[136:139]\n"
    "buffer_load_dwordx4 v[20:23], v240, s[28:31], s58 offen offset:3328\n"
    "v_mfma_f32_16x16x32_bf16 a[44:47], a[220:223], v[160:163], a[44:47]\n"
    "v_exp_f32 v218, v130\n"
    "v_mfma_f32_16x16x32_bf16 a[92:95], a[220:223], v[164:167], a[92:95]\n"
    "v_exp_f32 v219, v131\n"
    "v_mfma_f32_16x16x32_bf16 a[140:143], a[220:223], v[168:171], a[140:143]\n"
    "v_exp_f32 v220, v136\n"
    "v_exp_f32 v221, v137\n"
    "v_exp_f32 v222, v138\n"
    "v_exp_f32 v223, v139\n"
    "v_exp_f32 v224, v144\n"
    "v_exp_f32 v225, v145\n"
    "v_exp_f32 v226, v146\n"
    "v_cvt_pk_bf16_f32 v176, v216, v217\n"
    "v_add_f32 v212, v216, v217\n"
    "v_exp_f32 v227, v147\n"
    "v_cvt_pk_bf16_f32 v177, v218, v219\n"
    "v_add_f32 v212, v212, v218\n"
    "v_add_f32 v212, v212, v219\n"
    "v_cvt_pk_bf16_f32 v180, v220, v221\n"
    "v_exp_f32 v228, v132\n"
    "v_exp_f32 v229, v133\n"
    "v_add_f32 v213, v220, v221\n"
    "v_cvt_pk_bf16_f32 v181, v222, v223\n"
    "v_exp_f32 v230, v134\n"
    "v_exp_f32 v231, v135\n"
    "v_exp_f32 v216, v140\n"
    "v_add_f32 v213, v213, v222\n"
    "v_add_f32 v213, v213, v223\n"
    "v_cvt_pk_bf16_f32 v184, v224, v225\n"
    "v_add_f32 v214, v224, v225\n"
    "v_exp_f32 v217, v141\n"
    "v_exp_f32 v218, v142\n"
    "v_cvt_pk_bf16_f32 v185, v226, v227\n"
    "v_add_f32 v214, v214, v226\n"
    "v_exp_f32 v219, v143\n"
    "v_add_f32 v214, v214, v227\n"
    "v_cvt_pk_bf16_f32 v178, v228, v229\n"
    "v_add_f32 v212, v212, v228\n"
    "v_add_f32 v212, v212, v229\n"
    "v_exp_f32 v220, v148\n"
    "v_exp_f32 v221, v149\n"
    "v_cvt_pk_bf16_f32 v179, v230, v231\n"
    "v_add_f32 v212, v212, v230\n"
    "v_exp_f32 v222, v150\n"
    "v_exp_f32 v223, v151\n"
    "v_add_f32 v212, v212, v231\n"
    "v_cvt_pk_bf16_f32 v182, v216, v217\n"
    "v_add_f32 v213, v213, v216\n"
    "v_add_f32 v213, v213, v217\n"
    "v_cvt_pk_bf16_f32 v183, v218, v219\n"
    "v_add_f32 v213, v213, v218\n"
    "v_add_f32 v213, v213, v219\n"
    "v_cvt_pk_bf16_f32 v186, v220, v221\n"
    "v_add_f32 v214, v214, v220\n"
    "v_add_f32 v214, v214, v221\n"
    "v_cvt_pk_bf16_f32 v187, v222, v223\n"
    "v_add_f32 v214, v214, v222\n"
    "v_add_f32 v214, v214, v223\n"
    "v_max3_f32 v232, v212, v213, v214\n"
    "v_cmp_nge_f32 vcc, s49, v232\n"
    "s_cbranch_vccnz LRARE_o_3_%=\n"
    "LRET_o_3_F0_%=:\n"
    "v_mfma_f32_16x16x32_bf16 a[0:3], a[224:227], v[176:179], a[0:3]\n"
    "v_add_f32 v208, v208, v212\n"
    "v_add_f32 v209, v209, v213\n"
    "v_add_f32 v210, v210, v214\n"
    "v_mfma_f32_16x16x32_bf16 a[4:7], a[228:231], v[176:179], a[4:7]\n"
    "s_mul_i32 s62, s53, 6144\n"
    "v_mov_b32 v245, v208\n"
    "v_mov_b32 v247, v209\n"
    "v_mfma_f32_16x16x32_bf16 a[8:11], a[232:235], v[176:179], a[8:11]\n"
    "v_mov_b32 v249, v210\n"
    "s_add_u32 s62, s62, s42\n"
    "v_permlane32_swap_b32 v208, v245\n"
    "v_mfma_f32_16x16x32_bf16 a[12:15], a[236:239], v[176:179], a[12:15]\n"
    "v_permlane32_swap_b32 v209, v247\n"
    "v_permlane32_swap_b32 v210, v249\n"
    "s_add_u32 s62, s62, s56\n"
    "v_mfma_f32_16x16x32_bf16 a[16:19], a[240:243], v[176:179], a[16:19]\n"
    "v_add_f32 v208, v208, v245\n"
    "v_add_f32 v209, v209, v247\n"
    "v_add_f32 v210, v210, v249\n"
    "v_mfma_f32_16x16x32_bf16 a[20:23], a[244:247], v[176:179], a[20:23]\n"
    "s_add_u32 s62, s62, 73728\n"
    "v_mov_b32 v245, v208\n"
    "v_mov_b32 v247, v209\n"
    "v_mfma_f32_16x16x32_bf16 a[24:27], a[248:251], v[176:179], a[24:27]\n"
    "v_mov_b32 v249, v210\n"
    "v_lshrrev_b32 v216, 7, v253\n"
    "v_permlane16_swap_b32 v208, v245\n"
    "s_waitcnt lgkmcnt(0)\n"
    "v_mfma_f32_16x16x32_bf16 a[28:31], a[252:255], v[176:179], a[28:31]\n"
    "v_permlane16_swap_b32 v209, v247\n"
    "v_permlane16_swap_b32 v210, v249\n"
    "v_and_b32 v217, 15, v216\n"
    "v_mfma_f32_16x16x32_bf16 a[32:35], a[192:195], v[176:179], a[32:35]\n"
    "v_add_f32 v208, v208, v245\n"
    "v_add_f32 v209, v209, v247\n"
    "v_add_f32 v210, v210, v249\n"
    "v_mfma_f32_16x16x32_bf16 a[36:39], a[196:199], v[176:179], a[36:39]\n"
    "v_mul_u32_u24 v217, 144, v217\n"
    "v_rcp_f32 v244, v208\n"
    "v_rcp_f32 v246, v209\n"
    "v_mfma_f32_16x16x32_bf16 a[40:43], a[200:203], v[176:179], a[40:43]\n"
    "v_rcp_f32 v248, v210\n"
    "v_lshrrev_b32 v218, 4, v216\n"
    "v_lshl_add_u32 v217, v218, 3, v217\n"
    "v_mfma_f32_16x16x32_bf16 a[44:47], a[204:207], v[176:179], a[44:47]\n"
    "v_add_u32 v192, s62, v217\n"
    "v_add_u32 v193, 2304, v192\n"
    "v_lshrrev_b32 v217, 3, v216\n"
    "v_mul_u32_u24 v217, 144, v217\n"
    "v_and_b32 v218, 7, v216\n"
    "v_lshl_add_u32 v217, v218, 4, v217\n"
    "v_add_u32 v194, s62, v217\n"
    "s_add_u32 s80, s54, 0\n"
    "s_add_u32 s81, s54, 6144\n"
    "s_add_u32 s82, s54, 12288\n"
    "v_mfma_f32_16x16x32_bf16 a[48:51], a[224:227], v[180:183], a[48:51]\n"
    "v_accvgpr_read_b32 v96, a0\n"
    "v_accvgpr_read_b32 v97, a1\n"
    "buffer_load_dwordx4 v[24:27], v240, s[28:31], s59 offen offset:0\n"
    "buffer_load_dwordx4 v[28:31], v240, s[28:31], s59 offen offset:3072\n"
    "v_accvgpr_read_b32 v98, a2\n"
    "v_accvgpr_read_b32 v99, a3\n"
    "v_accvgpr_read_b32 v100, a4\n"
    "v_mfma_f32_16x16x32_bf16 a[52:55], a[228:231], v[180:183], a[52:55]\n"
    "v_accvgpr_read_b32 v101, a5\n"
    "v_accvgpr_read_b32 v102, a6\n"
    "v_accvgpr_read_b32 v103, a7\n"
    "v_accvgpr_read_b32 v104, a8\n"
    "v_accvgpr_read_b32 v105, a9\n"
    "v_accvgpr_read_b32 v106, a10\n"
    "v_accvgpr_read_b32 v107, a11\n"
    "v_accvgpr_read_b32 v108, a12\n"
    "v_accvgpr_read_b32 v109, a13\n"
    "v_accvgpr_read_b32 v110, a14\n"
    "v_accvgpr_read_b32 v111, a15\n"
    "v_pk_mul_f32 v[96:97], v[96:97], v[244:245] op_sel_hi:[1,0]\n"
    "v_pk_mul_f32 v[98:99], v[98:99], v[244:245] op_sel_hi:[1,0]\n"
    "v_pk_mul_f32 v[100:101], v[100:101], v[244:245] op_sel_hi:[1,0]\n"
    "v_pk_mul_f32 v[102:103], v[102:103], v[244:245] op_sel_hi:[1,0]\n"
    "v_pk_mul_f32 v[104:105], v[104:105], v[244:245] op_sel_hi:[1,0]\n"
    "v_pk_mul_f32 v[106:107], v[106:107], v[244:245] op_sel_hi:[1,0]\n"
    "v_pk_mul_f32 v[108:109], v[108:109], v[244:245] op_sel_hi:[1,0]\n"
    "v_pk_mul_f32 v[110:111], v[110:111], v[244:245] op_sel_hi:[1,0]\n"
    "v_mfma_f32_16x16x32_bf16 a[56:59], a[232:235], v[180:183], a[56:59]\n"
    "v_cvt_pk_bf16_f32 v128, v96, v97\n"
    "v_cvt_pk_bf16_f32 v129, v98, v99\n"
    "v_cvt_pk_bf16_f32 v130, v100, v101\n"
    "v_cvt_pk_bf16_f32 v131, v102, v103\n"
    "v_mfma_f32_16x16x32_bf16 a[60:63], a[236:239], v[180:183], a[60:63]\n"
    "v_cvt_pk_bf16_f32 v132, v104, v105\n"
    "v_cvt_pk_bf16_f32 v133, v106, v107\n"
    "v_cvt_pk_bf16_f32 v134, v108, v109\n"
    "v_cvt_pk_bf16_f32 v135, v110, v111\n"
    "ds_write2_b64 v192, v[128:129], v[130:131] offset0:0 offset1:4\n"
    "ds_write2_b64 v192, v[132:133], v[134:135] offset0:8 offset1:12\n"
    "v_mfma_f32_16x16x32_bf16 a[64:67], a[240:243], v[180:183], a[64:67]\n"
    "v_accvgpr_read_b32 v112, a16\n"
    "v_accvgpr_read_b32 v113, a17\n"
    "buffer_load_dwordx4 v[32:35], v240, s[28:31], s59 offen offset:128\n"
    "buffer_load_dwordx4 v[36:39], v240, s[28:31], s59 offen offset:3200\n"
    "v_accvgpr_read_b32 v114, a18\n"
    "v_accvgpr_read_b32 v115, a19\n"
    "v_accvgpr_read_b32 v116, a20\n"
    "v_mfma_f32_16x16x32_bf16 a[68:71], a[244:247], v[180:183], a[68:71]\n"
    "v_accvgpr_read_b32 v117, a21\n"
    "v_accvgpr_read_b32 v118, a22\n"
    "v_accvgpr_read_b32 v119, a23\n"
    "v_accvgpr_read_b32 v120, a24\n"
    "v_accvgpr_read_b32 v121, a25\n"
    "v_accvgpr_read_b32 v122, a26\n"
    "v_accvgpr_read_b32 v123, a27\n"
    "v_accvgpr_read_b32 v124, a28\n"
    "v_accvgpr_read_b32 v125, a29\n"
    "v_accvgpr_read_b32 v126, a30\n"
    "v_accvgpr_read_b32 v127, a31\n"
    "v_pk_mul_f32 v[112:113], v[112:113], v[244:245] op_sel_hi:[1,0]\n"
    "v_pk_mul_f32 v[114:115], v[114:115], v[244:245] op_sel_hi:[1,0]\n"
    "v_pk_mul_f32 v[116:117], v[116:117], v[244:245] op_sel_hi:[1,0]\n"
    "v_pk_mul_f32 v[118:119], v[118:119], v[244:245] op_sel_hi:[1,0]\n"
    "v_pk_mul_f32 v[120:121], v[120:121], v[244:245] op_sel_hi:[1,0]\n"
    "v_pk_mul_f32 v[122:123], v[122:123], v[244:245] op_sel_hi:[1,0]\n"
    "v_pk_mul_f32 v[124:125], v[124:125], v[244:245] op_sel_hi:[1,0]\n"
    "v_pk_mul_f32 v[126:127], v[126:127], v[244:245] op_sel_hi:[1,0]\n"
    "v_mfma_f32_16x16x32_bf16 a[72:75], a[248:251], v[180:183], a[72:75]\n"
    "v_cvt_pk_bf16_f32 v136, v112, v113\n"
    "v_cvt_pk_bf16_f32 v137, v114, v115\n"
    "v_cvt_pk_bf16_f32 v138, v116, v117\n"
    "v_cvt_pk_bf16_f32 v139, v118, v119\n"
    "v_mfma_f32_16x16x32_bf16 a[76:79], a[252:255], v[180:183], a[76:79]\n"
    "v_cvt_pk_bf16_f32 v140, v120, v121\n"
    "v_cvt_pk_bf16_f32 v141, v122, v123\n"
    "v_cvt_pk_bf16_f32 v142, v124, v125\n"
    "v_cvt_pk_bf16_f32 v143, v126, v127\n"
    "ds_read_b128 v[144:147], v194 offset:0\n"
    "ds_read_b128 v[148:151], v194 offset:1152\n"
    "ds_write2_b64 v193, v[136:137], v[138:139] offset0:0 offset1:4\n"
    "ds_write2_b64 v193, v[140:141], v[142:143] offset0:8 offset1:12\n"
    "v_mfma_f32_16x16x32_bf16 a[80:83], a[192:195], v[180:183], a[80:83]\n"
    "v_accvgpr_read_b32 v96, a32\n"
    "v_accvgpr_read_b32 v97, a33\n"
    "buffer_load_dwordx4 v[40:43], v240, s[28:31], s59 offen offset:256\n"
    "buffer_load_dwordx4 v[44:47], v240, s[28:31], s59 offen offset:3328\n"
    "v_accvgpr_read_b32 v98, a34\n"
    "v_accvgpr_read_b32 v99, a35\n"
    "v_accvgpr_read_b32 v100, a36\n"
    "v_mfma_f32_16x16x32_bf16 a[84:87], a[196:199], v[180:183], a[84:87]\n"
    "v_accvgpr_read_b32 v101, a37\n"
    "v_accvgpr_read_b32 v102, a38\n"
    "s_waitcnt lgkmcnt(2)\n"
    "buffer_store_dwordx4 v[144:147], v240, s[36:39], s80 offen offset:0\n"
    "buffer_store_dwordx4 v[148:151], v240, s[36:39], s80 offen offset:3072\n"
    "v_accvgpr_read_b32 v103, a39\n"
    "v_accvgpr_read_b32 v104, a40\n"
    "v_accvgpr_read_b32 v105, a41\n"
    "v_accvgpr_read_b32 v106, a42\n"
    "v_accvgpr_read_b32 v107, a43\n"
    "v_accvgpr_read_b32 v108, a44\n"
    "v_accvgpr_read_b32 v109, a45\n"
    "v_accvgpr_read_b32 v110, a46\n"
    "v_accvgpr_read_b32 v111, a47\n"
    "v_pk_mul_f32 v[96:97], v[96:97], v[244:245] op_sel_hi:[1,0]\n"
    "v_pk_mul_f32 v[98:99], v[98:99], v[244:245] op_sel_hi:[1,0]\n"
    "v_pk_mul_f32 v[100:101], v[100:101], v[244:245] op_sel_hi:[1,0]\n"
    "v_pk_mul_f32 v[102:103], v[102:103], v[244:245] op_sel_hi:[1,0]\n"
    "v_pk_mul_f32 v[104:105], v[104:105], v[244:245] op_sel_hi:[1,0]\n"
    "v_pk_mul_f32 v[106:107], v[106:107], v[244:245] op_sel_hi:[1,0]\n"
    "v_pk_mul_f32 v[108:109], v[108:109], v[244:245] op_sel_hi:[1,0]\n"
    "v_pk_mul_f32 v[110:111], v[110:111], v[244:245] op_sel_hi:[1,0]\n"
    "v_mfma_f32_16x16x32_bf16 a[88:91], a[200:203], v[180:183], a[88:91]\n"
    "v_cvt_pk_bf16_f32 v128, v96, v97\n"
    "v_cvt_pk_bf16_f32 v129, v98, v99\n"
    "v_cvt_pk_bf16_f32 v130, v100, v101\n"
    "v_cvt_pk_bf16_f32 v131, v102, v103\n"
    "v_mfma_f32_16x16x32_bf16 a[92:95], a[204:207], v[180:183], a[92:95]\n"
    "v_cvt_pk_bf16_f32 v132, v104, v105\n"
    "v_cvt_pk_bf16_f32 v133, v106, v107\n"
    "v_cvt_pk_bf16_f32 v134, v108, v109\n"
    "v_cvt_pk_bf16_f32 v135, v110, v111\n"
    "ds_read_b128 v[152:155], v194 offset:2304\n"
    "ds_read_b128 v[156:159], v194 offset:3456\n"
    "ds_write2_b64 v192, v[128:129], v[130:131] offset0:0 offset1:4\n"
    "ds_write2_b64 v192, v[132:133], v[134:135] offset0:8 offset1:12\n"
    "v_mfma_f32_16x16x32_bf16 a[96:99], a[224:227], v[184:187], a[96:99]\n"
    "v_accvgpr_read_b32 v112, a48\n"
    "v_accvgpr_read_b32 v113, a49\n"
    "buffer_load_dwordx4 v[48:51], v240, s[28:31], s60 offen offset:0\n"
    "buffer_load_dwordx4 v[52:55], v240, s[28:31], s60 offen offset:3072\n"
    "v_accvgpr_read_b32 v114, a50\n"
    "v_accvgpr_read_b32 v115, a51\n"
    "v_accvgpr_read_b32 v116, a52\n"
    "v_mfma_f32_16x16x32_bf16 a[100:103], a[228:231], v[184:187], a[100:103]\n"
    "v_accvgpr_read_b32 v117, a53\n"
    "v_accvgpr_read_b32 v118, a54\n"
    "s_waitcnt lgkmcnt(2)\n"
    "buffer_store_dwordx4 v[152:155], v240, s[36:39], s80 offen offset:128\n"
    "buffer_store_dwordx4 v[156:159], v240, s[36:39], s80 offen offset:3200\n"
    "v_accvgpr_read_b32 v119, a55\n"
    "v_accvgpr_read_b32 v120, a56\n"
    "v_accvgpr_read_b32 v121, a57\n"
    "v_accvgpr_read_b32 v122, a58\n"
    "v_accvgpr_read_b32 v123, a59\n"
    "v_accvgpr_read_b32 v124, a60\n"
    "v_accvgpr_read_b32 v125, a61\n"
    "v_accvgpr_read_b32 v126, a62\n"
    "v_accvgpr_read_b32 v127, a63\n"
    "v_pk_mul_f32 v[112:113], v[112:113], v[246:247] op_sel_hi:[1,0]\n"
    "v_pk_mul_f32 v[114:115], v[114:115], v[246:247] op_sel_hi:[1,0]\n"
    "v_pk_mul_f32 v[116:117], v[116:117], v[246:247] op_sel_hi:[1,0]\n"
    "v_pk_mul_f32 v[118:119], v[118:119], v[246:247] op_sel_hi:[1,0]\n"
    "v_pk_mul_f32 v[120:121], v[120:121], v[246:247] op_sel_hi:[1,0]\n"
    "v_pk_mul_f32 v[122:123], v[122:123], v[246:247] op_sel_hi:[1,0]\n"
    "v_pk_mul_f32 v[124:125], v[124:125], v[246:247] op_sel_hi:[1,0]\n"
    "v_pk_mul_f32 v[126:127], v[126:127], v[246:247] op_sel_hi:[1,0]\n"
    "v_mfma_f32_16x16x32_bf16 a[104:107], a[232:235], v[184:187], a[104:107]\n"
    "v_cvt_pk_bf16_f32 v136, v112, v113\n"
    "v_cvt_pk_bf16_f32 v137, v114, v115\n"
    "v_cvt_pk_bf16_f32 v138, v116, v117\n"
    "v_cvt_pk_bf16_f32 v139, v118, v119\n"
    "v_mfma_f32_16x16x32_bf16 a[108:111], a[236:239], v[184:187], a[108:111]\n"
    "v_cvt_pk_bf16_f32 v140, v120, v121\n"
    "v_cvt_pk_bf16_f32 v141, v122, v123\n"
    "v_cvt_pk_bf16_f32 v142, v124, v125\n"
    "v_cvt_pk_bf16_f32 v143, v126, v127\n"
    "ds_read_b128 v[216:219], v194 offset:0\n"
    "ds_read_b128 v[220:223], v194 offset:1152\n"
    "ds_write2_b64 v193, v[136:137], v[138:139] offset0:0 offset1:4\n"
    "ds_write2_b64 v193, v[140:141], v[142:143] offset0:8 offset1:12\n"
    "v_mfma_f32_16x16x32_bf16 a[112:115], a[240:243], v[184:187], a[112:115]\n"
    "v_accvgpr_read_b32 v96, a64\n"
    "v_accvgpr_read_b32 v97, a65\n"
    "buffer_load_dwordx4 v[56:59], v240, s[28:31], s60 offen offset:128\n"
    "buffer_load_dwordx4 v[60:63], v240, s[28:31], s60 offen offset:3200\n"
    "v_accvgpr_read_b32 v98, a66\n"
    "v_accvgpr_read_b32 v99, a67\n"
    "v_accvgpr_read_b32 v100, a68\n"
    "v_mfma_f32_16x16x32_bf16 a[116:119], a[244:247], v[184:187], a[116:119]\n"
    "v_accvgpr_read_b32 v101, a69\n"
    "v_accvgpr_read_b32 v102, a70\n"
    "s_waitcnt lgkmcnt(2)\n"
    "buffer_store_dwordx4 v[216:219], v240, s[36:39], s80 offen offset:256\n"
    "buffer_store_dwordx4 v[220:223], v240, s[36:39], s80 offen offset:3328\n"
    "v_accvgpr_read_b32 v103, a71\n"
    "v_accvgpr_read_b32 v104, a72\n"
    "v_accvgpr_read_b32 v105, a73\n"
    "v_accvgpr_read_b32 v106, a74\n"
    "v_accvgpr_read_b32 v107, a75\n"
    "v_accvgpr_read_b32 v108, a76\n"
    "v_accvgpr_read_b32 v109, a77\n"
    "v_accvgpr_read_b32 v110, a78\n"
    "v_accvgpr_read_b32 v111, a79\n"
    "v_pk_mul_f32 v[96:97], v[96:97], v[246:247] op_sel_hi:[1,0]\n"
    "v_pk_mul_f32 v[98:99], v[98:99], v[246:247] op_sel_hi:[1,0]\n"
    "v_pk_mul_f32 v[100:101], v[100:101], v[246:247] op_sel_hi:[1,0]\n"
    "v_pk_mul_f32 v[102:103], v[102:103], v[246:247] op_sel_hi:[1,0]\n"
    "v_pk_mul_f32 v[104:105], v[104:105], v[246:247] op_sel_hi:[1,0]\n"
    "v_pk_mul_f32 v[106:107], v[106:107], v[246:247] op_sel_hi:[1,0]\n"
    "v_pk_mul_f32 v[108:109], v[108:109], v[246:247] op_sel_hi:[1,0]\n"
    "v_pk_mul_f32 v[110:111], v[110:111], v[246:247] op_sel_hi:[1,0]\n"
    "v_mfma_f32_16x16x32_bf16 a[120:123], a[248:251], v[184:187], a[120:123]\n"
    "v_cvt_pk_bf16_f32 v128, v96, v97\n"
    "v_cvt_pk_bf16_f32 v129, v98, v99\n"
    "v_cvt_pk_bf16_f32 v130, v100, v101\n"
    "v_cvt_pk_bf16_f32 v131, v102, v103\n"
    "v_mfma_f32_16x16x32_bf16 a[124:127], a[252:255], v[184:187], a[124:127]\n"
    "v_cvt_pk_bf16_f32 v132, v104, v105\n"
    "v_cvt_pk_bf16_f32 v133, v106, v107\n"
    "v_cvt_pk_bf16_f32 v134, v108, v109\n"
    "v_cvt_pk_bf16_f32 v135, v110, v111\n"
    "ds_read_b128 v[144:147], v194 offset:2304\n"
    "ds_read_b128 v[148:151], v194 offset:3456\n"
    "ds_write2_b64 v192, v[128:129], v[130:131] offset0:0 offset1:4\n"
    "ds_write2_b64 v192, v[132:133], v[134:135] offset0:8 offset1:12\n"
    "v_mfma_f32_16x16x32_bf16 a[128:131], a[192:195], v[184:187], a[128:131]\n"
    "v_accvgpr_read_b32 v112, a80\n"
    "v_accvgpr_read_b32 v113, a81\n"
    "v_accvgpr_read_b32 v114, a82\n"
    "v_accvgpr_read_b32 v115, a83\n"
    "v_accvgpr_read_b32 v116, a84\n"
    "v_mfma_f32_16x16x32_bf16 a[132:135], a[196:199], v[184:187], a[132:135]\n"
    "v_accvgpr_read_b32 v117, a85\n"
    "v_accvgpr_read_b32 v118, a86\n"
    "s_waitcnt lgkmcnt(2)\n"
    "buffer_store_dwordx4 v[144:147], v240, s[36:39], s81 offen offset:0\n"
    "buffer_store_dwordx4 v[148:151], v240, s[36:39], s81 offen offset:3072\n"
    "v_accvgpr_read_b32 v119, a87\n"
    "v_accvgpr_read_b32 v120, a88\n"
    "v_accvgpr_read_b32 v121, a89\n"
    "v_accvgpr_read_b32 v122, a90\n"
    "v_accvgpr_read_b32 v123, a91\n"
    "v_accvgpr_read_b32 v124, a92\n"
    "v_accvgpr_read_b32 v125, a93\n"
    "v_accvgpr_read_b32 v126, a94\n"
    "v_accvgpr_read_b32 v127, a95\n"
    "v_pk_mul_f32 v[112:113], v[112:113], v[246:247] op_sel_hi:[1,0]\n"
    "v_pk_mul_f32 v[114:115], v[114:115], v[246:247] op_sel_hi:[1,0]\n"
    "v_pk_mul_f32 v[116:117], v[116:117], v[246:247] op_sel_hi:[1,0]\n"
    "v_pk_mul_f32 v[118:119], v[118:119], v[246:247] op_sel_hi:[1,0]\n"
    "v_pk_mul_f32 v[120:121], v[120:121], v[246:247] op_sel_hi:[1,0]\n"
    "v_pk_mul_f32 v[122:123], v[122:123], v[246:247] op_sel_hi:[1,0]\n"
    "v_pk_mul_f32 v[124:125], v[124:125], v[246:247] op_sel_hi:[1,0]\n"
    "v_pk_mul_f32 v[126:127], v[126:127], v[246:247] op_sel_hi:[1,0]\n"
    "v_mfma_f32_16x16x32_bf16 a[136:139], a[200:203], v[184:187], a[136:139]\n"
    "v_cvt_pk_bf16_f32 v136, v112, v113\n"
    "v_cvt_pk_bf16_f32 v137, v114, v115\n"
    "v_cvt_pk_bf16_f32 v138, v116, v117\n"
    "v_cvt_pk_bf16_f32 v139, v118, v119\n"
    "v_mfma_f32_16x16x32_bf16 a[140:143], a[204:207], v[184:187], a[140:143]\n"
    "v_cvt_pk_bf16_f32 v140, v120, v121\n"
    "v_cvt_pk_bf16_f32 v141, v122, v123\n"
    "v_cvt_pk_bf16_f32 v142, v124, v125\n"
    "v_cvt_pk_bf16_f32 v143, v126, v127\n"
    "ds_read_b128 v[152:155], v194 offset:0\n"
    "ds_read_b128 v[156:159], v194 offset:1152\n"
    "ds_write2_b64 v193, v[136:137], v[138:139] offset0:0 offset1:4\n"
    "ds_write2_b64 v193, v[140:141], v[142:143] offset0:8 offset1:12\n"
    "v_accvgpr_read_b32 v96, a96\n"
    "v_accvgpr_read_b32 v97, a97\n"
    "v_accvgpr_read_b32 v98, a98\n"
    "v_accvgpr_read_b32 v99, a99\n"
    "v_accvgpr_read_b32 v100, a100\n"
    "v_accvgpr_read_b32 v101, a101\n"
    "v_accvgpr_read_b32 v102, a102\n"
    "v_accvgpr_read_b32 v103, a103\n"
    "v_accvgpr_read_b32 v104, a104\n"
    "s_waitcnt lgkmcnt(2)\n"
    "buffer_store_dwordx4 v[152:155], v240, s[36:39], s81 offen offset:128\n"
    "buffer_store_dwordx4 v[156:159], v240, s[36:39], s81 offen offset:3200\n"
    "v_accvgpr_read_b32 v105, a105\n"
    "v_accvgpr_read_b32 v106, a106\n"
    "v_accvgpr_read_b32 v107, a107\n"
    "v_accvgpr_read_b32 v108, a108\n"
    "v_accvgpr_read_b32 v109, a109\n"
    "v_accvgpr_read_b32 v110, a110\n"
    "v_accvgpr_read_b32 v111, a111\n"
    "v_pk_mul_f32 v[96:97], v[96:97], v[248:249] op_sel_hi:[1,0]\n"
    "v_pk_mul_f32 v[98:99], v[98:99], v[248:249] op_sel_hi:[1,0]\n"
    "v_pk_mul_f32 v[100:101], v[100:101], v[248:249] op_sel_hi:[1,0]\n"
    "v_pk_mul_f32 v[102:103], v[102:103], v[248:249] op_sel_hi:[1,0]\n"
    "v_pk_mul_f32 v[104:105], v[104:105], v[248:249] op_sel_hi:[1,0]\n"
    "v_pk_mul_f32 v[106:107], v[106:107], v[248:249] op_sel_hi:[1,0]\n"
    "v_pk_mul_f32 v[108:109], v[108:109], v[248:249] op_sel_hi:[1,0]\n"
    "v_pk_mul_f32 v[110:111], v[110:111], v[248:249] op_sel_hi:[1,0]\n"
    "v_cvt_pk_bf16_f32 v128, v96, v97\n"
    "v_cvt_pk_bf16_f32 v129, v98, v99\n"
    "v_cvt_pk_bf16_f32 v130, v100, v101\n"
    "v_cvt_pk_bf16_f32 v131, v102, v103\n"
    "v_cvt_pk_bf16_f32 v132, v104, v105\n"
    "v_cvt_pk_bf16_f32 v133, v106, v107\n"
    "v_cvt_pk_bf16_f32 v134, v108, v109\n"
    "v_cvt_pk_bf16_f32 v135, v110, v111\n"
    "ds_read_b128 v[216:219], v194 offset:2304\n"
    "ds_read_b128 v[220:223], v194 offset:3456\n"
    "ds_write2_b64 v192, v[128:129], v[130:131] offset0:0 offset1:4\n"
    "ds_write2_b64 v192, v[132:133], v[134:135] offset0:8 offset1:12\n"
    "v_accvgpr_read_b32 v112, a112\n"
    "v_accvgpr_read_b32 v113, a113\n"
    "v_accvgpr_read_b32 v114, a114\n"
    "v_accvgpr_read_b32 v115, a115\n"
    "v_accvgpr_read_b32 v116, a116\n"
    "v_accvgpr_read_b32 v117, a117\n"
    "v_accvgpr_read_b32 v118, a118\n"
    "v_accvgpr_read_b32 v119, a119\n"
    "v_accvgpr_read_b32 v120, a120\n"
    "s_waitcnt lgkmcnt(2)\n"
    "buffer_store_dwordx4 v[216:219], v240, s[36:39], s81 offen offset:256\n"
    "buffer_store_dwordx4 v[220:223], v240, s[36:39], s81 offen offset:3328\n"
    "v_accvgpr_read_b32 v121, a121\n"
    "v_accvgpr_read_b32 v122, a122\n"
    "v_accvgpr_read_b32 v123, a123\n"
    "v_accvgpr_read_b32 v124, a124\n"
    "v_accvgpr_read_b32 v125, a125\n"
    "v_accvgpr_read_b32 v126, a126\n"
    "v_accvgpr_read_b32 v127, a127\n"
    "v_pk_mul_f32 v[112:113], v[112:113], v[248:249] op_sel_hi:[1,0]\n"
    "v_pk_mul_f32 v[114:115], v[114:115], v[248:249] op_sel_hi:[1,0]\n"
    "v_pk_mul_f32 v[116:117], v[116:117], v[248:249] op_sel_hi:[1,0]\n"
    "v_pk_mul_f32 v[118:119], v[118:119], v[248:249] op_sel_hi:[1,0]\n"
    "v_pk_mul_f32 v[120:121], v[120:121], v[248:249] op_sel_hi:[1,0]\n"
    "v_pk_mul_f32 v[122:123], v[122:123], v[248:249] op_sel_hi:[1,0]\n"
    "v_pk_mul_f32 v[124:125], v[124:125], v[248:249] op_sel_hi:[1,0]\n"
    "v_pk_mul_f32 v[126:127], v[126:127], v[248:249] op_sel_hi:[1,0]\n"
    "v_cvt_pk_bf16_f32 v136, v112, v113\n"
    "v_cvt_pk_bf16_f32 v137, v114, v115\n"
    "v_cvt_pk_bf16_f32 v138, v116, v117\n"
    "v_cvt_pk_bf16_f32 v139, v118, v119\n"
    "v_cvt_pk_bf16_f32 v140, v120, v121\n"
    "v_cvt_pk_bf16_f32 v141, v122, v123\n"
    "v_cvt_pk_bf16_f32 v142, v124, v125\n"
    "v_cvt_pk_bf16_f32 v143, v126, v127\n"
    "ds_read_b128 v[144:147], v194 offset:0\n"
    "ds_read_b128 v[148:151], v194 offset:1152\n"
    "ds_write2_b64 v193, v[136:137], v[138:139] offset0:0 offset1:4\n"
    "ds_write2_b64 v193, v[140:141], v[142:143] offset0:8 offset1:12\n"
    "v_accvgpr_read_b32 v96, a128\n"
    "v_accvgpr_read_b32 v97, a129\n"
    "v_accvgpr_read_b32 v98, a130\n"
    "v_accvgpr_read_b32 v99, a131\n"
    "v_accvgpr_read_b32 v100, a132\n"
    "v_accvgpr_read_b32 v101, a133\n"
    "v_accvgpr_read_b32 v102, a134\n"
    "v_accvgpr_read_b32 v103, a135\n"
    "v_accvgpr_read_b32 v104, a136\n"
    "s_waitcnt lgkmcnt(2)\n"
    "buffer_store_dwordx4 v[144:147], v240, s[36:39], s82 offen offset:0\n"
    "buffer_store_dwordx4 v[148:151], v240, s[36:39], s82 offen offset:3072\n"
    "v_accvgpr_read_b32 v105, a137\n"
    "v_accvgpr_read_b32 v106, a138\n"
    "v_accvgpr_read_b32 v107, a139\n"
    "v_accvgpr_read_b32 v108, a140\n"
    "v_accvgpr_read_b32 v109, a141\n"
    "v_accvgpr_read_b32 v110, a142\n"
    "v_accvgpr_read_b32 v111, a143\n"
    "v_pk_mul_f32 v[96:97], v[96:97], v[248:249] op_sel_hi:[1,0]\n"
    "v_pk_mul_f32 v[98:99], v[98:99], v[248:249] op_sel_hi:[1,0]\n"
    "v_pk_mul_f32 v[100:101], v[100:101], v[248:249] op_sel_hi:[1,0]\n"
    "v_pk_mul_f32 v[102:103], v[102:103], v[248:249] op_sel_hi:[1,0]\n"
    "v_pk_mul_f32 v[104:105], v[104:105], v[248:249] op_sel_hi:[1,0]\n"
    "v_pk_mul_f32 v[106:107], v[106:107], v[248:249] op_sel_hi:[1,0]\n"
    "v_pk_mul_f32 v[108:109], v[108:109], v[248:249] op_sel_hi:[1,0]\n"
    "v_pk_mul_f32 v[110:111], v[110:111], v[248:249] op_sel_hi:[1,0]\n"
    "v_cvt_pk_bf16_f32 v128, v96, v97\n"
    "v_cvt_pk_bf16_f32 v129, v98, v99\n"
    "v_cvt_pk_bf16_f32 v130, v100, v101\n"
    "v_cvt_pk_bf16_f32 v131, v102, v103\n"
    "v_cvt_pk_bf16_f32 v132, v104, v105\n"
    "v_cvt_pk_bf16_f32 v133, v106, v107\n"
    "v_cvt_pk_bf16_f32 v134, v108, v109\n"
    "v_cvt_pk_bf16_f32 v135, v110, v111\n"
    "ds_read_b128 v[152:155], v194 offset:2304\n"
    "ds_read_b128 v[156:159], v194 offset:3456\n"
    "ds_write2_b64 v192, v[128:129], v[130:131] offset0:0 offset1:4\n"
    "ds_write2_b64 v192, v[132:133], v[134:135] offset0:8 offset1:12\n"
    "s_waitcnt lgkmcnt(2)\n"
    "buffer_store_dwordx4 v[152:155], v240, s[36:39], s82 offen offset:128\n"
    "buffer_store_dwordx4 v[156:159], v240, s[36:39], s82 offen offset:3200\n"
    "ds_read_b128 v[216:219], v194 offset:0\n"
    "ds_read_b128 v[220:223], v194 offset:1152\n"
    "s_waitcnt lgkmcnt(0)\n"
    "buffer_store_dwordx4 v[216:219], v240, s[36:39], s82 offen offset:256\n"
    "buffer_store_dwordx4 v[220:223], v240, s[36:39], s82 offen offset:3328\n"
    "s_branch LNEXT_%=\n"
    "LFIN0_2_%=:\n"
    "s_mov_b32 s57, 1\n"
    "s_add_u32 s58, s55, 0\n"
    "s_add_u32 s59, s55, 6144\n"
    "s_add_u32 s60, s55, 12288\n"
    "s_mov_b32 s62, LPRO1_4_%=-LSTART_%=\n"
    "s_bfe_u32 s63, s53, 65536\n"
    "s_lshl_b32 s63, s63, 13\n"
    "s_add_u32 s62, s62, s63\n"
    "s_waitcnt lgkmcnt(4)\n"
    "v_mfma_f32_16x16x32_bf16 a[0:3], a[240:243], v[160:163], a[0:3]\n"
    "ds_read_b64_tr_b16 a[224:225], v236 offset:12288\n"
    "v_add_f32 v208, v208, v212\n"
    "v_mfma_f32_16x16x32_bf16 a[48:51], a[240:243], v[164:167], a[48:51]\n"
    "ds_read_b64_tr_b16 a[226:227], v236 offset:18432\n"
    "v_add_f32 v209, v209, v213\n"
    "ds_read_b64_tr_b16 a[228:229], v241 offset:12288\n"
    "v_exp_f32 v216, v128\n"
    "v_mfma_f32_16x16x32_bf16 a[4:7], a[244:247], v[160:163], a[4:7]\n"
    "ds_read_b64_tr_b16 a[230:231], v241 offset:18432\n"
    "v_mfma_f32_16x16x32_bf16 a[52:55], a[244:247], v[164:167], a[52:55]\n"
    "ds_read_b64_tr_b16 a[232:233], v236 offset:12800\n"
    "ds_read_b64_tr_b16 a[234:235], v236 offset:18944\n"
    "v_exp_f32 v217, v129\n"
    "v_mfma_f32_16x16x32_bf16 a[8:11], a[248:251], v[160:163], a[8:11]\n"
    "ds_read_b64_tr_b16 a[236:237], v241 offset:12800\n"
    "v_cvt_pk_bf16_f32 v176, v216, v217\n"
    "v_mfma_f32_16x16x32_bf16 a[56:59], a[248:251], v[164:167], a[56:59]\n"
    "ds_read_b64_tr_b16 a[238:239], v241 offset:18944\n"
    "v_add_f32 v212, v216, v217\n"
    "ds_read_b64_tr_b16 a[240:241], v236 offset:13312\n"
    "v_exp_f32 v218, v130\n"
    "v_mfma_f32_16x16x32_bf16 a[12:15], a[252:255], v[160:163], a[12:15]\n"
    "ds_read_b64_tr_b16 a[242:243], v236 offset:19456\n"
    "v_mfma_f32_16x16x32_bf16 a[60:63], a[252:255], v[164:167], a[60:63]\n"
    "ds_read_b64_tr_b16 a[244:245], v241 offset:13312\n"
    "ds_read_b64_tr_b16 a[246:247], v241 offset:19456\n"
    "v_exp_f32 v219, v131\n"
    "v_mfma_f32_16x16x32_bf16 a[16:19], a[192:195], v[160:163], a[16:19]\n"
    "ds_read_b64_tr_b16 a[248:249], v236 offset:13824\n"
    "v_cvt_pk_bf16_f32 v177, v218, v219\n"
    "v_mfma_f32_16x16x32_bf16 a[64:67], a[192:195], v[164:167], a[64:67]\n"
    "ds_read_b64_tr_b16 a[250:251], v236 offset:19968\n"
    "v_add_f32 v212, v212, v218\n"
    "ds_read_b64_tr_b16 a[252:253], v241 offset:13824\n"
    "v_exp_f32 v220, v136\n"
    "v_add_f32 v212, v212, v219\n"
    "v_mfma_f32_16x16x32_bf16 a[20:23], a[196:199], v[160:163], a[20:23]\n"
    "ds_read_b64_tr_b16 a[254:255], v241 offset:19968\n"
    "v_mfma_f32_16x16x32_bf16 a[68:71], a[196:199], v[164:167], a[68:71]\n"
    "ds_read_b64_tr_b16 a[192:193], v236 offset:14336\n"
    "ds_read_b64_tr_b16 a[194:195], v236 offset:20480\n"
    "v_exp_f32 v221, v137\n"
    "v_mfma_f32_16x16x32_bf16 a[24:27], a[200:203], v[160:163], a[24:27]\n"
    "ds_read_b64_tr_b16 a[196:197], v241 offset:14336\n"
    "v_cvt_pk_bf16_f32 v180, v220, v221\n"
    "v_mfma_f32_16x16x32_bf16 a[72:75], a[200:203], v[164:167], a[72:75]\n"
    "ds_read_b64_tr_b16 a[198:199], v241 offset:20480\n"
    "v_add_f32 v213, v220, v221\n"
    "ds_read_b64_tr_b16 a[200:201], v236 offset:14848\n"
    "v_exp_f32 v222, v138\n"
    "v_mfma_f32_16x16x32_bf16 a[28:31], a[204:207], v[160:163], a[28:31]\n"
    "ds_read_b64_tr_b16 a[202:203], v236 offset:20992\n"
    "v_mfma_f32_16x16x32_bf16 a[76:79], a[204:207], v[164:167], a[76:79]\n"
    "ds_read_b64_tr_b16 a[204:205], v241 offset:14848\n"
    "ds_read_b64_tr_b16 a[206:207], v241 offset:20992\n"
    "v_exp_f32 v223, v139\n"
    "v_mfma_f32_16x16x32_bf16 a[32:35], a[208:211], v[160:163], a[32:35]\n"
    "buffer_load_dwordx4 v[0:3], v240, s[28:31], s58 offen offset:0\n"
    "v_mfma_f32_16x16x32_bf16 a[80:83], a[208:211], v[164:167], a[80:83]\n"
    "v_exp_f32 v224, v132\n"
    "v_exp_f32 v225, v133\n"
    "v_exp_f32 v226, v134\n"
    "v_mfma_f32_16x16x32_bf16 a[36:39], a[212:215], v[160:163], a[36:39]\n"
    "v_exp_f32 v227, v135\n"
    "v_mfma_f32_16x16x32_bf16 a[84:87], a[212:215], v[164:167], a[84:87]\n"
    "buffer_load_dwordx4 v[4:7], v240, s[28:31], s58 offen offset:3072\n"
    "v_exp_f32 v228, v140\n"
    "v_exp_f32 v229, v141\n"
    "s_waitcnt lgkmcnt(5)\n"
    "v_mfma_f32_16x16x32_bf16 a[40:43], a[216:219], v[160:163], a[40:43]\n"
    "buffer_load_dword v254, v253, s[92:95], s62 offen\n"
    "v_mfma_f32_16x16x32_bf16 a[88:91], a[216:219], v[164:167], a[88:91]\n"
    "v_cvt_pk_bf16_f32 v181, v222, v223\n"
    "v_add_f32 v213, v213, v222\n"
    "buffer_load_dwordx4 v[8:11], v240, s[28:31], s58 offen offset:128\n"
    "v_exp_f32 v230, v142\n"
    "v_mfma_f32_16x16x32_bf16 a[44:47], a[220:223], v[160:163], a[44:47]\n"
    "v_exp_f32 v231, v143\n"
    "v_mfma_f32_16x16x32_bf16 a[92:95], a[220:223], v[164:167], a[92:95]\n"
    "v_add_f32 v213, v213, v223\n"
    "v_cvt_pk_bf16_f32 v178, v224, v225\n"
    "v_add_f32 v212, v212, v224\n"
    "v_add_f32 v212, v212, v225\n"
    "v_cvt_pk_bf16_f32 v179, v226, v227\n"
    "v_add_f32 v212, v212, v226\n"
    "v_add_f32 v212, v212, v227\n"
    "v_cvt_pk_bf16_f32 v182, v228, v229\n"
    "v_add_f32 v213, v213, v228\n"
    "v_add_f32 v213, v213, v229\n"
    "v_cvt_pk_bf16_f32 v183, v230, v231\n"
    "v_add_f32 v213, v213, v230\n"
    "v_add_f32 v213, v213, v231\n"
    "v_max_f32 v232, v212, v213\n"
    "v_cmp_nge_f32 vcc, s49, v232\n"
    "s_cbranch_vccnz LRARE_o_2_%=\n"
    "LRET_o_2_F0_%=:\n"
    "v_mfma_f32_16x16x32_bf16 a[0:3], a[224:227], v[176:179], a[0:3]\n"
    "v_add_f32 v208, v208, v212\n"
    "v_add_f32 v209, v209, v213\n"
    "v_mfma_f32_16x16x32_bf16 a[4:7], a[228:231], v[176:179], a[4:7]\n"
    "s_mul_i32 s62, s53, 6144\n"
    "v_mov_b32 v245, v208\n"
    "v_mfma_f32_16x16x32_bf16 a[8:11], a[232:235], v[176:179], a[8:11]\n"
    "v_mov_b32 v247, v209\n"
    "s_add_u32 s62, s62, s42\n"
    "v_permlane32_swap_b32 v208, v245\n"
    "v_mfma_f32_16x16x32_bf16 a[12:15], a[236:239], v[176:179], a[12:15]\n"
    "v_permlane32_swap_b32 v209, v247\n"
    "s_add_u32 s62, s62, s56\n"
    "v_mfma_f32_16x16x32_bf16 a[16:19], a[240:243], v[176:179], a[16:19]\n"
    "v_add_f32 v208, v208, v245\n"
    "v_add_f32 v209, v209, v247\n"
    "s_add_u32 s62, s62, 73728\n"
    "v_mfma_f32_16x16x32_bf16 a[20:23], a[244:247], v[176:179], a[20:23]\n"
    "v_mov_b32 v245, v208\n"
    "v_mov_b32 v247, v209\n"
    "v_mfma_f32_16x16x32_bf16 a[24:27], a[248:251], v[176:179], a[24:27]\n"
    "v_lshrrev_b32 v216, 7, v253\n"
    "v_permlane16_swap_b32 v208, v245\n"
    "v_permlane16_swap_b32 v209, v247\n"
    "v_mfma_f32_16x16x32_bf16 a[28:31], a[252:255], v[176:179], a[28:31]\n"
    "v_and_b32 v217, 15, v216\n"
    "v_add_f32 v208, v208, v245\n"
    "v_mfma_f32_16x16x32_bf16 a[32:35], a[192:195], v[176:179], a[32:35]\n"
    "v_add_f32 v209, v209, v247\n"
    "v_mul_u32_u24 v217, 144, v217\n"
    "s_waitcnt lgkmcnt(0)\n"
    "v_mfma_f32_16x16x32_bf16 a[36:39], a[196:199], v[176:179], a[36:39]\n"
    "v_rcp_f32 v244, v208\n"
    "v_rcp_f32 v246, v209\n"
    "v_lshrrev_b32 v218, 4, v216\n"
    "v_mfma_f32_16x16x32_bf16 a[40:43], a[200:203], v[176:179], a[40:43]\n"
    "v_lshl_add_u32 v217, v218, 3, v217\n"
    "v_add_u32 v192, s62, v217\n"
    "v_mfma_f32_16x16x32_bf16 a[44:47], a[204:207], v[176:179], a[44:47]\n"
    "v_add_u32 v193, 2304, v192\n"
    "v_lshrrev_b32 v217, 3, v216\n"
    "v_mul_u32_u24 v217, 144, v217\n"
    "v_and_b32 v218, 7, v216\n"
    "v_lshl_add_u32 v217, v218, 4, v217\n"
    "v_add_u32 v194, s62, v217\n"
    "s_add_u32 s80, s54, 0\n"
    "s_add_u32 s81, s54, 6144\n"
    "v_mfma_f32_16x16x32_bf16 a[48:51], a[224:227], v[180:183], a[48:51]\n"
    "v_accvgpr_read_b32 v96, a0\n"
    "v_accvgpr_read_b32 v97, a1\n"
    "buffer_load_dwordx4 v[12:15], v240, s[28:31], s58 offen offset:3200\n"
    "buffer_load_dwordx4 v[16:19], v240, s[28:31], s58 offen offset:256\n"
    "buffer_load_dwordx4 v[20:23], v240, s[28:31], s58 offen offset:3328\n"
    "buffer_load_dwordx4 v[24:27], v240, s[28:31], s59 offen offset:0\n"
    "v_accvgpr_read_b32 v98, a2\n"
    "v_accvgpr_read_b32 v99, a3\n"
    "v_accvgpr_read_b32 v100, a4\n"
    "v_mfma_f32_16x16x32_bf16 a[52:55], a[228:231], v[180:183], a[52:55]\n"
    "v_accvgpr_read_b32 v101, a5\n"
    "v_accvgpr_read_b32 v102, a6\n"
    "v_accvgpr_read_b32 v103, a7\n"
    "v_accvgpr_read_b32 v104, a8\n"
    "v_accvgpr_read_b32 v105, a9\n"
    "v_accvgpr_read_b32 v106, a10\n"
    "v_accvgpr_read_b32 v107, a11\n"
    "v_accvgpr_read_b32 v108, a12\n"
    "v_accvgpr_read_b32 v109, a13\n"
    "v_accvgpr_read_b32 v110, a14\n"
    "v_accvgpr_read_b32 v111, a15\n"
    "v_pk_mul_f32 v[96:97], v[96:97], v[244:245] op_sel_hi:[1,0]\n"
    "v_pk_mul_f32 v[98:99], v[98:99], v[244:245] op_sel_hi:[1,0]\n"
    "v_pk_mul_f32 v[100:101], v[100:101], v[244:245] op_sel_hi:[1,0]\n"
    "v_pk_mul_f32 v[102:103], v[102:103], v[244:245] op_sel_hi:[1,0]\n"
    "v_pk_mul_f32 v[104:105], v[104:105], v[244:245] op_sel_hi:[1,0]\n"
    "v_pk_mul_f32 v[106:107], v[106:107], v[244:245] op_sel_hi:[1,0]\n"
    "v_pk_mul_f32 v[108:109], v[108:109], v[244:245] op_sel_hi:[1,0]\n"
    "v_pk_mul_f32 v[110:111], v[110:111], v[244:245] op_sel_hi:[1,0]\n"
    "v_mfma_f32_16x16x32_bf16 a[56:59], a[232:235], v[180:183], a[56:59]\n"
    "v_cvt_pk_bf16_f32 v128, v96, v97\n"
    "v_cvt_pk_bf16_f32 v129, v98, v99\n"
    "v_cvt_pk_bf16_f32 v130, v100, v101\n"
    "v_cvt_pk_bf16_f32 v131, v102, v103\n"
    "v_mfma_f32_16x16x32_bf16 a[60:63], a[236:239], v[180:183], a[60:63]\n"
    "v_cvt_pk_bf16_f32 v132, v104, v105\n"
    "v_cvt_pk_bf16_f32 v133, v106, v107\n"
    "v_cvt_pk_bf16_f32 v134, v108, v109\n"
    "v_cvt_pk_bf16_f32 v135, v110, v111\n"
    "ds_write2_b64 v192, v[128:129], v[130:131] offset0:0 offset1:4\n"
    "ds_write2_b64 v192, v[132:133], v[134:135] offset0:8 offset1:12\n"
    "v_mfma_f32_16x16x32_bf16 a[64:67], a[240:243], v[180:183], a[64:67]\n"
    "v_accvgpr_read_b32 v112, a16\n"
    "v_accvgpr_read_b32 v113, a17\n"
    "buffer_load_dwordx4 v[28:31], v240, s[28:31], s59 offen offset:3072\n"
    "buffer_load_dwordx4 v[32:35], v240, s[28:31], s59 offen offset:128\n"
    "buffer_load_dwordx4 v[36:39], v240, s[28:31], s59 offen offset:3200\n"
    "buffer_load_dwordx4 v[40:43], v240, s[28:31], s59 offen offset:256\n"
    "v_accvgpr_read_b32 v114, a18\n"
    "v_accvgpr_read_b32 v115, a19\n"
    "v_accvgpr_read_b32 v116, a20\n"
    "v_mfma_f32_16x16x32_bf16 a[68:71], a[244:247], v[180:183], a[68:71]\n"
    "v_accvgpr_read_b32 v117, a21\n"
    "v_accvgpr_read_b32 v118, a22\n"
    "v_accvgpr_read_b32 v119, a23\n"
    "v_accvgpr_read_b32 v120, a24\n"
    "v_accvgpr_read_b32 v121, a25\n"
    "v_accvgpr_read_b32 v122, a26\n"
    "v_accvgpr_read_b32 v123, a27\n"
    "v_accvgpr_read_b32 v124, a28\n"
    "v_accvgpr_read_b32 v125, a29\n"
    "v_accvgpr_read_b32 v126, a30\n"
    "v_accvgpr_read_b32 v127, a31\n"
    "v_pk_mul_f32 v[112:113], v[112:113], v[244:245] op_sel_hi:[1,0]\n"
    "v_pk_mul_f32 v[114:115], v[114:115], v[244:245] op_sel_hi:[1,0]\n"
    "v_pk_mul_f32 v[116:117], v[116:117], v[244:245] op_sel_hi:[1,0]\n"
    "v_pk_mul_f32 v[118:119], v[118:119], v[244:245] op_sel_hi:[1,0]\n"
    "v_pk_mul_f32 v[120:121], v[120:121], v[244:245] op_sel_hi:[1,0]\n"
    "v_pk_mul_f32 v[122:123], v[122:123], v[244:245] op_sel_hi:[1,0]\n"
    "v_pk_mul_f32 v[124:125], v[124:125], v[244:245] op_sel_hi:[1,0]\n"
    "v_pk_mul_f32 v[126:127], v[126:127], v[244:245] op_sel_hi:[1,0]\n"
    "v_mfma_f32_16x16x32_bf16 a[72:75], a[248:251], v[180:183], a[72:75]\n"
    "v_cvt_pk_bf16_f32 v136, v112, v113\n"
    "v_cvt_pk_bf16_f32 v137, v114, v115\n"
    "v_cvt_pk_bf16_f32 v138, v116, v117\n"
    "v_cvt_pk_bf16_f32 v139, v118, v119\n"
    "v_mfma_f32_16x16x32_bf16 a[76:79], a[252:255], v[180:183], a[76:79]\n"
    "v_cvt_pk_bf16_f32 v140, v120, v121\n"
    "v_cvt_pk_bf16_f32 v141, v122, v123\n"
    "v_cvt_pk_bf16_f32 v142, v124, v125\n"
    "v_cvt_pk_bf16_f32 v143, v126, v127\n"
    "ds_read_b128 v[144:147], v194 offset:0\n"
    "ds_read_b128 v[148:151], v194 offset:1152\n"
    "ds_write2_b64 v193, v[136:137], v[138:139] offset0:0 offset1:4\n"
    "ds_write2_b64 v193, v[140:141], v[142:143] offset0:8 offset1:12\n"
    "v_mfma_f32_16x16x32_bf16 a[80:83], a[192:195], v[180:183], a[80:83]\n"
    "v_accvgpr_read_b32 v96, a32\n"
    "v_accvgpr_read_b32 v97, a33\n"
    "buffer_load_dwordx4 v[44:47], v240, s[28:31], s59 offen offset:3328\n"
    "buffer_load_dwordx4 v[48:51], v240, s[28:31], s60 offen offset:0\n"
    "buffer_load_dwordx4 v[52:55], v240, s[28:31], s60 offen offset:3072\n"
    "buffer_load_dwordx4 v[56:59], v240, s[28:31], s60 offen offset:128\n"
    "v_accvgpr_read_b32 v98, a34\n"
    "v_accvgpr_read_b32 v99, a35\n"
    "v_accvgpr_read_b32 v100, a36\n"
    "v_mfma_f32_16x16x32_bf16 a[84:87], a[196:199], v[180:183], a[84:87]\n"
    "v_accvgpr_read_b32 v101, a37\n"
    "v_accvgpr_read_b32 v102, a38\n"
    "s_waitcnt lgkmcnt(2)\n"
    "buffer_store_dwordx4 v[144:147], v240, s[36:39], s80 offen offset:0\n"
    "buffer_store_dwordx4 v[148:151], v240, s[36:39], s80 offen offset:3072\n"
    "v_accvgpr_read_b32 v103, a39\n"
    "v_accvgpr_read_b32 v104, a40\n"
    "v_accvgpr_read_b32 v105, a41\n"
    "v_accvgpr_read_b32 v106, a42\n"
    "v_accvgpr_read_b32 v107, a43\n"
    "v_accvgpr_read_b32 v108, a44\n"
    "v_accvgpr_read_b32 v109, a45\n"
    "v_accvgpr_read_b32 v110, a46\n"
    "v_accvgpr_read_b32 v111, a47\n"
    "v_pk_mul_f32 v[96:97], v[96:97], v[244:245] op_sel_hi:[1,0]\n"
    "v_pk_mul_f32 v[98:99], v[98:99], v[244:245] op_sel_hi:[1,0]\n"
    "v_pk_mul_f32 v[100:101], v[100:101], v[244:245] op_sel_hi:[1,0]\n"
    "v_pk_mul_f32 v[102:103], v[102:103], v[244:245] op_sel_hi:[1,0]\n"
    "v_pk_mul_f32 v[104:105], v[104:105], v[244:245] op_sel_hi:[1,0]\n"
    "v_pk_mul_f32 v[106:107], v[106:107], v[244:245] op_sel_hi:[1,0]\n"
    "v_pk_mul_f32 v[108:109], v[108:109], v[244:245] op_sel_hi:[1,0]\n"
    "v_pk_mul_f32 v[110:111], v[110:111], v[244:245] op_sel_hi:[1,0]\n"
    "v_mfma_f32_16x16x32_bf16 a[88:91], a[200:203], v[180:183], a[88:91]\n"
    "v_cvt_pk_bf16_f32 v128, v96, v97\n"
    "v_cvt_pk_bf16_f32 v129, v98, v99\n"
    "v_cvt_pk_bf16_f32 v130, v100, v101\n"
    "v_cvt_pk_bf16_f32 v131, v102, v103\n"
    "v_mfma_f32_16x16x32_bf16 a[92:95], a[204:207], v[180:183], a[92:95]\n"
    "v_cvt_pk_bf16_f32 v132, v104, v105\n"
    "v_cvt_pk_bf16_f32 v133, v106, v107\n"
    "v_cvt_pk_bf16_f32 v134, v108, v109\n"
    "v_cvt_pk_bf16_f32 v135, v110, v111\n"
    "ds_read_b128 v[152:155], v194 offset:2304\n"
    "ds_read_b128 v[156:159], v194 offset:3456\n"
    "ds_write2_b64 v192, v[128:129], v[130:131] offset0:0 offset1:4\n"
    "ds_write2_b64 v192, v[132:133], v[134:135] offset0:8 offset1:12\n"
    "v_accvgpr_read_b32 v112, a48\n"
    "v_accvgpr_read_b32 v113, a49\n"
    "v_accvgpr_read_b32 v114, a50\n"
    "buffer_load_dwordx4 v[60:63], v240, s[28:31], s60 offen offset:3200\n"
    "v_accvgpr_read_b32 v115, a51\n"
    "v_accvgpr_read_b32 v116, a52\n"
    "v_accvgpr_read_b32 v117, a53\n"
    "v_accvgpr_read_b32 v118, a54\n"
    "v_accvgpr_read_b32 v119, a55\n"
    "v_accvgpr_read_b32 v120, a56\n"
    "s_waitcnt lgkmcnt(2)\n"
    "buffer_store_dwordx4 v[152:155], v240, s[36:39], s80 offen offset:128\n"
    "buffer_store_dwordx4 v[156:159], v240, s[36:39], s80 offen offset:3200\n"
    "v_accvgpr_read_b32 v121, a57\n"
    "v_accvgpr_read_b32 v122, a58\n"
    "v_accvgpr_read_b32 v123, a59\n"
    "v_accvgpr_read_b32 v124, a60\n"
    "v_accvgpr_read_b32 v125, a61\n"
    "v_accvgpr_read_b32 v126, a62\n"
    "v_accvgpr_read_b32 v127, a63\n"
    "v_pk_mul_f32 v[112:113], v[112:113], v[246:247] op_sel_hi:[1,0]\n"
    "v_pk_mul_f32 v[114:115], v[114:115], v[246:247] op_sel_hi:[1,0]\n"
    "v_pk_mul_f32 v[116:117], v[116:117], v[246:247] op_sel_hi:[1,0]\n"
    "v_pk_mul_f32 v[118:119], v[118:119], v[246:247] op_sel_hi:[1,0]\n"
    "v_pk_mul_f32 v[120:121], v[120:121], v[246:247] op_sel_hi:[1,0]\n"
    "v_pk_mul_f32 v[122:123], v[122:123], v[246:247] op_sel_hi:[1,0]\n"
    "v_pk_mul_f32 v[124:125], v[124:125], v[246:247] op_sel_hi:[1,0]\n"
    "v_pk_mul_f32 v[126:127], v[126:127], v[246:247] op_sel_hi:[1,0]\n"
    "v_cvt_pk_bf16_f32 v136, v112, v113\n"
    "v_cvt_pk_bf16_f32 v137, v114, v115\n"
    "v_cvt_pk_bf16_f32 v138, v116, v117\n"
    "v_cvt_pk_bf16_f32 v139, v118, v119\n"
    "v_cvt_pk_bf16_f32 v140, v120, v121\n"
    "v_cvt_pk_bf16_f32 v141, v122, v123\n"
    "v_cvt_pk_bf16_f32 v142, v124, v125\n"
    "v_cvt_pk_bf16_f32 v143, v126, v127\n"
    "ds_read_b128 v[216:219], v194 offset:0\n"
    "ds_read_b128 v[220:223], v194 offset:1152\n"
    "ds_write2_b64 v193, v[136:137], v[138:139] offset0:0 offset1:4\n"
    "ds_write2_b64 v193, v[140:141], v[142:143] offset0:8 offset1:12\n"
    "v_accvgpr_read_b32 v96, a64\n"
    "v_accvgpr_read_b32 v97, a65\n"
    "v_accvgpr_read_b32 v98, a66\n"
    "v_accvgpr_read_b32 v99, a67\n"
    "v_accvgpr_read_b32 v100, a68\n"
    "v_accvgpr_read_b32 v101, a69\n"
    "v_accvgpr_read_b32 v102, a70\n"
    "v_accvgpr_read_b32 v103, a71\n"
    "v_accvgpr_read_b32 v104, a72\n"
    "s_waitcnt lgkmcnt(2)\n"
    "buffer_store_dwordx4 v[216:219], v240, s[36:39], s80 offen offset:256\n"
    "buffer_store_dwordx4 v[220:223], v240, s[36:39], s80 offen offset:3328\n"
    "v_accvgpr_read_b32 v105, a73\n"
    "v_accvgpr_read_b32 v106, a74\n"
    "v_accvgpr_read_b32 v107, a75\n"
    "v_accvgpr_read_b32 v108, a76\n"
    "v_accvgpr_read_b32 v109, a77\n"
    "v_accvgpr_read_b32 v110, a78\n"
    "v_accvgpr_read_b32 v111, a79\n"
    "v_pk_mul_f32 v[96:97], v[96:97], v[246:247] op_sel_hi:[1,0]\n"
    "v_pk_mul_f32 v[98:99], v[98:99], v[246:247] op_sel_hi:[1,0]\n"
    "v_pk_mul_f32 v[100:101], v[100:101], v[246:247] op_sel_hi:[1,0]\n"
    "v_pk_mul_f32 v[102:103], v[102:103], v[246:247] op_sel_hi:[1,0]\n"
    "v_pk_mul_f32 v[104:105], v[104:105], v[246:247] op_sel_hi:[1,0]\n"
    "v_pk_mul_f32 v[106:107], v[106:107], v[246:247] op_sel_hi:[1,0]\n"
    "v_pk_mul_f32 v[108:109], v[108:109], v[246:247] op_sel_hi:[1,0]\n"
    "v_pk_mul_f32 v[110:111], v[110:111], v[246:247] op_sel_hi:[1,0]\n"
    "v_cvt_pk_bf16_f32 v128, v96, v97\n"
    "v_cvt_pk_bf16_f32 v129, v98, v99\n"
    "v_cvt_pk_bf16_f32 v130, v100, v101\n"
    "v_cvt_pk_bf16_f32 v131, v102, v103\n"
    "v_cvt_pk_bf16_f32 v132, v104, v105\n"
    "v_cvt_pk_bf16_f32 v133, v106, v107\n"
    "v_cvt_pk_bf16_f32 v134, v108, v109\n"
    "v_cvt_pk_bf16_f32 v135, v110, v111\n"
    "ds_read_b128 v[144:147], v194 offset:2304\n"
    "ds_read_b128 v[148:151], v194 offset:3456\n"
    "ds_write2_b64 v192, v[128:129], v[130:131] offset0:0 offset1:4\n"
    "ds_write2_b64 v192, v[132:133], v[134:135] offset0:8 offset1:12\n"
    "v_accvgpr_read_b32 v112, a80\n"
    "v_accvgpr_read_b32 v113, a81\n"
    "v_accvgpr_read_b32 v114, a82\n"
    "v_accvgpr_read_b32 v115, a83\n"
    "v_accvgpr_read_b32 v116, a84\n"
    "v_accvgpr_read_b32 v117, a85\n"
    "v_accvgpr_read_b32 v118, a86\n"
    "v_accvgpr_read_b32 v119, a87\n"
    "v_accvgpr_read_b32 v120, a88\n"
    "s_waitcnt lgkmcnt(2)\n"
    "buffer_store_dwordx4 v[144:147], v240, s[36:39], s81 offen offset:0\n"
    "buffer_store_dwordx4 v[148:151], v240, s[36:39], s81 offen offset:3072\n"
    "v_accvgpr_read_b32 v121, a89\n"
    "v_accvgpr_read_b32 v122, a90\n"
    "v_accvgpr_read_b32 v123, a91\n"
    "v_accvgpr_read_b32 v124, a92\n"
    "v_accvgpr_read_b32 v125, a93\n"
    "v_accvgpr_read_b32 v126, a94\n"
    "v_accvgpr_read_b32 v127, a95\n"
    "v_pk_mul_f32 v[112:113], v[112:113], v[246:247] op_sel_hi:[1,0]\n"
    "v_pk_mul_f32 v[114:115], v[114:115], v[246:247] op_sel_hi:[1,0]\n"
    "v_pk_mul_f32 v[116:117], v[116:117], v[246:247] op_sel_hi:[1,0]\n"
    "v_pk_mul_f32 v[118:119], v[118:119], v[246:247] op_sel_hi:[1,0]\n"
    "v_pk_mul_f32 v[120:121], v[120:121], v[246:247] op_sel_hi:[1,0]\n"
    "v_pk_mul_f32 v[122:123], v[122:123], v[246:247] op_sel_hi:[1,0]\n"
    "v_pk_mul_f32 v[124:125], v[124:125], v[246:247] op_sel_hi:[1,0]\n"
    "v_pk_mul_f32 v[126:127], v[126:127], v[246:247] op_sel_hi:[1,0]\n"
    "v_cvt_pk_bf16_f32 v136, v112, v113\n"
    "v_cvt_pk_bf16_f32 v137, v114, v115\n"
    "v_cvt_pk_bf16_f32 v138, v116, v117\n"
    "v_cvt_pk_bf16_f32 v139, v118, v119\n"
    "v_cvt_pk_bf16_f32 v140, v120, v121\n"
    "v_cvt_pk_bf16_f32 v141, v122, v123\n"
    "v_cvt_pk_bf16_f32 v142, v124, v125\n"
    "v_cvt_pk_bf16_f32 v143, v126, v127\n"
    "ds_read_b128 v[152:155], v194 offset:0\n"
    "ds_read_b128 v[156:159], v194 offset:1152\n"
    "ds_write2_b64 v193, v[136:137], v[138:139] offset0:0 offset1:4\n"
    "ds_write2_b64 v193, v[140:141], v[142:143] offset0:8 offset1:12\n"
    "s_waitcnt lgkmcnt(2)\n"
    "buffer_store_dwordx4 v[152:155], v240, s[36:39], s81 offen offset:128\n"
    "buffer_store_dwordx4 v[156:159], v240, s[36:39], s81 offen offset:3200\n"
    "ds_read_b128 v[216:219], v194 offset:2304\n"
    "ds_read_b128 v[220:223], v194 offset:3456\n"
    "s_waitcnt lgkmcnt(0)\n"
    "buffer_store_dwordx4 v[216:219], v240, s[36:39], s81 offen offset:256\n"
    "buffer_store_dwordx4 v[220:223], v240, s[36:39], s81 offen offset:3328\n"
    "s_branch LNEXT_%=\n"
    "LFIN1_3_%=:\n"
    "s_mov_b32 s57, 2\n"
    "s_waitcnt lgkmcnt(6)\n"
    "v_mfma_f32_16x16x32_bf16 a[0:3], a[240:243], v[160:163], a[0:3]\n"
    "ds_read_b64_tr_b16 a[224:225], v236 offset:12288\n"
    "v_add_f32 v208, v208, v212\n"
    "v_mfma_f32_16x16x32_bf16 a[48:51], a[240:243], v[164:167], a[48:51]\n"
    "ds_read_b64_tr_b16 a[226:227], v236 offset:18432\n"
    "v_add_f32 v209, v209, v213\n"
    "v_mfma_f32_16x16x32_bf16 a[96:99], a[240:243], v[168:171], a[96:99]\n"
    "ds_read_b64_tr_b16 a[228:229], v241 offset:12288\n"
    "v_add_f32 v210, v210, v214\n"
    "v_mfma_f32_16x16x32_bf16 a[4:7], a[244:247], v[160:163], a[4:7]\n"
    "ds_read_b64_tr_b16 a[230:231], v241 offset:18432\n"
    "v_mfma_f32_16x16x32_bf16 a[52:55], a[244:247], v[164:167], a[52:55]\n"
    "ds_read_b64_tr_b16 a[232:233], v236 offset:12800\n"
    "v_mfma_f32_16x16x32_bf16 a[100:103], a[244:247], v[168:171], a[100:103]\n"
    "v_exp_f32 v216, v128\n"
    "v_mfma_f32_16x16x32_bf16 a[8:11], a[248:251], v[160:163], a[8:11]\n"
    "ds_read_b64_tr_b16 a[234:235], v236 offset:18944\n"
    "v_mfma_f32_16x16x32_bf16 a[56:59], a[248:251], v[164:167], a[56:59]\n"
    "ds_read_b64_tr_b16 a[236:237], v241 offset:12800\n"
    "v_mfma_f32_16x16x32_bf16 a[104:107], a[248:251], v[168:171], a[104:107]\n"
    "ds_read_b64_tr_b16 a[238:239], v241 offset:18944\n"
    "v_mfma_f32_16x16x32_bf16 a[12:15], a[252:255], v[160:163], a[12:15]\n"
    "ds_read_b64_tr_b16 a[240:241], v236 offset:13312\n"
    "v_mfma_f32_16x16x32_bf16 a[60:63], a[252:255], v[164:167], a[60:63]\n"
    "ds_read_b64_tr_b16 a[242:243], v236 offset:19456\n"
    "v_mfma_f32_16x16x32_bf16 a[108:111], a[252:255], v[168:171], a[108:111]\n"
    "v_exp_f32 v217, v129\n"
    "v_mfma_f32_16x16x32_bf16 a[16:19], a[192:195], v[160:163], a[16:19]\n"
    "ds_read_b64_tr_b16 a[244:245], v241 offset:13312\n"
    "v_cvt_pk_bf16_f32 v176, v216, v217\n"
    "v_mfma_f32_16x16x32_bf16 a[64:67], a[192:195], v[164:167], a[64:67]\n"
    "ds_read_b64_tr_b16 a[246:247], v241 offset:19456\n"
    "v_add_f32 v212, v216, v217\n"
    "v_mfma_f32_16x16x32_bf16 a[112:115], a[192:195], v[168:171], a[112:115]\n"
    "ds_read_b64_tr_b16 a[248:249], v236 offset:13824\n"
    "v_mfma_f32_16x16x32_bf16 a[20:23], a[196:199], v[160:163], a[20:23]\n"
    "ds_read_b64_tr_b16 a[250:251], v236 offset:19968\n"
    "v_mfma_f32_16x16x32_bf16 a[68:71], a[196:199], v[164:167], a[68:71]\n"
    "ds_read_b64_tr_b16 a[252:253], v241 offset:13824\n"
    "v_mfma_f32_16x16x32_bf16 a[116:119], a[196:199], v[168:171], a[116:119]\n"
    "v_exp_f32 v218, v130\n"
    "v_mfma_f32_16x16x32_bf16 a[24:27], a[200:203], v[160:163], a[24:27]\n"
    "ds_read_b64_tr_b16 a[254:255], v241 offset:19968\n"
    "v_mfma_f32_16x16x32_bf16 a[72:75], a[200:203], v[164:167], a[72:75]\n"
    "ds_read_b64_tr_b16 a[192:193], v236 offset:14336\n"
    "v_mfma_f32_16x16x32_bf16 a[120:123], a[200:203], v[168:171], a[120:123]\n"
    "ds_read_b64_tr_b16 a[194:195], v236 offset:20480\n"
    "v_mfma_f32_16x16x32_bf16 a[28:31], a[204:207], v[160:163], a[28:31]\n"
    "ds_read_b64_tr_b16 a[196:197], v241 offset:14336\n"
    "v_mfma_f32_16x16x32_bf16 a[76:79], a[204:207], v[164:167], a[76:79]\n"
    "ds_read_b64_tr_b16 a[198:199], v241 offset:20480\n"
    "v_mfma_f32_16x16x32_bf16 a[124:127], a[204:207], v[168:171], a[124:127]\n"
    "v_exp_f32 v219, v131\n"
    "v_mfma_f32_16x16x32_bf16 a[32:35], a[208:211], v[160:163], a[32:35]\n"
    "ds_read_b64_tr_b16 a[200:201], v236 offset:14848\n"
    "v_cvt_pk_bf16_f32 v177, v218, v219\n"
    "v_mfma_f32_16x16x32_bf16 a[80:83], a[208:211], v[164:167], a[80:83]\n"
    "ds_read_b64_tr_b16 a[202:203], v236 offset:20992\n"
    "v_add_f32 v212, v212, v218\n"
    "v_mfma_f32_16x16x32_bf16 a[128:131], a[208:211], v[168:171], a[128:131]\n"
    "ds_read_b64_tr_b16 a[204:205], v241 offset:14848\n"
    "v_add_f32 v212, v212, v219\n"
    "s_waitcnt lgkmcnt(8)\n"
    "v_mfma_f32_16x16x32_bf16 a[36:39], a[212:215], v[160:163], a[36:39]\n"
    "ds_read_b64_tr_b16 a[206:207], v241 offset:20992\n"
    "v_mfma_f32_16x16x32_bf16 a[84:87], a[212:215], v[164:167], a[84:87]\n"
    "v_exp_f32 v220, v136\n"
    "v_mfma_f32_16x16x32_bf16 a[132:135], a[212:215], v[168:171], a[132:135]\n"
    "v_exp_f32 v221, v137\n"
    "v_mfma_f32_16x16x32_bf16 a[40:43], a[216:219], v[160:163], a[40:43]\n"
    "v_exp_f32 v222, v138\n"
    "v_mfma_f32_16x16x32_bf16 a[88:91], a[216:219], v[164:167], a[88:91]\n"
    "v_exp_f32 v223, v139\n"
    "v_mfma_f32_16x16x32_bf16 a[136:139], a[216:219], v[168:171], a[136:139]\n"
    "v_exp_f32 v224, v144\n"
    "v_mfma_f32_16x16x32_bf16 a[44:47], a[220:223], v[160:163], a[44:47]\n"
    "v_exp_f32 v225, v145\n"
    "v_mfma_f32_16x16x32_bf16 a[92:95], a[220:223], v[164:167], a[92:95]\n"
    "v_exp_f32 v226, v146\n"
    "v_mfma_f32_16x16x32_bf16 a[140:143], a[220:223], v[168:171], a[140:143]\n"
    "v_exp_f32 v227, v147\n"
    "v_exp_f32 v228, v132\n"
    "v_exp_f32 v229, v133\n"
    "v_cvt_pk_bf16_f32 v180, v220, v221\n"
    "v_add_f32 v213, v220, v221\n"
    "v_cvt_pk_bf16_f32 v181, v222, v223\n"
    "v_add_f32 v213, v213, v222\n"
    "v_exp_f32 v230, v134\n"
    "v_exp_f32 v231, v135\n"
    "v_add_f32 v213, v213, v223\n"
    "v_cvt_pk_bf16_f32 v184, v224, v225\n"
    "v_exp_f32 v216, v140\n"
    "v_exp_f32 v217, v141\n"
    "v_exp_f32 v218, v142\n"
    "v_add_f32 v214, v224, v225\n"
    "v_cvt_pk_bf16_f32 v185, v226, v227\n"
    "v_add_f32 v214, v214, v226\n"
    "v_add_f32 v214, v214, v227\n"
    "v_exp_f32 v219, v143\n"
    "v_exp_f32 v220, v148\n"
    "v_cvt_pk_bf16_f32 v178, v228, v229\n"
    "v_add_f32 v212, v212, v228\n"
    "v_exp_f32 v221, v149\n"
    "v_add_f32 v212, v212, v229\n"
    "v_cvt_pk_bf16_f32 v179, v230, v231\n"
    "v_add_f32 v212, v212, v230\n"
    "v_add_f32 v212, v212, v231\n"
    "v_exp_f32 v222, v150\n"
    "v_exp_f32 v223, v151\n"
    "v_cvt_pk_bf16_f32 v182, v216, v217\n"
    "v_add_f32 v213, v213, v216\n"
    "v_add_f32 v213, v213, v217\n"
    "v_cvt_pk_bf16_f32 v183, v218, v219\n"
    "v_add_f32 v213, v213, v218\n"
    "v_add_f32 v213, v213, v219\n"
    "v_cvt_pk_bf16_f32 v186, v220, v221\n"
    "v_add_f32 v214, v214, v220\n"
    "v_add_f32 v214, v214, v221\n"
    "v_cvt_pk_bf16_f32 v187, v222, v223\n"
    "v_add_f32 v214, v214, v222\n"
    "v_add_f32 v214, v214, v223\n"
    "v_max3_f32 v232, v212, v213, v214\n"
    "v_cmp_nge_f32 vcc, s49, v232\n"
    "s_cbranch_vccnz LRARE_o_3_%=\n"
    "LRET_o_3_F1_%=:\n"
    "v_mfma_f32_16x16x32_bf16 a[0:3], a[224:227], v[176:179], a[0:3]\n"
    "v_add_f32 v208, v208, v212\n"
    "v_add_f32 v209, v209, v213\n"
    "v_add_f32 v210, v210, v214\n"
    "v_mfma_f32_16x16x32_bf16 a[4:7], a[228:231], v[176:179], a[4:7]\n"
    "s_mul_i32 s62, s53, 6144\n"
    "v_mov_b32 v245, v208\n"
    "v_mov_b32 v247, v209\n"
    "v_mfma_f32_16x16x32_bf16 a[8:11], a[232:235], v[176:179], a[8:11]\n"
    "v_mov_b32 v249, v210\n"
    "s_add_u32 s62, s62, s42\n"
    "v_permlane32_swap_b32 v208, v245\n"
    "v_mfma_f32_16x16x32_bf16 a[12:15], a[236:239], v[176:179], a[12:15]\n"
    "v_permlane32_swap_b32 v209, v247\n"
    "v_permlane32_swap_b32 v210, v249\n"
    "s_add_u32 s62, s62, s56\n"
    "v_mfma_f32_16x16x32_bf16 a[16:19], a[240:243], v[176:179], a[16:19]\n"
    "v_add_f32 v208, v208, v245\n"
    "v_add_f32 v209, v209, v247\n"
    "v_add_f32 v210, v210, v249\n"
    "v_mfma_f32_16x16x32_bf16 a[20:23], a[244:247], v[176:179], a[20:23]\n"
    "s_add_u32 s62, s62, 73728\n"
    "v_mov_b32 v245, v208\n"
    "v_mov_b32 v247, v209\n"
    "v_mfma_f32_16x16x32_bf16 a[24:27], a[248:251], v[176:179], a[24:27]\n"
    "v_mov_b32 v249, v210\n"
    "v_lshrrev_b32 v216, 7, v253\n"
    "v_permlane16_swap_b32 v208, v245\n"
    "s_waitcnt lgkmcnt(0)\n"
    "v_mfma_f32_16x16x32_bf16 a[28:31], a[252:255], v[176:179], a[28:31]\n"
    "v_permlane16_swap_b32 v209, v247\n"
    "v_permlane16_swap_b32 v210, v249\n"
    "v_and_b32 v217, 15, v216\n"
    "v_mfma_f32_16x16x32_bf16 a[32:35], a[192:195], v[176:179], a[32:35]\n"
    "v_add_f32 v208, v208, v245\n"
    "v_add_f32 v209, v209, v247\n"
    "v_add_f32 v210, v210, v249\n"
    "v_mfma_f32_16x16x32_bf16 a[36:39], a[196:199], v[176:179], a[36:39]\n"
    "v_mul_u32_u24 v217, 144, v217\n"
    "v_rcp_f32 v244, v208\n"
    "v_rcp_f32 v246, v209\n"
    "v_mfma_f32_16x16x32_bf16 a[40:43], a[200:203], v[176:179], a[40:43]\n"
    "v_rcp_f32 v248, v210\n"
    "v_lshrrev_b32 v218, 4, v216\n"
    "v_lshl_add_u32 v217, v218, 3, v217\n"
    "v_mfma_f32_16x16x32_bf16 a[44:47], a[204:207], v[176:179], a[44:47]\n"
    "v_add_u32 v192, s62, v217\n"
    "v_add_u32 v193, 2304, v192\n"
    "v_lshrrev_b32 v217, 3, v216\n"
    "v_mul_u32_u24 v217, 144, v217\n"
    "v_and_b32 v218, 7, v216\n"
    "v_lshl_add_u32 v217, v218, 4, v217\n"
    "v_add_u32 v194, s62, v217\n"
    "s_add_u32 s80, s54, 0\n"
    "s_add_u32 s81, s54, 6144\n"
    "s_add_u32 s82, s54, 12288\n"
    "v_mfma_f32_16x16x32_bf16 a[48:51], a[224:227], v[180:183], a[48:51]\n"
    "v_accvgpr_read_b32 v96, a0\n"
    "v_accvgpr_read_b32 v97, a1\n"
    "v_accvgpr_read_b32 v98, a2\n"
    "v_accvgpr_read_b32 v99, a3\n"
    "v_accvgpr_read_b32 v100, a4\n"
    "v_mfma_f32_16x16x32_bf16 a[52:55], a[228:231], v[180:183], a[52:55]\n"
    "v_accvgpr_read_b32 v101, a5\n"
    "v_accvgpr_read_b32 v102, a6\n"
    "v_accvgpr_read_b32 v103, a7\n"
    "v_accvgpr_read_b32 v104, a8\n"
    "v_accvgpr_read_b32 v105, a9\n"
    "v_accvgpr_read_b32 v106, a10\n"
    "v_accvgpr_read_b32 v107, a11\n"
    "v_accvgpr_read_b32 v108, a12\n"
    "v_accvgpr_read_b32 v109, a13\n"
    "v_accvgpr_read_b32 v110, a14\n"
    "v_accvgpr_read_b32 v111, a15\n"
    "v_pk_mul_f32 v[96:97], v[96:97], v[244:245] op_sel_hi:[1,0]\n"
    "v_pk_mul_f32 v[98:99], v[98:99], v[244:245] op_sel_hi:[1,0]\n"
    "v_pk_mul_f32 v[100:101], v[100:101], v[244:245] op_sel_hi:[1,0]\n"
    "v_pk_mul_f32 v[102:103], v[102:103], v[244:245] op_sel_hi:[1,0]\n"
    "v_pk_mul_f32 v[104:105], v[104:105], v[244:245] op_sel_hi:[1,0]\n"
    "v_pk_mul_f32 v[106:107], v[106:107], v[244:245] op_sel_hi:[1,0]\n"
    "v_pk_mul_f32 v[108:109], v[108:109], v[244:245] op_sel_hi:[1,0]\n"
    "v_pk_mul_f32 v[110:111], v[110:111], v[244:245] op_sel_hi:[1,0]\n"
    "v_mfma_f32_16x16x32_bf16 a[56:59], a[232:235], v[180:183], a[56:59]\n"
    "v_cvt_pk_bf16_f32 v128, v96, v97\n"
    "v_cvt_pk_bf16_f32 v129, v98, v99\n"
    "v_cvt_pk_bf16_f32 v130, v100, v101\n"
    "v_cvt_pk_bf16_f32 v131, v102, v103\n"
    "v_mfma_f32_16x16x32_bf16 a[60:63], a[236:239], v[180:183], a[60:63]\n"
    "v_cvt_pk_bf16_f32 v132, v104, v105\n"
    "v_cvt_pk_bf16_f32 v133, v106, v107\n"
    "v_cvt_pk_bf16_f32 v134, v108, v109\n"
    "v_cvt_pk_bf16_f32 v135, v110, v111\n"
    "ds_write2_b64 v192, v[128:129], v[130:131] offset0:0 offset1:4\n"
    "ds_write2_b64 v192, v[132:133], v[134:135] offset0:8 offset1:12\n"
    "v_mfma_f32_16x16x32_bf16 a[64:67], a[240:243], v[180:183], a[64:67]\n"
    "v_accvgpr_read_b32 v112, a16\n"
    "v_accvgpr_read_b32 v113, a17\n"
    "v_accvgpr_read_b32 v114, a18\n"
    "v_accvgpr_read_b32 v115, a19\n"
    "v_accvgpr_read_b32 v116, a20\n"
    "v_mfma_f32_16x16x32_bf16 a[68:71], a[244:247], v[180:183], a[68:71]\n"
    "v_accvgpr_read_b32 v117, a21\n"
    "v_accvgpr_read_b32 v118, a22\n"
    "v_accvgpr_read_b32 v119, a23\n"
    "v_accvgpr_read_b32 v120, a24\n"
    "v_accvgpr_read_b32 v121, a25\n"
    "v_accvgpr_read_b32 v122, a26\n"
    "v_accvgpr_read_b32 v123, a27\n"
    "v_accvgpr_read_b32 v124, a28\n"
    "v_accvgpr_read_b32 v125, a29\n"
    "v_accvgpr_read_b32 v126, a30\n"
    "v_accvgpr_read_b32 v127, a31\n"
    "v_pk_mul_f32 v[112:113], v[112:113], v[244:245] op_sel_hi:[1,0]\n"
    "v_pk_mul_f32 v[114:115], v[114:115], v[244:245] op_sel_hi:[1,0]\n"
    "v_pk_mul_f32 v[116:117], v[116:117], v[244:245] op_sel_hi:[1,0]\n"
    "v_pk_mul_f32 v[118:119], v[118:119], v[244:245] op_sel_hi:[1,0]\n"
    "v_pk_mul_f32 v[120:121], v[120:121], v[244:245] op_sel_hi:[1,0]\n"
    "v_pk_mul_f32 v[122:123], v[122:123], v[244:245] op_sel_hi:[1,0]\n"
    "v_pk_mul_f32 v[124:125], v[124:125], v[244:245] op_sel_hi:[1,0]\n"
    "v_pk_mul_f32 v[126:127], v[126:127], v[244:245] op_sel_hi:[1,0]\n"
    "v_mfma_f32_16x16x32_bf16 a[72:75], a[248:251], v[180:183], a[72:75]\n"
    "v_cvt_pk_bf16_f32 v136, v112, v113\n"
    "v_cvt_pk_bf16_f32 v137, v114, v115\n"
    "v_cvt_pk_bf16_f32 v138, v116, v117\n"
    "v_cvt_pk_bf16_f32 v139, v118, v119\n"
    "v_mfma_f32_16x16x32_bf16 a[76:79], a[252:255], v[180:183], a[76:79]\n"
    "v_cvt_pk_bf16_f32 v140, v120, v121\n"
    "v_cvt_pk_bf16_f32 v141, v122, v123\n"
    "v_cvt_pk_bf16_f32 v142, v124, v125\n"
    "v_cvt_pk_bf16_f32 v143, v126, v127\n"
    "ds_read_b128 v[144:147], v194 offset:0\n"
    "ds_read_b128 v[148:151], v194 offset:1152\n"
    "ds_write2_b64 v193, v[136:137], v[138:139] offset0:0 offset1:4\n"
    "ds_write2_b64 v193, v[140:141], v[142:143] offset0:8 offset1:12\n"
    "v_mfma_f32_16x16x32_bf16 a[80:83], a[192:195], v[180:183], a[80:83]\n"
    "v_accvgpr_read_b32 v96, a32\n"
    "v_accvgpr_read_b32 v97, a33\n"
    "v_accvgpr_read_b32 v98, a34\n"
    "v_accvgpr_read_b32 v99, a35\n"
    "v_accvgpr_read_b32 v100, a36\n"
    "v_mfma_f32_16x16x32_bf16 a[84:87], a[196:199], v[180:183], a[84:87]\n"
    "v_accvgpr_read_b32 v101, a37\n"
    "v_accvgpr_read_b32 v102, a38\n"
    "s_waitcnt lgkmcnt(2)\n"
    "buffer_store_dwordx4 v[144:147], v240, s[36:39], s80 offen offset:0 sc0 sc1\n"
    "buffer_store_dwordx4 v[148:151], v240, s[36:39], s80 offen offset:3072 sc0 sc1\n"
    "v_accvgpr_read_b32 v103, a39\n"
    "v_accvgpr_read_b32 v104, a40\n"
    "v_accvgpr_read_b32 v105, a41\n"
    "v_accvgpr_read_b32 v106, a42\n"
    "v_accvgpr_read_b32 v107, a43\n"
    "v_accvgpr_read_b32 v108, a44\n"
    "v_accvgpr_read_b32 v109, a45\n"
    "v_accvgpr_read_b32 v110, a46\n"
    "v_accvgpr_read_b32 v111, a47\n"
    "v_pk_mul_f32 v[96:97], v[96:97], v[244:245] op_sel_hi:[1,0]\n"
    "v_pk_mul_f32 v[98:99], v[98:99], v[244:245] op_sel_hi:[1,0]\n"
    "v_pk_mul_f32 v[100:101], v[100:101], v[244:245] op_sel_hi:[1,0]\n"
    "v_pk_mul_f32 v[102:103], v[102:103], v[244:245] op_sel_hi:[1,0]\n"
    "v_pk_mul_f32 v[104:105], v[104:105], v[244:245] op_sel_hi:[1,0]\n"
    "v_pk_mul_f32 v[106:107], v[106:107], v[244:245] op_sel_hi:[1,0]\n"
    "v_pk_mul_f32 v[108:109], v[108:109], v[244:245] op_sel_hi:[1,0]\n"
    "v_pk_mul_f32 v[110:111], v[110:111], v[244:245] op_sel_hi:[1,0]\n"
    "v_mfma_f32_16x16x32_bf16 a[88:91], a[200:203], v[180:183], a[88:91]\n"
    "v_cvt_pk_bf16_f32 v128, v96, v97\n"
    "v_cvt_pk_bf16_f32 v129, v98, v99\n"
    "v_cvt_pk_bf16_f32 v130, v100, v101\n"
    "v_cvt_pk_bf16_f32 v131, v102, v103\n"
    "v_mfma_f32_16x16x32_bf16 a[92:95], a[204:207], v[180:183], a[92:95]\n"
    "v_cvt_pk_bf16_f32 v132, v104, v105\n"
    "v_cvt_pk_bf16_f32 v133, v106, v107\n"
    "v_cvt_pk_bf16_f32 v134, v108, v109\n"
    "v_cvt_pk_bf16_f32 v135, v110, v111\n"
    "ds_read_b128 v[152:155], v194 offset:2304\n"
    "ds_read_b128 v[156:159], v194 offset:3456\n"
    "ds_write2_b64 v192, v[128:129], v[130:131] offset0:0 offset1:4\n"
    "ds_write2_b64 v192, v[132:133], v[134:135] offset0:8 offset1:12\n"
    "v_mfma_f32_16x16x32_bf16 a[96:99], a[224:227], v[184:187], a[96:99]\n"
    "v_accvgpr_read_b32 v112, a48\n"
    "v_accvgpr_read_b32 v113, a49\n"
    "v_accvgpr_read_b32 v114, a50\n"
    "v_accvgpr_read_b32 v115, a51\n"
    "v_accvgpr_read_b32 v116, a52\n"
    "v_mfma_f32_16x16x32_bf16 a[100:103], a[228:231], v[184:187], a[100:103]\n"
    "v_accvgpr_read_b32 v117, a53\n"
    "v_accvgpr_read_b32 v118, a54\n"
    "s_waitcnt lgkmcnt(2)\n"
    "buffer_store_dwordx4 v[152:155], v240, s[36:39], s80 offen offset:128 sc0 sc1\n"
    "buffer_store_dwordx4 v[156:159], v240, s[36:39], s80 offen offset:3200 sc0 sc1\n"
    "v_accvgpr_read_b32 v119, a55\n"
    "v_accvgpr_read_b32 v120, a56\n"
    "v_accvgpr_read_b32 v121, a57\n"
    "v_accvgpr_read_b32 v122, a58\n"
    "v_accvgpr_read_b32 v123, a59\n"
    "v_accvgpr_read_b32 v124, a60\n"
    "v_accvgpr_read_b32 v125, a61\n"
    "v_accvgpr_read_b32 v126, a62\n"
    "v_accvgpr_read_b32 v127, a63\n"
    "v_pk_mul_f32 v[112:113], v[112:113], v[246:247] op_sel_hi:[1,0]\n"
    "v_pk_mul_f32 v[114:115], v[114:115], v[246:247] op_sel_hi:[1,0]\n"
    "v_pk_mul_f32 v[116:117], v[116:117], v[246:247] op_sel_hi:[1,0]\n"
    "v_pk_mul_f32 v[118:119], v[118:119], v[246:247] op_sel_hi:[1,0]\n"
    "v_pk_mul_f32 v[120:121], v[120:121], v[246:247] op_sel_hi:[1,0]\n"
    "v_pk_mul_f32 v[122:123], v[122:123], v[246:247] op_sel_hi:[1,0]\n"
    "v_pk_mul_f32 v[124:125], v[124:125], v[246:247] op_sel_hi:[1,0]\n"
    "v_pk_mul_f32 v[126:127], v[126:127], v[246:247] op_sel_hi:[1,0]\n"
    "v_mfma_f32_16x16x32_bf16 a[104:107], a[232:235], v[184:187], a[104:107]\n"
    "v_cvt_pk_bf16_f32 v136, v112, v113\n"
    "v_cvt_pk_bf16_f32 v137, v114, v115\n"
    "v_cvt_pk_bf16_f32 v138, v116, v117\n"
    "v_cvt_pk_bf16_f32 v139, v118, v119\n"
    "v_mfma_f32_16x16x32_bf16 a[108:111], a[236:239], v[184:187], a[108:111]\n"
    "v_cvt_pk_bf16_f32 v140, v120, v121\n"
    "v_cvt_pk_bf16_f32 v141, v122, v123\n"
    "v_cvt_pk_bf16_f32 v142, v124, v125\n"
    "v_cvt_pk_bf16_f32 v143, v126, v127\n"
    "ds_read_b128 v[216:219], v194 offset:0\n"
    "ds_read_b128 v[220:223], v194 offset:1152\n"
    "ds_write2_b64 v193, v[136:137], v[138:139] offset0:0 offset1:4\n"
    "ds_write2_b64 v193, v[140:141], v[142:143] offset0:8 offset1:12\n"
    "v_mfma_f32_16x16x32_bf16 a[112:115], a[240:243], v[184:187], a[112:115]\n"
    "v_accvgpr_read_b32 v96, a64\n"
    "v_accvgpr_read_b32 v97, a65\n"
    "v_accvgpr_read_b32 v98, a66\n"
    "v_accvgpr_read_b32 v99, a67\n"
    "v_accvgpr_read_b32 v100, a68\n"
    "v_mfma_f32_16x16x32_bf16 a[116:119], a[244:247], v[184:187], a[116:119]\n"
    "v_accvgpr_read_b32 v101, a69\n"
    "v_accvgpr_read_b32 v102, a70\n"
    "s_waitcnt lgkmcnt(2)\n"
    "buffer_store_dwordx4 v[216:219], v240, s[36:39], s80 offen offset:256 sc0 sc1\n"
    "buffer_store_dwordx4 v[220:223], v240, s[36:39], s80 offen offset:3328 sc0 sc1\n"
    "v_accvgpr_read_b32 v103, a71\n"
    "v_accvgpr_read_b32 v104, a72\n"
    "v_accvgpr_read_b32 v105, a73\n"
    "v_accvgpr_read_b32 v106, a74\n"
    "v_accvgpr_read_b32 v107, a75\n"
    "v_accvgpr_read_b32 v108, a76\n"
    "v_accvgpr_read_b32 v109, a77\n"
    "v_accvgpr_read_b32 v110, a78\n"
    "v_accvgpr_read_b32 v111, a79\n"
    "v_pk_mul_f32 v[96:97], v[96:97], v[246:247] op_sel_hi:[1,0]\n"
    "v_pk_mul_f32 v[98:99], v[98:99], v[246:247] op_sel_hi:[1,0]\n"
    "v_pk_mul_f32 v[100:101], v[100:101], v[246:247] op_sel_hi:[1,0]\n"
    "v_pk_mul_f32 v[102:103], v[102:103], v[246:247] op_sel_hi:[1,0]\n"
    "v_pk_mul_f32 v[104:105], v[104:105], v[246:247] op_sel_hi:[1,0]\n"
    "v_pk_mul_f32 v[106:107], v[106:107], v[246:247] op_sel_hi:[1,0]\n"
    "v_pk_mul_f32 v[108:109], v[108:109], v[246:247] op_sel_hi:[1,0]\n"
    "v_pk_mul_f32 v[110:111], v[110:111], v[246:247] op_sel_hi:[1,0]\n"
    "v_mfma_f32_16x16x32_bf16 a[120:123], a[248:251], v[184:187], a[120:123]\n"
    "v_cvt_pk_bf16_f32 v128, v96, v97\n"
    "v_cvt_pk_bf16_f32 v129, v98, v99\n"
    "v_cvt_pk_bf16_f32 v130, v100, v101\n"
    "v_cvt_pk_bf16_f32 v131, v102, v103\n"
    "v_mfma_f32_16x16x32_bf16 a[124:127], a[252:255], v[184:187], a[124:127]\n"
    "v_cvt_pk_bf16_f32 v132, v104, v105\n"
    "v_cvt_pk_bf16_f32 v133, v106, v107\n"
    "v_cvt_pk_bf16_f32 v134, v108, v109\n"
    "v_cvt_pk_bf16_f32 v135, v110, v111\n"
    "ds_read_b128 v[144:147], v194 offset:2304\n"
    "ds_read_b128 v[148:151], v194 offset:3456\n"
    "ds_write2_b64 v192, v[128:129], v[130:131] offset0:0 offset1:4\n"
    "ds_write2_b64 v192, v[132:133], v[134:135] offset0:8 offset1:12\n"
    "v_mfma_f32_16x16x32_bf16 a[128:131], a[192:195], v[184:187], a[128:131]\n"
    "v_accvgpr_read_b32 v112, a80\n"
    "v_accvgpr_read_b32 v113, a81\n"
    "v_accvgpr_read_b32 v114, a82\n"
    "v_accvgpr_read_b32 v115, a83\n"
    "v_accvgpr_read_b32 v116, a84\n"
    "v_mfma_f32_16x16x32_bf16 a[132:135], a[196:199], v[184:187], a[132:135]\n"
    "v_accvgpr_read_b32 v117, a85\n"
    "v_accvgpr_read_b32 v118, a86\n"
    "s_waitcnt lgkmcnt(2)\n"
    "buffer_store_dwordx4 v[144:147], v240, s[36:39], s81 offen offset:0 sc0 sc1\n"
    "buffer_store_dwordx4 v[148:151], v240, s[36:39], s81 offen offset:3072 sc0 sc1\n"
    "v_accvgpr_read_b32 v119, a87\n"
    "v_accvgpr_read_b32 v120, a88\n"
    "v_accvgpr_read_b32 v121, a89\n"
    "v_accvgpr_read_b32 v122, a90\n"
    "v_accvgpr_read_b32 v123, a91\n"
    "v_accvgpr_read_b32 v124, a92\n"
    "v_accvgpr_read_b32 v125, a93\n"
    "v_accvgpr_read_b32 v126, a94\n"
    "v_accvgpr_read_b32 v127, a95\n"
    "v_pk_mul_f32 v[112:113], v[112:113], v[246:247] op_sel_hi:[1,0]\n"
    "v_pk_mul_f32 v[114:115], v[114:115], v[246:247] op_sel_hi:[1,0]\n"
    "v_pk_mul_f32 v[116:117], v[116:117], v[246:247] op_sel_hi:[1,0]\n"
    "v_pk_mul_f32 v[118:119], v[118:119], v[246:247] op_sel_hi:[1,0]\n"
    "v_pk_mul_f32 v[120:121], v[120:121], v[246:247] op_sel_hi:[1,0]\n"
    "v_pk_mul_f32 v[122:123], v[122:123], v[246:247] op_sel_hi:[1,0]\n"
    "v_pk_mul_f32 v[124:125], v[124:125], v[246:247] op_sel_hi:[1,0]\n"
    "v_pk_mul_f32 v[126:127], v[126:127], v[246:247] op_sel_hi:[1,0]\n"
    "v_mfma_f32_16x16x32_bf16 a[136:139], a[200:203], v[184:187], a[136:139]\n"
    "v_cvt_pk_bf16_f32 v136, v112, v113\n"
    "v_cvt_pk_bf16_f32 v137, v114, v115\n"
    "v_cvt_pk_bf16_f32 v138, v116, v117\n"
    "v_cvt_pk_bf16_f32 v139, v118, v119\n"
    "v_mfma_f32_16x16x32_bf16 a[140:143], a[204:207], v[184:187], a[140:143]\n"
    "v_cvt_pk_bf16_f32 v140, v120, v121\n"
    "v_cvt_pk_bf16_f32 v141, v122, v123\n"
    "v_cvt_pk_bf16_f32 v142, v124, v125\n"
    "v_cvt_pk_bf16_f32 v143, v126, v127\n"
    "ds_read_b128 v[152:155], v194 offset:0\n"
    "ds_read_b128 v[156:159], v194 offset:1152\n"
    "ds_write2_b64 v193, v[136:137], v[138:139] offset0:0 offset1:4\n"
    "ds_write2_b64 v193, v[140:141], v[142:143] offset0:8 offset1:12\n"
    "v_accvgpr_read_b32 v96, a96\n"
    "v_accvgpr_read_b32 v97, a97\n"
    "v_accvgpr_read_b32 v98, a98\n"
    "v_accvgpr_read_b32 v99, a99\n"
    "v_accvgpr_read_b32 v100, a100\n"
    "v_accvgpr_read_b32 v101, a101\n"
    "v_accvgpr_read_b32 v102, a102\n"
    "v_accvgpr_read_b32 v103, a103\n"
    "v_accvgpr_read_b32 v104, a104\n"
    "s_waitcnt lgkmcnt(2)\n"
    "buffer_store_dwordx4 v[152:155], v240, s[36:39], s81 offen offset:128 sc0 sc1\n"
    "buffer_store_dwordx4 v[156:159], v240, s[36:39], s81 offen offset:3200 sc0 sc1\n"
    "v_accvgpr_read_b32 v105, a105\n"
    "v_accvgpr_read_b32 v106, a106\n"
    "v_accvgpr_read_b32 v107, a107\n"
    "v_accvgpr_read_b32 v108, a108\n"
    "v_accvgpr_read_b32 v109, a109\n"
    "v_accvgpr_read_b32 v110, a110\n"
    "v_accvgpr_read_b32 v111, a111\n"
    "v_pk_mul_f32 v[96:97], v[96:97], v[248:249] op_sel_hi:[1,0]\n"
    "v_pk_mul_f32 v[98:99], v[98:99], v[248:249] op_sel_hi:[1,0]\n"
    "v_pk_mul_f32 v[100:101], v[100:101], v[248:249] op_sel_hi:[1,0]\n"
    "v_pk_mul_f32 v[102:103], v[102:103], v[248:249] op_sel_hi:[1,0]\n"
    "v_pk_mul_f32 v[104:105], v[104:105], v[248:249] op_sel_hi:[1,0]\n"
    "v_pk_mul_f32 v[106:107], v[106:107], v[248:249] op_sel_hi:[1,0]\n"
    "v_pk_mul_f32 v[108:109], v[108:109], v[248:249] op_sel_hi:[1,0]\n"
    "v_pk_mul_f32 v[110:111], v[110:111], v[248:249] op_sel_hi:[1,0]\n"
    "v_cvt_pk_bf16_f32 v128, v96, v97\n"
    "v_cvt_pk_bf16_f32 v129, v98, v99\n"
    "v_cvt_pk_bf16_f32 v130, v100, v101\n"
    "v_cvt_pk_bf16_f32 v131, v102, v103\n"
    "v_cvt_pk_bf16_f32 v132, v104, v105\n"
    "v_cvt_pk_bf16_f32 v133, v106, v107\n"
    "v_cvt_pk_bf16_f32 v134, v108, v109\n"
    "v_cvt_pk_bf16_f32 v135, v110, v111\n"
    "ds_read_b128 v[216:219], v194 offset:2304\n"
    "ds_read_b128 v[220:223], v194 offset:3456\n"
    "ds_write2_b64 v192, v[128:129], v[130:131] offset0:0 offset1:4\n"
    "ds_write2_b64 v192, v[132:133], v[134:135] offset0:8 offset1:12\n"
    "v_accvgpr_read_b32 v112, a112\n"
    "v_accvgpr_read_b32 v113, a113\n"
    "v_accvgpr_read_b32 v114, a114\n"
    "v_accvgpr_read_b32 v115, a115\n"
    "v_accvgpr_read_b32 v116, a116\n"
    "v_accvgpr_read_b32 v117, a117\n"
    "v_accvgpr_read_b32 v118, a118\n"
    "v_accvgpr_read_b32 v119, a119\n"
    "v_accvgpr_read_b32 v120, a120\n"
    "s_waitcnt lgkmcnt(2)\n"
    "buffer_store_dwordx4 v[216:219], v240, s[36:39], s81 offen offset:256 sc0 sc1\n"
    "buffer_store_dwordx4 v[220:223], v240, s[36:39], s81 offen offset:3328 sc0 sc1\n"
    "v_accvgpr_read_b32 v121, a121\n"
    "v_accvgpr_read_b32 v122, a122\n"
    "v_accvgpr_read_b32 v123, a123\n"
    "v_accvgpr_read_b32 v124, a124\n"
    "v_accvgpr_read_b32 v125, a125\n"
    "v_accvgpr_read_b32 v126, a126\n"
    "v_accvgpr_read_b32 v127, a127\n"
    "v_pk_mul_f32 v[112:113], v[112:113], v[248:249] op_sel_hi:[1,0]\n"
    "v_pk_mul_f32 v[114:115], v[114:115], v[248:249] op_sel_hi:[1,0]\n"
    "v_pk_mul_f32 v[116:117], v[116:117], v[248:249] op_sel_hi:[1,0]\n"
    "v_pk_mul_f32 v[118:119], v[118:119], v[248:249] op_sel_hi:[1,0]\n"
    "v_pk_mul_f32 v[120:121], v[120:121], v[248:249] op_sel_hi:[1,0]\n"
    "v_pk_mul_f32 v[122:123], v[122:123], v[248:249] op_sel_hi:[1,0]\n"
    "v_pk_mul_f32 v[124:125], v[124:125], v[248:249] op_sel_hi:[1,0]\n"
    "v_pk_mul_f32 v[126:127], v[126:127], v[248:249] op_sel_hi:[1,0]\n"
    "v_cvt_pk_bf16_f32 v136, v112, v113\n"
    "v_cvt_pk_bf16_f32 v137, v114, v115\n"
    "v_cvt_pk_bf16_f32 v138, v116, v117\n"
    "v_cvt_pk_bf16_f32 v139, v118, v119\n"
    "v_cvt_pk_bf16_f32 v140, v120, v121\n"
    "v_cvt_pk_bf16_f32 v141, v122, v123\n"
    "v_cvt_pk_bf16_f32 v142, v124, v125\n"
    "v_cvt_pk_bf16_f32 v143, v126, v127\n"
    "ds_read_b128 v[144:147], v194 offset:0\n"
    "ds_read_b128 v[148:151], v194 offset:1152\n"
    "ds_write2_b64 v193, v[136:137], v[138:139] offset0:0 offset1:4\n"
    "ds_write2_b64 v193, v[140:141], v[142:143] offset0:8 offset1:12\n"
    "v_accvgpr_read_b32 v96, a128\n"
    "v_accvgpr_read_b32 v97, a129\n"
    "v_accvgpr_read_b32 v98, a130\n"
    "v_accvgpr_read_b32 v99, a131\n"
    "v_accvgpr_read_b32 v100, a132\n"
    "v_accvgpr_read_b32 v101, a133\n"
    "v_accvgpr_read_b32 v102, a134\n"
    "v_accvgpr_read_b32 v103, a135\n"
    "v_accvgpr_read_b32 v104, a136\n"
    "s_waitcnt lgkmcnt(2)\n"
    "buffer_store_dwordx4 v[144:147], v240, s[36:39], s82 offen offset:0 sc0 sc1\n"
    "buffer_store_dwordx4 v[148:151], v240, s[36:39], s82 offen offset:3072 sc0 sc1\n"
    "v_accvgpr_read_b32 v105, a137\n"
    "v_accvgpr_read_b32 v106, a138\n"
    "v_accvgpr_read_b32 v107, a139\n"
    "v_accvgpr_read_b32 v108, a140\n"
    "v_accvgpr_read_b32 v109, a141\n"
    "v_accvgpr_read_b32 v110, a142\n"
    "v_accvgpr_read_b32 v111, a143\n"
    "v_pk_mul_f32 v[96:97], v[96:97], v[248:249] op_sel_hi:[1,0]\n"
    "v_pk_mul_f32 v[98:99], v[98:99], v[248:249] op_sel_hi:[1,0]\n"
    "v_pk_mul_f32 v[100:101], v[100:101], v[248:249] op_sel_hi:[1,0]\n"
    "v_pk_mul_f32 v[102:103], v[102:103], v[248:249] op_sel_hi:[1,0]\n"
    "v_pk_mul_f32 v[104:105], v[104:105], v[248:249] op_sel_hi:[1,0]\n"
    "v_pk_mul_f32 v[106:107], v[106:107], v[248:249] op_sel_hi:[1,0]\n"
    "v_pk_mul_f32 v[108:109], v[108:109], v[248:249] op_sel_hi:[1,0]\n"
    "v_pk_mul_f32 v[110:111], v[110:111], v[248:249] op_sel_hi:[1,0]\n"
    "v_cvt_pk_bf16_f32 v128, v96, v97\n"
    "v_cvt_pk_bf16_f32 v129, v98, v99\n"
    "v_cvt_pk_bf16_f32 v130, v100, v101\n"
    "v_cvt_pk_bf16_f32 v131, v102, v103\n"
    "v_cvt_pk_bf16_f32 v132, v104, v105\n"
    "v_cvt_pk_bf16_f32 v133, v106, v107\n"
    "v_cvt_pk_bf16_f32 v134, v108, v109\n"
    "v_cvt_pk_bf16_f32 v135, v110, v111\n"
    "ds_read_b128 v[152:155], v194 offset:2304\n"
    "ds_read_b128 v[156:159], v194 offset:3456\n"
    "ds_write2_b64 v192, v[128:129], v[130:131] offset0:0 offset1:4\n"
    "ds_write2_b64 v192, v[132:133], v[134:135] offset0:8 offset1:12\n"
    "s_waitcnt lgkmcnt(2)\n"
    "buffer_store_dwordx4 v[152:155], v240, s[36:39], s82 offen offset:128 sc0 sc1\n"
    "buffer_store_dwordx4 v[156:159], v240, s[36:39], s82 offen offset:3200 sc0 sc1\n"
    "ds_read_b128 v[216:219], v194 offset:0\n"
    "ds_read_b128 v[220:223], v194 offset:1152\n"
    "s_waitcnt lgkmcnt(0)\n"
    "buffer_store_dwordx4 v[216:219], v240, s[36:39], s82 offen offset:256 sc0 sc1\n"
    "buffer_store_dwordx4 v[220:223], v240, s[36:39], s82 offen offset:3328 sc0 sc1\n"
    "s_branch LNEXT_%=\n"
    "LFIN1_2_%=:\n"
    "s_mov_b32 s57, 2\n"
    "s_waitcnt lgkmcnt(4)\n"
    "v_mfma_f32_16x16x32_bf16 a[0:3], a[240:243], v[160:163], a[0:3]\n"
    "ds_read_b64_tr_b16 a[224:225], v236 offset:12288\n"
    "v_add_f32 v208, v208, v212\n"
    "v_mfma_f32_16x16x32_bf16 a[48:51], a[240:243], v[164:167], a[48:51]\n"
    "ds_read_b64_tr_b16 a[226:227], v236 offset:18432\n"
    "v_add_f32 v209, v209, v213\n"
    "ds_read_b64_tr_b16 a[228:229], v241 offset:12288\n"
    "v_exp_f32 v216, v128\n"
    "v_mfma_f32_16x16x32_bf16 a[4:7], a[244:247], v[160:163], a[4:7]\n"
    "ds_read_b64_tr_b16 a[230:231], v241 offset:18432\n"
    "v_mfma_f32_16x16x32_bf16 a[52:55], a[244:247], v[164:167], a[52:55]\n"
    "ds_read_b64_tr_b16 a[232:233], v236 offset:12800\n"
    "ds_read_b64_tr_b16 a[234:235], v236 offset:18944\n"
    "v_exp_f32 v217, v129\n"
    "v_mfma_f32_16x16x32_bf16 a[8:11], a[248:251], v[160:163], a[8:11]\n"
    "ds_read_b64_tr_b16 a[236:237], v241 offset:12800\n"
    "v_cvt_pk_bf16_f32 v176, v216, v217\n"
    "v_mfma_f32_16x16x32_bf16 a[56:59], a[248:251], v[164:167], a[56:59]\n"
    "ds_read_b64_tr_b16 a[238:239], v241 offset:18944\n"
    "v_add_f32 v212, v216, v217\n"
    "ds_read_b64_tr_b16 a[240:241], v236 offset:13312\n"
    "v_exp_f32 v218, v130\n"
    "v_mfma_f32_16x16x32_bf16 a[12:15], a[252:255], v[160:163], a[12:15]\n"
    "ds_read_b64_tr_b16 a[242:243], v236 offset:19456\n"
    "v_mfma_f32_16x16x32_bf16 a[60:63], a[252:255], v[164:167], a[60:63]\n"
    "ds_read_b64_tr_b16 a[244:245], v241 offset:13312\n"
    "ds_read_b64_tr_b16 a[246:247], v241 offset:19456\n"
    "v_exp_f32 v219, v131\n"
    "v_mfma_f32_16x16x32_bf16 a[16:19], a[192:195], v[160:163], a[16:19]\n"
    "ds_read_b64_tr_b16 a[248:249], v236 offset:13824\n"
    "v_cvt_pk_bf16_f32 v177, v218, v219\n"
    "v_mfma_f32_16x16x32_bf16 a[64:67], a[192:195], v[164:167], a[64:67]\n"
    "ds_read_b64_tr_b16 a[250:251], v236 offset:19968\n"
    "v_add_f32 v212, v212, v218\n"
    "ds_read_b64_tr_b16 a[252:253], v241 offset:13824\n"
    "v_exp_f32 v220, v136\n"
    "v_add_f32 v212, v212, v219\n"
    "v_mfma_f32_16x16x32_bf16 a[20:23], a[196:199], v[160:163], a[20:23]\n"
    "ds_read_b64_tr_b16 a[254:255], v241 offset:19968\n"
    "v_mfma_f32_16x16x32_bf16 a[68:71], a[196:199], v[164:167], a[68:71]\n"
    "ds_read_b64_tr_b16 a[192:193], v236 offset:14336\n"
    "ds_read_b64_tr_b16 a[194:195], v236 offset:20480\n"
    "v_exp_f32 v221, v137\n"
    "v_mfma_f32_16x16x32_bf16 a[24:27], a[200:203], v[160:163], a[24:27]\n"
    "ds_read_b64_tr_b16 a[196:197], v241 offset:14336\n"
    "v_cvt_pk_bf16_f32 v180, v220, v221\n"
    "v_mfma_f32_16x16x32_bf16 a[72:75], a[200:203], v[164:167], a[72:75]\n"
    "ds_read_b64_tr_b16 a[198:199], v241 offset:20480\n"
    "v_add_f32 v213, v220, v221\n"
    "ds_read_b64_tr_b16 a[200:201], v236 offset:14848\n"
    "v_exp_f32 v222, v138\n"
    "v_mfma_f32_16x16x32_bf16 a[28:31], a[204:207], v[160:163], a[28:31]\n"
    "ds_read_b64_tr_b16 a[202:203], v236 offset:20992\n"
    "v_mfma_f32_16x16x32_bf16 a[76:79], a[204:207], v[164:167], a[76:79]\n"
    "ds_read_b64_tr_b16 a[204:205], v241 offset:14848\n"
    "ds_read_b64_tr_b16 a[206:207], v241 offset:20992\n"
    "v_exp_f32 v223, v139\n"
    "v_mfma_f32_16x16x32_bf16 a[32:35], a[208:211], v[160:163], a[32:35]\n"
    "v_exp_f32 v224, v132\n"
    "v_mfma_f32_16x16x32_bf16 a[80:83], a[208:211], v[164:167], a[80:83]\n"
    "v_exp_f32 v225, v133\n"
    "v_exp_f32 v226, v134\n"
    "v_exp_f32 v227, v135\n"
    "v_mfma_f32_16x16x32_bf16 a[36:39], a[212:215], v[160:163], a[36:39]\n"
    "v_exp_f32 v228, v140\n"
    "v_mfma_f32_16x16x32_bf16 a[84:87], a[212:215], v[164:167], a[84:87]\n"
    "v_exp_f32 v229, v141\n"
    "v_cvt_pk_bf16_f32 v181, v222, v223\n"
    "v_add_f32 v213, v213, v222\n"
    "v_exp_f32 v230, v142\n"
    "s_waitcnt lgkmcnt(5)\n"
    "v_mfma_f32_16x16x32_bf16 a[40:43], a[216:219], v[160:163], a[40:43]\n"
    "v_exp_f32 v231, v143\n"
    "v_mfma_f32_16x16x32_bf16 a[88:91], a[216:219], v[164:167], a[88:91]\n"
    "v_add_f32 v213, v213, v223\n"
    "v_cvt_pk_bf16_f32 v178, v224, v225\n"
    "v_add_f32 v212, v212, v224\n"
    "v_add_f32 v212, v212, v225\n"
    "v_cvt_pk_bf16_f32 v179, v226, v227\n"
    "v_add_f32 v212, v212, v226\n"
    "v_mfma_f32_16x16x32_bf16 a[44:47], a[220:223], v[160:163], a[44:47]\n"
    "v_add_f32 v212, v212, v227\n"
    "v_cvt_pk_bf16_f32 v182, v228, v229\n"
    "v_mfma_f32_16x16x32_bf16 a[92:95], a[220:223], v[164:167], a[92:95]\n"
    "v_add_f32 v213, v213, v228\n"
    "v_add_f32 v213, v213, v229\n"
    "v_cvt_pk_bf16_f32 v183, v230, v231\n"
    "v_add_f32 v213, v213, v230\n"
    "v_add_f32 v213, v213, v231\n"
    "v_max_f32 v232, v212, v213\n"
    "v_cmp_nge_f32 vcc, s49, v232\n"
    "s_cbranch_vccnz LRARE_o_2_%=\n"
    "LRET_o_2_F1_%=:\n"
    "v_mfma_f32_16x16x32_bf16 a[0:3], a[224:227], v[176:179], a[0:3]\n"
    "v_add_f32 v208, v208, v212\n"
    "v_add_f32 v209, v209, v213\n"
    "v_mfma_f32_16x16x32_bf16 a[4:7], a[228:231], v[176:179], a[4:7]\n"
    "s_mul_i32 s62, s53, 6144\n"
    "v_mov_b32 v245, v208\n"
    "v_mfma_f32_16x16x32_bf16 a[8:11], a[232:235], v[176:179], a[8:11]\n"
    "v_mov_b32 v247, v209\n"
    "s_add_u32 s62, s62, s42\n"
    "v_permlane32_swap_b32 v208, v245\n"
    "v_mfma_f32_16x16x32_bf16 a[12:15], a[236:239], v[176:179], a[12:15]\n"
    "v_permlane32_swap_b32 v209, v247\n"
    "s_add_u32 s62, s62, s56\n"
    "v_mfma_f32_16x16x32_bf16 a[16:19], a[240:243], v[176:179], a[16:19]\n"
    "v_add_f32 v208, v208, v245\n"
    "v_add_f32 v209, v209, v247\n"
    "s_add_u32 s62, s62, 73728\n"
    "v_mfma_f32_16x16x32_bf16 a[20:23], a[244:247], v[176:179], a[20:23]\n"
    "v_mov_b32 v245, v208\n"
    "v_mov_b32 v247, v209\n"
    "v_mfma_f32_16x16x32_bf16 a[24:27], a[248:251], v[176:179], a[24:27]\n"
    "v_lshrrev_b32 v216, 7, v253\n"
    "v_permlane16_swap_b32 v208, v245\n"
    "v_permlane16_swap_b32 v209, v247\n"
    "v_mfma_f32_16x16x32_bf16 a[28:31], a[252:255], v[176:179], a[28:31]\n"
    "v_and_b32 v217, 15, v216\n"
    "v_add_f32 v208, v208, v245\n"
    "v_mfma_f32_16x16x32_bf16 a[32:35], a[192:195], v[176:179], a[32:35]\n"
    "v_add_f32 v209, v209, v247\n"
    "v_mul_u32_u24 v217, 144, v217\n"
    "s_waitcnt lgkmcnt(0)\n"
    "v_mfma_f32_16x16x32_bf16 a[36:39], a[196:199], v[176:179], a[36:39]\n"
    "v_rcp_f32 v244, v208\n"
    "v_rcp_f32 v246, v209\n"
    "v_lshrrev_b32 v218, 4, v216\n"
    "v_mfma_f32_16x16x32_bf16 a[40:43], a[200:203], v[176:179], a[40:43]\n"
    "v_lshl_add_u32 v217, v218, 3, v217\n"
    "v_add_u32 v192, s62, v217\n"
    "v_mfma_f32_16x16x32_bf16 a[44:47], a[204:207], v[176:179], a[44:47]\n"
    "v_add_u32 v193, 2304, v192\n"
    "v_lshrrev_b32 v217, 3, v216\n"
    "v_mul_u32_u24 v217, 144, v217\n"
    "v_and_b32 v218, 7, v216\n"
    "v_lshl_add_u32 v217, v218, 4, v217\n"
    "v_add_u32 v194, s62, v217\n"
    "s_add_u32 s80, s54, 0\n"
    "s_add_u32 s81, s54, 6144\n"
    "v_mfma_f32_16x16x32_bf16 a[48:51], a[224:227], v[180:183], a[48:51]\n"
    "v_accvgpr_read_b32 v96, a0\n"
    "v_accvgpr_read_b32 v97, a1\n"
    "v_accvgpr_read_b32 v98, a2\n"
    "v_accvgpr_read_b32 v99, a3\n"
    "v_accvgpr_read_b32 v100, a4\n"
    "v_mfma_f32_16x16x32_bf16 a[52:55], a[228:231], v[180:183], a[52:55]\n"
    "v_accvgpr_read_b32 v101, a5\n"
    "v_accvgpr_read_b32 v102, a6\n"
    "v_accvgpr_read_b32 v103, a7\n"
    "v_accvgpr_read_b32 v104, a8\n"
    "v_accvgpr_read_b32 v105, a9\n"
    "v_accvgpr_read_b32 v106, a10\n"
    "v_accvgpr_read_b32 v107, a11\n"
    "v_accvgpr_read_b32 v108, a12\n"
    "v_accvgpr_read_b32 v109, a13\n"
    "v_accvgpr_read_b32 v110, a14\n"
    "v_accvgpr_read_b32 v111, a15\n"
    "v_pk_mul_f32 v[96:97], v[96:97], v[244:245] op_sel_hi:[1,0]\n"
    "v_pk_mul_f32 v[98:99], v[98:99], v[244:245] op_sel_hi:[1,0]\n"
    "v_pk_mul_f32 v[100:101], v[100:101], v[244:245] op_sel_hi:[1,0]\n"
    "v_pk_mul_f32 v[102:103], v[102:103], v[244:245] op_sel_hi:[1,0]\n"
    "v_pk_mul_f32 v[104:105], v[104:105], v[244:245] op_sel_hi:[1,0]\n"
    "v_pk_mul_f32 v[106:107], v[106:107], v[244:245] op_sel_hi:[1,0]\n"
    "v_pk_mul_f32 v[108:109], v[108:109], v[244:245] op_sel_hi:[1,0]\n"
    "v_pk_mul_f32 v[110:111], v[110:111], v[244:245] op_sel_hi:[1,0]\n"
    "v_mfma_f32_16x16x32_bf16 a[56:59], a[232:235], v[180:183], a[56:59]\n"
    "v_cvt_pk_bf16_f32 v128, v96, v97\n"
    "v_cvt_pk_bf16_f32 v129, v98, v99\n"
    "v_cvt_pk_bf16_f32 v130, v100, v101\n"
    "v_cvt_pk_bf16_f32 v131, v102, v103\n"
    "v_mfma_f32_16x16x32_bf16 a[60:63], a[236:239], v[180:183], a[60:63]\n"
    "v_cvt_pk_bf16_f32 v132, v104, v105\n"
    "v_cvt_pk_bf16_f32 v133, v106, v107\n"
    "v_cvt_pk_bf16_f32 v134, v108, v109\n"
    "v_cvt_pk_bf16_f32 v135, v110, v111\n"
    "ds_write2_b64 v192, v[128:129], v[130:131] offset0:0 offset1:4\n"
    "ds_write2_b64 v192, v[132:133], v[134:135] offset0:8 offset1:12\n"
    "v_mfma_f32_16x16x32_bf16 a[64:67], a[240:243], v[180:183], a[64:67]\n"
    "v_accvgpr_read_b32 v112, a16\n"
    "v_accvgpr_read_b32 v113, a17\n"
    "v_accvgpr_read_b32 v114, a18\n"
    "v_accvgpr_read_b32 v115, a19\n"
    "v_accvgpr_read_b32 v116, a20\n"
    "v_mfma_f32_16x16x32_bf16 a[68:71], a[244:247], v[180:183], a[68:71]\n"
    "v_accvgpr_read_b32 v117, a21\n"
    "v_accvgpr_read_b32 v118, a22\n"
    "v_accvgpr_read_b32 v119, a23\n"
    "v_accvgpr_read_b32 v120, a24\n"
    "v_accvgpr_read_b32 v121, a25\n"
    "v_accvgpr_read_b32 v122, a26\n"
    "v_accvgpr_read_b32 v123, a27\n"
    "v_accvgpr_read_b32 v124, a28\n"
    "v_accvgpr_read_b32 v125, a29\n"
    "v_accvgpr_read_b32 v126, a30\n"
    "v_accvgpr_read_b32 v127, a31\n"
    "v_pk_mul_f32 v[112:113], v[112:113], v[244:245] op_sel_hi:[1,0]\n"
    "v_pk_mul_f32 v[114:115], v[114:115], v[244:245] op_sel_hi:[1,0]\n"
    "v_pk_mul_f32 v[116:117], v[116:117], v[244:245] op_sel_hi:[1,0]\n"
    "v_pk_mul_f32 v[118:119], v[118:119], v[244:245] op_sel_hi:[1,0]\n"
    "v_pk_mul_f32 v[120:121], v[120:121], v[244:245] op_sel_hi:[1,0]\n"
    "v_pk_mul_f32 v[122:123], v[122:123], v[244:245] op_sel_hi:[1,0]\n"
    "v_pk_mul_f32 v[124:125], v[124:125], v[244:245] op_sel_hi:[1,0]\n"
    "v_pk_mul_f32 v[126:127], v[126:127], v[244:245] op_sel_hi:[1,0]\n"
    "v_mfma_f32_16x16x32_bf16 a[72:75], a[248:251], v[180:183], a[72:75]\n"
    "v_cvt_pk_bf16_f32 v136, v112, v113\n"
    "v_cvt_pk_bf16_f32 v137, v114, v115\n"
    "v_cvt_pk_bf16_f32 v138, v116, v117\n"
    "v_cvt_pk_bf16_f32 v139, v118, v119\n"
    "v_mfma_f32_16x16x32_bf16 a[76:79], a[252:255], v[180:183], a[76:79]\n"
    "v_cvt_pk_bf16_f32 v140, v120, v121\n"
    "v_cvt_pk_bf16_f32 v141, v122, v123\n"
    "v_cvt_pk_bf16_f32 v142, v124, v125\n"
    "v_cvt_pk_bf16_f32 v143, v126, v127\n"
    "ds_read_b128 v[144:147], v194 offset:0\n"
    "ds_read_b128 v[148:151], v194 offset:1152\n"
    "ds_write2_b64 v193, v[136:137], v[138:139] offset0:0 offset1:4\n"
    "ds_write2_b64 v193, v[140:141], v[142:143] offset0:8 offset1:12\n"
    "v_mfma_f32_16x16x32_bf16 a[80:83], a[192:195], v[180:183], a[80:83]\n"
    "v_accvgpr_read_b32 v96, a32\n"
    "v_accvgpr_read_b32 v97, a33\n"
    "v_accvgpr_read_b32 v98, a34\n"
    "v_accvgpr_read_b32 v99, a35\n"
    "v_accvgpr_read_b32 v100, a36\n"
    "v_mfma_f32_16x16x32_bf16 a[84:87], a[196:199], v[180:183], a[84:87]\n"
    "v_accvgpr_read_b32 v101, a37\n"
    "v_accvgpr_read_b32 v102, a38\n"
    "s_waitcnt lgkmcnt(2)\n"
    "buffer_store_dwordx4 v[144:147], v240, s[36:39], s80 offen offset:0 sc0 sc1\n"
    "buffer_store_dwordx4 v[148:151], v240, s[36:39], s80 offen offset:3072 sc0 sc1\n"
    "v_accvgpr_read_b32 v103, a39\n"
    "v_accvgpr_read_b32 v104, a40\n"
    "v_accvgpr_read_b32 v105, a41\n"
    "v_accvgpr_read_b32 v106, a42\n"
    "v_accvgpr_read_b32 v107, a43\n"
    "v_accvgpr_read_b32 v108, a44\n"
    "v_accvgpr_read_b32 v109, a45\n"
    "v_accvgpr_read_b32 v110, a46\n"
    "v_accvgpr_read_b32 v111, a47\n"
    "v_pk_mul_f32 v[96:97], v[96:97], v[244:245] op_sel_hi:[1,0]\n"
    "v_pk_mul_f32 v[98:99], v[98:99], v[244:245] op_sel_hi:[1,0]\n"
    "v_pk_mul_f32 v[100:101], v[100:101], v[244:245] op_sel_hi:[1,0]\n"
    "v_pk_mul_f32 v[102:103], v[102:103], v[244:245] op_sel_hi:[1,0]\n"
    "v_pk_mul_f32 v[104:105], v[104:105], v[244:245] op_sel_hi:[1,0]\n"
    "v_pk_mul_f32 v[106:107], v[106:107], v[244:245] op_sel_hi:[1,0]\n"
    "v_pk_mul_f32 v[108:109], v[108:109], v[244:245] op_sel_hi:[1,0]\n"
    "v_pk_mul_f32 v[110:111], v[110:111], v[244:245] op_sel_hi:[1,0]\n"
    "v_mfma_f32_16x16x32_bf16 a[88:91], a[200:203], v[180:183], a[88:91]\n"
    "v_cvt_pk_bf16_f32 v128, v96, v97\n"
    "v_cvt_pk_bf16_f32 v129, v98, v99\n"
    "v_cvt_pk_bf16_f32 v130, v100, v101\n"
    "v_cvt_pk_bf16_f32 v131, v102, v103\n"
    "v_mfma_f32_16x16x32_bf16 a[92:95], a[204:207], v[180:183], a[92:95]\n"
    "v_cvt_pk_bf16_f32 v132, v104, v105\n"
    "v_cvt_pk_bf16_f32 v133, v106, v107\n"
    "v_cvt_pk_bf16_f32 v134, v108, v109\n"
    "v_cvt_pk_bf16_f32 v135, v110, v111\n"
    "ds_read_b128 v[152:155], v194 offset:2304\n"
    "ds_read_b128 v[156:159], v194 offset:3456\n"
    "ds_write2_b64 v192, v[128:129], v[130:131] offset0:0 offset1:4\n"
    "ds_write2_b64 v192, v[132:133], v[134:135] offset0:8 offset1:12\n"
    "v_accvgpr_read_b32 v112, a48\n"
    "v_accvgpr_read_b32 v113, a49\n"
    "v_accvgpr_read_b32 v114, a50\n"
    "v_accvgpr_read_b32 v115, a51\n"
    "v_accvgpr_read_b32 v116, a52\n"
    "v_accvgpr_read_b32 v117, a53\n"
    "v_accvgpr_read_b32 v118, a54\n"
    "v_accvgpr_read_b32 v119, a55\n"
    "v_accvgpr_read_b32 v120, a56\n"
    "s_waitcnt lgkmcnt(2)\n"
    "buffer_store_dwordx4 v[152:155], v240, s[36:39], s80 offen offset:128 sc0 sc1\n"
    "buffer_store_dwordx4 v[156:159], v240, s[36:39], s80 offen offset:3200 sc0 sc1\n"
    "v_accvgpr_read_b32 v121, a57\n"
    "v_accvgpr_read_b32 v122, a58\n"
    "v_accvgpr_read_b32 v123, a59\n"
    "v_accvgpr_read_b32 v124, a60\n"
    "v_accvgpr_read_b32 v125, a61\n"
    "v_accvgpr_read_b32 v126, a62\n"
    "v_accvgpr_read_b32 v127, a63\n"
    "v_pk_mul_f32 v[112:113], v[112:113], v[246:247] op_sel_hi:[1,0]\n"
    "v_pk_mul_f32 v[114:115], v[114:115], v[246:247] op_sel_hi:[1,0]\n"
    "v_pk_mul_f32 v[116:117], v[116:117], v[246:247] op_sel_hi:[1,0]\n"
    "v_pk_mul_f32 v[118:119], v[118:119], v[246:247] op_sel_hi:[1,0]\n"
    "v_pk_mul_f32 v[120:121], v[120:121], v[246:247] op_sel_hi:[1,0]\n"
    "v_pk_mul_f32 v[122:123], v[122:123], v[246:247] op_sel_hi:[1,0]\n"
    "v_pk_mul_f32 v[124:125], v[124:125], v[246:247] op_sel_hi:[1,0]\n"
    "v_pk_mul_f32 v[126:127], v[126:127], v[246:247] op_sel_hi:[1,0]\n"
    "v_cvt_pk_bf16_f32 v136, v112, v113\n"
    "v_cvt_pk_bf16_f32 v137, v114, v115\n"
    "v_cvt_pk_bf16_f32 v138, v116, v117\n"
    "v_cvt_pk_bf16_f32 v139, v118, v119\n"
    "v_cvt_pk_bf16_f32 v140, v120, v121\n"
    "v_cvt_pk_bf16_f32 v141, v122, v123\n"
    "v_cvt_pk_bf16_f32 v142, v124, v125\n"
    "v_cvt_pk_bf16_f32 v143, v126, v127\n"
    "ds_read_b128 v[216:219], v194 offset:0\n"
    "ds_read_b128 v[220:223], v194 offset:1152\n"
    "ds_write2_b64 v193, v[136:137], v[138:139] offset0:0 offset1:4\n"
    "ds_write2_b64 v193, v[140:141], v[142:143] offset0:8 offset1:12\n"
    "v_accvgpr_read_b32 v96, a64\n"
    "v_accvgpr_read_b32 v97, a65\n"
    "v_accvgpr_read_b32 v98, a66\n"
    "v_accvgpr_read_b32 v99, a67\n"
    "v_accvgpr_read_b32 v100, a68\n"
    "v_accvgpr_read_b32 v101, a69\n"
    "v_accvgpr_read_b32 v102, a70\n"
    "v_accvgpr_read_b32 v103, a71\n"
    "v_accvgpr_read_b32 v104, a72\n"
    "s_waitcnt lgkmcnt(2)\n"
    "buffer_store_dwordx4 v[216:219], v240, s[36:39], s80 offen offset:256 sc0 sc1\n"
    "buffer_store_dwordx4 v[220:223], v240, s[36:39], s80 offen offset:3328 sc0 sc1\n"
    "v_accvgpr_read_b32 v105, a73\n"
    "v_accvgpr_read_b32 v106, a74\n"
    "v_accvgpr_read_b32 v107, a75\n"
    "v_accvgpr_read_b32 v108, a76\n"
    "v_accvgpr_read_b32 v109, a77\n"
    "v_accvgpr_read_b32 v110, a78\n"
    "v_accvgpr_read_b32 v111, a79\n"
    "v_pk_mul_f32 v[96:97], v[96:97], v[246:247] op_sel_hi:[1,0]\n"
    "v_pk_mul_f32 v[98:99], v[98:99], v[246:247] op_sel_hi:[1,0]\n"
    "v_pk_mul_f32 v[100:101], v[100:101], v[246:247] op_sel_hi:[1,0]\n"
    "v_pk_mul_f32 v[102:103], v[102:103], v[246:247] op_sel_hi:[1,0]\n"
    "v_pk_mul_f32 v[104:105], v[104:105], v[246:247] op_sel_hi:[1,0]\n"
    "v_pk_mul_f32 v[106:107], v[106:107], v[246:247] op_sel_hi:[1,0]\n"
    "v_pk_mul_f32 v[108:109], v[108:109], v[246:247] op_sel_hi:[1,0]\n"
    "v_pk_mul_f32 v[110:111], v[110:111], v[246:247] op_sel_hi:[1,0]\n"
    "v_cvt_pk_bf16_f32 v128, v96, v97\n"
    "v_cvt_pk_bf16_f32 v129, v98, v99\n"
    "v_cvt_pk_bf16_f32 v130, v100, v101\n"
    "v_cvt_pk_bf16_f32 v131, v102, v103\n"
    "v_cvt_pk_bf16_f32 v132, v104, v105\n"
    "v_cvt_pk_bf16_f32 v133, v106, v107\n"
    "v_cvt_pk_bf16_f32 v134, v108, v109\n"
    "v_cvt_pk_bf16_f32 v135, v110, v111\n"
    "ds_read_b128 v[144:147], v194 offset:2304\n"
    "ds_read_b128 v[148:151], v194 offset:3456\n"
    "ds_write2_b64 v192, v[128:129], v[130:131] offset0:0 offset1:4\n"
    "ds_write2_b64 v192, v[132:133], v[134:135] offset0:8 offset1:12\n"
    "v_accvgpr_read_b32 v112, a80\n"
    "v_accvgpr_read_b32 v113, a81\n"
    "v_accvgpr_read_b32 v114, a82\n"
    "v_accvgpr_read_b32 v115, a83\n"
    "v_accvgpr_read_b32 v116, a84\n"
    "v_accvgpr_read_b32 v117, a85\n"
    "v_accvgpr_read_b32 v118, a86\n"
    "v_accvgpr_read_b32 v119, a87\n"
    "v_accvgpr_read_b32 v120, a88\n"
    "s_waitcnt lgkmcnt(2)\n"
    "buffer_store_dwordx4 v[144:147], v240, s[36:39], s81 offen offset:0 sc0 sc1\n"
    "buffer_store_dwordx4 v[148:151], v240, s[36:39], s81 offen offset:3072 sc0 sc1\n"
    "v_accvgpr_read_b32 v121, a89\n"
    "v_accvgpr_read_b32 v122, a90\n"
    "v_accvgpr_read_b32 v123, a91\n"
    "v_accvgpr_read_b32 v124, a92\n"
    "v_accvgpr_read_b32 v125, a93\n"
    "v_accvgpr_read_b32 v126, a94\n"
    "v_accvgpr_read_b32 v127, a95\n"
    "v_pk_mul_f32 v[112:113], v[112:113], v[246:247] op_sel_hi:[1,0]\n"
    "v_pk_mul_f32 v[114:115], v[114:115], v[246:247] op_sel_hi:[1,0]\n"
    "v_pk_mul_f32 v[116:117], v[116:117], v[246:247] op_sel_hi:[1,0]\n"
    "v_pk_mul_f32 v[118:119], v[118:119], v[246:247] op_sel_hi:[1,0]\n"
    "v_pk_mul_f32 v[120:121], v[120:121], v[246:247] op_sel_hi:[1,0]\n"
    "v_pk_mul_f32 v[122:123], v[122:123], v[246:247] op_sel_hi:[1,0]\n"
    "v_pk_mul_f32 v[124:125], v[124:125], v[246:247] op_sel_hi:[1,0]\n"
    "v_pk_mul_f32 v[126:127], v[126:127], v[246:247] op_sel_hi:[1,0]\n"
    "v_cvt_pk_bf16_f32 v136, v112, v113\n"
    "v_cvt_pk_bf16_f32 v137, v114, v115\n"
    "v_cvt_pk_bf16_f32 v138, v116, v117\n"
    "v_cvt_pk_bf16_f32 v139, v118, v119\n"
    "v_cvt_pk_bf16_f32 v140, v120, v121\n"
    "v_cvt_pk_bf16_f32 v141, v122, v123\n"
    "v_cvt_pk_bf16_f32 v142, v124, v125\n"
    "v_cvt_pk_bf16_f32 v143, v126, v127\n"
    "ds_read_b128 v[152:155], v194 offset:0\n"
    "ds_read_b128 v[156:159], v194 offset:1152\n"
    "ds_write2_b64 v193, v[136:137], v[138:139] offset0:0 offset1:4\n"
    "ds_write2_b64 v193, v[140:141], v[142:143] offset0:8 offset1:12\n"
    "s_waitcnt lgkmcnt(2)\n"
    "buffer_store_dwordx4 v[152:155], v240, s[36:39], s81 offen offset:128 sc0 sc1\n"
    "buffer_store_dwordx4 v[156:159], v240, s[36:39], s81 offen offset:3200 sc0 sc1\n"
    "ds_read_b128 v[216:219], v194 offset:2304\n"
    "ds_read_b128 v[220:223], v194 offset:3456\n"
    "s_waitcnt lgkmcnt(0)\n"
    "buffer_store_dwordx4 v[216:219], v240, s[36:39], s81 offen offset:256 sc0 sc1\n"
    "buffer_store_dwordx4 v[220:223], v240, s[36:39], s81 offen offset:3328 sc0 sc1\n"
    "s_branch LNEXT_%=\n"
    "LRARE_e_4_%=:\n"
    "s_nop 7\n"
    "s_nop 7\n"
    "v_max3_f32 v244, v96, v97, v98\n"
    "v_max3_f32 v246, v104, v105, v106\n"
    "v_max3_f32 v248, v112, v113, v114\n"
    "v_max3_f32 v250, v120, v121, v122\n"
    "v_max3_f32 v244, v244, v99, v100\n"
    "v_max3_f32 v246, v246, v107, v108\n"
    "v_max3_f32 v248, v248, v115, v116\n"
    "v_max3_f32 v250, v250, v123, v124\n"
    "v_max3_f32 v244, v244, v101, v102\n"
    "v_max3_f32 v246, v246, v109, v110\n"
    "v_max3_f32 v248, v248, v117, v118\n"
    "v_max3_f32 v250, v250, v125, v126\n"
    "v_max_f32 v244, v244, v103\n"
    "v_max_f32 v246, v246, v111\n"
    "v_max_f32 v248, v248, v119\n"
    "v_max_f32 v250, v250, v127\n"
    "v_mov_b32 v245, v244\n"
    "v_mov_b32 v247, v246\n"
    "v_mov_b32 v249, v248\n"
    "v_mov_b32 v251, v250\n"
    "v_permlane32_swap_b32 v244, v245\n"
    "v_permlane32_swap_b32 v246, v247\n"
    "v_permlane32_swap_b32 v248, v249\n"
    "v_permlane32_swap_b32 v250, v251\n"
    "v_max_f32 v244, v244, v245\n"
    "v_max_f32 v246, v246, v247\n"
    "v_max_f32 v248, v248, v249\n"
    "v_max_f32 v250, v250, v251\n"
    "v_mov_b32 v245, v244\n"
    "v_mov_b32 v247, v246\n"
    "v_mov_b32 v249, v248\n"
    "v_mov_b32 v251, v250\n"
    "v_permlane16_swap_b32 v244, v245\n"
    "v_permlane16_swap_b32 v246, v247\n"
    "v_permlane16_swap_b32 v248, v249\n"
    "v_permlane16_swap_b32 v250, v251\n"
    "v_max_f32 v244, v244, v245\n"
    "v_max_f32 v246, v246, v247\n"
    "v_max_f32 v248, v248, v249\n"
    "v_max_f32 v250, v250, v251\n"
    "v_max_f32 v244, 0, v244\n"
    "v_max_f32 v246, 0, v246\n"
    "v_max_f32 v248, 0, v248\n"
    "v_max_f32 v250, 0, v250\n"
    "v_exp_f32 v245, -v244\n"
    "v_exp_f32 v247, -v246\n"
    "v_exp_f32 v249, -v248\n"
    "v_exp_f32 v251, -v250\n"
    "v_sub_f32 v96, v96, v244\n"
    "v_sub_f32 v104, v104, v246\n"
    "v_sub_f32 v112, v112, v248\n"
    "v_sub_f32 v120, v120, v250\n"
    "v_sub_f32 v97, v97, v244\n"
    "v_sub_f32 v105, v105, v246\n"
    "v_sub_f32 v113, v113, v248\n"
    "v_sub_f32 v121, v121, v250\n"
    "v_sub_f32 v98, v98, v244\n"
    "v_sub_f32 v106, v106, v246\n"
    "v_sub_f32 v114, v114, v248\n"
    "v_sub_f32 v122, v122, v250\n"
    "v_sub_f32 v99, v99, v244\n"
    "v_sub_f32 v107, v107, v246\n"
    "v_sub_f32 v115, v115, v248\n"
    "v_sub_f32 v123, v123, v250\n"
    "v_sub_f32 v100, v100, v244\n"
    "v_sub_f32 v108, v108, v246\n"
    "v_sub_f32 v116, v116, v248\n"
    "v_sub_f32 v124, v124, v250\n"
    "v_sub_f32 v101, v101, v244\n"
    "v_sub_f32 v109, v109, v246\n"
    "v_sub_f32 v117, v117, v248\n"
    "v_sub_f32 v125, v125, v250\n"
    "v_sub_f32 v102, v102, v244\n"
    "v_sub_f32 v110, v110, v246\n"
    "v_sub_f32 v118, v118, v248\n"
    "v_sub_f32 v126, v126, v250\n"
    "v_sub_f32 v103, v103, v244\n"
    "v_sub_f32 v111, v111, v246\n"
    "v_sub_f32 v119, v119, v248\n"
    "v_sub_f32 v127, v127, v250\n"
    "v_sub_f32 v128, v128, v244\n"
    "v_sub_f32 v136, v136, v246\n"
    "v_sub_f32 v144, v144, v248\n"
    "v_sub_f32 v152, v152, v250\n"
    "v_sub_f32 v129, v129, v244\n"
    "v_sub_f32 v137, v137, v246\n"
    "v_sub_f32 v145, v145, v248\n"
    "v_sub_f32 v153, v153, v250\n"
    "v_sub_f32 v130, v130, v244\n"
    "v_sub_f32 v138, v138, v246\n"
    "v_sub_f32 v146, v146, v248\n"
    "v_sub_f32 v154, v154, v250\n"
    "v_sub_f32 v131, v131, v244\n"
    "v_sub_f32 v139, v139, v246\n"
    "v_sub_f32 v147, v147, v248\n"
    "v_sub_f32 v155, v155, v250\n"
    "v_sub_f32 v132, v132, v244\n"
    "v_sub_f32 v140, v140, v246\n"
    "v_sub_f32 v148, v148, v248\n"
    "v_sub_f32 v156, v156, v250\n"
    "v_sub_f32 v133, v133, v244\n"
    "v_sub_f32 v141, v141, v246\n"
    "v_sub_f32 v149, v149, v248\n"
    "v_sub_f32 v157, v157, v250\n"
    "v_sub_f32 v134, v134, v244\n"
    "v_sub_f32 v142, v142, v246\n"
    "v_sub_f32 v150, v150, v248\n"
    "v_sub_f32 v158, v158, v250\n"
    "v_sub_f32 v135, v135, v244\n"
    "v_sub_f32 v143, v143, v246\n"
    "v_sub_f32 v151, v151, v248\n"
    "v_sub_f32 v159, v159, v250\n"
    "v_sub_f32 v192, v192, v244\n"
    "v_sub_f32 v196, v196, v246\n"
    "v_sub_f32 v200, v200, v248\n"
    "v_sub_f32 v204, v204, v250\n"
    "v_sub_f32 v193, v193, v244\n"
    "v_sub_f32 v197, v197, v246\n"
    "v_sub_f32 v201, v201, v248\n"
    "v_sub_f32 v205, v205, v250\n"
    "v_sub_f32 v194, v194, v244\n"
    "v_sub_f32 v198, v198, v246\n"
    "v_sub_f32 v202, v202, v248\n"
    "v_sub_f32 v206, v206, v250\n"
    "v_sub_f32 v195, v195, v244\n"
    "v_sub_f32 v199, v199, v246\n"
    "v_sub_f32 v203, v203, v248\n"
    "v_sub_f32 v207, v207, v250\n"
    "v_mul_f32 v208, v208, v245\n"
    "v_mul_f32 v209, v209, v247\n"
    "v_mul_f32 v210, v210, v249\n"
    "v_mul_f32 v211, v211, v251\n"
    "v_accvgpr_read_b32 v216, a0\n"
    "v_accvgpr_read_b32 v220, a48\n"
    "v_accvgpr_read_b32 v224, a96\n"
    "v_accvgpr_read_b32 v228, a144\n"
    "v_accvgpr_read_b32 v217, a1\n"
    "v_accvgpr_read_b32 v221, a49\n"
    "v_accvgpr_read_b32 v225, a97\n"
    "v_accvgpr_read_b32 v229, a145\n"
    "v_accvgpr_read_b32 v218, a2\n"
    "v_accvgpr_read_b32 v222, a50\n"
    "v_accvgpr_read_b32 v226, a98\n"
    "v_accvgpr_read_b32 v230, a146\n"
    "v_accvgpr_read_b32 v219, a3\n"
    "v_accvgpr_read_b32 v223, a51\n"
    "v_accvgpr_read_b32 v227, a99\n"
    "v_accvgpr_read_b32 v231, a147\n"
    "v_mul_f32 v216, v216, v245\n"
    "v_mul_f32 v220, v220, v247\n"
    "v_mul_f32 v224, v224, v249\n"
    "v_mul_f32 v228, v228, v251\n"
    "v_mul_f32 v217, v217, v245\n"
    "v_mul_f32 v221, v221, v247\n"
    "v_mul_f32 v225, v225, v249\n"
    "v_mul_f32 v229, v229, v251\n"
    "v_mul_f32 v218, v218, v245\n"
    "v_mul_f32 v222, v222, v247\n"
    "v_mul_f32 v226, v226, v249\n"
    "v_mul_f32 v230, v230, v251\n"
    "v_mul_f32 v219, v219, v245\n"
    "v_mul_f32 v223, v223, v247\n"
    "v_mul_f32 v227, v227, v249\n"
    "v_mul_f32 v231, v231, v251\n"
    "v_accvgpr_write_b32 a0, v216\n"
    "v_accvgpr_write_b32 a48, v220\n"
    "v_accvgpr_write_b32 a96, v224\n"
    "v_accvgpr_write_b32 a144, v228\n"
    "v_accvgpr_write_b32 a1, v217\n"
    "v_accvgpr_write_b32 a49, v221\n"
    "v_accvgpr_write_b32 a97, v225\n"
    "v_accvgpr_write_b32 a145, v229\n"
    "v_accvgpr_write_b32 a2, v218\n"
    "v_accvgpr_write_b32 a50, v222\n"
    "v_accvgpr_write_b32 a98, v226\n"
    "v_accvgpr_write_b32 a146, v230\n"
    "v_accvgpr_write_b32 a3, v219\n"
    "v_accvgpr_write_b32 a51, v223\n"
    "v_accvgpr_write_b32 a99, v227\n"
    "v_accvgpr_write_b32 a147, v231\n"
    "v_accvgpr_read_b32 v216, a4\n"
    "v_accvgpr_read_b32 v220, a52\n"
    "v_accvgpr_read_b32 v224, a100\n"
    "v_accvgpr_read_b32 v228, a148\n"
    "v_accvgpr_read_b32 v217, a5\n"
    "v_accvgpr_read_b32 v221, a53\n"
    "v_accvgpr_read_b32 v225, a101\n"
    "v_accvgpr_read_b32 v229, a149\n"
    "v_accvgpr_read_b32 v218, a6\n"
    "v_accvgpr_read_b32 v222, a54\n"
    "v_accvgpr_read_b32 v226, a102\n"
    "v_accvgpr_read_b32 v230, a150\n"
    "v_accvgpr_read_b32 v219, a7\n"
    "v_accvgpr_read_b32 v223, a55\n"
    "v_accvgpr_read_b32 v227, a103\n"
    "v_accvgpr_read_b32 v231, a151\n"
    "v_mul_f32 v216, v216, v245\n"
    "v_mul_f32 v220, v220, v247\n"
    "v_mul_f32 v224, v224, v249\n"
    "v_mul_f32 v228, v228, v251\n"
    "v_mul_f32 v217, v217, v245\n"
    "v_mul_f32 v221, v221, v247\n"
    "v_mul_f32 v225, v225, v249\n"
    "v_mul_f32 v229, v229, v251\n"
    "v_mul_f32 v218, v218, v245\n"
    "v_mul_f32 v222, v222, v247\n"
    "v_mul_f32 v226, v226, v249\n"
    "v_mul_f32 v230, v230, v251\n"
    "v_mul_f32 v219, v219, v245\n"
    "v_mul_f32 v223, v223, v247\n"
    "v_mul_f32 v227, v227, v249\n"
    "v_mul_f32 v231, v231, v251\n"
    "v_accvgpr_write_b32 a4, v216\n"
    "v_accvgpr_write_b32 a52, v220\n"
    "v_accvgpr_write_b32 a100, v224\n"
    "v_accvgpr_write_b32 a148, v228\n"
    "v_accvgpr_write_b32 a5, v217\n"
    "v_accvgpr_write_b32 a53, v221\n"
    "v_accvgpr_write_b32 a101, v225\n"
    "v_accvgpr_write_b32 a149, v229\n"
    "v_accvgpr_write_b32 a6, v218\n"
    "v_accvgpr_write_b32 a54, v222\n"
    "v_accvgpr_write_b32 a102, v226\n"
    "v_accvgpr_write_b32 a150, v230\n"
    "v_accvgpr_write_b32 a7, v219\n"
    "v_accvgpr_write_b32 a55, v223\n"
    "v_accvgpr_write_b32 a103, v227\n"
    "v_accvgpr_write_b32 a151, v231\n"
    "v_accvgpr_read_b32 v216, a8\n"
    "v_accvgpr_read_b32 v220, a56\n"
    "v_accvgpr_read_b32 v224, a104\n"
    "v_accvgpr_read_b32 v228, a152\n"
    "v_accvgpr_read_b32 v217, a9\n"
    "v_accvgpr_read_b32 v221, a57\n"
    "v_accvgpr_read_b32 v225, a105\n"
    "v_accvgpr_read_b32 v229, a153\n"
    "v_accvgpr_read_b32 v218, a10\n"
    "v_accvgpr_read_b32 v222, a58\n"
    "v_accvgpr_read_b32 v226, a106\n"
    "v_accvgpr_read_b32 v230, a154\n"
    "v_accvgpr_read_b32 v219, a11\n"
    "v_accvgpr_read_b32 v223, a59\n"
    "v_accvgpr_read_b32 v227, a107\n"
    "v_accvgpr_read_b32 v231, a155\n"
    "v_mul_f32 v216, v216, v245\n"
    "v_mul_f32 v220, v220, v247\n"
    "v_mul_f32 v224, v224, v249\n"
    "v_mul_f32 v228, v228, v251\n"
    "v_mul_f32 v217, v217, v245\n"
    "v_mul_f32 v221, v221, v247\n"
    "v_mul_f32 v225, v225, v249\n"
    "v_mul_f32 v229, v229, v251\n"
    "v_mul_f32 v218, v218, v245\n"
    "v_mul_f32 v222, v222, v247\n"
    "v_mul_f32 v226, v226, v249\n"
    "v_mul_f32 v230, v230, v251\n"
    "v_mul_f32 v219, v219, v245\n"
    "v_mul_f32 v223, v223, v247\n"
    "v_mul_f32 v227, v227, v249\n"
    "v_mul_f32 v231, v231, v251\n"
    "v_accvgpr_write_b32 a8, v216\n"
    "v_accvgpr_write_b32 a56, v220\n"
    "v_accvgpr_write_b32 a104, v224\n"
    "v_accvgpr_write_b32 a152, v228\n"
    "v_accvgpr_write_b32 a9, v217\n"
    "v_accvgpr_write_b32 a57, v221\n"
    "v_accvgpr_write_b32 a105, v225\n"
    "v_accvgpr_write_b32 a153, v229\n"
    "v_accvgpr_write_b32 a10, v218\n"
    "v_accvgpr_write_b32 a58, v222\n"
    "v_accvgpr_write_b32 a106, v226\n"
    "v_accvgpr_write_b32 a154, v230\n"
    "v_accvgpr_write_b32 a11, v219\n"
    "v_accvgpr_write_b32 a59, v223\n"
    "v_accvgpr_write_b32 a107, v227\n"
    "v_accvgpr_write_b32 a155, v231\n"
    "v_accvgpr_read_b32 v216, a12\n"
    "v_accvgpr_read_b32 v220, a60\n"
    "v_accvgpr_read_b32 v224, a108\n"
    "v_accvgpr_read_b32 v228, a156\n"
    "v_accvgpr_read_b32 v217, a13\n"
    "v_accvgpr_read_b32 v221, a61\n"
    "v_accvgpr_read_b32 v225, a109\n"
    "v_accvgpr_read_b32 v229, a157\n"
    "v_accvgpr_read_b32 v218, a14\n"
    "v_accvgpr_read_b32 v222, a62\n"
    "v_accvgpr_read_b32 v226, a110\n"
    "v_accvgpr_read_b32 v230, a158\n"
    "v_accvgpr_read_b32 v219, a15\n"
    "v_accvgpr_read_b32 v223, a63\n"
    "v_accvgpr_read_b32 v227, a111\n"
    "v_accvgpr_read_b32 v231, a159\n"
    "v_mul_f32 v216, v216, v245\n"
    "v_mul_f32 v220, v220, v247\n"
    "v_mul_f32 v224, v224, v249\n"
    "v_mul_f32 v228, v228, v251\n"
    "v_mul_f32 v217, v217, v245\n"
    "v_mul_f32 v221, v221, v247\n"
    "v_mul_f32 v225, v225, v249\n"
    "v_mul_f32 v229, v229, v251\n"
    "v_mul_f32 v218, v218, v245\n"
    "v_mul_f32 v222, v222, v247\n"
    "v_mul_f32 v226, v226, v249\n"
    "v_mul_f32 v230, v230, v251\n"
    "v_mul_f32 v219, v219, v245\n"
    "v_mul_f32 v223, v223, v247\n"
    "v_mul_f32 v227, v227, v249\n"
    "v_mul_f32 v231, v231, v251\n"
    "v_accvgpr_write_b32 a12, v216\n"
    "v_accvgpr_write_b32 a60, v220\n"
    "v_accvgpr_write_b32 a108, v224\n"
    "v_accvgpr_write_b32 a156, v228\n"
    "v_accvgpr_write_b32 a13, v217\n"
    "v_accvgpr_write_b32 a61, v221\n"
    "v_accvgpr_write_b32 a109, v225\n"
    "v_accvgpr_write_b32 a157, v229\n"
    "v_accvgpr_write_b32 a14, v218\n"
    "v_accvgpr_write_b32 a62, v222\n"
    "v_accvgpr_write_b32 a110, v226\n"
    "v_accvgpr_write_b32 a158, v230\n"
    "v_accvgpr_write_b32 a15, v219\n"
    "v_accvgpr_write_b32 a63, v223\n"
    "v_accvgpr_write_b32 a111, v227\n"
    "v_accvgpr_write_b32 a159, v231\n"
    "v_accvgpr_read_b32 v216, a16\n"
    "v_accvgpr_read_b32 v220, a64\n"
    "v_accvgpr_read_b32 v224, a112\n"
    "v_accvgpr_read_b32 v228, a160\n"
    "v_accvgpr_read_b32 v217, a17\n"
    "v_accvgpr_read_b32 v221, a65\n"
    "v_accvgpr_read_b32 v225, a113\n"
    "v_accvgpr_read_b32 v229, a161\n"
    "v_accvgpr_read_b32 v218, a18\n"
    "v_accvgpr_read_b32 v222, a66\n"
    "v_accvgpr_read_b32 v226, a114\n"
    "v_accvgpr_read_b32 v230, a162\n"
    "v_accvgpr_read_b32 v219, a19\n"
    "v_accvgpr_read_b32 v223, a67\n"
    "v_accvgpr_read_b32 v227, a115\n"
    "v_accvgpr_read_b32 v231, a163\n"
    "v_mul_f32 v216, v216, v245\n"
    "v_mul_f32 v220, v220, v247\n"
    "v_mul_f32 v224, v224, v249\n"
    "v_mul_f32 v228, v228, v251\n"
    "v_mul_f32 v217, v217, v245\n"
    "v_mul_f32 v221, v221, v247\n"
    "v_mul_f32 v225, v225, v249\n"
    "v_mul_f32 v229, v229, v251\n"
    "v_mul_f32 v218, v218, v245\n"
    "v_mul_f32 v222, v222, v247\n"
    "v_mul_f32 v226, v226, v249\n"
    "v_mul_f32 v230, v230, v251\n"
    "v_mul_f32 v219, v219, v245\n"
    "v_mul_f32 v223, v223, v247\n"
    "v_mul_f32 v227, v227, v249\n"
    "v_mul_f32 v231, v231, v251\n"
    "v_accvgpr_write_b32 a16, v216\n"
    "v_accvgpr_write_b32 a64, v220\n"
    "v_accvgpr_write_b32 a112, v224\n"
    "v_accvgpr_write_b32 a160, v228\n"
    "v_accvgpr_write_b32 a17, v217\n"
    "v_accvgpr_write_b32 a65, v221\n"
    "v_accvgpr_write_b32 a113, v225\n"
    "v_accvgpr_write_b32 a161, v229\n"
    "v_accvgpr_write_b32 a18, v218\n"
    "v_accvgpr_write_b32 a66, v222\n"
    "v_accvgpr_write_b32 a114, v226\n"
    "v_accvgpr_write_b32 a162, v230\n"
    "v_accvgpr_write_b32 a19, v219\n"
    "v_accvgpr_write_b32 a67, v223\n"
    "v_accvgpr_write_b32 a115, v227\n"
    "v_accvgpr_write_b32 a163, v231\n"
    "v_accvgpr_read_b32 v216, a20\n"
    "v_accvgpr_read_b32 v220, a68\n"
    "v_accvgpr_read_b32 v224, a116\n"
    "v_accvgpr_read_b32 v228, a164\n"
    "v_accvgpr_read_b32 v217, a21\n"
    "v_accvgpr_read_b32 v221, a69\n"
    "v_accvgpr_read_b32 v225, a117\n"
    "v_accvgpr_read_b32 v229, a165\n"
    "v_accvgpr_read_b32 v218, a22\n"
    "v_accvgpr_read_b32 v222, a70\n"
    "v_accvgpr_read_b32 v226, a118\n"
    "v_accvgpr_read_b32 v230, a166\n"
    "v_accvgpr_read_b32 v219, a23\n"
    "v_accvgpr_read_b32 v223, a71\n"
    "v_accvgpr_read_b32 v227, a119\n"
    "v_accvgpr_read_b32 v231, a167\n"
    "v_mul_f32 v216, v216, v245\n"
    "v_mul_f32 v220, v220, v247\n"
    "v_mul_f32 v224, v224, v249\n"
    "v_mul_f32 v228, v228, v251\n"
    "v_mul_f32 v217, v217, v245\n"
    "v_mul_f32 v221, v221, v247\n"
    "v_mul_f32 v225, v225, v249\n"
    "v_mul_f32 v229, v229, v251\n"
    "v_mul_f32 v218, v218, v245\n"
    "v_mul_f32 v222, v222, v247\n"
    "v_mul_f32 v226, v226, v249\n"
    "v_mul_f32 v230, v230, v251\n"
    "v_mul_f32 v219, v219, v245\n"
    "v_mul_f32 v223, v223, v247\n"
    "v_mul_f32 v227, v227, v249\n"
    "v_mul_f32 v231, v231, v251\n"
    "v_accvgpr_write_b32 a20, v216\n"
    "v_accvgpr_write_b32 a68, v220\n"
    "v_accvgpr_write_b32 a116, v224\n"
    "v_accvgpr_write_b32 a164, v228\n"
    "v_accvgpr_write_b32 a21, v217\n"
    "v_accvgpr_write_b32 a69, v221\n"
    "v_accvgpr_write_b32 a117, v225\n"
    "v_accvgpr_write_b32 a165, v229\n"
    "v_accvgpr_write_b32 a22, v218\n"
    "v_accvgpr_write_b32 a70, v222\n"
    "v_accvgpr_write_b32 a118, v226\n"
    "v_accvgpr_write_b32 a166, v230\n"
    "v_accvgpr_write_b32 a23, v219\n"
    "v_accvgpr_write_b32 a71, v223\n"
    "v_accvgpr_write_b32 a119, v227\n"
    "v_accvgpr_write_b32 a167, v231\n"
    "v_accvgpr_read_b32 v216, a24\n"
    "v_accvgpr_read_b32 v220, a72\n"
    "v_accvgpr_read_b32 v224, a120\n"
    "v_accvgpr_read_b32 v228, a168\n"
    "v_accvgpr_read_b32 v217, a25\n"
    "v_accvgpr_read_b32 v221, a73\n"
    "v_accvgpr_read_b32 v225, a121\n"
    "v_accvgpr_read_b32 v229, a169\n"
    "v_accvgpr_read_b32 v218, a26\n"
    "v_accvgpr_read_b32 v222, a74\n"
    "v_accvgpr_read_b32 v226, a122\n"
    "v_accvgpr_read_b32 v230, a170\n"
    "v_accvgpr_read_b32 v219, a27\n"
    "v_accvgpr_read_b32 v223, a75\n"
    "v_accvgpr_read_b32 v227, a123\n"
    "v_accvgpr_read_b32 v231, a171\n"
    "v_mul_f32 v216, v216, v245\n"
    "v_mul_f32 v220, v220, v247\n"
    "v_mul_f32 v224, v224, v249\n"
    "v_mul_f32 v228, v228, v251\n"
    "v_mul_f32 v217, v217, v245\n"
    "v_mul_f32 v221, v221, v247\n"
    "v_mul_f32 v225, v225, v249\n"
    "v_mul_f32 v229, v229, v251\n"
    "v_mul_f32 v218, v218, v245\n"
    "v_mul_f32 v222, v222, v247\n"
    "v_mul_f32 v226, v226, v249\n"
    "v_mul_f32 v230, v230, v251\n"
    "v_mul_f32 v219, v219, v245\n"
    "v_mul_f32 v223, v223, v247\n"
    "v_mul_f32 v227, v227, v249\n"
    "v_mul_f32 v231, v231, v251\n"
    "v_accvgpr_write_b32 a24, v216\n"
    "v_accvgpr_write_b32 a72, v220\n"
    "v_accvgpr_write_b32 a120, v224\n"
    "v_accvgpr_write_b32 a168, v228\n"
    "v_accvgpr_write_b32 a25, v217\n"
    "v_accvgpr_write_b32 a73, v221\n"
    "v_accvgpr_write_b32 a121, v225\n"
    "v_accvgpr_write_b32 a169, v229\n"
    "v_accvgpr_write_b32 a26, v218\n"
    "v_accvgpr_write_b32 a74, v222\n"
    "v_accvgpr_write_b32 a122, v226\n"
    "v_accvgpr_write_b32 a170, v230\n"
    "v_accvgpr_write_b32 a27, v219\n"
    "v_accvgpr_write_b32 a75, v223\n"
    "v_accvgpr_write_b32 a123, v227\n"
    "v_accvgpr_write_b32 a171, v231\n"
    "v_accvgpr_read_b32 v216, a28\n"
    "v_accvgpr_read_b32 v220, a76\n"
    "v_accvgpr_read_b32 v224, a124\n"
    "v_accvgpr_read_b32 v228, a172\n"
    "v_accvgpr_read_b32 v217, a29\n"
    "v_accvgpr_read_b32 v221, a77\n"
    "v_accvgpr_read_b32 v225, a125\n"
    "v_accvgpr_read_b32 v229, a173\n"
    "v_accvgpr_read_b32 v218, a30\n"
    "v_accvgpr_read_b32 v222, a78\n"
    "v_accvgpr_read_b32 v226, a126\n"
    "v_accvgpr_read_b32 v230, a174\n"
    "v_accvgpr_read_b32 v219, a31\n"
    "v_accvgpr_read_b32 v223, a79\n"
    "v_accvgpr_read_b32 v227, a127\n"
    "v_accvgpr_read_b32 v231, a175\n"
    "v_mul_f32 v216, v216, v245\n"
    "v_mul_f32 v220, v220, v247\n"
    "v_mul_f32 v224, v224, v249\n"
    "v_mul_f32 v228, v228, v251\n"
    "v_mul_f32 v217, v217, v245\n"
    "v_mul_f32 v221, v221, v247\n"
    "v_mul_f32 v225, v225, v249\n"
    "v_mul_f32 v229, v229, v251\n"
    "v_mul_f32 v218, v218, v245\n"
    "v_mul_f32 v222, v222, v247\n"
    "v_mul_f32 v226, v226, v249\n"
    "v_mul_f32 v230, v230, v251\n"
    "v_mul_f32 v219, v219, v245\n"
    "v_mul_f32 v223, v223, v247\n"
    "v_mul_f32 v227, v227, v249\n"
    "v_mul_f32 v231, v231, v251\n"
    "v_accvgpr_write_b32 a28, v216\n"
    "v_accvgpr_write_b32 a76, v220\n"
    "v_accvgpr_write_b32 a124, v224\n"
    "v_accvgpr_write_b32 a172, v228\n"
    "v_accvgpr_write_b32 a29, v217\n"
    "v_accvgpr_write_b32 a77, v221\n"
    "v_accvgpr_write_b32 a125, v225\n"
    "v_accvgpr_write_b32 a173, v229\n"
    "v_accvgpr_write_b32 a30, v218\n"
    "v_accvgpr_write_b32 a78, v222\n"
    "v_accvgpr_write_b32 a126, v226\n"
    "v_accvgpr_write_b32 a174, v230\n"
    "v_accvgpr_write_b32 a31, v219\n"
    "v_accvgpr_write_b32 a79, v223\n"
    "v_accvgpr_write_b32 a127, v227\n"
    "v_accvgpr_write_b32 a175, v231\n"
    "v_accvgpr_read_b32 v216, a32\n"
    "v_accvgpr_read_b32 v220, a80\n"
    "v_accvgpr_read_b32 v224, a128\n"
    "v_accvgpr_read_b32 v228, a176\n"
    "v_accvgpr_read_b32 v217, a33\n"
    "v_accvgpr_read_b32 v221, a81\n"
    "v_accvgpr_read_b32 v225, a129\n"
    "v_accvgpr_read_b32 v229, a177\n"
    "v_accvgpr_read_b32 v218, a34\n"
    "v_accvgpr_read_b32 v222, a82\n"
    "v_accvgpr_read_b32 v226, a130\n"
    "v_accvgpr_read_b32 v230, a178\n"
    "v_accvgpr_read_b32 v219, a35\n"
    "v_accvgpr_read_b32 v223, a83\n"
    "v_accvgpr_read_b32 v227, a131\n"
    "v_accvgpr_read_b32 v231, a179\n"
    "v_mul_f32 v216, v216, v245\n"
    "v_mul_f32 v220, v220, v247\n"
    "v_mul_f32 v224, v224, v249\n"
    "v_mul_f32 v228, v228, v251\n"
    "v_mul_f32 v217, v217, v245\n"
    "v_mul_f32 v221, v221, v247\n"
    "v_mul_f32 v225, v225, v249\n"
    "v_mul_f32 v229, v229, v251\n"
    "v_mul_f32 v218, v218, v245\n"
    "v_mul_f32 v222, v222, v247\n"
    "v_mul_f32 v226, v226, v249\n"
    "v_mul_f32 v230, v230, v251\n"
    "v_mul_f32 v219, v219, v245\n"
    "v_mul_f32 v223, v223, v247\n"
    "v_mul_f32 v227, v227, v249\n"
    "v_mul_f32 v231, v231, v251\n"
    "v_accvgpr_write_b32 a32, v216\n"
    "v_accvgpr_write_b32 a80, v220\n"
    "v_accvgpr_write_b32 a128, v224\n"
    "v_accvgpr_write_b32 a176, v228\n"
    "v_accvgpr_write_b32 a33, v217\n"
    "v_accvgpr_write_b32 a81, v221\n"
    "v_accvgpr_write_b32 a129, v225\n"
    "v_accvgpr_write_b32 a177, v229\n"
    "v_accvgpr_write_b32 a34, v218\n"
    "v_accvgpr_write_b32 a82, v222\n"
    "v_accvgpr_write_b32 a130, v226\n"
    "v_accvgpr_write_b32 a178, v230\n"
    "v_accvgpr_write_b32 a35, v219\n"
    "v_accvgpr_write_b32 a83, v223\n"
    "v_accvgpr_write_b32 a131, v227\n"
    "v_accvgpr_write_b32 a179, v231\n"
    "v_accvgpr_read_b32 v216, a36\n"
    "v_accvgpr_read_b32 v220, a84\n"
    "v_accvgpr_read_b32 v224, a132\n"
    "v_accvgpr_read_b32 v228, a180\n"
    "v_accvgpr_read_b32 v217, a37\n"
    "v_accvgpr_read_b32 v221, a85\n"
    "v_accvgpr_read_b32 v225, a133\n"
    "v_accvgpr_read_b32 v229, a181\n"
    "v_accvgpr_read_b32 v218, a38\n"
    "v_accvgpr_read_b32 v222, a86\n"
    "v_accvgpr_read_b32 v226, a134\n"
    "v_accvgpr_read_b32 v230, a182\n"
    "v_accvgpr_read_b32 v219, a39\n"
    "v_accvgpr_read_b32 v223, a87\n"
    "v_accvgpr_read_b32 v227, a135\n"
    "v_accvgpr_read_b32 v231, a183\n"
    "v_mul_f32 v216, v216, v245\n"
    "v_mul_f32 v220, v220, v247\n"
    "v_mul_f32 v224, v224, v249\n"
    "v_mul_f32 v228, v228, v251\n"
    "v_mul_f32 v217, v217, v245\n"
    "v_mul_f32 v221, v221, v247\n"
    "v_mul_f32 v225, v225, v249\n"
    "v_mul_f32 v229, v229, v251\n"
    "v_mul_f32 v218, v218, v245\n"
    "v_mul_f32 v222, v222, v247\n"
    "v_mul_f32 v226, v226, v249\n"
    "v_mul_f32 v230, v230, v251\n"
    "v_mul_f32 v219, v219, v245\n"
    "v_mul_f32 v223, v223, v247\n"
    "v_mul_f32 v227, v227, v249\n"
    "v_mul_f32 v231, v231, v251\n"
    "v_accvgpr_write_b32 a36, v216\n"
    "v_accvgpr_write_b32 a84, v220\n"
    "v_accvgpr_write_b32 a132, v224\n"
    "v_accvgpr_write_b32 a180, v228\n"
    "v_accvgpr_write_b32 a37, v217\n"
    "v_accvgpr_write_b32 a85, v221\n"
    "v_accvgpr_write_b32 a133, v225\n"
    "v_accvgpr_write_b32 a181, v229\n"
    "v_accvgpr_write_b32 a38, v218\n"
    "v_accvgpr_write_b32 a86, v222\n"
    "v_accvgpr_write_b32 a134, v226\n"
    "v_accvgpr_write_b32 a182, v230\n"
    "v_accvgpr_write_b32 a39, v219\n"
    "v_accvgpr_write_b32 a87, v223\n"
    "v_accvgpr_write_b32 a135, v227\n"
    "v_accvgpr_write_b32 a183, v231\n"
    "v_accvgpr_read_b32 v216, a40\n"
    "v_accvgpr_read_b32 v220, a88\n"
    "v_accvgpr_read_b32 v224, a136\n"
    "v_accvgpr_read_b32 v228, a184\n"
    "v_accvgpr_read_b32 v217, a41\n"
    "v_accvgpr_read_b32 v221, a89\n"
    "v_accvgpr_read_b32 v225, a137\n"
    "v_accvgpr_read_b32 v229, a185\n"
    "v_accvgpr_read_b32 v218, a42\n"
    "v_accvgpr_read_b32 v222, a90\n"
    "v_accvgpr_read_b32 v226, a138\n"
    "v_accvgpr_read_b32 v230, a186\n"
    "v_accvgpr_read_b32 v219, a43\n"
    "v_accvgpr_read_b32 v223, a91\n"
    "v_accvgpr_read_b32 v227, a139\n"
    "v_accvgpr_read_b32 v231, a187\n"
    "v_mul_f32 v216, v216, v245\n"
    "v_mul_f32 v220, v220, v247\n"
    "v_mul_f32 v224, v224, v249\n"
    "v_mul_f32 v228, v228, v251\n"
    "v_mul_f32 v217, v217, v245\n"
    "v_mul_f32 v221, v221, v247\n"
    "v_mul_f32 v225, v225, v249\n"
    "v_mul_f32 v229, v229, v251\n"
    "v_mul_f32 v218, v218, v245\n"
    "v_mul_f32 v222, v222, v247\n"
    "v_mul_f32 v226, v226, v249\n"
    "v_mul_f32 v230, v230, v251\n"
    "v_mul_f32 v219, v219, v245\n"
    "v_mul_f32 v223, v223, v247\n"
    "v_mul_f32 v227, v227, v249\n"
    "v_mul_f32 v231, v231, v251\n"
    "v_accvgpr_write_b32 a40, v216\n"
    "v_accvgpr_write_b32 a88, v220\n"
    "v_accvgpr_write_b32 a136, v224\n"
    "v_accvgpr_write_b32 a184, v228\n"
    "v_accvgpr_write_b32 a41, v217\n"
    "v_accvgpr_write_b32 a89, v221\n"
    "v_accvgpr_write_b32 a137, v225\n"
    "v_accvgpr_write_b32 a185, v229\n"
    "v_accvgpr_write_b32 a42, v218\n"
    "v_accvgpr_write_b32 a90, v222\n"
    "v_accvgpr_write_b32 a138, v226\n"
    "v_accvgpr_write_b32 a186, v230\n"
    "v_accvgpr_write_b32 a43, v219\n"
    "v_accvgpr_write_b32 a91, v223\n"
    "v_accvgpr_write_b32 a139, v227\n"
    "v_accvgpr_write_b32 a187, v231\n"
    "v_accvgpr_read_b32 v216, a44\n"
    "v_accvgpr_read_b32 v220, a92\n"
    "v_accvgpr_read_b32 v224, a140\n"
    "v_accvgpr_read_b32 v228, a188\n"
    "v_accvgpr_read_b32 v217, a45\n"
    "v_accvgpr_read_b32 v221, a93\n"
    "v_accvgpr_read_b32 v225, a141\n"
    "v_accvgpr_read_b32 v229, a189\n"
    "v_accvgpr_read_b32 v218, a46\n"
    "v_accvgpr_read_b32 v222, a94\n"
    "v_accvgpr_read_b32 v226, a142\n"
    "v_accvgpr_read_b32 v230, a190\n"
    "v_accvgpr_read_b32 v219, a47\n"
    "v_accvgpr_read_b32 v223, a95\n"
    "v_accvgpr_read_b32 v227, a143\n"
    "v_accvgpr_read_b32 v231, a191\n"
    "v_mul_f32 v216, v216, v245\n"
    "v_mul_f32 v220, v220, v247\n"
    "v_mul_f32 v224, v224, v249\n"
    "v_mul_f32 v228, v228, v251\n"
    "v_mul_f32 v217, v217, v245\n"
    "v_mul_f32 v221, v221, v247\n"
    "v_mul_f32 v225, v225, v249\n"
    "v_mul_f32 v229, v229, v251\n"
    "v_mul_f32 v218, v218, v245\n"
    "v_mul_f32 v222, v222, v247\n"
    "v_mul_f32 v226, v226, v249\n"
    "v_mul_f32 v230, v230, v251\n"
    "v_mul_f32 v219, v219, v245\n"
    "v_mul_f32 v223, v223, v247\n"
    "v_mul_f32 v227, v227, v249\n"
    "v_mul_f32 v231, v231, v251\n"
    "v_accvgpr_write_b32 a44, v216\n"
    "v_accvgpr_write_b32 a92, v220\n"
    "v_accvgpr_write_b32 a140, v224\n"
    "v_accvgpr_write_b32 a188, v228\n"
    "v_accvgpr_write_b32 a45, v217\n"
    "v_accvgpr_write_b32 a93, v221\n"
    "v_accvgpr_write_b32 a141, v225\n"
    "v_accvgpr_write_b32 a189, v229\n"
    "v_accvgpr_write_b32 a46, v218\n"
    "v_accvgpr_write_b32 a94, v222\n"
    "v_accvgpr_write_b32 a142, v226\n"
    "v_accvgpr_write_b32 a190, v230\n"
    "v_accvgpr_write_b32 a47, v219\n"
    "v_accvgpr_write_b32 a95, v223\n"
    "v_accvgpr_write_b32 a143, v227\n"
    "v_accvgpr_write_b32 a191, v231\n"
    "v_exp_f32 v216, v96\n"
    "v_exp_f32 v220, v104\n"
    "v_exp_f32 v224, v112\n"
    "v_exp_f32 v228, v120\n"
    "v_exp_f32 v217, v97\n"
    "v_exp_f32 v221, v105\n"
    "v_exp_f32 v225, v113\n"
    "v_exp_f32 v229, v121\n"
    "v_exp_f32 v218, v98\n"
    "v_exp_f32 v222, v106\n"
    "v_exp_f32 v226, v114\n"
    "v_exp_f32 v230, v122\n"
    "v_exp_f32 v219, v99\n"
    "v_exp_f32 v223, v107\n"
    "v_exp_f32 v227, v115\n"
    "v_exp_f32 v231, v123\n"
    "v_cvt_pk_bf16_f32 v160, v216, v217\n"
    "v_cvt_pk_bf16_f32 v164, v220, v221\n"
    "v_cvt_pk_bf16_f32 v168, v224, v225\n"
    "v_cvt_pk_bf16_f32 v172, v228, v229\n"
    "v_cvt_pk_bf16_f32 v161, v218, v219\n"
    "v_cvt_pk_bf16_f32 v165, v222, v223\n"
    "v_cvt_pk_bf16_f32 v169, v226, v227\n"
    "v_cvt_pk_bf16_f32 v173, v230, v231\n"
    "v_add_f32 v212, v216, v217\n"
    "v_add_f32 v213, v220, v221\n"
    "v_add_f32 v214, v224, v225\n"
    "v_add_f32 v215, v228, v229\n"
    "v_add_f32 v212, v212, v218\n"
    "v_add_f32 v213, v213, v222\n"
    "v_add_f32 v214, v214, v226\n"
    "v_add_f32 v215, v215, v230\n"
    "v_add_f32 v212, v212, v219\n"
    "v_add_f32 v213, v213, v223\n"
    "v_add_f32 v214, v214, v227\n"
    "v_add_f32 v215, v215, v231\n"
    "v_exp_f32 v216, v100\n"
    "v_exp_f32 v220, v108\n"
    "v_exp_f32 v224, v116\n"
    "v_exp_f32 v228, v124\n"
    "v_exp_f32 v217, v101\n"
    "v_exp_f32 v221, v109\n"
    "v_exp_f32 v225, v117\n"
    "v_exp_f32 v229, v125\n"
    "v_exp_f32 v218, v102\n"
    "v_exp_f32 v222, v110\n"
    "v_exp_f32 v226, v118\n"
    "v_exp_f32 v230, v126\n"
    "v_exp_f32 v219, v103\n"
    "v_exp_f32 v223, v111\n"
    "v_exp_f32 v227, v119\n"
    "v_exp_f32 v231, v127\n"
    "v_cvt_pk_bf16_f32 v162, v216, v217\n"
    "v_cvt_pk_bf16_f32 v166, v220, v221\n"
    "v_cvt_pk_bf16_f32 v170, v224, v225\n"
    "v_cvt_pk_bf16_f32 v174, v228, v229\n"
    "v_cvt_pk_bf16_f32 v163, v218, v219\n"
    "v_cvt_pk_bf16_f32 v167, v222, v223\n"
    "v_cvt_pk_bf16_f32 v171, v226, v227\n"
    "v_cvt_pk_bf16_f32 v175, v230, v231\n"
    "v_add_f32 v212, v212, v216\n"
    "v_add_f32 v213, v213, v220\n"
    "v_add_f32 v214, v214, v224\n"
    "v_add_f32 v215, v215, v228\n"
    "v_add_f32 v212, v212, v217\n"
    "v_add_f32 v213, v213, v221\n"
    "v_add_f32 v214, v214, v225\n"
    "v_add_f32 v215, v215, v229\n"
    "v_add_f32 v212, v212, v218\n"
    "v_add_f32 v213, v213, v222\n"
    "v_add_f32 v214, v214, v226\n"
    "v_add_f32 v215, v215, v230\n"
    "v_add_f32 v212, v212, v219\n"
    "v_add_f32 v213, v213, v223\n"
    "v_add_f32 v214, v214, v227\n"
    "v_add_f32 v215, v215, v231\n"
    "s_nop 3\n"
    "s_branch LRET_e_4_L_%=\n"
    "LRARE_o_4_%=:\n"
    "s_nop 7\n"
    "s_nop 7\n"
    "v_max3_f32 v244, v128, v129, v130\n"
    "v_max3_f32 v246, v136, v137, v138\n"
    "v_max3_f32 v248, v144, v145, v146\n"
    "v_max3_f32 v250, v152, v153, v154\n"
    "v_max3_f32 v244, v244, v131, v132\n"
    "v_max3_f32 v246, v246, v139, v140\n"
    "v_max3_f32 v248, v248, v147, v148\n"
    "v_max3_f32 v250, v250, v155, v156\n"
    "v_max3_f32 v244, v244, v133, v134\n"
    "v_max3_f32 v246, v246, v141, v142\n"
    "v_max3_f32 v248, v248, v149, v150\n"
    "v_max3_f32 v250, v250, v157, v158\n"
    "v_max_f32 v244, v244, v135\n"
    "v_max_f32 v246, v246, v143\n"
    "v_max_f32 v248, v248, v151\n"
    "v_max_f32 v250, v250, v159\n"
    "v_mov_b32 v245, v244\n"
    "v_mov_b32 v247, v246\n"
    "v_mov_b32 v249, v248\n"
    "v_mov_b32 v251, v250\n"
    "v_permlane32_swap_b32 v244, v245\n"
    "v_permlane32_swap_b32 v246, v247\n"
    "v_permlane32_swap_b32 v248, v249\n"
    "v_permlane32_swap_b32 v250, v251\n"
    "v_max_f32 v244, v244, v245\n"
    "v_max_f32 v246, v246, v247\n"
    "v_max_f32 v248, v248, v249\n"
    "v_max_f32 v250, v250, v251\n"
    "v_mov_b32 v245, v244\n"
    "v_mov_b32 v247, v246\n"
    "v_mov_b32 v249, v248\n"
    "v_mov_b32 v251, v250\n"
    "v_permlane16_swap_b32 v244, v245\n"
    "v_permlane16_swap_b32 v246, v247\n"
    "v_permlane16_swap_b32 v248, v249\n"
    "v_permlane16_swap_b32 v250, v251\n"
    "v_max_f32 v244, v244, v245\n"
    "v_max_f32 v246, v246, v247\n"
    "v_max_f32 v248, v248, v249\n"
    "v_max_f32 v250, v250, v251\n"
    "v_max_f32 v244, 0, v244\n"
    "v_max_f32 v246, 0, v246\n"
    "v_max_f32 v248, 0, v248\n"
    "v_max_f32 v250, 0, v250\n"
    "v_exp_f32 v245, -v244\n"
    "v_exp_f32 v247, -v246\n"
    "v_exp_f32 v249, -v248\n"
    "v_exp_f32 v251, -v250\n"
    "v_sub_f32 v128, v128, v244\n"
    "v_sub_f32 v136, v136, v246\n"
    "v_sub_f32 v144, v144, v248\n"
    "v_sub_f32 v152, v152, v250\n"
    "v_sub_f32 v129, v129, v244\n"
    "v_sub_f32 v137, v137, v246\n"
    "v_sub_f32 v145, v145, v248\n"
    "v_sub_f32 v153, v153, v250\n"
    "v_sub_f32 v130, v130, v244\n"
    "v_sub_f32 v138, v138, v246\n"
    "v_sub_f32 v146, v146, v248\n"
    "v_sub_f32 v154, v154, v250\n"
    "v_sub_f32 v131, v131, v244\n"
    "v_sub_f32 v139, v139, v246\n"
    "v_sub_f32 v147, v147, v248\n"
    "v_sub_f32 v155, v155, v250\n"
    "v_sub_f32 v132, v132, v244\n"
    "v_sub_f32 v140, v140, v246\n"
    "v_sub_f32 v148, v148, v248\n"
    "v_sub_f32 v156, v156, v250\n"
    "v_sub_f32 v133, v133, v244\n"
    "v_sub_f32 v141, v141, v246\n"
    "v_sub_f32 v149, v149, v248\n"
    "v_sub_f32 v157, v157, v250\n"
    "v_sub_f32 v134, v134, v244\n"
    "v_sub_f32 v142, v142, v246\n"
    "v_sub_f32 v150, v150, v248\n"
    "v_sub_f32 v158, v158, v250\n"
    "v_sub_f32 v135, v135, v244\n"
    "v_sub_f32 v143, v143, v246\n"
    "v_sub_f32 v151, v151, v248\n"
    "v_sub_f32 v159, v159, v250\n"
    "v_sub_f32 v96, v96, v244\n"
    "v_sub_f32 v104, v104, v246\n"
    "v_sub_f32 v112, v112, v248\n"
    "v_sub_f32 v120, v120, v250\n"
    "v_sub_f32 v97, v97, v244\n"
    "v_sub_f32 v105, v105, v246\n"
    "v_sub_f32 v113, v113, v248\n"
    "v_sub_f32 v121, v121, v250\n"
    "v_sub_f32 v98, v98, v244\n"
    "v_sub_f32 v106, v106, v246\n"
    "v_sub_f32 v114, v114, v248\n"
    "v_sub_f32 v122, v122, v250\n"
    "v_sub_f32 v99, v99, v244\n"
    "v_sub_f32 v107, v107, v246\n"
    "v_sub_f32 v115, v115, v248\n"
    "v_sub_f32 v123, v123, v250\n"
    "v_sub_f32 v100, v100, v244\n"
    "v_sub_f32 v108, v108, v246\n"
    "v_sub_f32 v116, v116, v248\n"
    "v_sub_f32 v124, v124, v250\n"
    "v_sub_f32 v101, v101, v244\n"
    "v_sub_f32 v109, v109, v246\n"
    "v_sub_f32 v117, v117, v248\n"
    "v_sub_f32 v125, v125, v250\n"
    "v_sub_f32 v102, v102, v244\n"
    "v_sub_f32 v110, v110, v246\n"
    "v_sub_f32 v118, v118, v248\n"
    "v_sub_f32 v126, v126, v250\n"
    "v_sub_f32 v103, v103, v244\n"
    "v_sub_f32 v111, v111, v246\n"
    "v_sub_f32 v119, v119, v248\n"
    "v_sub_f32 v127, v127, v250\n"
    "v_sub_f32 v192, v192, v244\n"
    "v_sub_f32 v196, v196, v246\n"
    "v_sub_f32 v200, v200, v248\n"
    "v_sub_f32 v204, v204, v250\n"
    "v_sub_f32 v193, v193, v244\n"
    "v_sub_f32 v197, v197, v246\n"
    "v_sub_f32 v201, v201, v248\n"
    "v_sub_f32 v205, v205, v250\n"
    "v_sub_f32 v194, v194, v244\n"
    "v_sub_f32 v198, v198, v246\n"
    "v_sub_f32 v202, v202, v248\n"
    "v_sub_f32 v206, v206, v250\n"
    "v_sub_f32 v195, v195, v244\n"
    "v_sub_f32 v199, v199, v246\n"
    "v_sub_f32 v203, v203, v248\n"
    "v_sub_f32 v207, v207, v250\n"
    "v_mul_f32 v208, v208, v245\n"
    "v_mul_f32 v209, v209, v247\n"
    "v_mul_f32 v210, v210, v249\n"
    "v_mul_f32 v211, v211, v251\n"
    "v_accvgpr_read_b32 v216, a0\n"
    "v_accvgpr_read_b32 v220, a48\n"
    "v_accvgpr_read_b32 v224, a96\n"
    "v_accvgpr_read_b32 v228, a144\n"
    "v_accvgpr_read_b32 v217, a1\n"
    "v_accvgpr_read_b32 v221, a49\n"
    "v_accvgpr_read_b32 v225, a97\n"
    "v_accvgpr_read_b32 v229, a145\n"
    "v_accvgpr_read_b32 v218, a2\n"
    "v_accvgpr_read_b32 v222, a50\n"
    "v_accvgpr_read_b32 v226, a98\n"
    "v_accvgpr_read_b32 v230, a146\n"
    "v_accvgpr_read_b32 v219, a3\n"
    "v_accvgpr_read_b32 v223, a51\n"
    "v_accvgpr_read_b32 v227, a99\n"
    "v_accvgpr_read_b32 v231, a147\n"
    "v_mul_f32 v216, v216, v245\n"
    "v_mul_f32 v220, v220, v247\n"
    "v_mul_f32 v224, v224, v249\n"
    "v_mul_f32 v228, v228, v251\n"
    "v_mul_f32 v217, v217, v245\n"
    "v_mul_f32 v221, v221, v247\n"
    "v_mul_f32 v225, v225, v249\n"
    "v_mul_f32 v229, v229, v251\n"
    "v_mul_f32 v218, v218, v245\n"
    "v_mul_f32 v222, v222, v247\n"
    "v_mul_f32 v226, v226, v249\n"
    "v_mul_f32 v230, v230, v251\n"
    "v_mul_f32 v219, v219, v245\n"
    "v_mul_f32 v223, v223, v247\n"
    "v_mul_f32 v227, v227, v249\n"
    "v_mul_f32 v231, v231, v251\n"
    "v_accvgpr_write_b32 a0, v216\n"
    "v_accvgpr_write_b32 a48, v220\n"
    "v_accvgpr_write_b32 a96, v224\n"
    "v_accvgpr_write_b32 a144, v228\n"
    "v_accvgpr_write_b32 a1, v217\n"
    "v_accvgpr_write_b32 a49, v221\n"
    "v_accvgpr_write_b32 a97, v225\n"
    "v_accvgpr_write_b32 a145, v229\n"
    "v_accvgpr_write_b32 a2, v218\n"
    "v_accvgpr_write_b32 a50, v222\n"
    "v_accvgpr_write_b32 a98, v226\n"
    "v_accvgpr_write_b32 a146, v230\n"
    "v_accvgpr_write_b32 a3, v219\n"
    "v_accvgpr_write_b32 a51, v223\n"
    "v_accvgpr_write_b32 a99, v227\n"
    "v_accvgpr_write_b32 a147, v231\n"
    "v_accvgpr_read_b32 v216, a4\n"
    "v_accvgpr_read_b32 v220, a52\n"
    "v_accvgpr_read_b32 v224, a100\n"
    "v_accvgpr_read_b32 v228, a148\n"
    "v_accvgpr_read_b32 v217, a5\n"
    "v_accvgpr_read_b32 v221, a53\n"
    "v_accvgpr_read_b32 v225, a101\n"
    "v_accvgpr_read_b32 v229, a149\n"
    "v_accvgpr_read_b32 v218, a6\n"
    "v_accvgpr_read_b32 v222, a54\n"
    "v_accvgpr_read_b32 v226, a102\n"
    "v_accvgpr_read_b32 v230, a150\n"
    "v_accvgpr_read_b32 v219, a7\n"
    "v_accvgpr_read_b32 v223, a55\n"
    "v_accvgpr_read_b32 v227, a103\n"
    "v_accvgpr_read_b32 v231, a151\n"
    "v_mul_f32 v216, v216, v245\n"
    "v_mul_f32 v220, v220, v247\n"
    "v_mul_f32 v224, v224, v249\n"
    "v_mul_f32 v228, v228, v251\n"
    "v_mul_f32 v217, v217, v245\n"
    "v_mul_f32 v221, v221, v247\n"
    "v_mul_f32 v225, v225, v249\n"
    "v_mul_f32 v229, v229, v251\n"
    "v_mul_f32 v218, v218, v245\n"
    "v_mul_f32 v222, v222, v247\n"
    "v_mul_f32 v226, v226, v249\n"
    "v_mul_f32 v230, v230, v251\n"
    "v_mul_f32 v219, v219, v245\n"
    "v_mul_f32 v223, v223, v247\n"
    "v_mul_f32 v227, v227, v249\n"
    "v_mul_f32 v231, v231, v251\n"
    "v_accvgpr_write_b32 a4, v216\n"
    "v_accvgpr_write_b32 a52, v220\n"
    "v_accvgpr_write_b32 a100, v224\n"
    "v_accvgpr_write_b32 a148, v228\n"
    "v_accvgpr_write_b32 a5, v217\n"
    "v_accvgpr_write_b32 a53, v221\n"
    "v_accvgpr_write_b32 a101, v225\n"
    "v_accvgpr_write_b32 a149, v229\n"
    "v_accvgpr_write_b32 a6, v218\n"
    "v_accvgpr_write_b32 a54, v222\n"
    "v_accvgpr_write_b32 a102, v226\n"
    "v_accvgpr_write_b32 a150, v230\n"
    "v_accvgpr_write_b32 a7, v219\n"
    "v_accvgpr_write_b32 a55, v223\n"
    "v_accvgpr_write_b32 a103, v227\n"
    "v_accvgpr_write_b32 a151, v231\n"
    "v_accvgpr_read_b32 v216, a8\n"
    "v_accvgpr_read_b32 v220, a56\n"
    "v_accvgpr_read_b32 v224, a104\n"
    "v_accvgpr_read_b32 v228, a152\n"
    "v_accvgpr_read_b32 v217, a9\n"
    "v_accvgpr_read_b32 v221, a57\n"
    "v_accvgpr_read_b32 v225, a105\n"
    "v_accvgpr_read_b32 v229, a153\n"
    "v_accvgpr_read_b32 v218, a10\n"
    "v_accvgpr_read_b32 v222, a58\n"
    "v_accvgpr_read_b32 v226, a106\n"
    "v_accvgpr_read_b32 v230, a154\n"
    "v_accvgpr_read_b32 v219, a11\n"
    "v_accvgpr_read_b32 v223, a59\n"
    "v_accvgpr_read_b32 v227, a107\n"
    "v_accvgpr_read_b32 v231, a155\n"
    "v_mul_f32 v216, v216, v245\n"
    "v_mul_f32 v220, v220, v247\n"
    "v_mul_f32 v224, v224, v249\n"
    "v_mul_f32 v228, v228, v251\n"
    "v_mul_f32 v217, v217, v245\n"
    "v_mul_f32 v221, v221, v247\n"
    "v_mul_f32 v225, v225, v249\n"
    "v_mul_f32 v229, v229, v251\n"
    "v_mul_f32 v218, v218, v245\n"
    "v_mul_f32 v222, v222, v247\n"
    "v_mul_f32 v226, v226, v249\n"
    "v_mul_f32 v230, v230, v251\n"
    "v_mul_f32 v219, v219, v245\n"
    "v_mul_f32 v223, v223, v247\n"
    "v_mul_f32 v227, v227, v249\n"
    "v_mul_f32 v231, v231, v251\n"
    "v_accvgpr_write_b32 a8, v216\n"
    "v_accvgpr_write_b32 a56, v220\n"
    "v_accvgpr_write_b32 a104, v224\n"
    "v_accvgpr_write_b32 a152, v228\n"
    "v_accvgpr_write_b32 a9, v217\n"
    "v_accvgpr_write_b32 a57, v221\n"
    "v_accvgpr_write_b32 a105, v225\n"
    "v_accvgpr_write_b32 a153, v229\n"
    "v_accvgpr_write_b32 a10, v218\n"
    "v_accvgpr_write_b32 a58, v222\n"
    "v_accvgpr_write_b32 a106, v226\n"
    "v_accvgpr_write_b32 a154, v230\n"
    "v_accvgpr_write_b32 a11, v219\n"
    "v_accvgpr_write_b32 a59, v223\n"
    "v_accvgpr_write_b32 a107, v227\n"
    "v_accvgpr_write_b32 a155, v231\n"
    "v_accvgpr_read_b32 v216, a12\n"
    "v_accvgpr_read_b32 v220, a60\n"
    "v_accvgpr_read_b32 v224, a108\n"
    "v_accvgpr_read_b32 v228, a156\n"
    "v_accvgpr_read_b32 v217, a13\n"
    "v_accvgpr_read_b32 v221, a61\n"
    "v_accvgpr_read_b32 v225, a109\n"
    "v_accvgpr_read_b32 v229, a157\n"
    "v_accvgpr_read_b32 v218, a14\n"
    "v_accvgpr_read_b32 v222, a62\n"
    "v_accvgpr_read_b32 v226, a110\n"
    "v_accvgpr_read_b32 v230, a158\n"
    "v_accvgpr_read_b32 v219, a15\n"
    "v_accvgpr_read_b32 v223, a63\n"
    "v_accvgpr_read_b32 v227, a111\n"
    "v_accvgpr_read_b32 v231, a159\n"
    "v_mul_f32 v216, v216, v245\n"
    "v_mul_f32 v220, v220, v247\n"
    "v_mul_f32 v224, v224, v249\n"
    "v_mul_f32 v228, v228, v251\n"
    "v_mul_f32 v217, v217, v245\n"
    "v_mul_f32 v221, v221, v247\n"
    "v_mul_f32 v225, v225, v249\n"
    "v_mul_f32 v229, v229, v251\n"
    "v_mul_f32 v218, v218, v245\n"
    "v_mul_f32 v222, v222, v247\n"
    "v_mul_f32 v226, v226, v249\n"
    "v_mul_f32 v230, v230, v251\n"
    "v_mul_f32 v219, v219, v245\n"
    "v_mul_f32 v223, v223, v247\n"
    "v_mul_f32 v227, v227, v249\n"
    "v_mul_f32 v231, v231, v251\n"
    "v_accvgpr_write_b32 a12, v216\n"
    "v_accvgpr_write_b32 a60, v220\n"
    "v_accvgpr_write_b32 a108, v224\n"
    "v_accvgpr_write_b32 a156, v228\n"
    "v_accvgpr_write_b32 a13, v217\n"
    "v_accvgpr_write_b32 a61, v221\n"
    "v_accvgpr_write_b32 a109, v225\n"
    "v_accvgpr_write_b32 a157, v229\n"
    "v_accvgpr_write_b32 a14, v218\n"
    "v_accvgpr_write_b32 a62, v222\n"
    "v_accvgpr_write_b32 a110, v226\n"
    "v_accvgpr_write_b32 a158, v230\n"
    "v_accvgpr_write_b32 a15, v219\n"
    "v_accvgpr_write_b32 a63, v223\n"
    "v_accvgpr_write_b32 a111, v227\n"
    "v_accvgpr_write_b32 a159, v231\n"
    "v_accvgpr_read_b32 v216, a16\n"
    "v_accvgpr_read_b32 v220, a64\n"
    "v_accvgpr_read_b32 v224, a112\n"
    "v_accvgpr_read_b32 v228, a160\n"
    "v_accvgpr_read_b32 v217, a17\n"
    "v_accvgpr_read_b32 v221, a65\n"
    "v_accvgpr_read_b32 v225, a113\n"
    "v_accvgpr_read_b32 v229, a161\n"
    "v_accvgpr_read_b32 v218, a18\n"
    "v_accvgpr_read_b32 v222, a66\n"
    "v_accvgpr_read_b32 v226, a114\n"
    "v_accvgpr_read_b32 v230, a162\n"
    "v_accvgpr_read_b32 v219, a19\n"
    "v_accvgpr_read_b32 v223, a67\n"
    "v_accvgpr_read_b32 v227, a115\n"
    "v_accvgpr_read_b32 v231, a163\n"
    "v_mul_f32 v216, v216, v245\n"
    "v_mul_f32 v220, v220, v247\n"
    "v_mul_f32 v224, v224, v249\n"
    "v_mul_f32 v228, v228, v251\n"
    "v_mul_f32 v217, v217, v245\n"
    "v_mul_f32 v221, v221, v247\n"
    "v_mul_f32 v225, v225, v249\n"
    "v_mul_f32 v229, v229, v251\n"
    "v_mul_f32 v218, v218, v245\n"
    "v_mul_f32 v222, v222, v247\n"
    "v_mul_f32 v226, v226, v249\n"
    "v_mul_f32 v230, v230, v251\n"
    "v_mul_f32 v219, v219, v245\n"
    "v_mul_f32 v223, v223, v247\n"
    "v_mul_f32 v227, v227, v249\n"
    "v_mul_f32 v231, v231, v251\n"
    "v_accvgpr_write_b32 a16, v216\n"
    "v_accvgpr_write_b32 a64, v220\n"
    "v_accvgpr_write_b32 a112, v224\n"
    "v_accvgpr_write_b32 a160, v228\n"
    "v_accvgpr_write_b32 a17, v217\n"
    "v_accvgpr_write_b32 a65, v221\n"
    "v_accvgpr_write_b32 a113, v225\n"
    "v_accvgpr_write_b32 a161, v229\n"
    "v_accvgpr_write_b32 a18, v218\n"
    "v_accvgpr_write_b32 a66, v222\n"
    "v_accvgpr_write_b32 a114, v226\n"
    "v_accvgpr_write_b32 a162, v230\n"
    "v_accvgpr_write_b32 a19, v219\n"
    "v_accvgpr_write_b32 a67, v223\n"
    "v_accvgpr_write_b32 a115, v227\n"
    "v_accvgpr_write_b32 a163, v231\n"
    "v_accvgpr_read_b32 v216, a20\n"
    "v_accvgpr_read_b32 v220, a68\n"
    "v_accvgpr_read_b32 v224, a116\n"
    "v_accvgpr_read_b32 v228, a164\n"
    "v_accvgpr_read_b32 v217, a21\n"
    "v_accvgpr_read_b32 v221, a69\n"
    "v_accvgpr_read_b32 v225, a117\n"
    "v_accvgpr_read_b32 v229, a165\n"
    "v_accvgpr_read_b32 v218, a22\n"
    "v_accvgpr_read_b32 v222, a70\n"
    "v_accvgpr_read_b32 v226, a118\n"
    "v_accvgpr_read_b32 v230, a166\n"
    "v_accvgpr_read_b32 v219, a23\n"
    "v_accvgpr_read_b32 v223, a71\n"
    "v_accvgpr_read_b32 v227, a119\n"
    "v_accvgpr_read_b32 v231, a167\n"
    "v_mul_f32 v216, v216, v245\n"
    "v_mul_f32 v220, v220, v247\n"
    "v_mul_f32 v224, v224, v249\n"
    "v_mul_f32 v228, v228, v251\n"
    "v_mul_f32 v217, v217, v245\n"
    "v_mul_f32 v221, v221, v247\n"
    "v_mul_f32 v225, v225, v249\n"
    "v_mul_f32 v229, v229, v251\n"
    "v_mul_f32 v218, v218, v245\n"
    "v_mul_f32 v222, v222, v247\n"
    "v_mul_f32 v226, v226, v249\n"
    "v_mul_f32 v230, v230, v251\n"
    "v_mul_f32 v219, v219, v245\n"
    "v_mul_f32 v223, v223, v247\n"
    "v_mul_f32 v227, v227, v249\n"
    "v_mul_f32 v231, v231, v251\n"
    "v_accvgpr_write_b32 a20, v216\n"
    "v_accvgpr_write_b32 a68, v220\n"
    "v_accvgpr_write_b32 a116, v224\n"
    "v_accvgpr_write_b32 a164, v228\n"
    "v_accvgpr_write_b32 a21, v217\n"
    "v_accvgpr_write_b32 a69, v221\n"
    "v_accvgpr_write_b32 a117, v225\n"
    "v_accvgpr_write_b32 a165, v229\n"
    "v_accvgpr_write_b32 a22, v218\n"
    "v_accvgpr_write_b32 a70, v222\n"
    "v_accvgpr_write_b32 a118, v226\n"
    "v_accvgpr_write_b32 a166, v230\n"
    "v_accvgpr_write_b32 a23, v219\n"
    "v_accvgpr_write_b32 a71, v223\n"
    "v_accvgpr_write_b32 a119, v227\n"
    "v_accvgpr_write_b32 a167, v231\n"
    "v_accvgpr_read_b32 v216, a24\n"
    "v_accvgpr_read_b32 v220, a72\n"
    "v_accvgpr_read_b32 v224, a120\n"
    "v_accvgpr_read_b32 v228, a168\n"
    "v_accvgpr_read_b32 v217, a25\n"
    "v_accvgpr_read_b32 v221, a73\n"
    "v_accvgpr_read_b32 v225, a121\n"
    "v_accvgpr_read_b32 v229, a169\n"
    "v_accvgpr_read_b32 v218, a26\n"
    "v_accvgpr_read_b32 v222, a74\n"
    "v_accvgpr_read_b32 v226, a122\n"
    "v_accvgpr_read_b32 v230, a170\n"
    "v_accvgpr_read_b32 v219, a27\n"
    "v_accvgpr_read_b32 v223, a75\n"
    "v_accvgpr_read_b32 v227, a123\n"
    "v_accvgpr_read_b32 v231, a171\n"
    "v_mul_f32 v216, v216, v245\n"
    "v_mul_f32 v220, v220, v247\n"
    "v_mul_f32 v224, v224, v249\n"
    "v_mul_f32 v228, v228, v251\n"
    "v_mul_f32 v217, v217, v245\n"
    "v_mul_f32 v221, v221, v247\n"
    "v_mul_f32 v225, v225, v249\n"
    "v_mul_f32 v229, v229, v251\n"
    "v_mul_f32 v218, v218, v245\n"
    "v_mul_f32 v222, v222, v247\n"
    "v_mul_f32 v226, v226, v249\n"
    "v_mul_f32 v230, v230, v251\n"
    "v_mul_f32 v219, v219, v245\n"
    "v_mul_f32 v223, v223, v247\n"
    "v_mul_f32 v227, v227, v249\n"
    "v_mul_f32 v231, v231, v251\n"
    "v_accvgpr_write_b32 a24, v216\n"
    "v_accvgpr_write_b32 a72, v220\n"
    "v_accvgpr_write_b32 a120, v224\n"
    "v_accvgpr_write_b32 a168, v228\n"
    "v_accvgpr_write_b32 a25, v217\n"
    "v_accvgpr_write_b32 a73, v221\n"
    "v_accvgpr_write_b32 a121, v225\n"
    "v_accvgpr_write_b32 a169, v229\n"
    "v_accvgpr_write_b32 a26, v218\n"
    "v_accvgpr_write_b32 a74, v222\n"
    "v_accvgpr_write_b32 a122, v226\n"
    "v_accvgpr_write_b32 a170, v230\n"
    "v_accvgpr_write_b32 a27, v219\n"
    "v_accvgpr_write_b32 a75, v223\n"
    "v_accvgpr_write_b32 a123, v227\n"
    "v_accvgpr_write_b32 a171, v231\n"
    "v_accvgpr_read_b32 v216, a28\n"
    "v_accvgpr_read_b32 v220, a76\n"
    "v_accvgpr_read_b32 v224, a124\n"
    "v_accvgpr_read_b32 v228, a172\n"
    "v_accvgpr_read_b32 v217, a29\n"
    "v_accvgpr_read_b32 v221, a77\n"
    "v_accvgpr_read_b32 v225, a125\n"
    "v_accvgpr_read_b32 v229, a173\n"
    "v_accvgpr_read_b32 v218, a30\n"
    "v_accvgpr_read_b32 v222, a78\n"
    "v_accvgpr_read_b32 v226, a126\n"
    "v_accvgpr_read_b32 v230, a174\n"
    "v_accvgpr_read_b32 v219, a31\n"
    "v_accvgpr_read_b32 v223, a79\n"
    "v_accvgpr_read_b32 v227, a127\n"
    "v_accvgpr_read_b32 v231, a175\n"
    "v_mul_f32 v216, v216, v245\n"
    "v_mul_f32 v220, v220, v247\n"
    "v_mul_f32 v224, v224, v249\n"
    "v_mul_f32 v228, v228, v251\n"
    "v_mul_f32 v217, v217, v245\n"
    "v_mul_f32 v221, v221, v247\n"
    "v_mul_f32 v225, v225, v249\n"
    "v_mul_f32 v229, v229, v251\n"
    "v_mul_f32 v218, v218, v245\n"
    "v_mul_f32 v222, v222, v247\n"
    "v_mul_f32 v226, v226, v249\n"
    "v_mul_f32 v230, v230, v251\n"
    "v_mul_f32 v219, v219, v245\n"
    "v_mul_f32 v223, v223, v247\n"
    "v_mul_f32 v227, v227, v249\n"
    "v_mul_f32 v231, v231, v251\n"
    "v_accvgpr_write_b32 a28, v216\n"
    "v_accvgpr_write_b32 a76, v220\n"
    "v_accvgpr_write_b32 a124, v224\n"
    "v_accvgpr_write_b32 a172, v228\n"
    "v_accvgpr_write_b32 a29, v217\n"
    "v_accvgpr_write_b32 a77, v221\n"
    "v_accvgpr_write_b32 a125, v225\n"
    "v_accvgpr_write_b32 a173, v229\n"
    "v_accvgpr_write_b32 a30, v218\n"
    "v_accvgpr_write_b32 a78, v222\n"
    "v_accvgpr_write_b32 a126, v226\n"
    "v_accvgpr_write_b32 a174, v230\n"
    "v_accvgpr_write_b32 a31, v219\n"
    "v_accvgpr_write_b32 a79, v223\n"
    "v_accvgpr_write_b32 a127, v227\n"
    "v_accvgpr_write_b32 a175, v231\n"
    "v_accvgpr_read_b32 v216, a32\n"
    "v_accvgpr_read_b32 v220, a80\n"
    "v_accvgpr_read_b32 v224, a128\n"
    "v_accvgpr_read_b32 v228, a176\n"
    "v_accvgpr_read_b32 v217, a33\n"
    "v_accvgpr_read_b32 v221, a81\n"
    "v_accvgpr_read_b32 v225, a129\n"
    "v_accvgpr_read_b32 v229, a177\n"
    "v_accvgpr_read_b32 v218, a34\n"
    "v_accvgpr_read_b32 v222, a82\n"
    "v_accvgpr_read_b32 v226, a130\n"
    "v_accvgpr_read_b32 v230, a178\n"
    "v_accvgpr_read_b32 v219, a35\n"
    "v_accvgpr_read_b32 v223, a83\n"
    "v_accvgpr_read_b32 v227, a131\n"
    "v_accvgpr_read_b32 v231, a179\n"
    "v_mul_f32 v216, v216, v245\n"
    "v_mul_f32 v220, v220, v247\n"
    "v_mul_f32 v224, v224, v249\n"
    "v_mul_f32 v228, v228, v251\n"
    "v_mul_f32 v217, v217, v245\n"
    "v_mul_f32 v221, v221, v247\n"
    "v_mul_f32 v225, v225, v249\n"
    "v_mul_f32 v229, v229, v251\n"
    "v_mul_f32 v218, v218, v245\n"
    "v_mul_f32 v222, v222, v247\n"
    "v_mul_f32 v226, v226, v249\n"
    "v_mul_f32 v230, v230, v251\n"
    "v_mul_f32 v219, v219, v245\n"
    "v_mul_f32 v223, v223, v247\n"
    "v_mul_f32 v227, v227, v249\n"
    "v_mul_f32 v231, v231, v251\n"
    "v_accvgpr_write_b32 a32, v216\n"
    "v_accvgpr_write_b32 a80, v220\n"
    "v_accvgpr_write_b32 a128, v224\n"
    "v_accvgpr_write_b32 a176, v228\n"
    "v_accvgpr_write_b32 a33, v217\n"
    "v_accvgpr_write_b32 a81, v221\n"
    "v_accvgpr_write_b32 a129, v225\n"
    "v_accvgpr_write_b32 a177, v229\n"
    "v_accvgpr_write_b32 a34, v218\n"
    "v_accvgpr_write_b32 a82, v222\n"
    "v_accvgpr_write_b32 a130, v226\n"
    "v_accvgpr_write_b32 a178, v230\n"
    "v_accvgpr_write_b32 a35, v219\n"
    "v_accvgpr_write_b32 a83, v223\n"
    "v_accvgpr_write_b32 a131, v227\n"
    "v_accvgpr_write_b32 a179, v231\n"
    "v_accvgpr_read_b32 v216, a36\n"
    "v_accvgpr_read_b32 v220, a84\n"
    "v_accvgpr_read_b32 v224, a132\n"
    "v_accvgpr_read_b32 v228, a180\n"
    "v_accvgpr_read_b32 v217, a37\n"
    "v_accvgpr_read_b32 v221, a85\n"
    "v_accvgpr_read_b32 v225, a133\n"
    "v_accvgpr_read_b32 v229, a181\n"
    "v_accvgpr_read_b32 v218, a38\n"
    "v_accvgpr_read_b32 v222, a86\n"
    "v_accvgpr_read_b32 v226, a134\n"
    "v_accvgpr_read_b32 v230, a182\n"
    "v_accvgpr_read_b32 v219, a39\n"
    "v_accvgpr_read_b32 v223, a87\n"
    "v_accvgpr_read_b32 v227, a135\n"
    "v_accvgpr_read_b32 v231, a183\n"
    "v_mul_f32 v216, v216, v245\n"
    "v_mul_f32 v220, v220, v247\n"
    "v_mul_f32 v224, v224, v249\n"
    "v_mul_f32 v228, v228, v251\n"
    "v_mul_f32 v217, v217, v245\n"
    "v_mul_f32 v221, v221, v247\n"
    "v_mul_f32 v225, v225, v249\n"
    "v_mul_f32 v229, v229, v251\n"
    "v_mul_f32 v218, v218, v245\n"
    "v_mul_f32 v222, v222, v247\n"
    "v_mul_f32 v226, v226, v249\n"
    "v_mul_f32 v230, v230, v251\n"
    "v_mul_f32 v219, v219, v245\n"
    "v_mul_f32 v223, v223, v247\n"
    "v_mul_f32 v227, v227, v249\n"
    "v_mul_f32 v231, v231, v251\n"
    "v_accvgpr_write_b32 a36, v216\n"
    "v_accvgpr_write_b32 a84, v220\n"
    "v_accvgpr_write_b32 a132, v224\n"
    "v_accvgpr_write_b32 a180, v228\n"
    "v_accvgpr_write_b32 a37, v217\n"
    "v_accvgpr_write_b32 a85, v221\n"
    "v_accvgpr_write_b32 a133, v225\n"
    "v_accvgpr_write_b32 a181, v229\n"
    "v_accvgpr_write_b32 a38, v218\n"
    "v_accvgpr_write_b32 a86, v222\n"
    "v_accvgpr_write_b32 a134, v226\n"
    "v_accvgpr_write_b32 a182, v230\n"
    "v_accvgpr_write_b32 a39, v219\n"
    "v_accvgpr_write_b32 a87, v223\n"
    "v_accvgpr_write_b32 a135, v227\n"
    "v_accvgpr_write_b32 a183, v231\n"
    "v_accvgpr_read_b32 v216, a40\n"
    "v_accvgpr_read_b32 v220, a88\n"
    "v_accvgpr_read_b32 v224, a136\n"
    "v_accvgpr_read_b32 v228, a184\n"
    "v_accvgpr_read_b32 v217, a41\n"
    "v_accvgpr_read_b32 v221, a89\n"
    "v_accvgpr_read_b32 v225, a137\n"
    "v_accvgpr_read_b32 v229, a185\n"
    "v_accvgpr_read_b32 v218, a42\n"
    "v_accvgpr_read_b32 v222, a90\n"
    "v_accvgpr_read_b32 v226, a138\n"
    "v_accvgpr_read_b32 v230, a186\n"
    "v_accvgpr_read_b32 v219, a43\n"
    "v_accvgpr_read_b32 v223, a91\n"
    "v_accvgpr_read_b32 v227, a139\n"
    "v_accvgpr_read_b32 v231, a187\n"
    "v_mul_f32 v216, v216, v245\n"
    "v_mul_f32 v220, v220, v247\n"
    "v_mul_f32 v224, v224, v249\n"
    "v_mul_f32 v228, v228, v251\n"
    "v_mul_f32 v217, v217, v245\n"
    "v_mul_f32 v221, v221, v247\n"
    "v_mul_f32 v225, v225, v249\n"
    "v_mul_f32 v229, v229, v251\n"
    "v_mul_f32 v218, v218, v245\n"
    "v_mul_f32 v222, v222, v247\n"
    "v_mul_f32 v226, v226, v249\n"
    "v_mul_f32 v230, v230, v251\n"
    "v_mul_f32 v219, v219, v245\n"
    "v_mul_f32 v223, v223, v247\n"
    "v_mul_f32 v227, v227, v249\n"
    "v_mul_f32 v231, v231, v251\n"
    "v_accvgpr_write_b32 a40, v216\n"
    "v_accvgpr_write_b32 a88, v220\n"
    "v_accvgpr_write_b32 a136, v224\n"
    "v_accvgpr_write_b32 a184, v228\n"
    "v_accvgpr_write_b32 a41, v217\n"
    "v_accvgpr_write_b32 a89, v221\n"
    "v_accvgpr_write_b32 a137, v225\n"
    "v_accvgpr_write_b32 a185, v229\n"
    "v_accvgpr_write_b32 a42, v218\n"
    "v_accvgpr_write_b32 a90, v222\n"
    "v_accvgpr_write_b32 a138, v226\n"
    "v_accvgpr_write_b32 a186, v230\n"
    "v_accvgpr_write_b32 a43, v219\n"
    "v_accvgpr_write_b32 a91, v223\n"
    "v_accvgpr_write_b32 a139, v227\n"
    "v_accvgpr_write_b32 a187, v231\n"
    "v_accvgpr_read_b32 v216, a44\n"
    "v_accvgpr_read_b32 v220, a92\n"
    "v_accvgpr_read_b32 v224, a140\n"
    "v_accvgpr_read_b32 v228, a188\n"
    "v_accvgpr_read_b32 v217, a45\n"
    "v_accvgpr_read_b32 v221, a93\n"
    "v_accvgpr_read_b32 v225, a141\n"
    "v_accvgpr_read_b32 v229, a189\n"
    "v_accvgpr_read_b32 v218, a46\n"
    "v_accvgpr_read_b32 v222, a94\n"
    "v_accvgpr_read_b32 v226, a142\n"
    "v_accvgpr_read_b32 v230, a190\n"
    "v_accvgpr_read_b32 v219, a47\n"
    "v_accvgpr_read_b32 v223, a95\n"
    "v_accvgpr_read_b32 v227, a143\n"
    "v_accvgpr_read_b32 v231, a191\n"
    "v_mul_f32 v216, v216, v245\n"
    "v_mul_f32 v220, v220, v247\n"
    "v_mul_f32 v224, v224, v249\n"
    "v_mul_f32 v228, v228, v251\n"
    "v_mul_f32 v217, v217, v245\n"
    "v_mul_f32 v221, v221, v247\n"
    "v_mul_f32 v225, v225, v249\n"
    "v_mul_f32 v229, v229, v251\n"
    "v_mul_f32 v218, v218, v245\n"
    "v_mul_f32 v222, v222, v247\n"
    "v_mul_f32 v226, v226, v249\n"
    "v_mul_f32 v230, v230, v251\n"
    "v_mul_f32 v219, v219, v245\n"
    "v_mul_f32 v223, v223, v247\n"
    "v_mul_f32 v227, v227, v249\n"
    "v_mul_f32 v231, v231, v251\n"
    "v_accvgpr_write_b32 a44, v216\n"
    "v_accvgpr_write_b32 a92, v220\n"
    "v_accvgpr_write_b32 a140, v224\n"
    "v_accvgpr_write_b32 a188, v228\n"
    "v_accvgpr_write_b32 a45, v217\n"
    "v_accvgpr_write_b32 a93, v221\n"
    "v_accvgpr_write_b32 a141, v225\n"
    "v_accvgpr_write_b32 a189, v229\n"
    "v_accvgpr_write_b32 a46, v218\n"
    "v_accvgpr_write_b32 a94, v222\n"
    "v_accvgpr_write_b32 a142, v226\n"
    "v_accvgpr_write_b32 a190, v230\n"
    "v_accvgpr_write_b32 a47, v219\n"
    "v_accvgpr_write_b32 a95, v223\n"
    "v_accvgpr_write_b32 a143, v227\n"
    "v_accvgpr_write_b32 a191, v231\n"
    "v_exp_f32 v216, v128\n"
    "v_exp_f32 v220, v136\n"
    "v_exp_f32 v224, v144\n"
    "v_exp_f32 v228, v152\n"
    "v_exp_f32 v217, v129\n"
    "v_exp_f32 v221, v137\n"
    "v_exp_f32 v225, v145\n"
    "v_exp_f32 v229, v153\n"
    "v_exp_f32 v218, v130\n"
    "v_exp_f32 v222, v138\n"
    "v_exp_f32 v226, v146\n"
    "v_exp_f32 v230, v154\n"
    "v_exp_f32 v219, v131\n"
    "v_exp_f32 v223, v139\n"
    "v_exp_f32 v227, v147\n"
    "v_exp_f32 v231, v155\n"
    "v_cvt_pk_bf16_f32 v176, v216, v217\n"
    "v_cvt_pk_bf16_f32 v180, v220, v221\n"
    "v_cvt_pk_bf16_f32 v184, v224, v225\n"
    "v_cvt_pk_bf16_f32 v188, v228, v229\n"
    "v_cvt_pk_bf16_f32 v177, v218, v219\n"
    "v_cvt_pk_bf16_f32 v181, v222, v223\n"
    "v_cvt_pk_bf16_f32 v185, v226, v227\n"
    "v_cvt_pk_bf16_f32 v189, v230, v231\n"
    "v_add_f32 v212, v216, v217\n"
    "v_add_f32 v213, v220, v221\n"
    "v_add_f32 v214, v224, v225\n"
    "v_add_f32 v215, v228, v229\n"
    "v_add_f32 v212, v212, v218\n"
    "v_add_f32 v213, v213, v222\n"
    "v_add_f32 v214, v214, v226\n"
    "v_add_f32 v215, v215, v230\n"
    "v_add_f32 v212, v212, v219\n"
    "v_add_f32 v213, v213, v223\n"
    "v_add_f32 v214, v214, v227\n"
    "v_add_f32 v215, v215, v231\n"
    "v_exp_f32 v216, v132\n"
    "v_exp_f32 v220, v140\n"
    "v_exp_f32 v224, v148\n"
    "v_exp_f32 v228, v156\n"
    "v_exp_f32 v217, v133\n"
    "v_exp_f32 v221, v141\n"
    "v_exp_f32 v225, v149\n"
    "v_exp_f32 v229, v157\n"
    "v_exp_f32 v218, v134\n"
    "v_exp_f32 v222, v142\n"
    "v_exp_f32 v226, v150\n"
    "v_exp_f32 v230, v158\n"
    "v_exp_f32 v219, v135\n"
    "v_exp_f32 v223, v143\n"
    "v_exp_f32 v227, v151\n"
    "v_exp_f32 v231, v159\n"
    "v_cvt_pk_bf16_f32 v178, v216, v217\n"
    "v_cvt_pk_bf16_f32 v182, v220, v221\n"
    "v_cvt_pk_bf16_f32 v186, v224, v225\n"
    "v_cvt_pk_bf16_f32 v190, v228, v229\n"
    "v_cvt_pk_bf16_f32 v179, v218, v219\n"
    "v_cvt_pk_bf16_f32 v183, v222, v223\n"
    "v_cvt_pk_bf16_f32 v187, v226, v227\n"
    "v_cvt_pk_bf16_f32 v191, v230, v231\n"
    "v_add_f32 v212, v212, v216\n"
    "v_add_f32 v213, v213, v220\n"
    "v_add_f32 v214, v214, v224\n"
    "v_add_f32 v215, v215, v228\n"
    "v_add_f32 v212, v212, v217\n"
    "v_add_f32 v213, v213, v221\n"
    "v_add_f32 v214, v214, v225\n"
    "v_add_f32 v215, v215, v229\n"
    "v_add_f32 v212, v212, v218\n"
    "v_add_f32 v213, v213, v222\n"
    "v_add_f32 v214, v214, v226\n"
    "v_add_f32 v215, v215, v230\n"
    "v_add_f32 v212, v212, v219\n"
    "v_add_f32 v213, v213, v223\n"
    "v_add_f32 v214, v214, v227\n"
    "v_add_f32 v215, v215, v231\n"
    "s_nop 3\n"
    "s_cmp_eq_u32 s57, 0\n"
    "s_cbranch_scc1 LRET_o_4_L_%=\n"
    "s_cmp_eq_u32 s57, 1\n"
    "s_cbranch_scc1 LRET_o_4_F0_%=\n"
    "s_cmp_eq_u32 s57, 2\n"
    "s_cbranch_scc1 LRET_o_4_F1_%=\n"
    "s_branch LRET_o_4_P0_%=\n"
    "LRARE_e_3_%=:\n"
    "s_nop 7\n"
    "s_nop 7\n"
    "v_max3_f32 v244, v96, v97, v98\n"
    "v_max3_f32 v246, v104, v105, v106\n"
    "v_max3_f32 v248, v112, v113, v114\n"
    "v_max3_f32 v244, v244, v99, v100\n"
    "v_max3_f32 v246, v246, v107, v108\n"
    "v_max3_f32 v248, v248, v115, v116\n"
    "v_max3_f32 v244, v244, v101, v102\n"
    "v_max3_f32 v246, v246, v109, v110\n"
    "v_max3_f32 v248, v248, v117, v118\n"
    "v_max_f32 v244, v244, v103\n"
    "v_max_f32 v246, v246, v111\n"
    "v_max_f32 v248, v248, v119\n"
    "v_mov_b32 v245, v244\n"
    "v_mov_b32 v247, v246\n"
    "v_mov_b32 v249, v248\n"
    "v_permlane32_swap_b32 v244, v245\n"
    "v_permlane32_swap_b32 v246, v247\n"
    "v_permlane32_swap_b32 v248, v249\n"
    "v_max_f32 v244, v244, v245\n"
    "v_max_f32 v246, v246, v247\n"
    "v_max_f32 v248, v248, v249\n"
    "v_mov_b32 v245, v244\n"
    "v_mov_b32 v247, v246\n"
    "v_mov_b32 v249, v248\n"
    "v_permlane16_swap_b32 v244, v245\n"
    "v_permlane16_swap_b32 v246, v247\n"
    "v_permlane16_swap_b32 v248, v249\n"
    "v_max_f32 v244, v244, v245\n"
    "v_max_f32 v246, v246, v247\n"
    "v_max_f32 v248, v248, v249\n"
    "v_max_f32 v244, 0, v244\n"
    "v_max_f32 v246, 0, v246\n"
    "v_max_f32 v248, 0, v248\n"
    "v_exp_f32 v245, -v244\n"
    "v_exp_f32 v247, -v246\n"
    "v_exp_f32 v249, -v248\n"
    "v_sub_f32 v96, v96, v244\n"
    "v_sub_f32 v104, v104, v246\n"
    "v_sub_f32 v112, v112, v248\n"
    "v_sub_f32 v97, v97, v244\n"
    "v_sub_f32 v105, v105, v246\n"
    "v_sub_f32 v113, v113, v248\n"
    "v_sub_f32 v98, v98, v244\n"
    "v_sub_f32 v106, v106, v246\n"
    "v_sub_f32 v114, v114, v248\n"
    "v_sub_f32 v99, v99, v244\n"
    "v_sub_f32 v107, v107, v246\n"
    "v_sub_f32 v115, v115, v248\n"
    "v_sub_f32 v100, v100, v244\n"
    "v_sub_f32 v108, v108, v246\n"
    "v_sub_f32 v116, v116, v248\n"
    "v_sub_f32 v101, v101, v244\n"
    "v_sub_f32 v109, v109, v246\n"
    "v_sub_f32 v117, v117, v248\n"
    "v_sub_f32 v102, v102, v244\n"
    "v_sub_f32 v110, v110, v246\n"
    "v_sub_f32 v118, v118, v248\n"
    "v_sub_f32 v103, v103, v244\n"
    "v_sub_f32 v111, v111, v246\n"
    "v_sub_f32 v119, v119, v248\n"
    "v_sub_f32 v128, v128, v244\n"
    "v_sub_f32 v136, v136, v246\n"
    "v_sub_f32 v144, v144, v248\n"
    "v_sub_f32 v129, v129, v244\n"
    "v_sub_f32 v137, v137, v246\n"
    "v_sub_f32 v145, v145, v248\n"
    "v_sub_f32 v130, v130, v244\n"
    "v_sub_f32 v138, v138, v246\n"
    "v_sub_f32 v146, v146, v248\n"
    "v_sub_f32 v131, v131, v244\n"
    "v_sub_f32 v139, v139, v246\n"
    "v_sub_f32 v147, v147, v248\n"
    "v_sub_f32 v132, v132, v244\n"
    "v_sub_f32 v140, v140, v246\n"
    "v_sub_f32 v148, v148, v248\n"
    "v_sub_f32 v133, v133, v244\n"
    "v_sub_f32 v141, v141, v246\n"
    "v_sub_f32 v149, v149, v248\n"
    "v_sub_f32 v134, v134, v244\n"
    "v_sub_f32 v142, v142, v246\n"
    "v_sub_f32 v150, v150, v248\n"
    "v_sub_f32 v135, v135, v244\n"
    "v_sub_f32 v143, v143, v246\n"
    "v_sub_f32 v151, v151, v248\n"
    "v_sub_f32 v192, v192, v244\n"
    "v_sub_f32 v196, v196, v246\n"
    "v_sub_f32 v200, v200, v248\n"
    "v_sub_f32 v193, v193, v244\n"
    "v_sub_f32 v197, v197, v246\n"
    "v_sub_f32 v201, v201, v248\n"
    "v_sub_f32 v194, v194, v244\n"
    "v_sub_f32 v198, v198, v246\n"
    "v_sub_f32 v202, v202, v248\n"
    "v_sub_f32 v195, v195, v244\n"
    "v_sub_f32 v199, v199, v246\n"
    "v_sub_f32 v203, v203, v248\n"
    "v_mul_f32 v208, v208, v245\n"
    "v_mul_f32 v209, v209, v247\n"
    "v_mul_f32 v210, v210, v249\n"
    "v_accvgpr_read_b32 v216, a0\n"
    "v_accvgpr_read_b32 v220, a48\n"
    "v_accvgpr_read_b32 v224, a96\n"
    "v_accvgpr_read_b32 v217, a1\n"
    "v_accvgpr_read_b32 v221, a49\n"
    "v_accvgpr_read_b32 v225, a97\n"
    "v_accvgpr_read_b32 v218, a2\n"
    "v_accvgpr_read_b32 v222, a50\n"
    "v_accvgpr_read_b32 v226, a98\n"
    "v_accvgpr_read_b32 v219, a3\n"
    "v_accvgpr_read_b32 v223, a51\n"
    "v_accvgpr_read_b32 v227, a99\n"
    "v_mul_f32 v216, v216, v245\n"
    "v_mul_f32 v220, v220, v247\n"
    "v_mul_f32 v224, v224, v249\n"
    "v_mul_f32 v217, v217, v245\n"
    "v_mul_f32 v221, v221, v247\n"
    "v_mul_f32 v225, v225, v249\n"
    "v_mul_f32 v218, v218, v245\n"
    "v_mul_f32 v222, v222, v247\n"
    "v_mul_f32 v226, v226, v249\n"
    "v_mul_f32 v219, v219, v245\n"
    "v_mul_f32 v223, v223, v247\n"
    "v_mul_f32 v227, v227, v249\n"
    "v_accvgpr_write_b32 a0, v216\n"
    "v_accvgpr_write_b32 a48, v220\n"
    "v_accvgpr_write_b32 a96, v224\n"
    "v_accvgpr_write_b32 a1, v217\n"
    "v_accvgpr_write_b32 a49, v221\n"
    "v_accvgpr_write_b32 a97, v225\n"
    "v_accvgpr_write_b32 a2, v218\n"
    "v_accvgpr_write_b32 a50, v222\n"
    "v_accvgpr_write_b32 a98, v226\n"
    "v_accvgpr_write_b32 a3, v219\n"
    "v_accvgpr_write_b32 a51, v223\n"
    "v_accvgpr_write_b32 a99, v227\n"
    "v_accvgpr_read_b32 v216, a4\n"
    "v_accvgpr_read_b32 v220, a52\n"
    "v_accvgpr_read_b32 v224, a100\n"
    "v_accvgpr_read_b32 v217, a5\n"
    "v_accvgpr_read_b32 v221, a53\n"
    "v_accvgpr_read_b32 v225, a101\n"
    "v_accvgpr_read_b32 v218, a6\n"
    "v_accvgpr_read_b32 v222, a54\n"
    "v_accvgpr_read_b32 v226, a102\n"
    "v_accvgpr_read_b32 v219, a7\n"
    "v_accvgpr_read_b32 v223, a55\n"
    "v_accvgpr_read_b32 v227, a103\n"
    "v_mul_f32 v216, v216, v245\n"
    "v_mul_f32 v220, v220, v247\n"
    "v_mul_f32 v224, v224, v249\n"
    "v_mul_f32 v217, v217, v245\n"
    "v_mul_f32 v221, v221, v247\n"
    "v_mul_f32 v225, v225, v249\n"
    "v_mul_f32 v218, v218, v245\n"
    "v_mul_f32 v222, v222, v247\n"
    "v_mul_f32 v226, v226, v249\n"
    "v_mul_f32 v219, v219, v245\n"
    "v_mul_f32 v223, v223, v247\n"
    "v_mul_f32 v227, v227, v249\n"
    "v_accvgpr_write_b32 a4, v216\n"
    "v_accvgpr_write_b32 a52, v220\n"
    "v_accvgpr_write_b32 a100, v224\n"
    "v_accvgpr_write_b32 a5, v217\n"
    "v_accvgpr_write_b32 a53, v221\n"
    "v_accvgpr_write_b32 a101, v225\n"
    "v_accvgpr_write_b32 a6, v218\n"
    "v_accvgpr_write_b32 a54, v222\n"
    "v_accvgpr_write_b32 a102, v226\n"
    "v_accvgpr_write_b32 a7, v219\n"
    "v_accvgpr_write_b32 a55, v223\n"
    "v_accvgpr_write_b32 a103, v227\n"
    "v_accvgpr_read_b32 v216, a8\n"
    "v_accvgpr_read_b32 v220, a56\n"
    "v_accvgpr_read_b32 v224, a104\n"
    "v_accvgpr_read_b32 v217, a9\n"
    "v_accvgpr_read_b32 v221, a57\n"
    "v_accvgpr_read_b32 v225, a105\n"
    "v_accvgpr_read_b32 v218, a10\n"
    "v_accvgpr_read_b32 v222, a58\n"
    "v_accvgpr_read_b32 v226, a106\n"
    "v_accvgpr_read_b32 v219, a11\n"
    "v_accvgpr_read_b32 v223, a59\n"
    "v_accvgpr_read_b32 v227, a107\n"
    "v_mul_f32 v216, v216, v245\n"
    "v_mul_f32 v220, v220, v247\n"
    "v_mul_f32 v224, v224, v249\n"
    "v_mul_f32 v217, v217, v245\n"
    "v_mul_f32 v221, v221, v247\n"
    "v_mul_f32 v225, v225, v249\n"
    "v_mul_f32 v218, v218, v245\n"
    "v_mul_f32 v222, v222, v247\n"
    "v_mul_f32 v226, v226, v249\n"
    "v_mul_f32 v219, v219, v245\n"
    "v_mul_f32 v223, v223, v247\n"
    "v_mul_f32 v227, v227, v249\n"
    "v_accvgpr_write_b32 a8, v216\n"
    "v_accvgpr_write_b32 a56, v220\n"
    "v_accvgpr_write_b32 a104, v224\n"
    "v_accvgpr_write_b32 a9, v217\n"
    "v_accvgpr_write_b32 a57, v221\n"
    "v_accvgpr_write_b32 a105, v225\n"
    "v_accvgpr_write_b32 a10, v218\n"
    "v_accvgpr_write_b32 a58, v222\n"
    "v_accvgpr_write_b32 a106, v226\n"
    "v_accvgpr_write_b32 a11, v219\n"
    "v_accvgpr_write_b32 a59, v223\n"
    "v_accvgpr_write_b32 a107, v227\n"
    "v_accvgpr_read_b32 v216, a12\n"
    "v_accvgpr_read_b32 v220, a60\n"
    "v_accvgpr_read_b32 v224, a108\n"
    "v_accvgpr_read_b32 v217, a13\n"
    "v_accvgpr_read_b32 v221, a61\n"
    "v_accvgpr_read_b32 v225, a109\n"
    "v_accvgpr_read_b32 v218, a14\n"
    "v_accvgpr_read_b32 v222, a62\n"
    "v_accvgpr_read_b32 v226, a110\n"
    "v_accvgpr_read_b32 v219, a15\n"
    "v_accvgpr_read_b32 v223, a63\n"
    "v_accvgpr_read_b32 v227, a111\n"
    "v_mul_f32 v216, v216, v245\n"
    "v_mul_f32 v220, v220, v247\n"
    "v_mul_f32 v224, v224, v249\n"
    "v_mul_f32 v217, v217, v245\n"
    "v_mul_f32 v221, v221, v247\n"
    "v_mul_f32 v225, v225, v249\n"
    "v_mul_f32 v218, v218, v245\n"
    "v_mul_f32 v222, v222, v247\n"
    "v_mul_f32 v226, v226, v249\n"
    "v_mul_f32 v219, v219, v245\n"
    "v_mul_f32 v223, v223, v247\n"
    "v_mul_f32 v227, v227, v249\n"
    "v_accvgpr_write_b32 a12, v216\n"
    "v_accvgpr_write_b32 a60, v220\n"
    "v_accvgpr_write_b32 a108, v224\n"
    "v_accvgpr_write_b32 a13, v217\n"
    "v_accvgpr_write_b32 a61, v221\n"
    "v_accvgpr_write_b32 a109, v225\n"
    "v_accvgpr_write_b32 a14, v218\n"
    "v_accvgpr_write_b32 a62, v222\n"
    "v_accvgpr_write_b32 a110, v226\n"
    "v_accvgpr_write_b32 a15, v219\n"
    "v_accvgpr_write_b32 a63, v223\n"
    "v_accvgpr_write_b32 a111, v227\n"
    "v_accvgpr_read_b32 v216, a16\n"
    "v_accvgpr_read_b32 v220, a64\n"
    "v_accvgpr_read_b32 v224, a112\n"
    "v_accvgpr_read_b32 v217, a17\n"
    "v_accvgpr_read_b32 v221, a65\n"
    "v_accvgpr_read_b32 v225, a113\n"
    "v_accvgpr_read_b32 v218, a18\n"
    "v_accvgpr_read_b32 v222, a66\n"
    "v_accvgpr_read_b32 v226, a114\n"
    "v_accvgpr_read_b32 v219, a19\n"
    "v_accvgpr_read_b32 v223, a67\n"
    "v_accvgpr_read_b32 v227, a115\n"
    "v_mul_f32 v216, v216, v245\n"
    "v_mul_f32 v220, v220, v247\n"
    "v_mul_f32 v224, v224, v249\n"
    "v_mul_f32 v217, v217, v245\n"
    "v_mul_f32 v221, v221, v247\n"
    "v_mul_f32 v225, v225, v249\n"
    "v_mul_f32 v218, v218, v245\n"
    "v_mul_f32 v222, v222, v247\n"
    "v_mul_f32 v226, v226, v249\n"
    "v_mul_f32 v219, v219, v245\n"
    "v_mul_f32 v223, v223, v247\n"
    "v_mul_f32 v227, v227, v249\n"
    "v_accvgpr_write_b32 a16, v216\n"
    "v_accvgpr_write_b32 a64, v220\n"
    "v_accvgpr_write_b32 a112, v224\n"
    "v_accvgpr_write_b32 a17, v217\n"
    "v_accvgpr_write_b32 a65, v221\n"
    "v_accvgpr_write_b32 a113, v225\n"
    "v_accvgpr_write_b32 a18, v218\n"
    "v_accvgpr_write_b32 a66, v222\n"
    "v_accvgpr_write_b32 a114, v226\n"
    "v_accvgpr_write_b32 a19, v219\n"
    "v_accvgpr_write_b32 a67, v223\n"
    "v_accvgpr_write_b32 a115, v227\n"
    "v_accvgpr_read_b32 v216, a20\n"
    "v_accvgpr_read_b32 v220, a68\n"
    "v_accvgpr_read_b32 v224, a116\n"
    "v_accvgpr_read_b32 v217, a21\n"
    "v_accvgpr_read_b32 v221, a69\n"
    "v_accvgpr_read_b32 v225, a117\n"
    "v_accvgpr_read_b32 v218, a22\n"
    "v_accvgpr_read_b32 v222, a70\n"
    "v_accvgpr_read_b32 v226, a118\n"
    "v_accvgpr_read_b32 v219, a23\n"
    "v_accvgpr_read_b32 v223, a71\n"
    "v_accvgpr_read_b32 v227, a119\n"
    "v_mul_f32 v216, v216, v245\n"
    "v_mul_f32 v220, v220, v247\n"
    "v_mul_f32 v224, v224, v249\n"
    "v_mul_f32 v217, v217, v245\n"
    "v_mul_f32 v221, v221, v247\n"
    "v_mul_f32 v225, v225, v249\n"
    "v_mul_f32 v218, v218, v245\n"
    "v_mul_f32 v222, v222, v247\n"
    "v_mul_f32 v226, v226, v249\n"
    "v_mul_f32 v219, v219, v245\n"
    "v_mul_f32 v223, v223, v247\n"
    "v_mul_f32 v227, v227, v249\n"
    "v_accvgpr_write_b32 a20, v216\n"
    "v_accvgpr_write_b32 a68, v220\n"
    "v_accvgpr_write_b32 a116, v224\n"
    "v_accvgpr_write_b32 a21, v217\n"
    "v_accvgpr_write_b32 a69, v221\n"
    "v_accvgpr_write_b32 a117, v225\n"
    "v_accvgpr_write_b32 a22, v218\n"
    "v_accvgpr_write_b32 a70, v222\n"
    "v_accvgpr_write_b32 a118, v226\n"
    "v_accvgpr_write_b32 a23, v219\n"
    "v_accvgpr_write_b32 a71, v223\n"
    "v_accvgpr_write_b32 a119, v227\n"
    "v_accvgpr_read_b32 v216, a24\n"
    "v_accvgpr_read_b32 v220, a72\n"
    "v_accvgpr_read_b32 v224, a120\n"
    "v_accvgpr_read_b32 v217, a25\n"
    "v_accvgpr_read_b32 v221, a73\n"
    "v_accvgpr_read_b32 v225, a121\n"
    "v_accvgpr_read_b32 v218, a26\n"
    "v_accvgpr_read_b32 v222, a74\n"
    "v_accvgpr_read_b32 v226, a122\n"
    "v_accvgpr_read_b32 v219, a27\n"
    "v_accvgpr_read_b32 v223, a75\n"
    "v_accvgpr_read_b32 v227, a123\n"
    "v_mul_f32 v216, v216, v245\n"
    "v_mul_f32 v220, v220, v247\n"
    "v_mul_f32 v224, v224, v249\n"
    "v_mul_f32 v217, v217, v245\n"
    "v_mul_f32 v221, v221, v247\n"
    "v_mul_f32 v225, v225, v249\n"
    "v_mul_f32 v218, v218, v245\n"
    "v_mul_f32 v222, v222, v247\n"
    "v_mul_f32 v226, v226, v249\n"
    "v_mul_f32 v219, v219, v245\n"
    "v_mul_f32 v223, v223, v247\n"
    "v_mul_f32 v227, v227, v249\n"
    "v_accvgpr_write_b32 a24, v216\n"
    "v_accvgpr_write_b32 a72, v220\n"
    "v_accvgpr_write_b32 a120, v224\n"
    "v_accvgpr_write_b32 a25, v217\n"
    "v_accvgpr_write_b32 a73, v221\n"
    "v_accvgpr_write_b32 a121, v225\n"
    "v_accvgpr_write_b32 a26, v218\n"
    "v_accvgpr_write_b32 a74, v222\n"
    "v_accvgpr_write_b32 a122, v226\n"
    "v_accvgpr_write_b32 a27, v219\n"
    "v_accvgpr_write_b32 a75, v223\n"
    "v_accvgpr_write_b32 a123, v227\n"
    "v_accvgpr_read_b32 v216, a28\n"
    "v_accvgpr_read_b32 v220, a76\n"
    "v_accvgpr_read_b32 v224, a124\n"
    "v_accvgpr_read_b32 v217, a29\n"
    "v_accvgpr_read_b32 v221, a77\n"
    "v_accvgpr_read_b32 v225, a125\n"
    "v_accvgpr_read_b32 v218, a30\n"
    "v_accvgpr_read_b32 v222, a78\n"
    "v_accvgpr_read_b32 v226, a126\n"
    "v_accvgpr_read_b32 v219, a31\n"
    "v_accvgpr_read_b32 v223, a79\n"
    "v_accvgpr_read_b32 v227, a127\n"
    "v_mul_f32 v216, v216, v245\n"
    "v_mul_f32 v220, v220, v247\n"
    "v_mul_f32 v224, v224, v249\n"
    "v_mul_f32 v217, v217, v245\n"
    "v_mul_f32 v221, v221, v247\n"
    "v_mul_f32 v225, v225, v249\n"
    "v_mul_f32 v218, v218, v245\n"
    "v_mul_f32 v222, v222, v247\n"
    "v_mul_f32 v226, v226, v249\n"
    "v_mul_f32 v219, v219, v245\n"
    "v_mul_f32 v223, v223, v247\n"
    "v_mul_f32 v227, v227, v249\n"
    "v_accvgpr_write_b32 a28, v216\n"
    "v_accvgpr_write_b32 a76, v220\n"
    "v_accvgpr_write_b32 a124, v224\n"
    "v_accvgpr_write_b32 a29, v217\n"
    "v_accvgpr_write_b32 a77, v221\n"
    "v_accvgpr_write_b32 a125, v225\n"
    "v_accvgpr_write_b32 a30, v218\n"
    "v_accvgpr_write_b32 a78, v222\n"
    "v_accvgpr_write_b32 a126, v226\n"
    "v_accvgpr_write_b32 a31, v219\n"
    "v_accvgpr_write_b32 a79, v223\n"
    "v_accvgpr_write_b32 a127, v227\n"
    "v_accvgpr_read_b32 v216, a32\n"
    "v_accvgpr_read_b32 v220, a80\n"
    "v_accvgpr_read_b32 v224, a128\n"
    "v_accvgpr_read_b32 v217, a33\n"
    "v_accvgpr_read_b32 v221, a81\n"
    "v_accvgpr_read_b32 v225, a129\n"
    "v_accvgpr_read_b32 v218, a34\n"
    "v_accvgpr_read_b32 v222, a82\n"
    "v_accvgpr_read_b32 v226, a130\n"
    "v_accvgpr_read_b32 v219, a35\n"
    "v_accvgpr_read_b32 v223, a83\n"
    "v_accvgpr_read_b32 v227, a131\n"
    "v_mul_f32 v216, v216, v245\n"
    "v_mul_f32 v220, v220, v247\n"
    "v_mul_f32 v224, v224, v249\n"
    "v_mul_f32 v217, v217, v245\n"
    "v_mul_f32 v221, v221, v247\n"
    "v_mul_f32 v225, v225, v249\n"
    "v_mul_f32 v218, v218, v245\n"
    "v_mul_f32 v222, v222, v247\n"
    "v_mul_f32 v226, v226, v249\n"
    "v_mul_f32 v219, v219, v245\n"
    "v_mul_f32 v223, v223, v247\n"
    "v_mul_f32 v227, v227, v249\n"
    "v_accvgpr_write_b32 a32, v216\n"
    "v_accvgpr_write_b32 a80, v220\n"
    "v_accvgpr_write_b32 a128, v224\n"
    "v_accvgpr_write_b32 a33, v217\n"
    "v_accvgpr_write_b32 a81, v221\n"
    "v_accvgpr_write_b32 a129, v225\n"
    "v_accvgpr_write_b32 a34, v218\n"
    "v_accvgpr_write_b32 a82, v222\n"
    "v_accvgpr_write_b32 a130, v226\n"
    "v_accvgpr_write_b32 a35, v219\n"
    "v_accvgpr_write_b32 a83, v223\n"
    "v_accvgpr_write_b32 a131, v227\n"
    "v_accvgpr_read_b32 v216, a36\n"
    "v_accvgpr_read_b32 v220, a84\n"
    "v_accvgpr_read_b32 v224, a132\n"
    "v_accvgpr_read_b32 v217, a37\n"
    "v_accvgpr_read_b32 v221, a85\n"
    "v_accvgpr_read_b32 v225, a133\n"
    "v_accvgpr_read_b32 v218, a38\n"
    "v_accvgpr_read_b32 v222, a86\n"
    "v_accvgpr_read_b32 v226, a134\n"
    "v_accvgpr_read_b32 v219, a39\n"
    "v_accvgpr_read_b32 v223, a87\n"
    "v_accvgpr_read_b32 v227, a135\n"
    "v_mul_f32 v216, v216, v245\n"
    "v_mul_f32 v220, v220, v247\n"
    "v_mul_f32 v224, v224, v249\n"
    "v_mul_f32 v217, v217, v245\n"
    "v_mul_f32 v221, v221, v247\n"
    "v_mul_f32 v225, v225, v249\n"
    "v_mul_f32 v218, v218, v245\n"
    "v_mul_f32 v222, v222, v247\n"
    "v_mul_f32 v226, v226, v249\n"
    "v_mul_f32 v219, v219, v245\n"
    "v_mul_f32 v223, v223, v247\n"
    "v_mul_f32 v227, v227, v249\n"
    "v_accvgpr_write_b32 a36, v216\n"
    "v_accvgpr_write_b32 a84, v220\n"
    "v_accvgpr_write_b32 a132, v224\n"
    "v_accvgpr_write_b32 a37, v217\n"
    "v_accvgpr_write_b32 a85, v221\n"
    "v_accvgpr_write_b32 a133, v225\n"
    "v_accvgpr_write_b32 a38, v218\n"
    "v_accvgpr_write_b32 a86, v222\n"
    "v_accvgpr_write_b32 a134, v226\n"
    "v_accvgpr_write_b32 a39, v219\n"
    "v_accvgpr_write_b32 a87, v223\n"
    "v_accvgpr_write_b32 a135, v227\n"
    "v_accvgpr_read_b32 v216, a40\n"
    "v_accvgpr_read_b32 v220, a88\n"
    "v_accvgpr_read_b32 v224, a136\n"
    "v_accvgpr_read_b32 v217, a41\n"
    "v_accvgpr_read_b32 v221, a89\n"
    "v_accvgpr_read_b32 v225, a137\n"
    "v_accvgpr_read_b32 v218, a42\n"
    "v_accvgpr_read_b32 v222, a90\n"
    "v_accvgpr_read_b32 v226, a138\n"
    "v_accvgpr_read_b32 v219, a43\n"
    "v_accvgpr_read_b32 v223, a91\n"
    "v_accvgpr_read_b32 v227, a139\n"
    "v_mul_f32 v216, v216, v245\n"
    "v_mul_f32 v220, v220, v247\n"
    "v_mul_f32 v224, v224, v249\n"
    "v_mul_f32 v217, v217, v245\n"
    "v_mul_f32 v221, v221, v247\n"
    "v_mul_f32 v225, v225, v249\n"
    "v_mul_f32 v218, v218, v245\n"
    "v_mul_f32 v222, v222, v247\n"
    "v_mul_f32 v226, v226, v249\n"
    "v_mul_f32 v219, v219, v245\n"
    "v_mul_f32 v223, v223, v247\n"
    "v_mul_f32 v227, v227, v249\n"
    "v_accvgpr_write_b32 a40, v216\n"
    "v_accvgpr_write_b32 a88, v220\n"
    "v_accvgpr_write_b32 a136, v224\n"
    "v_accvgpr_write_b32 a41, v217\n"
    "v_accvgpr_write_b32 a89, v221\n"
    "v_accvgpr_write_b32 a137, v225\n"
    "v_accvgpr_write_b32 a42, v218\n"
    "v_accvgpr_write_b32 a90, v222\n"
    "v_accvgpr_write_b32 a138, v226\n"
    "v_accvgpr_write_b32 a43, v219\n"
    "v_accvgpr_write_b32 a91, v223\n"
    "v_accvgpr_write_b32 a139, v227\n"
    "v_accvgpr_read_b32 v216, a44\n"
    "v_accvgpr_read_b32 v220, a92\n"
    "v_accvgpr_read_b32 v224, a140\n"
    "v_accvgpr_read_b32 v217, a45\n"
    "v_accvgpr_read_b32 v221, a93\n"
    "v_accvgpr_read_b32 v225, a141\n"
    "v_accvgpr_read_b32 v218, a46\n"
    "v_accvgpr_read_b32 v222, a94\n"
    "v_accvgpr_read_b32 v226, a142\n"
    "v_accvgpr_read_b32 v219, a47\n"
    "v_accvgpr_read_b32 v223, a95\n"
    "v_accvgpr_read_b32 v227, a143\n"
    "v_mul_f32 v216, v216, v245\n"
    "v_mul_f32 v220, v220, v247\n"
    "v_mul_f32 v224, v224, v249\n"
    "v_mul_f32 v217, v217, v245\n"
    "v_mul_f32 v221, v221, v247\n"
    "v_mul_f32 v225, v225, v249\n"
    "v_mul_f32 v218, v218, v245\n"
    "v_mul_f32 v222, v222, v247\n"
    "v_mul_f32 v226, v226, v249\n"
    "v_mul_f32 v219, v219, v245\n"
    "v_mul_f32 v223, v223, v247\n"
    "v_mul_f32 v227, v227, v249\n"
    "v_accvgpr_write_b32 a44, v216\n"
    "v_accvgpr_write_b32 a92, v220\n"
    "v_accvgpr_write_b32 a140, v224\n"
    "v_accvgpr_write_b32 a45, v217\n"
    "v_accvgpr_write_b32 a93, v221\n"
    "v_accvgpr_write_b32 a141, v225\n"
    "v_accvgpr_write_b32 a46, v218\n"
    "v_accvgpr_write_b32 a94, v222\n"
    "v_accvgpr_write_b32 a142, v226\n"
    "v_accvgpr_write_b32 a47, v219\n"
    "v_accvgpr_write_b32 a95, v223\n"
    "v_accvgpr_write_b32 a143, v227\n"
    "v_exp_f32 v216, v96\n"
    "v_exp_f32 v220, v104\n"
    "v_exp_f32 v224, v112\n"
    "v_exp_f32 v217, v97\n"
    "v_exp_f32 v221, v105\n"
    "v_exp_f32 v225, v113\n"
    "v_exp_f32 v218, v98\n"
    "v_exp_f32 v222, v106\n"
    "v_exp_f32 v226, v114\n"
    "v_exp_f32 v219, v99\n"
    "v_exp_f32 v223, v107\n"
    "v_exp_f32 v227, v115\n"
    "v_cvt_pk_bf16_f32 v160, v216, v217\n"
    "v_cvt_pk_bf16_f32 v164, v220, v221\n"
    "v_cvt_pk_bf16_f32 v168, v224, v225\n"
    "v_cvt_pk_bf16_f32 v161, v218, v219\n"
    "v_cvt_pk_bf16_f32 v165, v222, v223\n"
    "v_cvt_pk_bf16_f32 v169, v226, v227\n"
    "v_add_f32 v212, v216, v217\n"
    "v_add_f32 v213, v220, v221\n"
    "v_add_f32 v214, v224, v225\n"
    "v_add_f32 v212, v212, v218\n"
    "v_add_f32 v213, v213, v222\n"
    "v_add_f32 v214, v214, v226\n"
    "v_add_f32 v212, v212, v219\n"
    "v_add_f32 v213, v213, v223\n"
    "v_add_f32 v214, v214, v227\n"
    "v_exp_f32 v216, v100\n"
    "v_exp_f32 v220, v108\n"
    "v_exp_f32 v224, v116\n"
    "v_exp_f32 v217, v101\n"
    "v_exp_f32 v221, v109\n"
    "v_exp_f32 v225, v117\n"
    "v_exp_f32 v218, v102\n"
    "v_exp_f32 v222, v110\n"
    "v_exp_f32 v226, v118\n"
    "v_exp_f32 v219, v103\n"
    "v_exp_f32 v223, v111\n"
    "v_exp_f32 v227, v119\n"
    "v_cvt_pk_bf16_f32 v162, v216, v217\n"
    "v_cvt_pk_bf16_f32 v166, v220, v221\n"
    "v_cvt_pk_bf16_f32 v170, v224, v225\n"
    "v_cvt_pk_bf16_f32 v163, v218, v219\n"
    "v_cvt_pk_bf16_f32 v167, v222, v223\n"
    "v_cvt_pk_bf16_f32 v171, v226, v227\n"
    "v_add_f32 v212, v212, v216\n"
    "v_add_f32 v213, v213, v220\n"
    "v_add_f32 v214, v214, v224\n"
    "v_add_f32 v212, v212, v217\n"
    "v_add_f32 v213, v213, v221\n"
    "v_add_f32 v214, v214, v225\n"
    "v_add_f32 v212, v212, v218\n"
    "v_add_f32 v213, v213, v222\n"
    "v_add_f32 v214, v214, v226\n"
    "v_add_f32 v212, v212, v219\n"
    "v_add_f32 v213, v213, v223\n"
    "v_add_f32 v214, v214, v227\n"
    "s_nop 3\n"
    "s_branch LRET_e_3_L_%=\n"
    "LRARE_o_3_%=:\n"
    "s_nop 7\n"
    "s_nop 7\n"
    "v_max3_f32 v244, v128, v129, v130\n"
    "v_max3_f32 v246, v136, v137, v138\n"
    "v_max3_f32 v248, v144, v145, v146\n"
    "v_max3_f32 v244, v244, v131, v132\n"
    "v_max3_f32 v246, v246, v139, v140\n"
    "v_max3_f32 v248, v248, v147, v148\n"
    "v_max3_f32 v244, v244, v133, v134\n"
    "v_max3_f32 v246, v246, v141, v142\n"
    "v_max3_f32 v248, v248, v149, v150\n"
    "v_max_f32 v244, v244, v135\n"
    "v_max_f32 v246, v246, v143\n"
    "v_max_f32 v248, v248, v151\n"
    "v_mov_b32 v245, v244\n"
    "v_mov_b32 v247, v246\n"
    "v_mov_b32 v249, v248\n"
    "v_permlane32_swap_b32 v244, v245\n"
    "v_permlane32_swap_b32 v246, v247\n"
    "v_permlane32_swap_b32 v248, v249\n"
    "v_max_f32 v244, v244, v245\n"
    "v_max_f32 v246, v246, v247\n"
    "v_max_f32 v248, v248, v249\n"
    "v_mov_b32 v245, v244\n"
    "v_mov_b32 v247, v246\n"
    "v_mov_b32 v249, v248\n"
    "v_permlane16_swap_b32 v244, v245\n"
    "v_permlane16_swap_b32 v246, v247\n"
    "v_permlane16_swap_b32 v248, v249\n"
    "v_max_f32 v244, v244, v245\n"
    "v_max_f32 v246, v246, v247\n"
    "v_max_f32 v248, v248, v249\n"
    "v_max_f32 v244, 0, v244\n"
    "v_max_f32 v246, 0, v246\n"
    "v_max_f32 v248, 0, v248\n"
    "v_exp_f32 v245, -v244\n"
    "v_exp_f32 v247, -v246\n"
    "v_exp_f32 v249, -v248\n"
    "v_sub_f32 v128, v128, v244\n"
    "v_sub_f32 v136, v136, v246\n"
    "v_sub_f32 v144, v144, v248\n"
    "v_sub_f32 v129, v129, v244\n"
    "v_sub_f32 v137, v137, v246\n"
    "v_sub_f32 v145, v145, v248\n"
    "v_sub_f32 v130, v130, v244\n"
    "v_sub_f32 v138, v138, v246\n"
    "v_sub_f32 v146, v146, v248\n"
    "v_sub_f32 v131, v131, v244\n"
    "v_sub_f32 v139, v139, v246\n"
    "v_sub_f32 v147, v147, v248\n"
    "v_sub_f32 v132, v132, v244\n"
    "v_sub_f32 v140, v140, v246\n"
    "v_sub_f32 v148, v148, v248\n"
    "v_sub_f32 v133, v133, v244\n"
    "v_sub_f32 v141, v141, v246\n"
    "v_sub_f32 v149, v149, v248\n"
    "v_sub_f32 v134, v134, v244\n"
    "v_sub_f32 v142, v142, v246\n"
    "v_sub_f32 v150, v150, v248\n"
    "v_sub_f32 v135, v135, v244\n"
    "v_sub_f32 v143, v143, v246\n"
    "v_sub_f32 v151, v151, v248\n"
    "v_sub_f32 v96, v96, v244\n"
    "v_sub_f32 v104, v104, v246\n"
    "v_sub_f32 v112, v112, v248\n"
    "v_sub_f32 v97, v97, v244\n"
    "v_sub_f32 v105, v105, v246\n"
    "v_sub_f32 v113, v113, v248\n"
    "v_sub_f32 v98, v98, v244\n"
    "v_sub_f32 v106, v106, v246\n"
    "v_sub_f32 v114, v114, v248\n"
    "v_sub_f32 v99, v99, v244\n"
    "v_sub_f32 v107, v107, v246\n"
    "v_sub_f32 v115, v115, v248\n"
    "v_sub_f32 v100, v100, v244\n"
    "v_sub_f32 v108, v108, v246\n"
    "v_sub_f32 v116, v116, v248\n"
    "v_sub_f32 v101, v101, v244\n"
    "v_sub_f32 v109, v109, v246\n"
    "v_sub_f32 v117, v117, v248\n"
    "v_sub_f32 v102, v102, v244\n"
    "v_sub_f32 v110, v110, v246\n"
    "v_sub_f32 v118, v118, v248\n"
    "v_sub_f32 v103, v103, v244\n"
    "v_sub_f32 v111, v111, v246\n"
    "v_sub_f32 v119, v119, v248\n"
    "v_sub_f32 v192, v192, v244\n"
    "v_sub_f32 v196, v196, v246\n"
    "v_sub_f32 v200, v200, v248\n"
    "v_sub_f32 v193, v193, v244\n"
    "v_sub_f32 v197, v197, v246\n"
    "v_sub_f32 v201, v201, v248\n"
    "v_sub_f32 v194, v194, v244\n"
    "v_sub_f32 v198, v198, v246\n"
    "v_sub_f32 v202, v202, v248\n"
    "v_sub_f32 v195, v195, v244\n"
    "v_sub_f32 v199, v199, v246\n"
    "v_sub_f32 v203, v203, v248\n"
    "v_mul_f32 v208, v208, v245\n"
    "v_mul_f32 v209, v209, v247\n"
    "v_mul_f32 v210, v210, v249\n"
    "v_accvgpr_read_b32 v216, a0\n"
    "v_accvgpr_read_b32 v220, a48\n"
    "v_accvgpr_read_b32 v224, a96\n"
    "v_accvgpr_read_b32 v217, a1\n"
    "v_accvgpr_read_b32 v221, a49\n"
    "v_accvgpr_read_b32 v225, a97\n"
    "v_accvgpr_read_b32 v218, a2\n"
    "v_accvgpr_read_b32 v222, a50\n"
    "v_accvgpr_read_b32 v226, a98\n"
    "v_accvgpr_read_b32 v219, a3\n"
    "v_accvgpr_read_b32 v223, a51\n"
    "v_accvgpr_read_b32 v227, a99\n"
    "v_mul_f32 v216, v216, v245\n"
    "v_mul_f32 v220, v220, v247\n"
    "v_mul_f32 v224, v224, v249\n"
    "v_mul_f32 v217, v217, v245\n"
    "v_mul_f32 v221, v221, v247\n"
    "v_mul_f32 v225, v225, v249\n"
    "v_mul_f32 v218, v218, v245\n"
    "v_mul_f32 v222, v222, v247\n"
    "v_mul_f32 v226, v226, v249\n"
    "v_mul_f32 v219, v219, v245\n"
    "v_mul_f32 v223, v223, v247\n"
    "v_mul_f32 v227, v227, v249\n"
    "v_accvgpr_write_b32 a0, v216\n"
    "v_accvgpr_write_b32 a48, v220\n"
    "v_accvgpr_write_b32 a96, v224\n"
    "v_accvgpr_write_b32 a1, v217\n"
    "v_accvgpr_write_b32 a49, v221\n"
    "v_accvgpr_write_b32 a97, v225\n"
    "v_accvgpr_write_b32 a2, v218\n"
    "v_accvgpr_write_b32 a50, v222\n"
    "v_accvgpr_write_b32 a98, v226\n"
    "v_accvgpr_write_b32 a3, v219\n"
    "v_accvgpr_write_b32 a51, v223\n"
    "v_accvgpr_write_b32 a99, v227\n"
    "v_accvgpr_read_b32 v216, a4\n"
    "v_accvgpr_read_b32 v220, a52\n"
    "v_accvgpr_read_b32 v224, a100\n"
    "v_accvgpr_read_b32 v217, a5\n"
    "v_accvgpr_read_b32 v221, a53\n"
    "v_accvgpr_read_b32 v225, a101\n"
    "v_accvgpr_read_b32 v218, a6\n"
    "v_accvgpr_read_b32 v222, a54\n"
    "v_accvgpr_read_b32 v226, a102\n"
    "v_accvgpr_read_b32 v219, a7\n"
    "v_accvgpr_read_b32 v223, a55\n"
    "v_accvgpr_read_b32 v227, a103\n"
    "v_mul_f32 v216, v216, v245\n"
    "v_mul_f32 v220, v220, v247\n"
    "v_mul_f32 v224, v224, v249\n"
    "v_mul_f32 v217, v217, v245\n"
    "v_mul_f32 v221, v221, v247\n"
    "v_mul_f32 v225, v225, v249\n"
    "v_mul_f32 v218, v218, v245\n"
    "v_mul_f32 v222, v222, v247\n"
    "v_mul_f32 v226, v226, v249\n"
    "v_mul_f32 v219, v219, v245\n"
    "v_mul_f32 v223, v223, v247\n"
    "v_mul_f32 v227, v227, v249\n"
    "v_accvgpr_write_b32 a4, v216\n"
    "v_accvgpr_write_b32 a52, v220\n"
    "v_accvgpr_write_b32 a100, v224\n"
    "v_accvgpr_write_b32 a5, v217\n"
    "v_accvgpr_write_b32 a53, v221\n"
    "v_accvgpr_write_b32 a101, v225\n"
    "v_accvgpr_write_b32 a6, v218\n"
    "v_accvgpr_write_b32 a54, v222\n"
    "v_accvgpr_write_b32 a102, v226\n"
    "v_accvgpr_write_b32 a7, v219\n"
    "v_accvgpr_write_b32 a55, v223\n"
    "v_accvgpr_write_b32 a103, v227\n"
    "v_accvgpr_read_b32 v216, a8\n"
    "v_accvgpr_read_b32 v220, a56\n"
    "v_accvgpr_read_b32 v224, a104\n"
    "v_accvgpr_read_b32 v217, a9\n"
    "v_accvgpr_read_b32 v221, a57\n"
    "v_accvgpr_read_b32 v225, a105\n"
    "v_accvgpr_read_b32 v218, a10\n"
    "v_accvgpr_read_b32 v222, a58\n"
    "v_accvgpr_read_b32 v226, a106\n"
    "v_accvgpr_read_b32 v219, a11\n"
    "v_accvgpr_read_b32 v223, a59\n"
    "v_accvgpr_read_b32 v227, a107\n"
    "v_mul_f32 v216, v216, v245\n"
    "v_mul_f32 v220, v220, v247\n"
    "v_mul_f32 v224, v224, v249\n"
    "v_mul_f32 v217, v217, v245\n"
    "v_mul_f32 v221, v221, v247\n"
    "v_mul_f32 v225, v225, v249\n"
    "v_mul_f32 v218, v218, v245\n"
    "v_mul_f32 v222, v222, v247\n"
    "v_mul_f32 v226, v226, v249\n"
    "v_mul_f32 v219, v219, v245\n"
    "v_mul_f32 v223, v223, v247\n"
    "v_mul_f32 v227, v227, v249\n"
    "v_accvgpr_write_b32 a8, v216\n"
    "v_accvgpr_write_b32 a56, v220\n"
    "v_accvgpr_write_b32 a104, v224\n"
    "v_accvgpr_write_b32 a9, v217\n"
    "v_accvgpr_write_b32 a57, v221\n"
    "v_accvgpr_write_b32 a105, v225\n"
    "v_accvgpr_write_b32 a10, v218\n"
    "v_accvgpr_write_b32 a58, v222\n"
    "v_accvgpr_write_b32 a106, v226\n"
    "v_accvgpr_write_b32 a11, v219\n"
    "v_accvgpr_write_b32 a59, v223\n"
    "v_accvgpr_write_b32 a107, v227\n"
    "v_accvgpr_read_b32 v216, a12\n"
    "v_accvgpr_read_b32 v220, a60\n"
    "v_accvgpr_read_b32 v224, a108\n"
    "v_accvgpr_read_b32 v217, a13\n"
    "v_accvgpr_read_b32 v221, a61\n"
    "v_accvgpr_read_b32 v225, a109\n"
    "v_accvgpr_read_b32 v218, a14\n"
    "v_accvgpr_read_b32 v222, a62\n"
    "v_accvgpr_read_b32 v226, a110\n"
    "v_accvgpr_read_b32 v219, a15\n"
    "v_accvgpr_read_b32 v223, a63\n"
    "v_accvgpr_read_b32 v227, a111\n"
    "v_mul_f32 v216, v216, v245\n"
    "v_mul_f32 v220, v220, v247\n"
    "v_mul_f32 v224, v224, v249\n"
    "v_mul_f32 v217, v217, v245\n"
    "v_mul_f32 v221, v221, v247\n"
    "v_mul_f32 v225, v225, v249\n"
    "v_mul_f32 v218, v218, v245\n"
    "v_mul_f32 v222, v222, v247\n"
    "v_mul_f32 v226, v226, v249\n"
    "v_mul_f32 v219, v219, v245\n"
    "v_mul_f32 v223, v223, v247\n"
    "v_mul_f32 v227, v227, v249\n"
    "v_accvgpr_write_b32 a12, v216\n"
    "v_accvgpr_write_b32 a60, v220\n"
    "v_accvgpr_write_b32 a108, v224\n"
    "v_accvgpr_write_b32 a13, v217\n"
    "v_accvgpr_write_b32 a61, v221\n"
    "v_accvgpr_write_b32 a109, v225\n"
    "v_accvgpr_write_b32 a14, v218\n"
    "v_accvgpr_write_b32 a62, v222\n"
    "v_accvgpr_write_b32 a110, v226\n"
    "v_accvgpr_write_b32 a15, v219\n"
    "v_accvgpr_write_b32 a63, v223\n"
    "v_accvgpr_write_b32 a111, v227\n"
    "v_accvgpr_read_b32 v216, a16\n"
    "v_accvgpr_read_b32 v220, a64\n"
    "v_accvgpr_read_b32 v224, a112\n"
    "v_accvgpr_read_b32 v217, a17\n"
    "v_accvgpr_read_b32 v221, a65\n"
    "v_accvgpr_read_b32 v225, a113\n"
    "v_accvgpr_read_b32 v218, a18\n"
    "v_accvgpr_read_b32 v222, a66\n"
    "v_accvgpr_read_b32 v226, a114\n"
    "v_accvgpr_read_b32 v219, a19\n"
    "v_accvgpr_read_b32 v223, a67\n"
    "v_accvgpr_read_b32 v227, a115\n"
    "v_mul_f32 v216, v216, v245\n"
    "v_mul_f32 v220, v220, v247\n"
    "v_mul_f32 v224, v224, v249\n"
    "v_mul_f32 v217, v217, v245\n"
    "v_mul_f32 v221, v221, v247\n"
    "v_mul_f32 v225, v225, v249\n"
    "v_mul_f32 v218, v218, v245\n"
    "v_mul_f32 v222, v222, v247\n"
    "v_mul_f32 v226, v226, v249\n"
    "v_mul_f32 v219, v219, v245\n"
    "v_mul_f32 v223, v223, v247\n"
    "v_mul_f32 v227, v227, v249\n"
    "v_accvgpr_write_b32 a16, v216\n"
    "v_accvgpr_write_b32 a64, v220\n"
    "v_accvgpr_write_b32 a112, v224\n"
    "v_accvgpr_write_b32 a17, v217\n"
    "v_accvgpr_write_b32 a65, v221\n"
    "v_accvgpr_write_b32 a113, v225\n"
    "v_accvgpr_write_b32 a18, v218\n"
    "v_accvgpr_write_b32 a66, v222\n"
    "v_accvgpr_write_b32 a114, v226\n"
    "v_accvgpr_write_b32 a19, v219\n"
    "v_accvgpr_write_b32 a67, v223\n"
    "v_accvgpr_write_b32 a115, v227\n"
    "v_accvgpr_read_b32 v216, a20\n"
    "v_accvgpr_read_b32 v220, a68\n"
    "v_accvgpr_read_b32 v224, a116\n"
    "v_accvgpr_read_b32 v217, a21\n"
    "v_accvgpr_read_b32 v221, a69\n"
    "v_accvgpr_read_b32 v225, a117\n"
    "v_accvgpr_read_b32 v218, a22\n"
    "v_accvgpr_read_b32 v222, a70\n"
    "v_accvgpr_read_b32 v226, a118\n"
    "v_accvgpr_read_b32 v219, a23\n"
    "v_accvgpr_read_b32 v223, a71\n"
    "v_accvgpr_read_b32 v227, a119\n"
    "v_mul_f32 v216, v216, v245\n"
    "v_mul_f32 v220, v220, v247\n"
    "v_mul_f32 v224, v224, v249\n"
    "v_mul_f32 v217, v217, v245\n"
    "v_mul_f32 v221, v221, v247\n"
    "v_mul_f32 v225, v225, v249\n"
    "v_mul_f32 v218, v218, v245\n"
    "v_mul_f32 v222, v222, v247\n"
    "v_mul_f32 v226, v226, v249\n"
    "v_mul_f32 v219, v219, v245\n"
    "v_mul_f32 v223, v223, v247\n"
    "v_mul_f32 v227, v227, v249\n"
    "v_accvgpr_write_b32 a20, v216\n"
    "v_accvgpr_write_b32 a68, v220\n"
    "v_accvgpr_write_b32 a116, v224\n"
    "v_accvgpr_write_b32 a21, v217\n"
    "v_accvgpr_write_b32 a69, v221\n"
    "v_accvgpr_write_b32 a117, v225\n"
    "v_accvgpr_write_b32 a22, v218\n"
    "v_accvgpr_write_b32 a70, v222\n"
    "v_accvgpr_write_b32 a118, v226\n"
    "v_accvgpr_write_b32 a23, v219\n"
    "v_accvgpr_write_b32 a71, v223\n"
    "v_accvgpr_write_b32 a119, v227\n"
    "v_accvgpr_read_b32 v216, a24\n"
    "v_accvgpr_read_b32 v220, a72\n"
    "v_accvgpr_read_b32 v224, a120\n"
    "v_accvgpr_read_b32 v217, a25\n"
    "v_accvgpr_read_b32 v221, a73\n"
    "v_accvgpr_read_b32 v225, a121\n"
    "v_accvgpr_read_b32 v218, a26\n"
    "v_accvgpr_read_b32 v222, a74\n"
    "v_accvgpr_read_b32 v226, a122\n"
    "v_accvgpr_read_b32 v219, a27\n"
    "v_accvgpr_read_b32 v223, a75\n"
    "v_accvgpr_read_b32 v227, a123\n"
    "v_mul_f32 v216, v216, v245\n"
    "v_mul_f32 v220, v220, v247\n"
    "v_mul_f32 v224, v224, v249\n"
    "v_mul_f32 v217, v217, v245\n"
    "v_mul_f32 v221, v221, v247\n"
    "v_mul_f32 v225, v225, v249\n"
    "v_mul_f32 v218, v218, v245\n"
    "v_mul_f32 v222, v222, v247\n"
    "v_mul_f32 v226, v226, v249\n"
    "v_mul_f32 v219, v219, v245\n"
    "v_mul_f32 v223, v223, v247\n"
    "v_mul_f32 v227, v227, v249\n"
    "v_accvgpr_write_b32 a24, v216\n"
    "v_accvgpr_write_b32 a72, v220\n"
    "v_accvgpr_write_b32 a120, v224\n"
    "v_accvgpr_write_b32 a25, v217\n"
    "v_accvgpr_write_b32 a73, v221\n"
    "v_accvgpr_write_b32 a121, v225\n"
    "v_accvgpr_write_b32 a26, v218\n"
    "v_accvgpr_write_b32 a74, v222\n"
    "v_accvgpr_write_b32 a122, v226\n"
    "v_accvgpr_write_b32 a27, v219\n"
    "v_accvgpr_write_b32 a75, v223\n"
    "v_accvgpr_write_b32 a123, v227\n"
    "v_accvgpr_read_b32 v216, a28\n"
    "v_accvgpr_read_b32 v220, a76\n"
    "v_accvgpr_read_b32 v224, a124\n"
    "v_accvgpr_read_b32 v217, a29\n"
    "v_accvgpr_read_b32 v221, a77\n"
    "v_accvgpr_read_b32 v225, a125\n"
    "v_accvgpr_read_b32 v218, a30\n"
    "v_accvgpr_read_b32 v222, a78\n"
    "v_accvgpr_read_b32 v226, a126\n"
    "v_accvgpr_read_b32 v219, a31\n"
    "v_accvgpr_read_b32 v223, a79\n"
    "v_accvgpr_read_b32 v227, a127\n"
    "v_mul_f32 v216, v216, v245\n"
    "v_mul_f32 v220, v220, v247\n"
    "v_mul_f32 v224, v224, v249\n"
    "v_mul_f32 v217, v217, v245\n"
    "v_mul_f32 v221, v221, v247\n"
    "v_mul_f32 v225, v225, v249\n"
    "v_mul_f32 v218, v218, v245\n"
    "v_mul_f32 v222, v222, v247\n"
    "v_mul_f32 v226, v226, v249\n"
    "v_mul_f32 v219, v219, v245\n"
    "v_mul_f32 v223, v223, v247\n"
    "v_mul_f32 v227, v227, v249\n"
    "v_accvgpr_write_b32 a28, v216\n"
    "v_accvgpr_write_b32 a76, v220\n"
    "v_accvgpr_write_b32 a124, v224\n"
    "v_accvgpr_write_b32 a29, v217\n"
    "v_accvgpr_write_b32 a77, v221\n"
    "v_accvgpr_write_b32 a125, v225\n"
    "v_accvgpr_write_b32 a30, v218\n"
    "v_accvgpr_write_b32 a78, v222\n"
    "v_accvgpr_write_b32 a126, v226\n"
    "v_accvgpr_write_b32 a31, v219\n"
    "v_accvgpr_write_b32 a79, v223\n"
    "v_accvgpr_write_b32 a127, v227\n"
    "v_accvgpr_read_b32 v216, a32\n"
    "v_accvgpr_read_b32 v220, a80\n"
    "v_accvgpr_read_b32 v224, a128\n"
    "v_accvgpr_read_b32 v217, a33\n"
    "v_accvgpr_read_b32 v221, a81\n"
    "v_accvgpr_read_b32 v225, a129\n"
    "v_accvgpr_read_b32 v218, a34\n"
    "v_accvgpr_read_b32 v222, a82\n"
    "v_accvgpr_read_b32 v226, a130\n"
    "v_accvgpr_read_b32 v219, a35\n"
    "v_accvgpr_read_b32 v223, a83\n"
    "v_accvgpr_read_b32 v227, a131\n"
    "v_mul_f32 v216, v216, v245\n"
    "v_mul_f32 v220, v220, v247\n"
    "v_mul_f32 v224, v224, v249\n"
    "v_mul_f32 v217, v217, v245\n"
    "v_mul_f32 v221, v221, v247\n"
    "v_mul_f32 v225, v225, v249\n"
    "v_mul_f32 v218, v218, v245\n"
    "v_mul_f32 v222, v222, v247\n"
    "v_mul_f32 v226, v226, v249\n"
    "v_mul_f32 v219, v219, v245\n"
    "v_mul_f32 v223, v223, v247\n"
    "v_mul_f32 v227, v227, v249\n"
    "v_accvgpr_write_b32 a32, v216\n"
    "v_accvgpr_write_b32 a80, v220\n"
    "v_accvgpr_write_b32 a128, v224\n"
    "v_accvgpr_write_b32 a33, v217\n"
    "v_accvgpr_write_b32 a81, v221\n"
    "v_accvgpr_write_b32 a129, v225\n"
    "v_accvgpr_write_b32 a34, v218\n"
    "v_accvgpr_write_b32 a82, v222\n"
    "v_accvgpr_write_b32 a130, v226\n"
    "v_accvgpr_write_b32 a35, v219\n"
    "v_accvgpr_write_b32 a83, v223\n"
    "v_accvgpr_write_b32 a131, v227\n"
    "v_accvgpr_read_b32 v216, a36\n"
    "v_accvgpr_read_b32 v220, a84\n"
    "v_accvgpr_read_b32 v224, a132\n"
    "v_accvgpr_read_b32 v217, a37\n"
    "v_accvgpr_read_b32 v221, a85\n"
    "v_accvgpr_read_b32 v225, a133\n"
    "v_accvgpr_read_b32 v218, a38\n"
    "v_accvgpr_read_b32 v222, a86\n"
    "v_accvgpr_read_b32 v226, a134\n"
    "v_accvgpr_read_b32 v219, a39\n"
    "v_accvgpr_read_b32 v223, a87\n"
    "v_accvgpr_read_b32 v227, a135\n"
    "v_mul_f32 v216, v216, v245\n"
    "v_mul_f32 v220, v220, v247\n"
    "v_mul_f32 v224, v224, v249\n"
    "v_mul_f32 v217, v217, v245\n"
    "v_mul_f32 v221, v221, v247\n"
    "v_mul_f32 v225, v225, v249\n"
    "v_mul_f32 v218, v218, v245\n"
    "v_mul_f32 v222, v222, v247\n"
    "v_mul_f32 v226, v226, v249\n"
    "v_mul_f32 v219, v219, v245\n"
    "v_mul_f32 v223, v223, v247\n"
    "v_mul_f32 v227, v227, v249\n"
    "v_accvgpr_write_b32 a36, v216\n"
    "v_accvgpr_write_b32 a84, v220\n"
    "v_accvgpr_write_b32 a132, v224\n"
    "v_accvgpr_write_b32 a37, v217\n"
    "v_accvgpr_write_b32 a85, v221\n"
    "v_accvgpr_write_b32 a133, v225\n"
    "v_accvgpr_write_b32 a38, v218\n"
    "v_accvgpr_write_b32 a86, v222\n"
    "v_accvgpr_write_b32 a134, v226\n"
    "v_accvgpr_write_b32 a39, v219\n"
    "v_accvgpr_write_b32 a87, v223\n"
    "v_accvgpr_write_b32 a135, v227\n"
    "v_accvgpr_read_b32 v216, a40\n"
    "v_accvgpr_read_b32 v220, a88\n"
    "v_accvgpr_read_b32 v224, a136\n"
    "v_accvgpr_read_b32 v217, a41\n"
    "v_accvgpr_read_b32 v221, a89\n"
    "v_accvgpr_read_b32 v225, a137\n"
    "v_accvgpr_read_b32 v218, a42\n"
    "v_accvgpr_read_b32 v222, a90\n"
    "v_accvgpr_read_b32 v226, a138\n"
    "v_accvgpr_read_b32 v219, a43\n"
    "v_accvgpr_read_b32 v223, a91\n"
    "v_accvgpr_read_b32 v227, a139\n"
    "v_mul_f32 v216, v216, v245\n"
    "v_mul_f32 v220, v220, v247\n"
    "v_mul_f32 v224, v224, v249\n"
    "v_mul_f32 v217, v217, v245\n"
    "v_mul_f32 v221, v221, v247\n"
    "v_mul_f32 v225, v225, v249\n"
    "v_mul_f32 v218, v218, v245\n"
    "v_mul_f32 v222, v222, v247\n"
    "v_mul_f32 v226, v226, v249\n"
    "v_mul_f32 v219, v219, v245\n"
    "v_mul_f32 v223, v223, v247\n"
    "v_mul_f32 v227, v227, v249\n"
    "v_accvgpr_write_b32 a40, v216\n"
    "v_accvgpr_write_b32 a88, v220\n"
    "v_accvgpr_write_b32 a136, v224\n"
    "v_accvgpr_write_b32 a41, v217\n"
    "v_accvgpr_write_b32 a89, v221\n"
    "v_accvgpr_write_b32 a137, v225\n"
    "v_accvgpr_write_b32 a42, v218\n"
    "v_accvgpr_write_b32 a90, v222\n"
    "v_accvgpr_write_b32 a138, v226\n"
    "v_accvgpr_write_b32 a43, v219\n"
    "v_accvgpr_write_b32 a91, v223\n"
    "v_accvgpr_write_b32 a139, v227\n"
    "v_accvgpr_read_b32 v216, a44\n"
    "v_accvgpr_read_b32 v220, a92\n"
    "v_accvgpr_read_b32 v224, a140\n"
    "v_accvgpr_read_b32 v217, a45\n"
    "v_accvgpr_read_b32 v221, a93\n"
    "v_accvgpr_read_b32 v225, a141\n"
    "v_accvgpr_read_b32 v218, a46\n"
    "v_accvgpr_read_b32 v222, a94\n"
    "v_accvgpr_read_b32 v226, a142\n"
    "v_accvgpr_read_b32 v219, a47\n"
    "v_accvgpr_read_b32 v223, a95\n"
    "v_accvgpr_read_b32 v227, a143\n"
    "v_mul_f32 v216, v216, v245\n"
    "v_mul_f32 v220, v220, v247\n"
    "v_mul_f32 v224, v224, v249\n"
    "v_mul_f32 v217, v217, v245\n"
    "v_mul_f32 v221, v221, v247\n"
    "v_mul_f32 v225, v225, v249\n"
    "v_mul_f32 v218, v218, v245\n"
    "v_mul_f32 v222, v222, v247\n"
    "v_mul_f32 v226, v226, v249\n"
    "v_mul_f32 v219, v219, v245\n"
    "v_mul_f32 v223, v223, v247\n"
    "v_mul_f32 v227, v227, v249\n"
    "v_accvgpr_write_b32 a44, v216\n"
    "v_accvgpr_write_b32 a92, v220\n"
    "v_accvgpr_write_b32 a140, v224\n"
    "v_accvgpr_write_b32 a45, v217\n"
    "v_accvgpr_write_b32 a93, v221\n"
    "v_accvgpr_write_b32 a141, v225\n"
    "v_accvgpr_write_b32 a46, v218\n"
    "v_accvgpr_write_b32 a94, v222\n"
    "v_accvgpr_write_b32 a142, v226\n"
    "v_accvgpr_write_b32 a47, v219\n"
    "v_accvgpr_write_b32 a95, v223\n"
    "v_accvgpr_write_b32 a143, v227\n"
    "v_exp_f32 v216, v128\n"
    "v_exp_f32 v220, v136\n"
    "v_exp_f32 v224, v144\n"
    "v_exp_f32 v217, v129\n"
    "v_exp_f32 v221, v137\n"
    "v_exp_f32 v225, v145\n"
    "v_exp_f32 v218, v130\n"
    "v_exp_f32 v222, v138\n"
    "v_exp_f32 v226, v146\n"
    "v_exp_f32 v219, v131\n"
    "v_exp_f32 v223, v139\n"
    "v_exp_f32 v227, v147\n"
    "v_cvt_pk_bf16_f32 v176, v216, v217\n"
    "v_cvt_pk_bf16_f32 v180, v220, v221\n"
    "v_cvt_pk_bf16_f32 v184, v224, v225\n"
    "v_cvt_pk_bf16_f32 v177, v218, v219\n"
    "v_cvt_pk_bf16_f32 v181, v222, v223\n"
    "v_cvt_pk_bf16_f32 v185, v226, v227\n"
    "v_add_f32 v212, v216, v217\n"
    "v_add_f32 v213, v220, v221\n"
    "v_add_f32 v214, v224, v225\n"
    "v_add_f32 v212, v212, v218\n"
    "v_add_f32 v213, v213, v222\n"
    "v_add_f32 v214, v214, v226\n"
    "v_add_f32 v212, v212, v219\n"
    "v_add_f32 v213, v213, v223\n"
    "v_add_f32 v214, v214, v227\n"
    "v_exp_f32 v216, v132\n"
    "v_exp_f32 v220, v140\n"
    "v_exp_f32 v224, v148\n"
    "v_exp_f32 v217, v133\n"
    "v_exp_f32 v221, v141\n"
    "v_exp_f32 v225, v149\n"
    "v_exp_f32 v218, v134\n"
    "v_exp_f32 v222, v142\n"
    "v_exp_f32 v226, v150\n"
    "v_exp_f32 v219, v135\n"
    "v_exp_f32 v223, v143\n"
    "v_exp_f32 v227, v151\n"
    "v_cvt_pk_bf16_f32 v178, v216, v217\n"
    "v_cvt_pk_bf16_f32 v182, v220, v221\n"
    "v_cvt_pk_bf16_f32 v186, v224, v225\n"
    "v_cvt_pk_bf16_f32 v179, v218, v219\n"
    "v_cvt_pk_bf16_f32 v183, v222, v223\n"
    "v_cvt_pk_bf16_f32 v187, v226, v227\n"
    "v_add_f32 v212, v212, v216\n"
    "v_add_f32 v213, v213, v220\n"
    "v_add_f32 v214, v214, v224\n"
    "v_add_f32 v212, v212, v217\n"
    "v_add_f32 v213, v213, v221\n"
    "v_add_f32 v214, v214, v225\n"
    "v_add_f32 v212, v212, v218\n"
    "v_add_f32 v213, v213, v222\n"
    "v_add_f32 v214, v214, v226\n"
    "v_add_f32 v212, v212, v219\n"
    "v_add_f32 v213, v213, v223\n"
    "v_add_f32 v214, v214, v227\n"
    "s_nop 3\n"
    "s_cmp_eq_u32 s57, 0\n"
    "s_cbranch_scc1 LRET_o_3_L_%=\n"
    "s_cmp_eq_u32 s57, 1\n"
    "s_cbranch_scc1 LRET_o_3_F0_%=\n"
    "s_cmp_eq_u32 s57, 2\n"
    "s_cbranch_scc1 LRET_o_3_F1_%=\n"
    "s_branch LRET_o_3_P0_%=\n"
    "LRARE_e_2_%=:\n"
    "s_nop 7\n"
    "s_nop 7\n"
    "v_max3_f32 v244, v96, v97, v98\n"
    "v_max3_f32 v246, v104, v105, v106\n"
    "v_max3_f32 v244, v244, v99, v100\n"
    "v_max3_f32 v246, v246, v107, v108\n"
    "v_max3_f32 v244, v244, v101, v102\n"
    "v_max3_f32 v246, v246, v109, v110\n"
    "v_max_f32 v244, v244, v103\n"
    "v_max_f32 v246, v246, v111\n"
    "v_mov_b32 v245, v244\n"
    "v_mov_b32 v247, v246\n"
    "s_nop 0\n"
    "v_permlane32_swap_b32 v244, v245\n"
    "v_permlane32_swap_b32 v246, v247\n"
    "v_max_f32 v244, v244, v245\n"
    "v_max_f32 v246, v246, v247\n"
    "v_mov_b32 v245, v244\n"
    "v_mov_b32 v247, v246\n"
    "s_nop 0\n"
    "v_permlane16_swap_b32 v244, v245\n"
    "v_permlane16_swap_b32 v246, v247\n"
    "v_max_f32 v244, v244, v245\n"
    "v_max_f32 v246, v246, v247\n"
    "v_max_f32 v244, 0, v244\n"
    "v_max_f32 v246, 0, v246\n"
    "v_exp_f32 v245, -v244\n"
    "v_exp_f32 v247, -v246\n"
    "v_sub_f32 v96, v96, v244\n"
    "v_sub_f32 v104, v104, v246\n"
    "v_sub_f32 v97, v97, v244\n"
    "v_sub_f32 v105, v105, v246\n"
    "v_sub_f32 v98, v98, v244\n"
    "v_sub_f32 v106, v106, v246\n"
    "v_sub_f32 v99, v99, v244\n"
    "v_sub_f32 v107, v107, v246\n"
    "v_sub_f32 v100, v100, v244\n"
    "v_sub_f32 v108, v108, v246\n"
    "v_sub_f32 v101, v101, v244\n"
    "v_sub_f32 v109, v109, v246\n"
    "v_sub_f32 v102, v102, v244\n"
    "v_sub_f32 v110, v110, v246\n"
    "v_sub_f32 v103, v103, v244\n"
    "v_sub_f32 v111, v111, v246\n"
    "v_sub_f32 v128, v128, v244\n"
    "v_sub_f32 v136, v136, v246\n"
    "v_sub_f32 v129, v129, v244\n"
    "v_sub_f32 v137, v137, v246\n"
    "v_sub_f32 v130, v130, v244\n"
    "v_sub_f32 v138, v138, v246\n"
    "v_sub_f32 v131, v131, v244\n"
    "v_sub_f32 v139, v139, v246\n"
    "v_sub_f32 v132, v132, v244\n"
    "v_sub_f32 v140, v140, v246\n"
    "v_sub_f32 v133, v133, v244\n"
    "v_sub_f32 v141, v141, v246\n"
    "v_sub_f32 v134, v134, v244\n"
    "v_sub_f32 v142, v142, v246\n"
    "v_sub_f32 v135, v135, v244\n"
    "v_sub_f32 v143, v143, v246\n"
    "v_sub_f32 v192, v192, v244\n"
    "v_sub_f32 v196, v196, v246\n"
    "v_sub_f32 v193, v193, v244\n"
    "v_sub_f32 v197, v197, v246\n"
    "v_sub_f32 v194, v194, v244\n"
    "v_sub_f32 v198, v198, v246\n"
    "v_sub_f32 v195, v195, v244\n"
    "v_sub_f32 v199, v199, v246\n"
    "v_mul_f32 v208, v208, v245\n"
    "v_mul_f32 v209, v209, v247\n"
    "v_accvgpr_read_b32 v216, a0\n"
    "v_accvgpr_read_b32 v220, a48\n"
    "v_accvgpr_read_b32 v217, a1\n"
    "v_accvgpr_read_b32 v221, a49\n"
    "v_accvgpr_read_b32 v218, a2\n"
    "v_accvgpr_read_b32 v222, a50\n"
    "v_accvgpr_read_b32 v219, a3\n"
    "v_accvgpr_read_b32 v223, a51\n"
    "v_mul_f32 v216, v216, v245\n"
    "v_mul_f32 v220, v220, v247\n"
    "v_mul_f32 v217, v217, v245\n"
    "v_mul_f32 v221, v221, v247\n"
    "v_mul_f32 v218, v218, v245\n"
    "v_mul_f32 v222, v222, v247\n"
    "v_mul_f32 v219, v219, v245\n"
    "v_mul_f32 v223, v223, v247\n"
    "v_accvgpr_write_b32 a0, v216\n"
    "v_accvgpr_write_b32 a48, v220\n"
    "v_accvgpr_write_b32 a1, v217\n"
    "v_accvgpr_write_b32 a49, v221\n"
    "v_accvgpr_write_b32 a2, v218\n"
    "v_accvgpr_write_b32 a50, v222\n"
    "v_accvgpr_write_b32 a3, v219\n"
    "v_accvgpr_write_b32 a51, v223\n"
    "v_accvgpr_read_b32 v216, a4\n"
    "v_accvgpr_read_b32 v220, a52\n"
    "v_accvgpr_read_b32 v217, a5\n"
    "v_accvgpr_read_b32 v221, a53\n"
    "v_accvgpr_read_b32 v218, a6\n"
    "v_accvgpr_read_b32 v222, a54\n"
    "v_accvgpr_read_b32 v219, a7\n"
    "v_accvgpr_read_b32 v223, a55\n"
    "v_mul_f32 v216, v216, v245\n"
    "v_mul_f32 v220, v220, v247\n"
    "v_mul_f32 v217, v217, v245\n"
    "v_mul_f32 v221, v221, v247\n"
    "v_mul_f32 v218, v218, v245\n"
    "v_mul_f32 v222, v222, v247\n"
    "v_mul_f32 v219, v219, v245\n"
    "v_mul_f32 v223, v223, v247\n"
    "v_accvgpr_write_b32 a4, v216\n"
    "v_accvgpr_write_b32 a52, v220\n"
    "v_accvgpr_write_b32 a5, v217\n"
    "v_accvgpr_write_b32 a53, v221\n"
    "v_accvgpr_write_b32 a6, v218\n"
    "v_accvgpr_write_b32 a54, v222\n"
    "v_accvgpr_write_b32 a7, v219\n"
    "v_accvgpr_write_b32 a55, v223\n"
    "v_accvgpr_read_b32 v216, a8\n"
    "v_accvgpr_read_b32 v220, a56\n"
    "v_accvgpr_read_b32 v217, a9\n"
    "v_accvgpr_read_b32 v221, a57\n"
    "v_accvgpr_read_b32 v218, a10\n"
    "v_accvgpr_read_b32 v222, a58\n"
    "v_accvgpr_read_b32 v219, a11\n"
    "v_accvgpr_read_b32 v223, a59\n"
    "v_mul_f32 v216, v216, v245\n"
    "v_mul_f32 v220, v220, v247\n"
    "v_mul_f32 v217, v217, v245\n"
    "v_mul_f32 v221, v221, v247\n"
    "v_mul_f32 v218, v218, v245\n"
    "v_mul_f32 v222, v222, v247\n"
    "v_mul_f32 v219, v219, v245\n"
    "v_mul_f32 v223, v223, v247\n"
    "v_accvgpr_write_b32 a8, v216\n"
    "v_accvgpr_write_b32 a56, v220\n"
    "v_accvgpr_write_b32 a9, v217\n"
    "v_accvgpr_write_b32 a57, v221\n"
    "v_accvgpr_write_b32 a10, v218\n"
    "v_accvgpr_write_b32 a58, v222\n"
    "v_accvgpr_write_b32 a11, v219\n"
    "v_accvgpr_write_b32 a59, v223\n"
    "v_accvgpr_read_b32 v216, a12\n"
    "v_accvgpr_read_b32 v220, a60\n"
    "v_accvgpr_read_b32 v217, a13\n"
    "v_accvgpr_read_b32 v221, a61\n"
    "v_accvgpr_read_b32 v218, a14\n"
    "v_accvgpr_read_b32 v222, a62\n"
    "v_accvgpr_read_b32 v219, a15\n"
    "v_accvgpr_read_b32 v223, a63\n"
    "v_mul_f32 v216, v216, v245\n"
    "v_mul_f32 v220, v220, v247\n"
    "v_mul_f32 v217, v217, v245\n"
    "v_mul_f32 v221, v221, v247\n"
    "v_mul_f32 v218, v218, v245\n"
    "v_mul_f32 v222, v222, v247\n"
    "v_mul_f32 v219, v219, v245\n"
    "v_mul_f32 v223, v223, v247\n"
    "v_accvgpr_write_b32 a12, v216\n"
    "v_accvgpr_write_b32 a60, v220\n"
    "v_accvgpr_write_b32 a13, v217\n"
    "v_accvgpr_write_b32 a61, v221\n"
    "v_accvgpr_write_b32 a14, v218\n"
    "v_accvgpr_write_b32 a62, v222\n"
    "v_accvgpr_write_b32 a15, v219\n"
    "v_accvgpr_write_b32 a63, v223\n"
    "v_accvgpr_read_b32 v216, a16\n"
    "v_accvgpr_read_b32 v220, a64\n"
    "v_accvgpr_read_b32 v217, a17\n"
    "v_accvgpr_read_b32 v221, a65\n"
    "v_accvgpr_read_b32 v218, a18\n"
    "v_accvgpr_read_b32 v222, a66\n"
    "v_accvgpr_read_b32 v219, a19\n"
    "v_accvgpr_read_b32 v223, a67\n"
    "v_mul_f32 v216, v216, v245\n"
    "v_mul_f32 v220, v220, v247\n"
    "v_mul_f32 v217, v217, v245\n"
    "v_mul_f32 v221, v221, v247\n"
    "v_mul_f32 v218, v218, v245\n"
    "v_mul_f32 v222, v222, v247\n"
    "v_mul_f32 v219, v219, v245\n"
    "v_mul_f32 v223, v223, v247\n"
    "v_accvgpr_write_b32 a16, v216\n"
    "v_accvgpr_write_b32 a64, v220\n"
    "v_accvgpr_write_b32 a17, v217\n"
    "v_accvgpr_write_b32 a65, v221\n"
    "v_accvgpr_write_b32 a18, v218\n"
    "v_accvgpr_write_b32 a66, v222\n"
    "v_accvgpr_write_b32 a19, v219\n"
    "v_accvgpr_write_b32 a67, v223\n"
    "v_accvgpr_read_b32 v216, a20\n"
    "v_accvgpr_read_b32 v220, a68\n"
    "v_accvgpr_read_b32 v217, a21\n"
    "v_accvgpr_read_b32 v221, a69\n"
    "v_accvgpr_read_b32 v218, a22\n"
    "v_accvgpr_read_b32 v222, a70\n"
    "v_accvgpr_read_b32 v219, a23\n"
    "v_accvgpr_read_b32 v223, a71\n"
    "v_mul_f32 v216, v216, v245\n"
    "v_mul_f32 v220, v220, v247\n"
    "v_mul_f32 v217, v217, v245\n"
    "v_mul_f32 v221, v221, v247\n"
    "v_mul_f32 v218, v218, v245\n"
    "v_mul_f32 v222, v222, v247\n"
    "v_mul_f32 v219, v219, v245\n"
    "v_mul_f32 v223, v223, v247\n"
    "v_accvgpr_write_b32 a20, v216\n"
    "v_accvgpr_write_b32 a68, v220\n"
    "v_accvgpr_write_b32 a21, v217\n"
    "v_accvgpr_write_b32 a69, v221\n"
    "v_accvgpr_write_b32 a22, v218\n"
    "v_accvgpr_write_b32 a70, v222\n"
    "v_accvgpr_write_b32 a23, v219\n"
    "v_accvgpr_write_b32 a71, v223\n"
    "v_accvgpr_read_b32 v216, a24\n"
    "v_accvgpr_read_b32 v220, a72\n"
    "v_accvgpr_read_b32 v217, a25\n"
    "v_accvgpr_read_b32 v221, a73\n"
    "v_accvgpr_read_b32 v218, a26\n"
    "v_accvgpr_read_b32 v222, a74\n"
    "v_accvgpr_read_b32 v219, a27\n"
    "v_accvgpr_read_b32 v223, a75\n"
    "v_mul_f32 v216, v216, v245\n"
    "v_mul_f32 v220, v220, v247\n"
    "v_mul_f32 v217, v217, v245\n"
    "v_mul_f32 v221, v221, v247\n"
    "v_mul_f32 v218, v218, v245\n"
    "v_mul_f32 v222, v222, v247\n"
    "v_mul_f32 v219, v219, v245\n"
    "v_mul_f32 v223, v223, v247\n"
    "v_accvgpr_write_b32 a24, v216\n"
    "v_accvgpr_write_b32 a72, v220\n"
    "v_accvgpr_write_b32 a25, v217\n"
    "v_accvgpr_write_b32 a73, v221\n"
    "v_accvgpr_write_b32 a26, v218\n"
    "v_accvgpr_write_b32 a74, v222\n"
    "v_accvgpr_write_b32 a27, v219\n"
    "v_accvgpr_write_b32 a75, v223\n"
    "v_accvgpr_read_b32 v216, a28\n"
    "v_accvgpr_read_b32 v220, a76\n"
    "v_accvgpr_read_b32 v217, a29\n"
    "v_accvgpr_read_b32 v221, a77\n"
    "v_accvgpr_read_b32 v218, a30\n"
    "v_accvgpr_read_b32 v222, a78\n"
    "v_accvgpr_read_b32 v219, a31\n"
    "v_accvgpr_read_b32 v223, a79\n"
    "v_mul_f32 v216, v216, v245\n"
    "v_mul_f32 v220, v220, v247\n"
    "v_mul_f32 v217, v217, v245\n"
    "v_mul_f32 v221, v221, v247\n"
    "v_mul_f32 v218, v218, v245\n"
    "v_mul_f32 v222, v222, v247\n"
    "v_mul_f32 v219, v219, v245\n"
    "v_mul_f32 v223, v223, v247\n"
    "v_accvgpr_write_b32 a28, v216\n"
    "v_accvgpr_write_b32 a76, v220\n"
    "v_accvgpr_write_b32 a29, v217\n"
    "v_accvgpr_write_b32 a77, v221\n"
    "v_accvgpr_write_b32 a30, v218\n"
    "v_accvgpr_write_b32 a78, v222\n"
    "v_accvgpr_write_b32 a31, v219\n"
    "v_accvgpr_write_b32 a79, v223\n"
    "v_accvgpr_read_b32 v216, a32\n"
    "v_accvgpr_read_b32 v220, a80\n"
    "v_accvgpr_read_b32 v217, a33\n"
    "v_accvgpr_read_b32 v221, a81\n"
    "v_accvgpr_read_b32 v218, a34\n"
    "v_accvgpr_read_b32 v222, a82\n"
    "v_accvgpr_read_b32 v219, a35\n"
    "v_accvgpr_read_b32 v223, a83\n"
    "v_mul_f32 v216, v216, v245\n"
    "v_mul_f32 v220, v220, v247\n"
    "v_mul_f32 v217, v217, v245\n"
    "v_mul_f32 v221, v221, v247\n"
    "v_mul_f32 v218, v218, v245\n"
    "v_mul_f32 v222, v222, v247\n"
    "v_mul_f32 v219, v219, v245\n"
    "v_mul_f32 v223, v223, v247\n"
    "v_accvgpr_write_b32 a32, v216\n"
    "v_accvgpr_write_b32 a80, v220\n"
    "v_accvgpr_write_b32 a33, v217\n"
    "v_accvgpr_write_b32 a81, v221\n"
    "v_accvgpr_write_b32 a34, v218\n"
    "v_accvgpr_write_b32 a82, v222\n"
    "v_accvgpr_write_b32 a35, v219\n"
    "v_accvgpr_write_b32 a83, v223\n"
    "v_accvgpr_read_b32 v216, a36\n"
    "v_accvgpr_read_b32 v220, a84\n"
    "v_accvgpr_read_b32 v217, a37\n"
    "v_accvgpr_read_b32 v221, a85\n"
    "v_accvgpr_read_b32 v218, a38\n"
    "v_accvgpr_read_b32 v222, a86\n"
    "v_accvgpr_read_b32 v219, a39\n"
    "v_accvgpr_read_b32 v223, a87\n"
    "v_mul_f32 v216, v216, v245\n"
    "v_mul_f32 v220, v220, v247\n"
    "v_mul_f32 v217, v217, v245\n"
    "v_mul_f32 v221, v221, v247\n"
    "v_mul_f32 v218, v218, v245\n"
    "v_mul_f32 v222, v222, v247\n"
    "v_mul_f32 v219, v219, v245\n"
    "v_mul_f32 v223, v223, v247\n"
    "v_accvgpr_write_b32 a36, v216\n"
    "v_accvgpr_write_b32 a84, v220\n"
    "v_accvgpr_write_b32 a37, v217\n"
    "v_accvgpr_write_b32 a85, v221\n"
    "v_accvgpr_write_b32 a38, v218\n"
    "v_accvgpr_write_b32 a86, v222\n"
    "v_accvgpr_write_b32 a39, v219\n"
    "v_accvgpr_write_b32 a87, v223\n"
    "v_accvgpr_read_b32 v216, a40\n"
    "v_accvgpr_read_b32 v220, a88\n"
    "v_accvgpr_read_b32 v217, a41\n"
    "v_accvgpr_read_b32 v221, a89\n"
    "v_accvgpr_read_b32 v218, a42\n"
    "v_accvgpr_read_b32 v222, a90\n"
    "v_accvgpr_read_b32 v219, a43\n"
    "v_accvgpr_read_b32 v223, a91\n"
    "v_mul_f32 v216, v216, v245\n"
    "v_mul_f32 v220, v220, v247\n"
    "v_mul_f32 v217, v217, v245\n"
    "v_mul_f32 v221, v221, v247\n"
    "v_mul_f32 v218, v218, v245\n"
    "v_mul_f32 v222, v222, v247\n"
    "v_mul_f32 v219, v219, v245\n"
    "v_mul_f32 v223, v223, v247\n"
    "v_accvgpr_write_b32 a40, v216\n"
    "v_accvgpr_write_b32 a88, v220\n"
    "v_accvgpr_write_b32 a41, v217\n"
    "v_accvgpr_write_b32 a89, v221\n"
    "v_accvgpr_write_b32 a42, v218\n"
    "v_accvgpr_write_b32 a90, v222\n"
    "v_accvgpr_write_b32 a43, v219\n"
    "v_accvgpr_write_b32 a91, v223\n"
    "v_accvgpr_read_b32 v216, a44\n"
    "v_accvgpr_read_b32 v220, a92\n"
    "v_accvgpr_read_b32 v217, a45\n"
    "v_accvgpr_read_b32 v221, a93\n"
    "v_accvgpr_read_b32 v218, a46\n"
    "v_accvgpr_read_b32 v222, a94\n"
    "v_accvgpr_read_b32 v219, a47\n"
    "v_accvgpr_read_b32 v223, a95\n"
    "v_mul_f32 v216, v216, v245\n"
    "v_mul_f32 v220, v220, v247\n"
    "v_mul_f32 v217, v217, v245\n"
    "v_mul_f32 v221, v221, v247\n"
    "v_mul_f32 v218, v218, v245\n"
    "v_mul_f32 v222, v222, v247\n"
    "v_mul_f32 v219, v219, v245\n"
    "v_mul_f32 v223, v223, v247\n"
    "v_accvgpr_write_b32 a44, v216\n"
    "v_accvgpr_write_b32 a92, v220\n"
    "v_accvgpr_write_b32 a45, v217\n"
    "v_accvgpr_write_b32 a93, v221\n"
    "v_accvgpr_write_b32 a46, v218\n"
    "v_accvgpr_write_b32 a94, v222\n"
    "v_accvgpr_write_b32 a47, v219\n"
    "v_accvgpr_write_b32 a95, v223\n"
    "v_exp_f32 v216, v96\n"
    "v_exp_f32 v220, v104\n"
    "v_exp_f32 v217, v97\n"
    "v_exp_f32 v221, v105\n"
    "v_exp_f32 v218, v98\n"
    "v_exp_f32 v222, v106\n"
    "v_exp_f32 v219, v99\n"
    "v_exp_f32 v223, v107\n"
    "v_cvt_pk_bf16_f32 v160, v216, v217\n"
    "v_cvt_pk_bf16_f32 v164, v220, v221\n"
    "v_cvt_pk_bf16_f32 v161, v218, v219\n"
    "v_cvt_pk_bf16_f32 v165, v222, v223\n"
    "v_add_f32 v212, v216, v217\n"
    "v_add_f32 v213, v220, v221\n"
    "v_add_f32 v212, v212, v218\n"
    "v_add_f32 v213, v213, v222\n"
    "v_add_f32 v212, v212, v219\n"
    "v_add_f32 v213, v213, v223\n"
    "v_exp_f32 v216, v100\n"
    "v_exp_f32 v220, v108\n"
    "v_exp_f32 v217, v101\n"
    "v_exp_f32 v221, v109\n"
    "v_exp_f32 v218, v102\n"
    "v_exp_f32 v222, v110\n"
    "v_exp_f32 v219, v103\n"
    "v_exp_f32 v223, v111\n"
    "v_cvt_pk_bf16_f32 v162, v216, v217\n"
    "v_cvt_pk_bf16_f32 v166, v220, v221\n"
    "v_cvt_pk_bf16_f32 v163, v218, v219\n"
    "v_cvt_pk_bf16_f32 v167, v222, v223\n"
    "v_add_f32 v212, v212, v216\n"
    "v_add_f32 v213, v213, v220\n"
    "v_add_f32 v212, v212, v217\n"
    "v_add_f32 v213, v213, v221\n"
    "v_add_f32 v212, v212, v218\n"
    "v_add_f32 v213, v213, v222\n"
    "v_add_f32 v212, v212, v219\n"
    "v_add_f32 v213, v213, v223\n"
    "s_nop 3\n"
    "s_branch LRET_e_2_L_%=\n"
    "LRARE_o_2_%=:\n"
    "s_nop 7\n"
    "s_nop 7\n"
    "v_max3_f32 v244, v128, v129, v130\n"
    "v_max3_f32 v246, v136, v137, v138\n"
    "v_max3_f32 v244, v244, v131, v132\n"
    "v_max3_f32 v246, v246, v139, v140\n"
    "v_max3_f32 v244, v244, v133, v134\n"
    "v_max3_f32 v246, v246, v141, v142\n"
    "v_max_f32 v244, v244, v135\n"
    "v_max_f32 v246, v246, v143\n"
    "v_mov_b32 v245, v244\n"
    "v_mov_b32 v247, v246\n"
    "s_nop 0\n"
    "v_permlane32_swap_b32 v244, v245\n"
    "v_permlane32_swap_b32 v246, v247\n"
    "v_max_f32 v244, v244, v245\n"
    "v_max_f32 v246, v246, v247\n"
    "v_mov_b32 v245, v244\n"
    "v_mov_b32 v247, v246\n"
    "s_nop 0\n"
    "v_permlane16_swap_b32 v244, v245\n"
    "v_permlane16_swap_b32 v246, v247\n"
    "v_max_f32 v244, v244, v245\n"
    "v_max_f32 v246, v246, v247\n"
    "v_max_f32 v244, 0, v244\n"
    "v_max_f32 v246, 0, v246\n"
    "v_exp_f32 v245, -v244\n"
    "v_exp_f32 v247, -v246\n"
    "v_sub_f32 v128, v128, v244\n"
    "v_sub_f32 v136, v136, v246\n"
    "v_sub_f32 v129, v129, v244\n"
    "v_sub_f32 v137, v137, v246\n"
    "v_sub_f32 v130, v130, v244\n"
    "v_sub_f32 v138, v138, v246\n"
    "v_sub_f32 v131, v131, v244\n"
    "v_sub_f32 v139, v139, v246\n"
    "v_sub_f32 v132, v132, v244\n"
    "v_sub_f32 v140, v140, v246\n"
    "v_sub_f32 v133, v133, v244\n"
    "v_sub_f32 v141, v141, v246\n"
    "v_sub_f32 v134, v134, v244\n"
    "v_sub_f32 v142, v142, v246\n"
    "v_sub_f32 v135, v135, v244\n"
    "v_sub_f32 v143, v143, v246\n"
    "v_sub_f32 v96, v96, v244\n"
    "v_sub_f32 v104, v104, v246\n"
    "v_sub_f32 v97, v97, v244\n"
    "v_sub_f32 v105, v105, v246\n"
    "v_sub_f32 v98, v98, v244\n"
    "v_sub_f32 v106, v106, v246\n"
    "v_sub_f32 v99, v99, v244\n"
    "v_sub_f32 v107, v107, v246\n"
    "v_sub_f32 v100, v100, v244\n"
    "v_sub_f32 v108, v108, v246\n"
    "v_sub_f32 v101, v101, v244\n"
    "v_sub_f32 v109, v109, v246\n"
    "v_sub_f32 v102, v102, v244\n"
    "v_sub_f32 v110, v110, v246\n"
    "v_sub_f32 v103, v103, v244\n"
    "v_sub_f32 v111, v111, v246\n"
    "v_sub_f32 v192, v192, v244\n"
    "v_sub_f32 v196, v196, v246\n"
    "v_sub_f32 v193, v193, v244\n"
    "v_sub_f32 v197, v197, v246\n"
    "v_sub_f32 v194, v194, v244\n"
    "v_sub_f32 v198, v198, v246\n"
    "v_sub_f32 v195, v195, v244\n"
    "v_sub_f32 v199, v199, v246\n"
    "v_mul_f32 v208, v208, v245\n"
    "v_mul_f32 v209, v209, v247\n"
    "v_accvgpr_read_b32 v216, a0\n"
    "v_accvgpr_read_b32 v220, a48\n"
    "v_accvgpr_read_b32 v217, a1\n"
    "v_accvgpr_read_b32 v221, a49\n"
    "v_accvgpr_read_b32 v218, a2\n"
    "v_accvgpr_read_b32 v222, a50\n"
    "v_accvgpr_read_b32 v219, a3\n"
    "v_accvgpr_read_b32 v223, a51\n"
    "v_mul_f32 v216, v216, v245\n"
    "v_mul_f32 v220, v220, v247\n"
    "v_mul_f32 v217, v217, v245\n"
    "v_mul_f32 v221, v221, v247\n"
    "v_mul_f32 v218, v218, v245\n"
    "v_mul_f32 v222, v222, v247\n"
    "v_mul_f32 v219, v219, v245\n"
    "v_mul_f32 v223, v223, v247\n"
    "v_accvgpr_write_b32 a0, v216\n"
    "v_accvgpr_write_b32 a48, v220\n"
    "v_accvgpr_write_b32 a1, v217\n"
    "v_accvgpr_write_b32 a49, v221\n"
    "v_accvgpr_write_b32 a2, v218\n"
    "v_accvgpr_write_b32 a50, v222\n"
    "v_accvgpr_write_b32 a3, v219\n"
    "v_accvgpr_write_b32 a51, v223\n"
    "v_accvgpr_read_b32 v216, a4\n"
    "v_accvgpr_read_b32 v220, a52\n"
    "v_accvgpr_read_b32 v217, a5\n"
    "v_accvgpr_read_b32 v221, a53\n"
    "v_accvgpr_read_b32 v218, a6\n"
    "v_accvgpr_read_b32 v222, a54\n"
    "v_accvgpr_read_b32 v219, a7\n"
    "v_accvgpr_read_b32 v223, a55\n"
    "v_mul_f32 v216, v216, v245\n"
    "v_mul_f32 v220, v220, v247\n"
    "v_mul_f32 v217, v217, v245\n"
    "v_mul_f32 v221, v221, v247\n"
    "v_mul_f32 v218, v218, v245\n"
    "v_mul_f32 v222, v222, v247\n"
    "v_mul_f32 v219, v219, v245\n"
    "v_mul_f32 v223, v223, v247\n"
    "v_accvgpr_write_b32 a4, v216\n"
    "v_accvgpr_write_b32 a52, v220\n"
    "v_accvgpr_write_b32 a5, v217\n"
    "v_accvgpr_write_b32 a53, v221\n"
    "v_accvgpr_write_b32 a6, v218\n"
    "v_accvgpr_write_b32 a54, v222\n"
    "v_accvgpr_write_b32 a7, v219\n"
    "v_accvgpr_write_b32 a55, v223\n"
    "v_accvgpr_read_b32 v216, a8\n"
    "v_accvgpr_read_b32 v220, a56\n"
    "v_accvgpr_read_b32 v217, a9\n"
    "v_accvgpr_read_b32 v221, a57\n"
    "v_accvgpr_read_b32 v218, a10\n"
    "v_accvgpr_read_b32 v222, a58\n"
    "v_accvgpr_read_b32 v219, a11\n"
    "v_accvgpr_read_b32 v223, a59\n"
    "v_mul_f32 v216, v216, v245\n"
    "v_mul_f32 v220, v220, v247\n"
    "v_mul_f32 v217, v217, v245\n"
    "v_mul_f32 v221, v221, v247\n"
    "v_mul_f32 v218, v218, v245\n"
    "v_mul_f32 v222, v222, v247\n"
    "v_mul_f32 v219, v219, v245\n"
    "v_mul_f32 v223, v223, v247\n"
    "v_accvgpr_write_b32 a8, v216\n"
    "v_accvgpr_write_b32 a56, v220\n"
    "v_accvgpr_write_b32 a9, v217\n"
    "v_accvgpr_write_b32 a57, v221\n"
    "v_accvgpr_write_b32 a10, v218\n"
    "v_accvgpr_write_b32 a58, v222\n"
    "v_accvgpr_write_b32 a11, v219\n"
    "v_accvgpr_write_b32 a59, v223\n"
    "v_accvgpr_read_b32 v216, a12\n"
    "v_accvgpr_read_b32 v220, a60\n"
    "v_accvgpr_read_b32 v217, a13\n"
    "v_accvgpr_read_b32 v221, a61\n"
    "v_accvgpr_read_b32 v218, a14\n"
    "v_accvgpr_read_b32 v222, a62\n"
    "v_accvgpr_read_b32 v219, a15\n"
    "v_accvgpr_read_b32 v223, a63\n"
    "v_mul_f32 v216, v216, v245\n"
    "v_mul_f32 v220, v220, v247\n"
    "v_mul_f32 v217, v217, v245\n"
    "v_mul_f32 v221, v221, v247\n"
    "v_mul_f32 v218, v218, v245\n"
    "v_mul_f32 v222, v222, v247\n"
    "v_mul_f32 v219, v219, v245\n"
    "v_mul_f32 v223, v223, v247\n"
    "v_accvgpr_write_b32 a12, v216\n"
    "v_accvgpr_write_b32 a60, v220\n"
    "v_accvgpr_write_b32 a13, v217\n"
    "v_accvgpr_write_b32 a61, v221\n"
    "v_accvgpr_write_b32 a14, v218\n"
    "v_accvgpr_write_b32 a62, v222\n"
    "v_accvgpr_write_b32 a15, v219\n"
    "v_accvgpr_write_b32 a63, v223\n"
    "v_accvgpr_read_b32 v216, a16\n"
    "v_accvgpr_read_b32 v220, a64\n"
    "v_accvgpr_read_b32 v217, a17\n"
    "v_accvgpr_read_b32 v221, a65\n"
    "v_accvgpr_read_b32 v218, a18\n"
    "v_accvgpr_read_b32 v222, a66\n"
    "v_accvgpr_read_b32 v219, a19\n"
    "v_accvgpr_read_b32 v223, a67\n"
    "v_mul_f32 v216, v216, v245\n"
    "v_mul_f32 v220, v220, v247\n"
    "v_mul_f32 v217, v217, v245\n"
    "v_mul_f32 v221, v221, v247\n"
    "v_mul_f32 v218, v218, v245\n"
    "v_mul_f32 v222, v222, v247\n"
    "v_mul_f32 v219, v219, v245\n"
    "v_mul_f32 v223, v223, v247\n"
    "v_accvgpr_write_b32 a16, v216\n"
    "v_accvgpr_write_b32 a64, v220\n"
    "v_accvgpr_write_b32 a17, v217\n"
    "v_accvgpr_write_b32 a65, v221\n"
    "v_accvgpr_write_b32 a18, v218\n"
    "v_accvgpr_write_b32 a66, v222\n"
    "v_accvgpr_write_b32 a19, v219\n"
    "v_accvgpr_write_b32 a67, v223\n"
    "v_accvgpr_read_b32 v216, a20\n"
    "v_accvgpr_read_b32 v220, a68\n"
    "v_accvgpr_read_b32 v217, a21\n"
    "v_accvgpr_read_b32 v221, a69\n"
    "v_accvgpr_read_b32 v218, a22\n"
    "v_accvgpr_read_b32 v222, a70\n"
    "v_accvgpr_read_b32 v219, a23\n"
    "v_accvgpr_read_b32 v223, a71\n"
    "v_mul_f32 v216, v216, v245\n"
    "v_mul_f32 v220, v220, v247\n"
    "v_mul_f32 v217, v217, v245\n"
    "v_mul_f32 v221, v221, v247\n"
    "v_mul_f32 v218, v218, v245\n"
    "v_mul_f32 v222, v222, v247\n"
    "v_mul_f32 v219, v219, v245\n"
    "v_mul_f32 v223, v223, v247\n"
    "v_accvgpr_write_b32 a20, v216\n"
    "v_accvgpr_write_b32 a68, v220\n"
    "v_accvgpr_write_b32 a21, v217\n"
    "v_accvgpr_write_b32 a69, v221\n"
    "v_accvgpr_write_b32 a22, v218\n"
    "v_accvgpr_write_b32 a70, v222\n"
    "v_accvgpr_write_b32 a23, v219\n"
    "v_accvgpr_write_b32 a71, v223\n"
    "v_accvgpr_read_b32 v216, a24\n"
    "v_accvgpr_read_b32 v220, a72\n"
    "v_accvgpr_read_b32 v217, a25\n"
    "v_accvgpr_read_b32 v221, a73\n"
    "v_accvgpr_read_b32 v218, a26\n"
    "v_accvgpr_read_b32 v222, a74\n"
    "v_accvgpr_read_b32 v219, a27\n"
    "v_accvgpr_read_b32 v223, a75\n"
    "v_mul_f32 v216, v216, v245\n"
    "v_mul_f32 v220, v220, v247\n"
    "v_mul_f32 v217, v217, v245\n"
    "v_mul_f32 v221, v221, v247\n"
    "v_mul_f32 v218, v218, v245\n"
    "v_mul_f32 v222, v222, v247\n"
    "v_mul_f32 v219, v219, v245\n"
    "v_mul_f32 v223, v223, v247\n"
    "v_accvgpr_write_b32 a24, v216\n"
    "v_accvgpr_write_b32 a72, v220\n"
    "v_accvgpr_write_b32 a25, v217\n"
    "v_accvgpr_write_b32 a73, v221\n"
    "v_accvgpr_write_b32 a26, v218\n"
    "v_accvgpr_write_b32 a74, v222\n"
    "v_accvgpr_write_b32 a27, v219\n"
    "v_accvgpr_write_b32 a75, v223\n"
    "v_accvgpr_read_b32 v216, a28\n"
    "v_accvgpr_read_b32 v220, a76\n"
    "v_accvgpr_read_b32 v217, a29\n"
    "v_accvgpr_read_b32 v221, a77\n"
    "v_accvgpr_read_b32 v218, a30\n"
    "v_accvgpr_read_b32 v222, a78\n"
    "v_accvgpr_read_b32 v219, a31\n"
    "v_accvgpr_read_b32 v223, a79\n"
    "v_mul_f32 v216, v216, v245\n"
    "v_mul_f32 v220, v220, v247\n"
    "v_mul_f32 v217, v217, v245\n"
    "v_mul_f32 v221, v221, v247\n"
    "v_mul_f32 v218, v218, v245\n"
    "v_mul_f32 v222, v222, v247\n"
    "v_mul_f32 v219, v219, v245\n"
    "v_mul_f32 v223, v223, v247\n"
    "v_accvgpr_write_b32 a28, v216\n"
    "v_accvgpr_write_b32 a76, v220\n"
    "v_accvgpr_write_b32 a29, v217\n"
    "v_accvgpr_write_b32 a77, v221\n"
    "v_accvgpr_write_b32 a30, v218\n"
    "v_accvgpr_write_b32 a78, v222\n"
    "v_accvgpr_write_b32 a31, v219\n"
    "v_accvgpr_write_b32 a79, v223\n"
    "v_accvgpr_read_b32 v216, a32\n"
    "v_accvgpr_read_b32 v220, a80\n"
    "v_accvgpr_read_b32 v217, a33\n"
    "v_accvgpr_read_b32 v221, a81\n"
    "v_accvgpr_read_b32 v218, a34\n"
    "v_accvgpr_read_b32 v222, a82\n"
    "v_accvgpr_read_b32 v219, a35\n"
    "v_accvgpr_read_b32 v223, a83\n"
    "v_mul_f32 v216, v216, v245\n"
    "v_mul_f32 v220, v220, v247\n"
    "v_mul_f32 v217, v217, v245\n"
    "v_mul_f32 v221, v221, v247\n"
    "v_mul_f32 v218, v218, v245\n"
    "v_mul_f32 v222, v222, v247\n"
    "v_mul_f32 v219, v219, v245\n"
    "v_mul_f32 v223, v223, v247\n"
    "v_accvgpr_write_b32 a32, v216\n"
    "v_accvgpr_write_b32 a80, v220\n"
    "v_accvgpr_write_b32 a33, v217\n"
    "v_accvgpr_write_b32 a81, v221\n"
    "v_accvgpr_write_b32 a34, v218\n"
    "v_accvgpr_write_b32 a82, v222\n"
    "v_accvgpr_write_b32 a35, v219\n"
    "v_accvgpr_write_b32 a83, v223\n"
    "v_accvgpr_read_b32 v216, a36\n"
    "v_accvgpr_read_b32 v220, a84\n"
    "v_accvgpr_read_b32 v217, a37\n"
    "v_accvgpr_read_b32 v221, a85\n"
    "v_accvgpr_read_b32 v218, a38\n"
    "v_accvgpr_read_b32 v222, a86\n"
    "v_accvgpr_read_b32 v219, a39\n"
    "v_accvgpr_read_b32 v223, a87\n"
    "v_mul_f32 v216, v216, v245\n"
    "v_mul_f32 v220, v220, v247\n"
    "v_mul_f32 v217, v217, v245\n"
    "v_mul_f32 v221, v221, v247\n"
    "v_mul_f32 v218, v218, v245\n"
    "v_mul_f32 v222, v222, v247\n"
    "v_mul_f32 v219, v219, v245\n"
    "v_mul_f32 v223, v223, v247\n"
    "v_accvgpr_write_b32 a36, v216\n"
    "v_accvgpr_write_b32 a84, v220\n"
    "v_accvgpr_write_b32 a37, v217\n"
    "v_accvgpr_write_b32 a85, v221\n"
    "v_accvgpr_write_b32 a38, v218\n"
    "v_accvgpr_write_b32 a86, v222\n"
    "v_accvgpr_write_b32 a39, v219\n"
    "v_accvgpr_write_b32 a87, v223\n"
    "v_accvgpr_read_b32 v216, a40\n"
    "v_accvgpr_read_b32 v220, a88\n"
    "v_accvgpr_read_b32 v217, a41\n"
    "v_accvgpr_read_b32 v221, a89\n"
    "v_accvgpr_read_b32 v218, a42\n"
    "v_accvgpr_read_b32 v222, a90\n"
    "v_accvgpr_read_b32 v219, a43\n"
    "v_accvgpr_read_b32 v223, a91\n"
    "v_mul_f32 v216, v216, v245\n"
    "v_mul_f32 v220, v220, v247\n"
    "v_mul_f32 v217, v217, v245\n"
    "v_mul_f32 v221, v221, v247\n"
    "v_mul_f32 v218, v218, v245\n"
    "v_mul_f32 v222, v222, v247\n"
    "v_mul_f32 v219, v219, v245\n"
    "v_mul_f32 v223, v223, v247\n"
    "v_accvgpr_write_b32 a40, v216\n"
    "v_accvgpr_write_b32 a88, v220\n"
    "v_accvgpr_write_b32 a41, v217\n"
    "v_accvgpr_write_b32 a89, v221\n"
    "v_accvgpr_write_b32 a42, v218\n"
    "v_accvgpr_write_b32 a90, v222\n"
    "v_accvgpr_write_b32 a43, v219\n"
    "v_accvgpr_write_b32 a91, v223\n"
    "v_accvgpr_read_b32 v216, a44\n"
    "v_accvgpr_read_b32 v220, a92\n"
    "v_accvgpr_read_b32 v217, a45\n"
    "v_accvgpr_read_b32 v221, a93\n"
    "v_accvgpr_read_b32 v218, a46\n"
    "v_accvgpr_read_b32 v222, a94\n"
    "v_accvgpr_read_b32 v219, a47\n"
    "v_accvgpr_read_b32 v223, a95\n"
    "v_mul_f32 v216, v216, v245\n"
    "v_mul_f32 v220, v220, v247\n"
    "v_mul_f32 v217, v217, v245\n"
    "v_mul_f32 v221, v221, v247\n"
    "v_mul_f32 v218, v218, v245\n"
    "v_mul_f32 v222, v222, v247\n"
    "v_mul_f32 v219, v219, v245\n"
    "v_mul_f32 v223, v223, v247\n"
    "v_accvgpr_write_b32 a44, v216\n"
    "v_accvgpr_write_b32 a92, v220\n"
    "v_accvgpr_write_b32 a45, v217\n"
    "v_accvgpr_write_b32 a93, v221\n"
    "v_accvgpr_write_b32 a46, v218\n"
    "v_accvgpr_write_b32 a94, v222\n"
    "v_accvgpr_write_b32 a47, v219\n"
    "v_accvgpr_write_b32 a95, v223\n"
    "v_exp_f32 v216, v128\n"
    "v_exp_f32 v220, v136\n"
    "v_exp_f32 v217, v129\n"
    "v_exp_f32 v221, v137\n"
    "v_exp_f32 v218, v130\n"
    "v_exp_f32 v222, v138\n"
    "v_exp_f32 v219, v131\n"
    "v_exp_f32 v223, v139\n"
    "v_cvt_pk_bf16_f32 v176, v216, v217\n"
    "v_cvt_pk_bf16_f32 v180, v220, v221\n"
    "v_cvt_pk_bf16_f32 v177, v218, v219\n"
    "v_cvt_pk_bf16_f32 v181, v222, v223\n"
    "v_add_f32 v212, v216, v217\n"
    "v_add_f32 v213, v220, v221\n"
    "v_add_f32 v212, v212, v218\n"
    "v_add_f32 v213, v213, v222\n"
    "v_add_f32 v212, v212, v219\n"
    "v_add_f32 v213, v213, v223\n"
    "v_exp_f32 v216, v132\n"
    "v_exp_f32 v220, v140\n"
    "v_exp_f32 v217, v133\n"
    "v_exp_f32 v221, v141\n"
    "v_exp_f32 v218, v134\n"
    "v_exp_f32 v222, v142\n"
    "v_exp_f32 v219, v135\n"
    "v_exp_f32 v223, v143\n"
    "v_cvt_pk_bf16_f32 v178, v216, v217\n"
    "v_cvt_pk_bf16_f32 v182, v220, v221\n"
    "v_cvt_pk_bf16_f32 v179, v218, v219\n"
    "v_cvt_pk_bf16_f32 v183, v222, v223\n"
    "v_add_f32 v212, v212, v216\n"
    "v_add_f32 v213, v213, v220\n"
    "v_add_f32 v212, v212, v217\n"
    "v_add_f32 v213, v213, v221\n"
    "v_add_f32 v212, v212, v218\n"
    "v_add_f32 v213, v213, v222\n"
    "v_add_f32 v212, v212, v219\n"
    "v_add_f32 v213, v213, v223\n"
    "s_nop 3\n"
    "s_cmp_eq_u32 s57, 0\n"
    "s_cbranch_scc1 LRET_o_2_L_%=\n"
    "s_cmp_eq_u32 s57, 1\n"
    "s_cbranch_scc1 LRET_o_2_F0_%=\n"
    "s_cmp_eq_u32 s57, 2\n"
    "s_cbranch_scc1 LRET_o_2_F1_%=\n"
    "s_branch LRET_o_2_P0_%=\n"
    "LEND_%=:\n"
    "s_waitcnt vmcnt(0) lgkmcnt(0)\n"
    :
    : "s"(__builtin_amdgcn_kernarg_segment_ptr()), "s"(blockIdx.x), "s"(wave), "s"(ldsb)
    : "v0","v1","v2","v3","v4","v5","v6","v7","v8","v9","v10","v11","v12","v13","v14","v15","v16","v17","v18","v19","v20","v21","v22","v23","v24","v25","v26","v27","v28","v29","v30","v31","v32","v33","v34","v35","v36","v37","v38","v39","v40","v41","v42","v43","v44","v45","v46","v47","v48","v49","v50","v51","v52","v53","v54","v55","v56","v57","v58","v59","v60","v61","v62","v63","v64","v65","v66","v67","v68","v69","v70","v71","v72","v73","v74","v75","v76","v77","v78","v79","v80","v81","v82","v83","v84","v85","v86","v87","v88","v89","v90","v91","v92","v93","v94","v95","v96","v97","v98","v99","v100","v101","v102","v103","v104","v105","v106","v107","v108","v109","v110","v111","v112","v113","v114","v115","v116","v117","v118","v119","v120","v121","v122","v123","v124","v125","v126","v127","v128","v129","v130","v131","v132","v133","v134","v135","v136","v137","v138","v139","v140","v141","v142","v143","v144","v145","v146","v147","v148","v149","v150","v151","v152","v153","v154","v155","v156","v157","v158","v159","v160","v161","v162","v163","v164","v165","v166","v167","v168","v169","v170","v171","v172","v173","v174","v175","v176","v177","v178","v179","v180","v181","v182","v183","v184","v185","v186","v187","v188","v189","v190","v191","v192","v193","v194","v195","v196","v197","v198","v199","v200","v201","v202","v203","v204","v205","v206","v207","v208","v209","v210","v211","v212","v213","v214","v215","v216","v217","v218","v219","v220","v221","v222","v223","v224","v225","v226","v227","v228","v229","v230","v231","v232","v233","v234","v235","v236","v237","v238","v239","v240","v241","v242","v243","v244","v245","v246","v247","v248","v249","v250","v251","v252","v253","v254","v255","a0","a1","a2","a3","a4","a5","a6","a7","a8","a9","a10","a11","a12","a13","a14","a15","a16","a17","a18","a19","a20","a21","a22","a23","a24","a25","a26","a27","a28","a29","a30","a31","a32","a33","a34","a35","a36","a37","a38","a39","a40","a41","a42","a43","a44","a45","a46","a47","a48","a49","a50","a51","a52","a53","a54","a55","a56","a57","a58","a59","a60","a61","a62","a63","a64","a65","a66","a67","a68","a69","a70","a71","a72","a73","a74","a75","a76","a77","a78","a79","a80","a81","a82","a83","a84","a85","a86","a87","a88","a89","a90","a91","a92","a93","a94","a95","a96","a97","a98","a99","a100","a101","a102","a103","a104","a105","a106","a107","a108","a109","a110","a111","a112","a113","a114","a115","a116","a117","a118","a119","a120","a121","a122","a123","a124","a125","a126","a127","a128","a129","a130","a131","a132","a133","a134","a135","a136","a137","a138","a139","a140","a141","a142","a143","a144","a145","a146","a147","a148","a149","a150","a151","a152","a153","a154","a155","a156","a157","a158","a159","a160","a161","a162","a163","a164","a165","a166","a167","a168","a169","a170","a171","a172","a173","a174","a175","a176","a177","a178","a179","a180","a181","a182","a183","a184","a185","a186","a187","a188","a189","a190","a191","a192","a193","a194","a195","a196","a197","a198","a199","a200","a201","a202","a203","a204","a205","a206","a207","a208","a209","a210","a211","a212","a213","a214","a215","a216","a217","a218","a219","a220","a221","a222","a223","a224","a225","a226","a227","a228","a229","a230","a231","a232","a233","a234","a235","a236","a237","a238","a239","a240","a241","a242","a243","a244","a245","a246","a247","a248","a249","a250","a251","a252","a253","a254","a255","s8","s9","s10","s11","s12","s13","s14","s15","s16","s17","s18","s19","s20","s21","s22","s23","s24","s25","s26","s27","s28","s29","s30","s31","s34","s35","s36","s37","s38","s39","s40","s41","s42","s43","s44","s45","s46","s47","s48","s49","s50","s51","s52","s53","s54","s55","s56","s57","s58","s59","s60","s61","s62","s63","s64","s65","s66","s67","s68","s69","s70","s71","s72","s73","s74","s75","s76","s77","s78","s79","s80","s81","s82","s83","s84","s85","s86","s87","s88","s89","s90","s91","s92","s93","s94","s95","vcc","scc","memory");
}

extern "C" void kernel_launch(void* const* d_in, const int* in_sizes, int n_in, void* d_out, int out_size, void* d_ws, size_t ws_size,
                              hipStream_t stream) {
  attn_fwd_d192<<<dim3(256), dim3(256), 0, stream>>>(d_in[0], d_in[1], d_in[2], d_out);
}
```
